```python
import torch

def _softmax(x, axis=-1):
    return torch.softmax(x, dim=axis)
def _gelu(x):
    return torch.nn.functional.gelu(x, approximate="tanh")
def _silu(x):
    return torch.nn.functional.silu(x)
def _sigmoid(x):
    return torch.sigmoid(x)
def _layernorm(x, axis=-1, eps=1e-5):
    m = x.mean(dim=axis, keepdim=True)
    v = x.var(dim=axis, keepdim=True, unbiased=False)
    return (x - m) / torch.sqrt(v + eps)
def _scatter_add(tgt, idx, src, axis=0):
    return tgt.clone().index_add_(axis, idx, src)
def _segment_sum(src, seg, n):
    out = torch.zeros((n,) + src.shape[1:], dtype=src.dtype)
    return out.index_add_(0, seg, src)
def _pad(x, pw):
    flat = tuple(v for pair in reversed(pw) for v in pair)
    return torch.nn.functional.pad(x, flat)

kernel_name = 'standalone__structured_sparse_n_m__ragged'

def setup_inputs():

    _w = (lambda a: (lambda t: t.to(torch.bfloat16) if t.is_floating_point() else t)(torch.as_tensor(a)))
    B, S, D = 16, 4096, 576
    assert D % 4 == 0, "d_model must be divisible by M=4 for 2:4 sparsity"
    return {
        "x": _w((lambda *s: torch.randn(*s, dtype=torch.bfloat16) * 0.05)(B, S, D)),
        "w": _w((lambda *s: torch.randn(*s, dtype=torch.bfloat16) * 0.05)(D, D)),
        "b": _w((lambda *s: torch.randn(*s, dtype=torch.bfloat16) * 0.05)(D)),
    }

def reference(x, w, b):
    B, S, D = x.shape
    N, M = 2, 4
    G = D // M
    wg = w.reshape(G, M, D)
    mag = torch.abs(wg)
    order = torch.argsort(mag, 1)
    thr = (lambda x, i, axis: torch.take_along_dim(x, i, dim=axis))(mag, order[:, M - N:M - N + 1, :], 1)
    mask = (lambda x, dt: x.to(dt))(mag >= thr, w.dtype)
    wp = (wg * mask).reshape(D, D)
    return torch.einsum("bsd,df->bsf", x, wp) + b
```

```cpp
#include <hip/hip_runtime.h>
#include <hip/hip_bf16.h>
#include <stdint.h>

namespace {
#define SPMM_ASM \
  ".Lstart%=:\n\t" \
  "s_load_dwordx8 s[4:11], %[sKA], 0\n\t" \
  "s_load_dwordx2 s[12:13], %[sKA], 0x20\n\t" \
  "s_memrealtime s[72:73]\n\t" \
  "s_getpc_b64 s[68:69]\n\t" \
  ".Lpc%=:\n\t" \
  "v_readfirstlane_b32 s24, v0\n\t" \
  "s_lshr_b32 s59, %[sBID], 6\n\t" \
  "s_and_b32 s25, %[sBID], 31\n\t" \
  "s_lshl_b32 s59, s59, 5\n\t" \
  "s_or_b32 s59, s59, s25\n\t" \
  "s_and_b32 s25, %[sBID], 32\n\t" \
  "s_lshl2_add_u32 s59, s25, s59\n\t" \
  "s_lshr_b32 s24, s24, 6\n\t" \
  "v_mov_b32 v1, 0\n\t" \
  "s_cmp_gt_u32 s59, 81\n\t" \
  "s_cbranch_scc1 .Le1%=\n\t" \
  "s_lshr_b32 s25, s59, 1\n\t" \
  "s_mul_i32 s38, s25, 57\n\t" \
  "s_lshr_b32 s38, s38, 9\n\t" \
  "s_mul_i32 s29, s38, -9\n\t" \
  "s_add_u32 s29, s29, s25\n\t" \
  "s_and_b32 s25, s59, 1\n\t" \
  "s_and_b32 s36, s24, 1\n\t" \
  "s_lshl1_add_u32 s36, s25, s36\n\t" \
  "s_cmp_gt_u32 s59, 79\n\t" \
  "s_cselect_b32 s42, 0, 73728\n\t" \
  "s_cmp_gt_u32 s59, 71\n\t" \
  "s_cselect_b32 s37, 64, 128\n\t" \
  "s_cselect_b32 s40, 8, 64\n\t" \
  "s_cselect_b32 s42, s42, 8\n\t" \
  "s_cselect_b32 s25, 3, 2\n\t" \
  "s_lshl_b32 s25, s29, s25\n\t" \
  "s_add_u32 s25, s25, s36\n\t" \
  "s_mul_i32 s25, s25, 18432\n\t" \
  "s_lshr_b32 s26, s24, 1\n\t" \
  "s_lshl3_add_u32 s26, s38, s26\n\t" \
  "s_lshl_b32 s26, s26, 5\n\t" \
  "s_add_u32 s25, s25, s26\n\t" \
  "s_add_u32 s25, s25, 4032\n\t" \
  "v_and_b32 v2, 2, v0\n\t" \
  "v_and_b32 v3, 4, v0\n\t" \
  "v_lshl_add_u32 v2, v3, 1, v2\n\t" \
  "v_lshl_add_u32 v2, v2, 1, s25\n\t" \
  "v_and_b32 v3, 1, v0\n\t" \
  "s_movk_i32 s26, 9216\n\t" \
  "v_mad_u32_u24 v2, v3, s26, v2\n\t" \
  "v_bfe_u32 v3, v0, 5, 1\n\t" \
  "v_mad_u32_u24 v2, v3, s37, v2\n\t" \
  "v_bfe_u32 v3, v0, 4, 1\n\t" \
  "v_mad_u32_u24 v2, v3, s40, v2\n\t" \
  "v_bfe_u32 v3, v0, 3, 1\n\t" \
  "v_mad_u32_u24 v2, v3, s42, v2\n\t" \
  "s_waitcnt lgkmcnt(0)\n\t" \
  "global_load_dwordx2 v[142:143], v1, s[12:13] sc1\n\t" \
  "global_load_dword v72, v2, s[6:7] offset:-4032\n\t" \
  "global_load_dword v73, v2, s[6:7] offset:-2880\n\t" \
  "global_load_dword v74, v2, s[6:7] offset:-1728\n\t" \
  "global_load_dword v75, v2, s[6:7] offset:-576\n\t" \
  "global_load_dword v76, v2, s[6:7] offset:576\n\t" \
  "global_load_dword v77, v2, s[6:7] offset:1728\n\t" \
  "global_load_dword v78, v2, s[6:7] offset:2880\n\t" \
  "global_load_dword v79, v2, s[6:7] offset:4032\n\t" \
  "s_mul_i32 s25, s38, 81\n\t" \
  "s_mul_i32 s26, s29, 9\n\t" \
  "s_add_u32 s25, s25, s26\n\t" \
  "s_lshl1_add_u32 s26, s26, 8\n\t" \
  "s_mul_i32 s27, s38, 10\n\t" \
  "s_add_u32 s27, s27, s29\n\t" \
  "s_lshl_b32 s27, s27, 10\n\t" \
  "s_add_u32 s27, s27, 1024\n\t" \
  "s_mul_i32 s28, s29, 10240\n\t" \
  "s_cmp_gt_u32 s59, 71\n\t" \
  "s_cselect_b32 s25, s26, s25\n\t" \
  "s_cselect_b32 s27, s28, s27\n\t" \
  "s_movk_i32 s37, 2048\n\t" \
  "s_cmov_b32 s37, 82944\n\t" \
  "s_movk_i32 s42, 1024\n\t" \
  "s_cmovk_i32 s42, 9216\n\t" \
  "s_cmp_gt_u32 s59, 79\n\t" \
  "s_cmovk_i32 s42, 0\n\t" \
  "s_lshl_b32 s25, s25, 10\n\t" \
  "s_lshr_b32 s26, s24, 1\n\t" \
  "s_lshl1_add_u32 s26, s36, s26\n\t" \
  "s_lshl_b32 s26, s26, 7\n\t" \
  "v_and_b32 v2, 7, v0\n\t" \
  "v_lshl_add_u32 v2, v2, 4, s26\n\t" \
  "v_bfe_u32 v4, v0, 4, 2\n\t" \
  "v_mad_u32_u24 v146, v4, s37, v2\n\t" \
  "v_mad_u32_u24 v146, v3, s42, v146\n\t" \
  "v_lshl_add_u32 v147, v4, 2, v2\n\t" \
  "s_add_u32 s25, s25, 30720\n\t" \
  "s_add_u32 s27, s27, 362496\n\t" \
  "v_add_u32 v146, s25, v146\n\t" \
  "v_add_u32 v147, s27, v147\n\t" \
  "s_branch .Le3%=\n\t" \
  ".Le1%=:\n\t" \
  "s_cmp_ge_u32 s59, 109\n\t" \
  "s_cbranch_scc1 .Le2%=\n\t" \
  "s_sub_u32 s25, s59, 82\n\t" \
  "s_mul_i32 s55, s25, 86\n\t" \
  "s_lshr_b32 s55, s55, 8\n\t" \
  "s_mul_i32 s56, s55, -3\n\t" \
  "s_add_u32 s56, s56, s25\n\t" \
  "s_lshl2_add_u32 s25, s56, s24\n\t" \
  "s_mul_i32 s25, s25, 55296\n\t" \
  "s_lshl_b32 s26, s55, 7\n\t" \
  "s_add_u32 s25, s25, s26\n\t" \
  "v_and_b32 v2, 7, v0\n\t" \
  "v_lshl_add_u32 v2, v2, 4, s25\n\t" \
  "v_bfe_u32 v3, v0, 3, 3\n\t" \
  "s_movk_i32 s26, 1152\n\t" \
  "v_mad_u32_u24 v2, v3, s26, v2\n\t" \
  "s_mul_i32 s25, s24, 6528\n\t" \
  "s_waitcnt lgkmcnt(0)\n\t" \
  "global_load_dwordx2 v[142:143], v1, s[12:13] sc1\n\t" \
  "s_add_u32 m0, s25, 131584\n\t" \
  "s_nop 0\n\t" \
  "global_load_lds_dwordx4 v2, s[6:7]\n\t" \
  "s_add_u32 m0, s25, 132672\n\t" \
  "v_add_u32 v2, 9216, v2\n\t" \
  "global_load_lds_dwordx4 v2, s[6:7]\n\t" \
  "s_add_u32 m0, s25, 133760\n\t" \
  "v_add_u32 v2, 9216, v2\n\t" \
  "global_load_lds_dwordx4 v2, s[6:7]\n\t" \
  "s_add_u32 m0, s25, 134848\n\t" \
  "v_add_u32 v2, 9216, v2\n\t" \
  "global_load_lds_dwordx4 v2, s[6:7]\n\t" \
  "s_add_u32 m0, s25, 135936\n\t" \
  "v_add_u32 v2, 9216, v2\n\t" \
  "global_load_lds_dwordx4 v2, s[6:7]\n\t" \
  "s_add_u32 m0, s25, 137024\n\t" \
  "v_add_u32 v2, 9216, v2\n\t" \
  "global_load_lds_dwordx4 v2, s[6:7]\n\t" \
  "s_branch .Le3%=\n\t" \
  ".Le2%=:\n\t" \
  "s_cmp_lt_u32 s59, 160\n\t" \
  "s_cbranch_scc0 .Lnp%=\n\t" \
  "v_lshlrev_b32 v2, 7, v0\n\t" \
  "v_add_u32 v3, 0x8000, v2\n\t" \
  "v_min_u32 v3, .Lstop%=-.Lpc%=-16, v3\n\t" \
  "global_load_dword v2, v2, s[68:69]\n\t" \
  "global_load_dword v3, v3, s[68:69]\n\t" \
  ".Lnp%=:\n\t" \
  "s_waitcnt lgkmcnt(0)\n\t" \
  "global_load_dwordx2 v[142:143], v1, s[12:13] sc1\n\t" \
  ".Le3%=:\n\t" \
  "s_memrealtime s[70:71]\n\t" \
  "v_and_b32 v6, 63, v0\n\t" \
  "v_lshlrev_b32 v187, 4, v6\n\t" \
  "s_lshr_b32 s25, s24, 1\n\t" \
  "s_and_b32 s26, s24, 1\n\t" \
  "s_lshl3_add_u32 s41, s25, s26\n\t" \
  "s_lshl_b32 s41, s41, 3\n\t" \
  "s_lshl_b32 s39, s24, 8\n\t" \
  "v_and_b32 v7, 56, v0\n\t" \
  "v_and_b32 v4, 4, v0\n\t" \
  "v_lshl_add_u32 v7, v7, 2, v4\n\t" \
  "v_lshl_add_u32 v7, v7, 1, s39\n\t" \
  "s_add_u32 s27, s41, 1024\n\t" \
  "s_mov_b32 exec_lo, 0\n\t" \
  "v_mov_b32 v7, s27\n\t" \
  "s_mov_b32 exec_lo, -1\n\t" \
  "v_and_b32 v4, 48, v187\n\t" \
  "v_add_u32 v7, v7, v4\n\t" \
  "global_load_dwordx2 v[144:145], v7, s[8:9]\n\t" \
  "v_bfe_u32 v7, v0, 3, 3\n\t" \
  "v_lshl_add_u32 v7, s25, 3, v7\n\t" \
  "v_lshrrev_b32 v4, 1, v7\n\t" \
  "v_xor_b32 v4, v4, v0\n\t" \
  "v_and_b32 v4, 7, v4\n\t" \
  "s_mul_i32 s27, s26, 640\n\t" \
  "v_lshl_add_u32 v4, v4, 4, s27\n\t" \
  "s_movk_i32 s28, 1152\n\t" \
  "v_mad_u32_u24 v186, v7, s28, v4\n\t" \
  "s_lshl_b32 s32, s25, 10\n\t" \
  "s_lshl4_add_u32 s32, s27, s32\n\t" \
  "s_mov_b32 s33, s32\n\t" \
  "s_mov_b32 s31, 0\n\t" \
  "s_mov_b32 s16, s4\n\t" \
  "s_and_b32 s17, s5, 0xffff\n\t" \
  "s_mov_b32 s18, 75497472\n\t" \
  "s_mov_b32 s19, 0x20000\n\t" \
  "s_mul_i32 s34, %[sBID], 18432\n\t" \
  "s_mov_b32 s35, s34\n\t" \
  "s_and_b32 s13, s13, 0xffff\n\t" \
  "s_mov_b32 s14, 740352\n\t" \
  "s_mov_b32 s15, s19\n\t" \
  "s_waitcnt lgkmcnt(0)\n\t" \
  "s_add_u32 s53, s70, 195\n\t" \
  "s_cmp_lt_u32 s59, 82\n\t" \
  "s_cbranch_scc1 .Le5%=\n\t" \
  "s_cmp_lt_u32 s59, 109\n\t" \
  "s_cselect_b32 s61, 1, 2\n\t" \
  "s_add_u32 s74, s68, .Le5%=-.Lpc%=\n\t" \
  "s_addc_u32 s75, s69, 0\n\t" \
  ".Lxs%=:\n\t" \
  "s_add_u32 m0, s33, 0\n\t" \
  "s_nop 0\n\t" \
  "buffer_load_dwordx4 v186, s[16:19], s34 offen offset:0 nt lds\n\t" \
  "s_add_u32 m0, s33, 1920\n\t" \
  "s_nop 0\n\t" \
  "buffer_load_dwordx4 v186, s[16:19], s34 offen offset:128 nt lds\n\t" \
  "s_add_u32 m0, s33, 3840\n\t" \
  "s_nop 0\n\t" \
  "buffer_load_dwordx4 v186, s[16:19], s34 offen offset:256 nt lds\n\t" \
  "s_add_u32 m0, s33, 5760\n\t" \
  "s_nop 0\n\t" \
  "buffer_load_dwordx4 v186, s[16:19], s34 offen offset:384 nt lds\n\t" \
  "s_bitcmp1_b32 s24, 0\n\t" \
  "s_cbranch_scc1 .Lx5%=\n\t" \
  "s_add_u32 m0, s33, 7680\n\t" \
  "s_nop 0\n\t" \
  "buffer_load_dwordx4 v186, s[16:19], s34 offen offset:512 nt lds\n\t" \
  ".Lx5%=:\n\t" \
  "s_add_u32 s31, s31, 18432\n\t" \
  "s_sub_u32 s26, s31, 129024\n\t" \
  "s_min_u32 s31, s31, s26\n\t" \
  "s_add_u32 s33, s31, s32\n\t" \
  "s_add_u32 s34, s34, 4718592\n\t" \
  "s_sub_u32 s61, s61, 1\n\t" \
  "s_cmp_lg_u32 s61, 0\n\t" \
  "s_cbranch_scc1 .Lxs%=\n\t" \
  "s_setpc_b64 s[74:75]\n\t" \
  ".Le5%=:\n\t" \
  "v_mov_b64 v[204:205], 0\n\t" \
  "v_mov_b64 v[206:207], 0\n\t" \
  "s_mul_i32 s25, s24, 9216\n\t" \
  "s_add_u32 s25, s25, 55296\n\t" \
  "v_add_u32 v208, s25, v187\n\t" \
  "s_mul_i32 s26, s24, 640\n\t" \
  "s_add_u32 s26, s26, 129024\n\t" \
  "v_min_u32 v246, 624, v187\n\t" \
  "v_add_u32 v246, s26, v246\n\t" \
  "ds_write_b128 v208, v[204:207] offset:36864\n\t" \
  "ds_write_b128 v208, v[204:207] offset:37888\n\t" \
  "ds_write_b128 v208, v[204:207] offset:38912\n\t" \
  "ds_write_b128 v208, v[204:207] offset:39936\n\t" \
  "s_mov_b32 s25, 0x7fff7fff\n\t" \
  "s_sub_u32 s26, s59, 82\n\t" \
  "s_cmp_lt_u32 s26, 27\n\t" \
  "s_cbranch_scc1 .Lrc%=\n\t" \
  ".Le6%=:\n\t" \
  "ds_write_b128 v208, v[204:207] offset:7168\n\t" \
  "ds_write_b128 v208, v[204:207] offset:8192\n\t" \
  "ds_write_b128 v208, v[204:207] offset:40960\n\t" \
  "ds_write_b128 v208, v[204:207] offset:41984\n\t" \
  "ds_write_b128 v208, v[204:207] offset:43008\n\t" \
  "ds_write_b128 v208, v[204:207] offset:44032\n\t" \
  "ds_write_b128 v208, v[204:207] offset:45056\n\t" \
  "ds_write_b128 v246, v[204:207]\n\t" \
  "s_cmp_ge_u32 s59, 82\n\t" \
  "s_cbranch_scc1 .Lf3%=\n\t" \
  "v_mov_b32 v148, 0x00010001\n\t" \
  "v_mov_b32 v149, 0x00080008\n\t" \
  "v_mov_b32 v150, 0x000c000c\n\t" \
  "s_mov_b32 s26, 0x00020002\n\t" \
  "s_mov_b32 s27, 0x00040004\n\t" \
  "s_mov_b32 s70, 0x05040100\n\t" \
  "s_mov_b32 s71, 0x07060302\n\t" \
  "s_mov_b32 vcc_lo, 0x55555555\n\t" \
  "s_mov_b32 vcc_hi, 0x55555555\n\t" \
  "v_mov_b32 v151, s70\n\t" \
  "v_mov_b32 v152, s71\n\t" \
  "v_cndmask_b32 v151, v152, v151, vcc\n\t" \
  "v_and_b32 v152, 1, v0\n\t" \
  "v_lshlrev_b32 v152, 3, v152\n\t" \
  "s_waitcnt vmcnt(1)\n\t" \
  "v_and_b32 v154, s25, v72\n\t" \
  "v_and_b32 v170, s25, v76\n\t" \
  "v_and_b32 v155, s25, v73\n\t" \
  "v_and_b32 v171, s25, v77\n\t" \
  "v_and_b32 v156, s25, v74\n\t" \
  "v_and_b32 v172, s25, v78\n\t" \
  "v_and_b32 v157, s25, v75\n\t" \
  "v_and_b32 v173, s25, v79\n\t" \
  "v_pk_max_u16 v158, v154, v155\n\t" \
  "v_pk_max_u16 v174, v170, v171\n\t" \
  "v_pk_max_u16 v159, v156, v157\n\t" \
  "v_pk_max_u16 v175, v172, v173\n\t" \
  "v_pk_min_u16 v158, v158, v159\n\t" \
  "v_pk_min_u16 v174, v174, v175\n\t" \
  "v_pk_min_u16 v159, v154, v155\n\t" \
  "v_pk_min_u16 v175, v170, v171\n\t" \
  "v_pk_max_u16 v158, v158, v159\n\t" \
  "v_pk_max_u16 v174, v174, v175\n\t" \
  "v_pk_min_u16 v159, v156, v157\n\t" \
  "v_pk_min_u16 v175, v172, v173\n\t" \
  "v_pk_max_u16 v158, v158, v159\n\t" \
  "v_pk_max_u16 v174, v174, v175\n\t" \
  "v_pk_sub_i16 v154, v154, v158\n\t" \
  "v_pk_sub_i16 v170, v170, v174\n\t" \
  "v_pk_sub_i16 v155, v155, v158\n\t" \
  "v_pk_sub_i16 v171, v171, v174\n\t" \
  "v_pk_sub_i16 v156, v156, v158\n\t" \
  "v_pk_sub_i16 v172, v172, v174\n\t" \
  "v_pk_sub_i16 v157, v157, v158\n\t" \
  "v_pk_sub_i16 v173, v173, v174\n\t" \
  "v_pk_ashrrev_i16 v154, 15, v154 op_sel_hi:[0,1]\n\t" \
  "v_pk_ashrrev_i16 v170, 15, v170 op_sel_hi:[0,1]\n\t" \
  "v_pk_ashrrev_i16 v155, 15, v155 op_sel_hi:[0,1]\n\t" \
  "v_pk_ashrrev_i16 v171, 15, v171 op_sel_hi:[0,1]\n\t" \
  "v_pk_ashrrev_i16 v156, 15, v156 op_sel_hi:[0,1]\n\t" \
  "v_pk_ashrrev_i16 v172, 15, v172 op_sel_hi:[0,1]\n\t" \
  "v_pk_ashrrev_i16 v157, 15, v157 op_sel_hi:[0,1]\n\t" \
  "v_pk_ashrrev_i16 v173, 15, v173 op_sel_hi:[0,1]\n\t" \
  "v_bfi_b32 v158, v155, v74, v73\n\t" \
  "v_bfi_b32 v174, v171, v78, v77\n\t" \
  "v_bfi_b32 v159, v156, v73, v74\n\t" \
  "v_bfi_b32 v175, v172, v77, v78\n\t" \
  "v_bfi_b32 v158, v154, v158, v72\n\t" \
  "v_bfi_b32 v174, v170, v174, v76\n\t" \
  "v_bfi_b32 v159, v157, v159, v75\n\t" \
  "v_bfi_b32 v175, v173, v175, v79\n\t" \
  "v_perm_b32 v160, v159, v158, s70\n\t" \
  "v_perm_b32 v162, v175, v174, s70\n\t" \
  "v_perm_b32 v161, v159, v158, s71\n\t" \
  "v_perm_b32 v163, v175, v174, s71\n\t" \
  "v_bfi_b32 v158, v155, s26, v148\n\t" \
  "v_bfi_b32 v174, v171, s26, v148\n\t" \
  "v_cndmask_b32_dpp v166, v161, v160, vcc quad_perm:[1,0,3,2] row_mask:0xf bank_mask:0xf\n\t" \
  "v_cndmask_b32_dpp v167, v163, v162, vcc quad_perm:[1,0,3,2] row_mask:0xf bank_mask:0xf\n\t" \
  "s_not_b64 vcc, vcc\n\t" \
  "v_cndmask_b32_dpp v168, v160, v161, vcc quad_perm:[1,0,3,2] row_mask:0xf bank_mask:0xf\n\t" \
  "v_cndmask_b32_dpp v169, v162, v163, vcc quad_perm:[1,0,3,2] row_mask:0xf bank_mask:0xf\n\t" \
  "buffer_store_dwordx4 v[166:169], v146, s[12:15], 0 offen sc1\n\t" \
  "v_bfi_b32 v159, v156, s27, v149\n\t" \
  "v_bfi_b32 v175, v172, s27, v149\n\t" \
  "v_bfi_b32 v159, v157, v159, v150\n\t" \
  "v_bfi_b32 v175, v173, v175, v150\n\t" \
  "v_and_or_b32 v164, v158, v154, v159\n\t" \
  "v_and_or_b32 v165, v174, v170, v175\n\t" \
  "v_lshl_or_b32 v174, v165, 4, v164\n\t" \
  "v_lshlrev_b32 v174, v152, v174\n\t" \
  "s_nop 1\n\t" \
  "v_or_b32_dpp v174, v174, v174 quad_perm:[1,0,3,2] row_mask:0xf bank_mask:0xf\n\t" \
  "s_nop 1\n\t" \
  "v_mov_b32_dpp v175, v174 row_ror:8 row_mask:0xf bank_mask:0xf\n\t" \
  "s_mov_b32 exec_lo, 0x00ff00ff\n\t" \
  "s_mov_b32 exec_hi, 0x00ff00ff\n\t" \
  "v_perm_b32 v174, v175, v174, v151\n\t" \
  "buffer_store_dword v174, v147, s[12:15], 0 offen sc1\n\t" \
  "s_mov_b64 exec, -1\n\t" \
  ".Lf3%=:\n\t" \
  "s_mov_b32 s20, s10\n\t" \
  "s_and_b32 s21, s11, 0xffff\n\t" \
  "s_mov_b32 s22, s18\n\t" \
  "s_mov_b32 s23, s19\n\t" \
  "v_and_b32 v7, 15, v6\n\t" \
  "v_lshrrev_b32 v4, 4, v6\n\t" \
  "v_lshrrev_b32 v5, 1, v7\n\t" \
  "v_xor_b32 v5, v5, v4\n\t" \
  "v_lshlrev_b32 v5, 4, v5\n\t" \
  "v_lshl_add_u32 v183, v7, 7, v5\n\t" \
  "v_xor_b32 v184, 64, v183\n\t" \
  "v_and_b32 v5, 7, v6\n\t" \
  "v_xor_b32 v4, v4, v5\n\t" \
  "v_lshlrev_b32 v4, 4, v4\n\t" \
  "v_lshl_add_u32 v4, v7, 8, v4\n\t" \
  "s_lshl_b32 s49, s24, 12\n\t" \
  "v_add_u32 v185, s49, v4\n\t" \
  "s_mul_i32 s45, s24, 576\n\t" \
  "s_mul_i32 s36, s24, 3456\n\t" \
  "s_add_u32 s45, s45, 161536\n\t" \
  "s_add_u32 s36, s36, 4096\n\t" \
  "s_mul_i32 s46, s24, 82944\n\t" \
  "s_add_u32 s46, s46, 30720\n\t" \
  "s_mul_i32 s47, s24, 10240\n\t" \
  "s_add_u32 s47, s47, 362496\n\t" \
  "s_lshl_b32 s40, s24, 2\n\t" \
  "s_add_u32 s40, s40, 403456\n\t" \
  "s_mul_i32 s60, s24, 6400\n\t" \
  "s_add_u32 s60, s60, 135680\n\t" \
  "s_mov_b32 s62, 0\n\t" \
  "s_movk_i32 s50, 0x7fff\n\t" \
  "s_mul_i32 s25, s24, 12288\n\t" \
  "s_add_u32 s25, s25, .Lvar%=-.Lpc%=-4096\n\t" \
  "s_add_u32 s68, s68, s25\n\t" \
  "s_addc_u32 s67, s69, 0\n\t" \
  "s_bfe_u32 s25, %[sBID], 0x20003\n\t" \
  "s_cmp_eq_u32 s25, 0\n\t" \
  "s_cmov_b32 s64, 0xeeeddc9a\n\t" \
  "s_cmov_b32 s65, 0x566eee9\n\t" \
  "s_cmp_eq_u32 s25, 1\n\t" \
  "s_cmov_b32 s64, 0xeeddd9ba\n\t" \
  "s_cmov_b32 s65, 0x566eee9\n\t" \
  "s_cmp_eq_u32 s25, 2\n\t" \
  "s_cmov_b32 s64, 0xeddd9cba\n\t" \
  "s_cmov_b32 s65, 0x566eee9\n\t" \
  "s_cmp_eq_u32 s25, 3\n\t" \
  "s_cmov_b32 s64, 0xddd9ccba\n\t" \
  "s_cmov_b32 s65, 0x566eee9\n\t" \
  "s_xor_b32 s25, s59, 128\n\t" \
  "s_cmp_lt_u32 s25, 72\n\t" \
  "s_cselect_b32 s55, 0, 1\n\t" \
  "s_cmov_b32 s64, 0x9cccccba\n\t" \
  "s_cmov_b32 s65, 0x44cccc\n\t" \
  "s_lshl2_add_u32 s25, %[sBID], s24\n\t" \
  "s_cmp_eq_u32 s25, 130\n\t" \
  "s_cselect_b32 s63, 1, 0\n\t" \
  "s_mul_i32 s25, s24, 9216\n\t" \
  "s_add_u32 s25, s25, 92160\n\t" \
  "s_mul_i32 s26, s24, 640\n\t" \
  "s_add_u32 s26, s26, 129024\n\t" \
  "s_lshr_b32 s30, %[sBID], 3\n\t" \
  "s_lshl2_add_u32 s30, s30, s24\n\t" \
  "s_mul_i32 s30, s30, 2944\n\t" \
  "s_add_u32 s30, s30, 30720\n\t" \
  "v_lshlrev_b32 v213, 3, v187\n\t" \
  "v_min_u32 v203, 992, v187\n\t" \
  "v_lshrrev_b32 v246, 8, v187\n\t" \
  "v_lshlrev_b32 v247, 4, v246\n\t" \
  "v_xor_b32 v247, v247, v183\n\t" \
  "v_lshl_or_b32 v247, v247, 16, v247\n\t" \
  "v_lshl_add_u32 v246, v246, 2, s26\n\t" \
  "s_lshl2_add_u32 s27, s39, 131584\n\t" \
  "v_add_u32 v210, s27, v187\n\t" \
  "s_lshl2_add_u32 s27, s24, 131584\n\t" \
  "v_add_u32 v211, s27, v187\n\t" \
  "v_mul_u32_u24 v209, 37, v203\n\t" \
  "v_lshrrev_b32 v209, 12, v209\n\t" \
  "v_lshl_add_u32 v202, v209, 6, s26\n\t" \
  "v_lshl_add_u32 v203, v209, 4, v203\n\t" \
  "v_bfe_u32 v200, v203, 3, 4\n\t" \
  "v_lshl_add_u32 v214, v200, 2, 16\n\t" \
  "v_lshl_add_u32 v209, v209, 10, s25\n\t" \
  "s_mov_b32 s44, s25\n\t" \
  "s_mov_b32 s37, -37888\n\t" \
  "s_mov_b32 s42, 0x10000\n\t" \
  "s_movk_i32 s38, 240\n\t" \
  "s_mov_b32 s43, 0x78787878\n\t" \
  "s_mov_b32 s61, 1\n\t" \
  "s_cmp_ge_u32 s59, 82\n\t" \
  "s_cbranch_scc1 .Lf2%=\n\t" \
  "s_mul_i32 s28, s59, 64\n\t" \
  "s_add_u32 s28, s28, 18432\n\t" \
  "v_readfirstlane_b32 s9, v142\n\t" \
  "v_readfirstlane_b32 s58, v143\n\t" \
  "s_mul_i32 s57, s9, 0x9E3779B1\n\t" \
  "s_add_u32 s57, s57, 0x632BE5AB\n\t" \
  "s_and_b32 s57, s57, 0xffffff00\n\t" \
  "s_mul_i32 s70, s9, 0x85EBCA6B\n\t" \
  "s_xor_b32 s58, s58, s70\n\t" \
  "s_add_u32 s58, s58, 0x3C6EF372\n\t" \
  "s_memrealtime s[70:71]\n\t" \
  "v_mov_b32 v134, s57\n\t" \
  "v_mov_b32 v135, s58\n\t" \
  "v_mov_b32 v136, s57\n\t" \
  "v_mov_b32 v137, s58\n\t" \
  "s_mov_b32 s61, 3\n\t" \
  "s_cmp_lg_u32 s24, 1\n\t" \
  "s_waitcnt vmcnt(0) lgkmcnt(0)\n\t" \
  "s_barrier\n\t" \
  "s_cbranch_scc1 .Lf2%=\n\t" \
  "s_mov_b64 exec, 15\n\t" \
  "buffer_store_dwordx4 v[134:137], v187, s[12:15], s28 offen sc1\n\t" \
  "s_cmp_lg_u32 s59, 0\n\t" \
  "s_cbranch_scc1 .Lp3%=\n\t" \
  "s_mov_b64 exec, 255\n\t" \
  "s_add_u32 s28, s28, 5248\n\t" \
  "v_mov_b32 v134, s70\n\t" \
  "v_mov_b32 v136, s70\n\t" \
  "buffer_store_dwordx4 v[134:137], v187, s[12:15], s28 offen sc1\n\t" \
  ".Lp3%=:\n\t" \
  "s_mov_b64 exec, -1\n\t" \
  "s_memrealtime s[70:71]\n\t" \
  "s_waitcnt lgkmcnt(0)\n\t" \
  "s_add_u32 s53, s70, 40\n\t" \
  ".Lf2%=:\n\t" \
  "s_call_b64 s[74:75], .Lxs%=\n\t" \
  "ds_write_b128 v208, v[204:207] offset:0\n\t" \
  "ds_write_b128 v208, v[204:207] offset:1024\n\t" \
  "ds_write_b128 v208, v[204:207] offset:2048\n\t" \
  "ds_write_b128 v208, v[204:207] offset:3072\n\t" \
  "ds_write_b128 v208, v[204:207] offset:4096\n\t" \
  "ds_write_b128 v208, v[204:207] offset:5120\n\t" \
  "ds_write_b128 v208, v[204:207] offset:6144\n\t" \
  "s_waitcnt vmcnt(12)\n\t" \
  "v_readfirstlane_b32 s9, v142\n\t" \
  "v_readfirstlane_b32 s58, v143\n\t" \
  "s_mul_i32 s57, s9, 0x9E3779B1\n\t" \
  "s_add_u32 s57, s57, 0x632BE5AB\n\t" \
  "s_and_b32 s57, s57, 0xffffff00\n\t" \
  "s_mul_i32 s70, s9, 0x85EBCA6B\n\t" \
  "s_xor_b32 s58, s58, s70\n\t" \
  "s_add_u32 s58, s58, 0x3C6EF372\n\t" \
  "v_add_u32 v8, s45, v187\n\t" \
  "v_lshlrev_b32 v4, 16, v144\n\t" \
  "v_and_b32 v5, 0xffff0000, v144\n\t" \
  "v_lshlrev_b32 v6, 16, v145\n\t" \
  "v_and_b32 v7, 0xffff0000, v145\n\t" \
  "s_mov_b32 exec_hi, 15\n\t" \
  "ds_write_b128 v8, v[4:7]\n\t" \
  "s_mov_b32 exec_hi, -1\n\t" \
  "v_lshrrev_b32 v126, 4, v187\n\t" \
  "v_and_or_b32 v126, v126, 48, s45\n\t" \
  "ds_read_b128 v[0:3], v126 offset:0\n\t" \
  "ds_read_b128 v[4:7], v126 offset:64\n\t" \
  "ds_read_b128 v[8:11], v126 offset:128\n\t" \
  "ds_read_b128 v[12:15], v126 offset:192\n\t" \
  "ds_read_b128 v[16:19], v126 offset:256\n\t" \
  "ds_read_b128 v[20:23], v126 offset:320\n\t" \
  "ds_read_b128 v[24:27], v126 offset:384\n\t" \
  "ds_read_b128 v[28:31], v126 offset:448\n\t" \
  "ds_read_b128 v[32:35], v126 offset:512\n\t" \
  "ds_read_b128 v[72:75], v126 offset:0\n\t" \
  "ds_read_b128 v[76:79], v126 offset:64\n\t" \
  "ds_read_b128 v[80:83], v126 offset:128\n\t" \
  "ds_read_b128 v[84:87], v126 offset:192\n\t" \
  "ds_read_b128 v[88:91], v126 offset:256\n\t" \
  "ds_read_b128 v[92:95], v126 offset:320\n\t" \
  "ds_read_b128 v[96:99], v126 offset:384\n\t" \
  "ds_read_b128 v[100:103], v126 offset:448\n\t" \
  "ds_read_b128 v[104:107], v126 offset:512\n\t" \
  "ds_read_b128 v[36:39], v126 offset:0\n\t" \
  "ds_read_b128 v[40:43], v126 offset:64\n\t" \
  "ds_read_b128 v[44:47], v126 offset:128\n\t" \
  "ds_read_b128 v[48:51], v126 offset:192\n\t" \
  "ds_read_b128 v[52:55], v126 offset:256\n\t" \
  "ds_read_b128 v[56:59], v126 offset:320\n\t" \
  "ds_read_b128 v[60:63], v126 offset:384\n\t" \
  "ds_read_b128 v[64:67], v126 offset:448\n\t" \
  "ds_read_b128 v[68:71], v126 offset:512\n\t" \
  "s_mov_b64 exec, 0\n\t" \
  "s_cmp_eq_u32 s24, 0\n\t" \
  "s_cbranch_scc1 .Lhop0%=\n\t" \
  "s_cmp_eq_u32 s24, 1\n\t" \
  "s_cbranch_scc1 .Lhop1%=\n\t" \
  "s_cmp_eq_u32 s24, 2\n\t" \
  "s_cbranch_scc1 .Lhop2%=\n\t" \
  "s_branch .Lhop3%=\n\t" \
  ".Lhopy%=:\n\t" \
  ".Lhopx%=:\n\t" \
  "s_mov_b64 exec, -1\n\t" \
  "v_min_u32 v138, 656, v187\n\t" \
  "v_mul_u32_u24 v138, 8, v138\n\t" \
  "s_movk_i32 s26, 18488\n\t" \
  "s_xor_b32 s54, s59, 128\n\t" \
  "s_mul_i32 s54, s54, 100\n\t" \
  "s_lshr_b32 s54, s54, 4\n\t" \
  "s_mul_i32 s25, s54, 0\n\t" \
  "s_lshr_b32 s25, s25, 8\n\t" \
  "s_bitcmp0_b32 s55, 0\n\t" \
  "s_cmov_b32 s25, 0\n\t" \
  "s_sub_u32 s27, 2080, s25\n\t" \
  "s_add_u32 s51, s27, 288\n\t" \
  "s_add_u32 s54, s54, 7280\n\t" \
  "s_lshr_b32 s52, s54, 4\n\t" \
  "s_cmp_lt_u32 s59, 109\n\t" \
  "s_cselect_b32 s29, 50, 0\n\t" \
  "s_cselect_b32 s101, 14, 2\n\t" \
  "s_cmp_lt_u32 s59, 82\n\t" \
  "s_cmov_b32 s29, 50\n\t" \
  "s_bitcmp0_b32 s55, 0\n\t" \
  "s_cmov_b32 s101, 8\n\t" \
  ".Lpw%=:\n\t" \
  "s_memrealtime s[70:71]\n\t" \
  "s_waitcnt lgkmcnt(0)\n\t" \
  "s_sub_i32 s25, s70, s53\n\t" \
  "s_cmp_lt_i32 s25, 0\n\t" \
  "s_cbranch_scc1 .Lpw%=\n\t" \
  "s_mov_b32 s53, s27\n\t" \
  "s_cmp_lg_u32 s24, 1\n\t" \
  "s_cbranch_scc1 .Lp2%=\n\t" \
  ".Lpp%=:\n\t" \
  "buffer_load_dwordx4 v[134:137], v138, s[12:15], s26 offen sc1\n\t" \
  "s_waitcnt vmcnt(0)\n\t" \
  "v_xor_b32 v135, s58, v135\n\t" \
  "v_bitop3_b32 v135, v135, s58, v137 bitop3:0xf6\n\t" \
  "s_mov_b32 exec_hi, 0x1ff\n\t" \
  "v_bitop3_b32 v135, v135, s57, v136 bitop3:0xf6\n\t" \
  "v_bitop3_b32 v135, v135, s57, v134 bitop3:0xf6\n\t" \
  "s_mov_b32 exec_hi, -1\n\t" \
  "v_cmp_ne_u32 vcc, 0, v135\n\t" \
  "v_readlane_b32 s56, v134, 63\n\t" \
  "s_cbranch_vccz .Lp2%=\n\t" \
  "s_sleep 2\n\t" \
  "s_sub_u32 s50, s50, 1\n\t" \
  "s_cbranch_scc0 .Lpp%=\n\t" \
  ".Lp2%=:\n\t" \
  "s_movk_i32 s50, 0x7fff\n\t" \
  "s_sub_u32 s25, s59, 82\n\t" \
  "s_cmp_lg_u32 s24, 3\n\t" \
  "s_cselect_b32 s25, 99, s25\n\t" \
  "s_cmp_lt_u32 s25, 27\n\t" \
  "s_waitcnt vmcnt(0) lgkmcnt(0)\n\t" \
  "s_barrier\n\t" \
  "s_cbranch_scc1 .Lp5%=\n\t" \
  ".Lp6%=:\n\t" \
  "s_memrealtime s[72:73]\n\t" \
  "s_add_u32 s56, s56, 190\n\t" \
  "ds_read_b128 v[126:129], v183 offset:0\n\t" \
  "ds_read_b128 v[130:133], v184 offset:0\n\t" \
  "buffer_load_dwordx4 v[188:191], v203, s[12:15], s36 offen offset:0 sc1\n\t" \
  "buffer_load_dwordx4 v[192:195], v203, s[12:15], s36 offen offset:1152 sc1\n\t" \
  "buffer_load_dwordx4 v[196:199], v203, s[12:15], s36 offen offset:2304 sc1\n\t" \
  "s_mov_b64 exec, 0x7fffff\n\t" \
  "buffer_load_dword v213, v213, s[12:15], s30 offen sc1\n\t" \
  "s_mov_b64 exec, -1\n\t" \
  "buffer_load_dwordx4 v[178:181], v187, s[12:15], s47 offen sc1\n\t" \
  "buffer_load_dword v182, v187, s[12:15], s40 offen sc1\n\t" \
  "buffer_load_dwordx4 v[142:145], v187, s[12:15], s47 offen offset:1024 sc1\n\t" \
  "s_add_u32 s48, s46, 0\n\t" \
  "buffer_load_dwordx4 a[0:3], v187, s[12:15], s48 offen offset:0 sc1\n\t" \
  "buffer_load_dwordx4 a[4:7], v187, s[12:15], s48 offen offset:1024 sc1\n\t" \
  "s_waitcnt vmcnt(6)\n\t" \
  ".Lrk%=:\n\t" \
  "v_xor_b32 v204, s57, v190\n\t" \
  "v_xor_b32 v205, s58, v191\n\t" \
  "v_and_b32 v248, 63, v190\n\t" \
  "v_bitop3_b32 v204, v204, s57, v194 bitop3:0xf6\n\t" \
  "v_bitop3_b32 v205, v205, s58, v195 bitop3:0xf6\n\t" \
  "v_and_b32 v249, 63, v194\n\t" \
  "v_bitop3_b32 v204, v204, s57, v198 bitop3:0xf6\n\t" \
  "v_bitop3_b32 v205, v205, s58, v199 bitop3:0xf6\n\t" \
  "v_and_b32 v250, 63, v198\n\t" \
  "v_lshrrev_b32 v204, 8, v204\n\t" \
  "v_add3_u32 v206, v248, v249, v250\n\t" \
  "v_max3_u32 v207, v248, v249, v250\n\t" \
  "v_or_b32 v204, v204, v205\n\t" \
  "v_cmp_lt_u32 s[4:5], v214, v206\n\t" \
  "v_add_u32 v207, 18, v207\n\t" \
  "v_cmp_ne_u32 vcc, 0, v204\n\t" \
  "v_max_u32 v207, v207, v206\n\t" \
  "v_add_u32 v222, v200, v248\n\t" \
  "v_cmp_lt_u32 s[10:11], 32, v207\n\t" \
  "v_mov_b32 v220, v200\n\t" \
  "v_add_u32 v221, 1, v200\n\t" \
  "v_add_u32 v224, v222, v249\n\t" \
  "v_add_u32 v223, 1, v222\n\t" \
  "v_add_u32 v225, 1, v224\n\t" \
  "v_lshrrev_b32 v226, 4, v220\n\t" \
  "v_lshrrev_b32 v227, 4, v221\n\t" \
  "v_lshrrev_b32 v228, 4, v222\n\t" \
  "v_lshrrev_b32 v229, 4, v223\n\t" \
  "v_lshrrev_b32 v230, 4, v224\n\t" \
  "v_lshrrev_b32 v231, 4, v225\n\t" \
  "s_cbranch_vccnz .Lrr%=\n\t" \
  ".Lrg%=:\n\t" \
  "s_bcnt1_i32_b64 s61, s[4:5]\n\t" \
  "v_mad_i32_i24 v226, v226, s37, v209\n\t" \
  "v_mad_i32_i24 v227, v227, s37, v209\n\t" \
  "v_mad_i32_i24 v228, v228, s37, v209\n\t" \
  "v_mad_i32_i24 v229, v229, s37, v209\n\t" \
  "v_mad_i32_i24 v230, v230, s37, v209\n\t" \
  "v_mad_i32_i24 v231, v231, s37, v209\n\t" \
  "s_cmp_gt_u32 s61, 5\n\t" \
  "s_cselect_b64 vcc, s[4:5], s[10:11]\n\t" \
  "s_cmp_lg_u64 vcc, 0\n\t" \
  "s_cbranch_scc1 .Lss%=\n\t" \
  ".Lsg%=:\n\t" \
  "buffer_load_dwordx4 a[8:11], v187, s[12:15], s48 offen offset:2048 sc1\n\t" \
  "v_sub_u32 v232, v220, v248\n\t" \
  "v_sub_u32 v233, v221, v248\n\t" \
  "v_sub_u32 v234, v220, v249\n\t" \
  "v_sub_u32 v235, v221, v249\n\t" \
  "v_sub_u32 v236, v220, v250\n\t" \
  "v_sub_u32 v237, v221, v250\n\t" \
  "buffer_load_dwordx4 a[12:15], v187, s[12:15], s48 offen offset:3072 sc1\n\t" \
  "v_bitop3_b32 v238, v220, v232, s42 bitop3:0xf2\n\t" \
  "v_bitop3_b32 v239, v221, v233, s42 bitop3:0xf2\n\t" \
  "v_bitop3_b32 v222, v222, v234, s42 bitop3:0xf2\n\t" \
  "v_bitop3_b32 v223, v223, v235, s42 bitop3:0xf2\n\t" \
  "v_bitop3_b32 v224, v224, v236, s42 bitop3:0xf2\n\t" \
  "v_bitop3_b32 v225, v225, v237, s42 bitop3:0xf2\n\t" \
  "s_add_u32 s48, s46, 4096\n\t" \
  "buffer_load_dwordx4 a[16:19], v187, s[12:15], s48 offen offset:0 sc1\n\t" \
  "v_bfe_u32 v232, v188, 7, 4\n\t" \
  "v_bfe_u32 v233, v189, 7, 4\n\t" \
  "v_bfe_u32 v234, v192, 7, 4\n\t" \
  "v_bfe_u32 v235, v193, 7, 4\n\t" \
  "v_bfe_u32 v236, v196, 7, 4\n\t" \
  "v_bfe_u32 v237, v197, 7, 4\n\t" \
  "buffer_load_dwordx4 a[20:23], v187, s[12:15], s48 offen offset:1024 sc1\n\t" \
  "v_lshl_add_u32 v226, v232, 4, v226\n\t" \
  "v_lshl_add_u32 v227, v233, 4, v227\n\t" \
  "v_lshl_add_u32 v228, v234, 4, v228\n\t" \
  "v_lshl_add_u32 v229, v235, 4, v229\n\t" \
  "v_lshl_add_u32 v230, v236, 4, v230\n\t" \
  "v_lshl_add_u32 v231, v237, 4, v231\n\t" \
  "buffer_load_dwordx4 a[24:27], v187, s[12:15], s48 offen offset:2048 sc1\n\t" \
  "v_lshrrev_b32 v232, 2, v238\n\t" \
  "v_lshrrev_b32 v233, 2, v239\n\t" \
  "v_lshrrev_b32 v234, 2, v222\n\t" \
  "v_lshrrev_b32 v235, 2, v223\n\t" \
  "v_lshrrev_b32 v236, 2, v224\n\t" \
  "v_lshrrev_b32 v237, 2, v225\n\t" \
  "buffer_load_dwordx4 a[28:31], v187, s[12:15], s48 offen offset:3072 sc1\n\t" \
  "v_and_or_b32 v226, v188, 2, v226\n\t" \
  "v_and_or_b32 v227, v189, 2, v227\n\t" \
  "v_and_or_b32 v228, v192, 2, v228\n\t" \
  "v_and_or_b32 v229, v193, 2, v229\n\t" \
  "v_and_or_b32 v230, v196, 2, v230\n\t" \
  "v_and_or_b32 v231, v197, 2, v231\n\t" \
  "v_mad_u32_u24 v226, v232, s38, v226\n\t" \
  "v_mad_u32_u24 v227, v233, s38, v227\n\t" \
  "v_mad_u32_u24 v228, v234, s38, v228\n\t" \
  "v_mad_u32_u24 v229, v235, s38, v229\n\t" \
  "v_mad_u32_u24 v230, v236, s38, v230\n\t" \
  "v_mad_u32_u24 v231, v237, s38, v231\n\t" \
  "s_add_u32 s48, s46, 8192\n\t" \
  "buffer_load_dwordx4 a[32:35], v187, s[12:15], s48 offen offset:0 sc1\n\t" \
  "v_lshl_add_u32 v232, v238, 1, v202\n\t" \
  "v_lshl_add_u32 v233, v239, 1, v202\n\t" \
  "v_lshl_add_u32 v234, v222, 1, v202\n\t" \
  "v_lshl_add_u32 v235, v223, 1, v202\n\t" \
  "v_lshl_add_u32 v236, v224, 1, v202\n\t" \
  "v_lshl_add_u32 v237, v225, 1, v202\n\t" \
  "buffer_load_dwordx4 v[146:149], v187, s[12:15], s47 offen offset:2048 sc1\n\t" \
  "ds_write_b16 v232, v188\n\t" \
  "v_lshl_add_u32 v226, v238, 2, v226\n\t" \
  "ds_write_b16 v233, v189\n\t" \
  "v_lshl_add_u32 v227, v239, 2, v227\n\t" \
  "ds_write_b16 v234, v192\n\t" \
  "v_lshl_add_u32 v228, v222, 2, v228\n\t" \
  "buffer_load_dwordx4 a[36:39], v187, s[12:15], s48 offen offset:1024 sc1\n\t" \
  "ds_write_b16 v235, v193\n\t" \
  "v_lshl_add_u32 v229, v223, 2, v229\n\t" \
  "ds_write_b16 v236, v196\n\t" \
  "v_lshl_add_u32 v230, v224, 2, v230\n\t" \
  "ds_write_b16 v237, v197\n\t" \
  "v_lshl_add_u32 v231, v225, 2, v231\n\t" \
  "buffer_load_dwordx4 a[40:43], v187, s[12:15], s48 offen offset:2048 sc1\n\t" \
  "ds_write_b16_d16_hi v226, v188\n\t" \
  "ds_write_b16_d16_hi v227, v189\n\t" \
  "ds_write_b16_d16_hi v228, v192\n\t" \
  "ds_write_b16_d16_hi v229, v193\n\t" \
  "ds_write_b16_d16_hi v230, v196\n\t" \
  "ds_write_b16_d16_hi v231, v197\n\t" \
  "buffer_load_dwordx4 a[44:47], v187, s[12:15], s48 offen offset:3072 sc1\n\t" \
  "ds_read2_b32 v[108:109], v246 offset0:0 offset1:4\n\t" \
  "ds_read2_b32 v[110:111], v246 offset0:16 offset1:20\n\t" \
  "ds_read2_b32 v[112:113], v246 offset0:32 offset1:36\n\t" \
  "ds_read2_b32 v[114:115], v246 offset0:48 offset1:52\n\t" \
  "ds_read2_b32 v[116:117], v246 offset0:64 offset1:68\n\t" \
  "ds_read2_b32 v[118:119], v246 offset0:80 offset1:84\n\t" \
  "s_add_u32 s48, s46, 12288\n\t" \
  "buffer_load_dwordx4 a[48:51], v187, s[12:15], s48 offen offset:0 sc1\n\t" \
  "ds_read2_b32 v[120:121], v246 offset0:96 offset1:100\n\t" \
  "ds_read2_b32 v[122:123], v246 offset0:112 offset1:116\n\t" \
  "ds_read2_b32 v[124:125], v246 offset0:128 offset1:132\n\t" \
  "v_add_u32 v236, s44, v187\n\t" \
  "ds_read_b128 v[236:239], v236 offset:0\n\t" \
  "s_waitcnt vmcnt(16)\n\t" \
  "ds_write_b128 v210, v[178:181]\n\t" \
  "s_waitcnt vmcnt(15)\n\t" \
  "v_and_b32 v182, 0xffff, v182\n\t" \
  "v_or_b32 v182, 0x99990000, v182\n\t" \
  "s_waitcnt lgkmcnt(0)\n\t" \
  "v_bitop3_b32 v108, v108, s43, v247 bitop3:0x6a\n\t" \
  "v_bitop3_b32 v109, v109, s43, v247 bitop3:0x6a\n\t" \
  "s_barrier\n\t" \
  "ds_read2st64_b32 v[178:179], v211 offset0:0 offset1:4\n\t" \
  "ds_read2st64_b32 v[180:181], v211 offset0:8 offset1:12\n\t" \
  "s_waitcnt vmcnt(13)\n\t" \
  "v_smfmac_f32_16x16x64_bf16 v[0:3], a[0:3], v[126:133], v142\n\t" \
  "s_waitcnt vmcnt(12)\n\t" \
  "buffer_load_dwordx4 a[52:55], v187, s[12:15], s48 offen offset:1024 sc1\n\t" \
  "ds_read_b128 v[134:137], v183 offset:18432\n\t" \
  "v_add_u32_sdwa v248, 0, v108 dst_sel:DWORD dst_unused:UNUSED_PAD src0_sel:DWORD src1_sel:WORD_0\n\t" \
  "v_smfmac_f32_16x16x64_bf16 v[4:7], a[4:7], v[126:133], v142 abid:1\n\t" \
  "s_waitcnt vmcnt(12)\n\t" \
  "buffer_load_dwordx4 a[56:59], v187, s[12:15], s48 offen offset:2048 sc1\n\t" \
  "ds_read_b128 v[138:141], v184 offset:18432\n\t" \
  "v_add_u32_sdwa v249, 0, v108 dst_sel:DWORD dst_unused:UNUSED_PAD src0_sel:DWORD src1_sel:WORD_1\n\t" \
  "v_smfmac_f32_16x16x64_bf16 v[8:11], a[8:11], v[126:133], v143\n\t" \
  "s_waitcnt vmcnt(12)\n\t" \
  "buffer_load_dwordx4 a[60:63], v187, s[12:15], s48 offen offset:3072 sc1\n\t" \
  "ds_read_b64 v[220:221], v248 offset:0\n\t" \
  "v_add_u32_sdwa v250, 0, v109 dst_sel:DWORD dst_unused:UNUSED_PAD src0_sel:DWORD src1_sel:WORD_0\n\t" \
  "v_smfmac_f32_16x16x64_bf16 v[12:15], a[12:15], v[126:133], v143 abid:1\n\t" \
  "s_add_u32 s48, s46, 16384\n\t" \
  "s_waitcnt vmcnt(12)\n\t" \
  "buffer_load_dwordx4 a[64:67], v187, s[12:15], s48 offen offset:0 sc1\n\t" \
  "ds_read_b64 v[222:223], v249 offset:0\n\t" \
  "v_add_u32_sdwa v251, 0, v109 dst_sel:DWORD dst_unused:UNUSED_PAD src0_sel:DWORD src1_sel:WORD_1\n\t" \
  "v_smfmac_f32_16x16x64_bf16 v[16:19], a[16:19], v[126:133], v144\n\t" \
  "s_waitcnt vmcnt(12)\n\t" \
  "buffer_load_dwordx4 a[68:71], v187, s[12:15], s48 offen offset:1024 sc1\n\t" \
  "ds_read_b64 v[224:225], v250 offset:0\n\t" \
  "v_bitop3_b32 v110, v110, s43, v247 bitop3:0x6a\n\t" \
  "v_smfmac_f32_16x16x64_bf16 v[20:23], a[20:23], v[126:133], v144 abid:1\n\t" \
  "s_waitcnt vmcnt(12)\n\t" \
  "ds_read_b64 v[226:227], v251 offset:0\n\t" \
  "v_bitop3_b32 v111, v111, s43, v247 bitop3:0x6a\n\t" \
  "v_smfmac_f32_16x16x64_bf16 v[24:27], a[24:27], v[126:133], v145\n\t" \
  "s_waitcnt vmcnt(11)\n\t" \
  "buffer_load_dwordx4 v[150:153], v187, s[12:15], s47 offen offset:3072 sc1\n\t" \
  "ds_read_b64 v[228:229], v248 offset:18432\n\t" \
  "v_bitop3_b32 v112, v112, s43, v247 bitop3:0x6a\n\t" \
  "v_smfmac_f32_16x16x64_bf16 v[28:31], a[28:31], v[126:133], v145 abid:1\n\t" \
  "s_waitcnt vmcnt(11) lgkmcnt(7)\n\t" \
  "ds_read_b64 v[230:231], v249 offset:18432\n\t" \
  "v_bitop3_b32 v113, v113, s43, v247 bitop3:0x6a\n\t" \
  "v_smfmac_f32_16x16x64_bf16 v[32:35], a[32:35], v[126:133], v178\n\t" \
  "s_waitcnt lgkmcnt(2)\n\t" \
  "ds_read_b64 v[232:233], v250 offset:18432\n\t" \
  "v_bitop3_b32 v114, v114, s43, v247 bitop3:0x6a\n\t" \
  "v_smfmac_f32_16x16x64_bf16 v[0:3], v[236:239], v[220:227], v182 abid:1\n\t" \
  "s_ff1_i32_b64 s25, s[4:5]\n\t" \
  "ds_read_b64 v[234:235], v251 offset:18432\n\t" \
  "v_add_u32 v240, s44, v187\n\t" \
  "v_smfmac_f32_16x16x64_bf16 v[72:75], a[0:3], v[134:141], v142\n\t" \
  "s_bitset0_b64 s[4:5], s25\n\t" \
  "buffer_load_dwordx4 a[72:75], v187, s[12:15], s48 offen offset:2048 sc1\n\t" \
  "ds_read_b128 v[126:129], v183 offset:36864\n\t" \
  "v_bitop3_b32 v115, v115, s43, v247 bitop3:0x6a\n\t" \
  "v_smfmac_f32_16x16x64_bf16 v[76:79], a[4:7], v[134:141], v142 abid:1\n\t" \
  "s_max_i32 s25, s25, 0\n\t" \
  "ds_read_b128 v[130:133], v184 offset:36864\n\t" \
  "v_bitop3_b32 v116, v116, s43, v247 bitop3:0x6a\n\t" \
  "v_smfmac_f32_16x16x64_bf16 v[80:83], a[8:11], v[134:141], v143\n\t" \
  "s_mul_i32 s26, s25, 37\n\t" \
  "s_waitcnt lgkmcnt(2)\n\t" \
  "ds_read_b128 v[240:243], v240 offset:1024\n\t" \
  "ds_read_b64 v[220:221], v248 offset:36864\n\t" \
  "v_add_u32_sdwa v248, 0, v110 dst_sel:DWORD dst_unused:UNUSED_PAD src0_sel:DWORD src1_sel:WORD_0\n\t" \
  "v_smfmac_f32_16x16x64_bf16 v[72:75], v[236:239], v[228:235], v182 abid:1\n\t" \
  "s_lshr_b32 s26, s26, 8\n\t" \
  "ds_read_b64 v[222:223], v249 offset:36864\n\t" \
  "v_add_u32_sdwa v249, 0, v110 dst_sel:DWORD dst_unused:UNUSED_PAD src0_sel:DWORD src1_sel:WORD_1\n\t" \
  "v_smfmac_f32_16x16x64_bf16 v[84:87], a[12:15], v[134:141], v143 abid:1\n\t" \
  "s_mul_i32 s70, s26, -3\n\t" \
  "buffer_load_dwordx4 a[76:79], v187, s[12:15], s48 offen offset:3072 sc1\n\t" \
  "ds_read_b64 v[224:225], v250 offset:36864\n\t" \
  "v_add_u32_sdwa v250, 0, v111 dst_sel:DWORD dst_unused:UNUSED_PAD src0_sel:DWORD src1_sel:WORD_0\n\t" \
  "v_smfmac_f32_16x16x64_bf16 v[88:91], a[16:19], v[134:141], v144\n\t" \
  "s_add_u32 s70, s70, s25\n\t" \
  "ds_read_b64 v[226:227], v251 offset:36864\n\t" \
  "v_add_u32_sdwa v251, 0, v111 dst_sel:DWORD dst_unused:UNUSED_PAD src0_sel:DWORD src1_sel:WORD_1\n\t" \
  "v_smfmac_f32_16x16x64_bf16 v[92:95], a[20:23], v[134:141], v144 abid:1\n\t" \
  "s_mul_i32 s71, s26, -5\n\t" \
  "ds_read_b64 v[228:229], v248 offset:0\n\t" \
  "v_bitop3_b32 v117, v117, s43, v247 bitop3:0x6a\n\t" \
  "v_smfmac_f32_16x16x64_bf16 v[96:99], a[24:27], v[134:141], v145\n\t" \
  "s_add_u32 s48, s46, 20480\n\t" \
  "buffer_load_dwordx4 a[80:83], v187, s[12:15], s48 offen offset:0 sc1\n\t" \
  "ds_read_b64 v[230:231], v249 offset:0\n\t" \
  "v_bitop3_b32 v118, v118, s43, v247 bitop3:0x6a\n\t" \
  "v_smfmac_f32_16x16x64_bf16 v[100:103], a[28:31], v[134:141], v145 abid:1\n\t" \
  "s_add_u32 s71, s71, s25\n\t" \
  "s_waitcnt lgkmcnt(2)\n\t" \
  "ds_read_b64 v[232:233], v250 offset:0\n\t" \
  "v_bitop3_b32 v119, v119, s43, v247 bitop3:0x6a\n\t" \
  "v_smfmac_f32_16x16x64_bf16 v[36:39], v[236:239], v[220:227], v182 abid:1\n\t" \
  "s_lshl_b32 s71, s71, 9\n\t" \
  "ds_read_b64 v[234:235], v251 offset:0\n\t" \
  "v_bitop3_b32 v120, v120, s43, v247 bitop3:0x6a\n\t" \
  "v_smfmac_f32_16x16x64_bf16 v[104:107], a[32:35], v[134:141], v178\n\t" \
  "s_add_u32 s71, s71, s44\n\t" \
  "ds_read_b64 v[220:221], v248 offset:18432\n\t" \
  "v_bitop3_b32 v121, v121, s43, v247 bitop3:0x6a\n\t" \
  "v_smfmac_f32_16x16x64_bf16 v[36:39], a[0:3], v[126:133], v142\n\t" \
  "s_sub_u32 s71, s71, 36864\n\t" \
  "buffer_load_dwordx4 a[84:87], v187, s[12:15], s48 offen offset:1024 sc1\n\t" \
  "ds_read_b128 v[134:137], v183 offset:2048\n\t" \
  "v_bitop3_b32 v122, v122, s43, v247 bitop3:0x6a\n\t" \
  "v_smfmac_f32_16x16x64_bf16 v[40:43], a[4:7], v[126:133], v142 abid:1\n\t" \
  "s_lshl_b32 s6, s26, 2\n\t" \
  "s_waitcnt lgkmcnt(2)\n\t" \
  "ds_read_b128 v[138:141], v184 offset:2048\n\t" \
  "v_bitop3_b32 v123, v123, s43, v247 bitop3:0x6a\n\t" \
  "v_smfmac_f32_16x16x64_bf16 v[4:7], v[240:243], v[228:235], v182 abid:1\n\t" \
  "s_max_u32 s27, s61, 1\n\t" \
  "ds_read_b64 v[222:223], v249 offset:18432\n\t" \
  "v_add_u32 v236, s44, v187\n\t" \
  "v_smfmac_f32_16x16x64_bf16 v[44:47], a[8:11], v[126:133], v143\n\t" \
  "s_min_u32 s27, s27, 3\n\t" \
  "ds_read_b128 v[236:239], v236 offset:2048\n\t" \
  "v_bitop3_b32 v124, v124, s43, v247 bitop3:0x6a\n\t" \
  "v_smfmac_f32_16x16x64_bf16 v[48:51], a[12:15], v[126:133], v143 abid:1\n\t" \
  "s_mul_i32 s27, s27, 4096\n\t" \
  "buffer_load_dwordx4 a[88:91], v187, s[12:15], s48 offen offset:2048 sc1\n\t" \
  "ds_read_b64 v[224:225], v250 offset:18432\n\t" \
  "v_bitop3_b32 v125, v125, s43, v247 bitop3:0x6a\n\t" \
  "v_smfmac_f32_16x16x64_bf16 v[52:55], a[16:19], v[126:133], v144\n\t" \
  "s_add_u32 s68, s68, s27\n\t" \
  "ds_read_b64 v[226:227], v251 offset:18432\n\t" \
  "v_smfmac_f32_16x16x64_bf16 v[56:59], a[20:23], v[126:133], v144 abid:1\n\t" \
  "ds_read_b64 v[228:229], v248 offset:36864\n\t" \
  "v_add_u32_sdwa v248, 0, v112 dst_sel:DWORD dst_unused:UNUSED_PAD src0_sel:DWORD src1_sel:WORD_0\n\t" \
  "v_smfmac_f32_16x16x64_bf16 v[60:63], a[24:27], v[126:133], v145\n\t" \
  "buffer_load_dwordx4 a[92:95], v187, s[12:15], s48 offen offset:3072 sc1\n\t" \
  "ds_read_b64 v[230:231], v249 offset:36864\n\t" \
  "v_add_u32_sdwa v249, 0, v112 dst_sel:DWORD dst_unused:UNUSED_PAD src0_sel:DWORD src1_sel:WORD_1\n\t" \
  "v_smfmac_f32_16x16x64_bf16 v[64:67], a[28:31], v[126:133], v145 abid:1\n\t" \
  "s_waitcnt lgkmcnt(2)\n\t" \
  "ds_read_b64 v[232:233], v250 offset:36864\n\t" \
  "v_add_u32_sdwa v250, 0, v113 dst_sel:DWORD dst_unused:UNUSED_PAD src0_sel:DWORD src1_sel:WORD_0\n\t" \
  "v_smfmac_f32_16x16x64_bf16 v[76:79], v[240:243], v[220:227], v182 abid:1\n\t" \
  "ds_read_b64 v[234:235], v251 offset:36864\n\t" \
  "v_add_u32_sdwa v251, 0, v113 dst_sel:DWORD dst_unused:UNUSED_PAD src0_sel:DWORD src1_sel:WORD_1\n\t" \
  "v_smfmac_f32_16x16x64_bf16 v[68:71], a[32:35], v[126:133], v178\n\t" \
  "s_waitcnt vmcnt(15)\n\t" \
  "ds_read_b64 v[220:221], v248 offset:0\n\t" \
  "v_lshl_add_u32 v244, s70, 4, v246\n\t" \
  "v_smfmac_f32_16x16x64_bf16 v[0:3], a[36:39], v[134:141], v146\n\t" \
  "s_add_u32 s48, s46, 24576\n\t" \
  "s_waitcnt vmcnt(14)\n\t" \
  "buffer_load_dwordx4 a[96:99], v187, s[12:15], s48 offen offset:0 sc1\n\t" \
  "ds_read_b128 v[126:129], v183 offset:20480\n\t" \
  "ds_read_b32 v244, v244 offset:32\n\t" \
  "v_smfmac_f32_16x16x64_bf16 v[4:7], a[40:43], v[134:141], v146 abid:1\n\t" \
  "s_waitcnt lgkmcnt(3)\n\t" \
  "buffer_load_dwordx4 a[100:103], v187, s[12:15], s48 offen offset:1024 sc1\n\t" \
  "ds_read_b128 v[130:133], v184 offset:20480\n\t" \
  "v_lshrrev_b32 v245, 2, v187\n\t" \
  "v_smfmac_f32_16x16x64_bf16 v[40:43], v[240:243], v[228:235], v182 abid:1\n\t" \
  "s_waitcnt vmcnt(15)\n\t" \
  "ds_read_b64 v[222:223], v249 offset:0\n\t" \
  "v_add_u32 v245, s60, v245\n\t" \
  "v_smfmac_f32_16x16x64_bf16 v[8:11], a[44:47], v[134:141], v147\n\t" \
  "s_waitcnt vmcnt(14)\n\t" \
  "ds_read_b64 v[224:225], v250 offset:0\n\t" \
  "v_smfmac_f32_16x16x64_bf16 v[12:15], a[48:51], v[134:141], v147 abid:1\n\t" \
  "s_waitcnt vmcnt(13)\n\t" \
  "buffer_load_dwordx4 a[104:107], v187, s[12:15], s48 offen offset:2048 sc1\n\t" \
  "ds_read_b64 v[226:227], v251 offset:0\n\t" \
  "v_smfmac_f32_16x16x64_bf16 v[16:19], a[52:55], v[134:141], v148\n\t" \
  "s_waitcnt vmcnt(13)\n\t" \
  "ds_read_b64 v[228:229], v248 offset:18432\n\t" \
  "s_waitcnt lgkmcnt(5)\n\t" \
  "v_bitop3_b32 v244, v244, s43, v247 bitop3:0x6a\n\t" \
  "v_smfmac_f32_16x16x64_bf16 v[20:23], a[56:59], v[134:141], v148 abid:1\n\t" \
  "s_waitcnt vmcnt(12)\n\t" \
  "ds_read_b64 v[230:231], v249 offset:18432\n\t" \
  "ds_write_b32 v245, v244 offset:1024\n\t" \
  "v_smfmac_f32_16x16x64_bf16 v[24:27], a[60:63], v[134:141], v149\n\t" \
  "s_add_u32 s47, s47, 4096\n\t" \
  "s_waitcnt lgkmcnt(3)\n\t" \
  "buffer_load_dwordx4 v[154:157], v187, s[12:15], s47 offen offset:0 sc1\n\t" \
  "ds_read_b64 v[232:233], v250 offset:18432\n\t" \
  "v_smfmac_f32_16x16x64_bf16 v[8:11], v[236:239], v[220:227], v182 abid:1\n\t" \
  "s_waitcnt vmcnt(12)\n\t" \
  "ds_read_b64 v[234:235], v251 offset:18432\n\t" \
  "v_add_u32 v240, s44, v187\n\t" \
  "v_smfmac_f32_16x16x64_bf16 v[28:31], a[64:67], v[134:141], v149 abid:1\n\t" \
  "s_waitcnt vmcnt(11)\n\t" \
  "ds_read_b128 v[240:243], v240 offset:3072\n\t" \
  "v_smfmac_f32_16x16x64_bf16 v[32:35], a[68:71], v[134:141], v178 abid:1\n\t" \
  "ds_read_b64 v[220:221], v248 offset:36864\n\t" \
  "v_add_u32_sdwa v248, 0, v114 dst_sel:DWORD dst_unused:UNUSED_PAD src0_sel:DWORD src1_sel:WORD_0\n\t" \
  "v_smfmac_f32_16x16x64_bf16 v[72:75], a[36:39], v[126:133], v146\n\t" \
  "s_waitcnt lgkmcnt(2)\n\t" \
  "buffer_load_dwordx4 a[108:111], v187, s[12:15], s48 offen offset:3072 sc1\n\t" \
  "ds_read_b128 v[134:137], v183 offset:38912\n\t" \
  "v_smfmac_f32_16x16x64_bf16 v[80:83], v[236:239], v[228:235], v182 abid:1\n\t" \
  "ds_read_b128 v[138:141], v184 offset:38912\n\t" \
  "v_smfmac_f32_16x16x64_bf16 v[76:79], a[40:43], v[126:133], v146 abid:1\n\t" \
  "ds_read_b64 v[222:223], v249 offset:36864\n\t" \
  "v_add_u32_sdwa v249, 0, v114 dst_sel:DWORD dst_unused:UNUSED_PAD src0_sel:DWORD src1_sel:WORD_1\n\t" \
  "v_smfmac_f32_16x16x64_bf16 v[80:83], a[44:47], v[126:133], v147\n\t" \
  "ds_read_b64 v[224:225], v250 offset:36864\n\t" \
  "v_add_u32_sdwa v250, 0, v115 dst_sel:DWORD dst_unused:UNUSED_PAD src0_sel:DWORD src1_sel:WORD_0\n\t" \
  "v_smfmac_f32_16x16x64_bf16 v[84:87], a[48:51], v[126:133], v147 abid:1\n\t" \
  "s_add_u32 s48, s46, 28672\n\t" \
  "buffer_load_dwordx4 a[112:115], v187, s[12:15], s48 offen offset:0 sc1\n\t" \
  "ds_read_b64 v[226:227], v251 offset:36864\n\t" \
  "v_add_u32_sdwa v251, 0, v115 dst_sel:DWORD dst_unused:UNUSED_PAD src0_sel:DWORD src1_sel:WORD_1\n\t" \
  "v_smfmac_f32_16x16x64_bf16 v[88:91], a[52:55], v[126:133], v148\n\t" \
  "ds_read_b64 v[228:229], v248 offset:0\n\t" \
  "v_smfmac_f32_16x16x64_bf16 v[92:95], a[56:59], v[126:133], v148 abid:1\n\t" \
  "ds_read_b64 v[230:231], v249 offset:0\n\t" \
  "v_smfmac_f32_16x16x64_bf16 v[96:99], a[60:63], v[126:133], v149\n\t" \
  "s_waitcnt lgkmcnt(2)\n\t" \
  "buffer_load_dwordx4 a[116:119], v187, s[12:15], s48 offen offset:1024 sc1\n\t" \
  "ds_read_b64 v[232:233], v250 offset:0\n\t" \
  "v_smfmac_f32_16x16x64_bf16 v[44:47], v[236:239], v[220:227], v182 abid:1\n\t" \
  "ds_read_b64 v[234:235], v251 offset:0\n\t" \
  "v_smfmac_f32_16x16x64_bf16 v[100:103], a[64:67], v[126:133], v149 abid:1\n\t" \
  "ds_read_b64 v[220:221], v248 offset:18432\n\t" \
  "v_smfmac_f32_16x16x64_bf16 v[104:107], a[68:71], v[126:133], v178 abid:1\n\t" \
  "ds_read_b64 v[222:223], v249 offset:18432\n\t" \
  "v_smfmac_f32_16x16x64_bf16 v[36:39], a[36:39], v[134:141], v146\n\t" \
  "s_waitcnt lgkmcnt(2)\n\t" \
  "buffer_load_dwordx4 a[120:123], v187, s[12:15], s48 offen offset:2048 sc1\n\t" \
  "ds_read_b128 v[126:129], v183 offset:4096\n\t" \
  "v_smfmac_f32_16x16x64_bf16 v[12:15], v[240:243], v[228:235], v182 abid:1\n\t" \
  "ds_read_b128 v[130:133], v184 offset:4096\n\t" \
  "v_add_u32 v236, s44, v187\n\t" \
  "v_smfmac_f32_16x16x64_bf16 v[40:43], a[40:43], v[134:141], v146 abid:1\n\t" \
  "ds_read_b128 v[236:239], v236 offset:4096\n\t" \
  "v_smfmac_f32_16x16x64_bf16 v[44:47], a[44:47], v[134:141], v147\n\t" \
  "ds_read_b64 v[224:225], v250 offset:18432\n\t" \
  "v_smfmac_f32_16x16x64_bf16 v[48:51], a[48:51], v[134:141], v147 abid:1\n\t" \
  "buffer_load_dwordx4 a[124:127], v187, s[12:15], s48 offen offset:3072 sc1\n\t" \
  "ds_read_b64 v[226:227], v251 offset:18432\n\t" \
  "v_smfmac_f32_16x16x64_bf16 v[52:55], a[52:55], v[134:141], v148\n\t" \
  "ds_read_b64 v[228:229], v248 offset:36864\n\t" \
  "v_add_u32_sdwa v248, 0, v116 dst_sel:DWORD dst_unused:UNUSED_PAD src0_sel:DWORD src1_sel:WORD_0\n\t" \
  "v_smfmac_f32_16x16x64_bf16 v[56:59], a[56:59], v[134:141], v148 abid:1\n\t" \
  "ds_read_b64 v[230:231], v249 offset:36864\n\t" \
  "v_add_u32_sdwa v249, 0, v116 dst_sel:DWORD dst_unused:UNUSED_PAD src0_sel:DWORD src1_sel:WORD_1\n\t" \
  "v_smfmac_f32_16x16x64_bf16 v[60:63], a[60:63], v[134:141], v149\n\t" \
  "s_add_u32 s48, s46, 32768\n\t" \
  "s_waitcnt lgkmcnt(2)\n\t" \
  "buffer_load_dwordx4 a[128:131], v187, s[12:15], s48 offen offset:0 sc1\n\t" \
  "ds_read_b64 v[232:233], v250 offset:36864\n\t" \
  "v_add_u32_sdwa v250, 0, v117 dst_sel:DWORD dst_unused:UNUSED_PAD src0_sel:DWORD src1_sel:WORD_0\n\t" \
  "v_smfmac_f32_16x16x64_bf16 v[84:87], v[240:243], v[220:227], v182 abid:1\n\t" \
  "ds_read_b64 v[234:235], v251 offset:36864\n\t" \
  "v_add_u32_sdwa v251, 0, v117 dst_sel:DWORD dst_unused:UNUSED_PAD src0_sel:DWORD src1_sel:WORD_1\n\t" \
  "v_smfmac_f32_16x16x64_bf16 v[64:67], a[64:67], v[134:141], v149 abid:1\n\t" \
  "ds_read_b64 v[220:221], v248 offset:0\n\t" \
  "v_smfmac_f32_16x16x64_bf16 v[68:71], a[68:71], v[134:141], v178 abid:1\n\t" \
  "s_waitcnt vmcnt(15)\n\t" \
  "ds_read_b64 v[222:223], v249 offset:0\n\t" \
  "v_smfmac_f32_16x16x64_bf16 v[0:3], a[72:75], v[126:133], v150\n\t" \
  "s_waitcnt lgkmcnt(2)\n\t" \
  "buffer_load_dwordx4 a[132:135], v187, s[12:15], s48 offen offset:1024 sc1\n\t" \
  "ds_read_b128 v[134:137], v183 offset:22528\n\t" \
  "v_smfmac_f32_16x16x64_bf16 v[48:51], v[240:243], v[228:235], v182 abid:1\n\t" \
  "s_waitcnt vmcnt(15)\n\t" \
  "buffer_load_dwordx4 a[136:139], v187, s[12:15], s48 offen offset:2048 sc1\n\t" \
  "ds_read_b128 v[138:141], v184 offset:22528\n\t" \
  "v_smfmac_f32_16x16x64_bf16 v[4:7], a[76:79], v[126:133], v150 abid:1\n\t" \
  "s_waitcnt vmcnt(15)\n\t" \
  "ds_read_b64 v[224:225], v250 offset:0\n\t" \
  "v_smfmac_f32_16x16x64_bf16 v[8:11], a[80:83], v[126:133], v151\n\t" \
  "s_waitcnt vmcnt(14)\n\t" \
  "ds_read_b64 v[226:227], v251 offset:0\n\t" \
  "v_smfmac_f32_16x16x64_bf16 v[12:15], a[84:87], v[126:133], v151 abid:1\n\t" \
  "s_waitcnt vmcnt(13)\n\t" \
  "buffer_load_dwordx4 a[140:143], v187, s[12:15], s48 offen offset:3072 sc1\n\t" \
  "ds_read_b64 v[228:229], v248 offset:18432\n\t" \
  "v_smfmac_f32_16x16x64_bf16 v[16:19], a[88:91], v[126:133], v152\n\t" \
  "s_waitcnt vmcnt(13)\n\t" \
  "ds_read_b64 v[230:231], v249 offset:18432\n\t" \
  "v_smfmac_f32_16x16x64_bf16 v[20:23], a[92:95], v[126:133], v152 abid:1\n\t" \
  "s_waitcnt lgkmcnt(2)\n\t" \
  "ds_read_b64 v[232:233], v250 offset:18432\n\t" \
  "v_smfmac_f32_16x16x64_bf16 v[16:19], v[236:239], v[220:227], v182 abid:1\n\t" \
  "s_waitcnt vmcnt(12)\n\t" \
  "ds_read_b64 v[234:235], v251 offset:18432\n\t" \
  "v_add_u32 v240, s44, v187\n\t" \
  "v_smfmac_f32_16x16x64_bf16 v[24:27], a[96:99], v[126:133], v153\n\t" \
  "s_waitcnt vmcnt(11)\n\t" \
  "buffer_load_dwordx4 v[158:161], v187, s[12:15], s47 offen offset:1024 sc1\n\t" \
  "ds_read_b128 v[240:243], v240 offset:5120\n\t" \
  "v_smfmac_f32_16x16x64_bf16 v[28:31], a[100:103], v[126:133], v153 abid:1\n\t" \
  "s_waitcnt vmcnt(11)\n\t" \
  "ds_read_b64 v[220:221], v248 offset:36864\n\t" \
  "v_add_u32_sdwa v248, 0, v118 dst_sel:DWORD dst_unused:UNUSED_PAD src0_sel:DWORD src1_sel:WORD_0\n\t" \
  "v_smfmac_f32_16x16x64_bf16 v[32:35], a[104:107], v[126:133], v179\n\t" \
  "s_waitcnt lgkmcnt(2)\n\t" \
  "ds_read_b64 v[222:223], v249 offset:36864\n\t" \
  "v_add_u32_sdwa v249, 0, v118 dst_sel:DWORD dst_unused:UNUSED_PAD src0_sel:DWORD src1_sel:WORD_1\n\t" \
  "v_smfmac_f32_16x16x64_bf16 v[88:91], v[236:239], v[228:235], v182 abid:1\n\t" \
  "ds_read_b64 v[224:225], v250 offset:36864\n\t" \
  "v_add_u32_sdwa v250, 0, v119 dst_sel:DWORD dst_unused:UNUSED_PAD src0_sel:DWORD src1_sel:WORD_0\n\t" \
  "v_smfmac_f32_16x16x64_bf16 v[72:75], a[72:75], v[134:141], v150\n\t" \
  "s_add_u32 s48, s46, 36864\n\t" \
  "buffer_load_dwordx4 a[144:147], v187, s[12:15], s48 offen offset:0 sc1\n\t" \
  "ds_read_b128 v[126:129], v183 offset:40960\n\t" \
  "v_smfmac_f32_16x16x64_bf16 v[76:79], a[76:79], v[134:141], v150 abid:1\n\t" \
  "ds_read_b128 v[130:133], v184 offset:40960\n\t" \
  "v_smfmac_f32_16x16x64_bf16 v[80:83], a[80:83], v[134:141], v151\n\t" \
  "ds_read_b64 v[226:227], v251 offset:36864\n\t" \
  "v_add_u32_sdwa v251, 0, v119 dst_sel:DWORD dst_unused:UNUSED_PAD src0_sel:DWORD src1_sel:WORD_1\n\t" \
  "v_smfmac_f32_16x16x64_bf16 v[84:87], a[84:87], v[134:141], v151 abid:1\n\t" \
  "buffer_load_dwordx4 a[148:151], v187, s[12:15], s48 offen offset:1024 sc1\n\t" \
  "ds_read_b64 v[228:229], v248 offset:0\n\t" \
  "v_smfmac_f32_16x16x64_bf16 v[88:91], a[88:91], v[134:141], v152\n\t" \
  "ds_read_b64 v[230:231], v249 offset:0\n\t" \
  "v_smfmac_f32_16x16x64_bf16 v[92:95], a[92:95], v[134:141], v152 abid:1\n\t" \
  "s_waitcnt lgkmcnt(2)\n\t" \
  "ds_read_b64 v[232:233], v250 offset:0\n\t" \
  "v_smfmac_f32_16x16x64_bf16 v[52:55], v[236:239], v[220:227], v182 abid:1\n\t" \
  "ds_read_b64 v[234:235], v251 offset:0\n\t" \
  "v_smfmac_f32_16x16x64_bf16 v[96:99], a[96:99], v[134:141], v153\n\t" \
  "buffer_load_dwordx4 a[152:155], v187, s[12:15], s48 offen offset:2048 sc1\n\t" \
  "ds_read_b64 v[220:221], v248 offset:18432\n\t" \
  "v_smfmac_f32_16x16x64_bf16 v[100:103], a[100:103], v[134:141], v153 abid:1\n\t" \
  "ds_read_b64 v[222:223], v249 offset:18432\n\t" \
  "v_smfmac_f32_16x16x64_bf16 v[104:107], a[104:107], v[134:141], v179\n\t" \
  "s_waitcnt lgkmcnt(2)\n\t" \
  "ds_read_b64 v[224:225], v250 offset:18432\n\t" \
  "v_smfmac_f32_16x16x64_bf16 v[20:23], v[240:243], v[228:235], v182 abid:1\n\t" \
  "ds_read_b64 v[226:227], v251 offset:18432\n\t" \
  "v_add_u32 v236, s44, v187\n\t" \
  "v_smfmac_f32_16x16x64_bf16 v[36:39], a[72:75], v[126:133], v150\n\t" \
  "buffer_load_dwordx4 a[156:159], v187, s[12:15], s48 offen offset:3072 sc1\n\t" \
  "ds_read_b128 v[134:137], v183 offset:6144\n\t" \
  "v_smfmac_f32_16x16x64_bf16 v[40:43], a[76:79], v[126:133], v150 abid:1\n\t" \
  "ds_read_b128 v[138:141], v184 offset:6144\n\t" \
  "v_smfmac_f32_16x16x64_bf16 v[44:47], a[80:83], v[126:133], v151\n\t" \
  "s_waitcnt lgkmcnt(2)\n\t" \
  "ds_read_b128 v[236:239], v236 offset:6144\n\t" \
  "v_smfmac_f32_16x16x64_bf16 v[92:95], v[240:243], v[220:227], v182 abid:1\n\t" \
  "ds_read_b64 v[228:229], v248 offset:36864\n\t" \
  "v_add_u32_sdwa v248, 0, v120 dst_sel:DWORD dst_unused:UNUSED_PAD src0_sel:DWORD src1_sel:WORD_0\n\t" \
  "v_smfmac_f32_16x16x64_bf16 v[48:51], a[84:87], v[126:133], v151 abid:1\n\t" \
  "s_add_u32 s48, s46, 40960\n\t" \
  "buffer_load_dwordx4 a[160:163], v187, s[12:15], s48 offen offset:0 sc1\n\t" \
  "ds_read_b64 v[230:231], v249 offset:36864\n\t" \
  "v_add_u32_sdwa v249, 0, v120 dst_sel:DWORD dst_unused:UNUSED_PAD src0_sel:DWORD src1_sel:WORD_1\n\t" \
  "v_smfmac_f32_16x16x64_bf16 v[52:55], a[88:91], v[126:133], v152\n\t" \
  "ds_read_b64 v[232:233], v250 offset:36864\n\t" \
  "v_add_u32_sdwa v250, 0, v121 dst_sel:DWORD dst_unused:UNUSED_PAD src0_sel:DWORD src1_sel:WORD_0\n\t" \
  "v_smfmac_f32_16x16x64_bf16 v[56:59], a[92:95], v[126:133], v152 abid:1\n\t" \
  "ds_read_b64 v[234:235], v251 offset:36864\n\t" \
  "v_add_u32_sdwa v251, 0, v121 dst_sel:DWORD dst_unused:UNUSED_PAD src0_sel:DWORD src1_sel:WORD_1\n\t" \
  "v_smfmac_f32_16x16x64_bf16 v[60:63], a[96:99], v[126:133], v153\n\t" \
  "buffer_load_dwordx4 a[164:167], v187, s[12:15], s48 offen offset:1024 sc1\n\t" \
  "ds_read_b64 v[220:221], v248 offset:0\n\t" \
  "v_smfmac_f32_16x16x64_bf16 v[64:67], a[100:103], v[126:133], v153 abid:1\n\t" \
  "ds_read_b64 v[222:223], v249 offset:0\n\t" \
  "v_smfmac_f32_16x16x64_bf16 v[68:71], a[104:107], v[126:133], v179\n\t" \
  "s_waitcnt lgkmcnt(2)\n\t" \
  "ds_read_b64 v[224:225], v250 offset:0\n\t" \
  "v_smfmac_f32_16x16x64_bf16 v[56:59], v[240:243], v[228:235], v182 abid:1\n\t" \
  "s_waitcnt vmcnt(15)\n\t" \
  "ds_read_b64 v[226:227], v251 offset:0\n\t" \
  "v_smfmac_f32_16x16x64_bf16 v[0:3], a[108:111], v[134:141], v154\n\t" \
  "s_waitcnt vmcnt(14)\n\t" \
  "buffer_load_dwordx4 a[168:171], v187, s[12:15], s48 offen offset:2048 sc1\n\t" \
  "ds_read_b128 v[126:129], v183 offset:24576\n\t" \
  "v_smfmac_f32_16x16x64_bf16 v[4:7], a[112:115], v[134:141], v154 abid:1\n\t" \
  "s_waitcnt vmcnt(14)\n\t" \
  "buffer_load_dwordx4 a[172:175], v187, s[12:15], s48 offen offset:3072 sc1\n\t" \
  "ds_read_b128 v[130:133], v184 offset:24576\n\t" \
  "v_smfmac_f32_16x16x64_bf16 v[8:11], a[116:119], v[134:141], v155\n\t" \
  "s_waitcnt lgkmcnt(2)\n\t" \
  "ds_read_b64 v[228:229], v248 offset:18432\n\t" \
  "v_smfmac_f32_16x16x64_bf16 v[24:27], v[236:239], v[220:227], v182 abid:1\n\t" \
  "s_waitcnt vmcnt(14)\n\t" \
  "ds_read_b64 v[230:231], v249 offset:18432\n\t" \
  "v_add_u32 v240, s44, v187\n\t" \
  "v_smfmac_f32_16x16x64_bf16 v[12:15], a[120:123], v[134:141], v155 abid:1\n\t" \
  "s_add_u32 s48, s46, 45056\n\t" \
  "s_waitcnt vmcnt(13)\n\t" \
  "buffer_load_dwordx4 a[176:179], v187, s[12:15], s48 offen offset:0 sc1\n\t" \
  "ds_read_b128 v[240:243], v240 offset:7168\n\t" \
  "v_smfmac_f32_16x16x64_bf16 v[16:19], a[124:127], v[134:141], v156\n\t" \
  "s_waitcnt vmcnt(13)\n\t" \
  "ds_read_b64 v[232:233], v250 offset:18432\n\t" \
  "v_smfmac_f32_16x16x64_bf16 v[20:23], a[128:131], v[134:141], v156 abid:1\n\t" \
  "s_waitcnt vmcnt(12)\n\t" \
  "ds_read_b64 v[234:235], v251 offset:18432\n\t" \
  "v_smfmac_f32_16x16x64_bf16 v[24:27], a[132:135], v[134:141], v157\n\t" \
  "s_waitcnt vmcnt(11)\n\t" \
  "buffer_load_dwordx4 v[162:165], v187, s[12:15], s47 offen offset:2048 sc1\n\t" \
  "ds_read_b64 v[220:221], v248 offset:36864\n\t" \
  "v_add_u32_sdwa v248, 0, v122 dst_sel:DWORD dst_unused:UNUSED_PAD src0_sel:DWORD src1_sel:WORD_0\n\t" \
  "v_smfmac_f32_16x16x64_bf16 v[28:31], a[136:139], v[134:141], v157 abid:1\n\t" \
  "s_waitcnt vmcnt(11)\n\t" \
  "ds_read_b64 v[222:223], v249 offset:36864\n\t" \
  "v_add_u32_sdwa v249, 0, v122 dst_sel:DWORD dst_unused:UNUSED_PAD src0_sel:DWORD src1_sel:WORD_1\n\t" \
  "v_smfmac_f32_16x16x64_bf16 v[32:35], a[140:143], v[134:141], v179 abid:1\n\t" \
  "s_waitcnt lgkmcnt(2)\n\t" \
  "ds_read_b64 v[224:225], v250 offset:36864\n\t" \
  "v_add_u32_sdwa v250, 0, v123 dst_sel:DWORD dst_unused:UNUSED_PAD src0_sel:DWORD src1_sel:WORD_0\n\t" \
  "v_smfmac_f32_16x16x64_bf16 v[96:99], v[236:239], v[228:235], v182 abid:1\n\t" \
  "ds_read_b64 v[226:227], v251 offset:36864\n\t" \
  "v_add_u32_sdwa v251, 0, v123 dst_sel:DWORD dst_unused:UNUSED_PAD src0_sel:DWORD src1_sel:WORD_1\n\t" \
  "v_smfmac_f32_16x16x64_bf16 v[72:75], a[108:111], v[126:133], v154\n\t" \
  "buffer_load_dwordx4 a[180:183], v187, s[12:15], s48 offen offset:1024 sc1\n\t" \
  "ds_read_b128 v[134:137], v183 offset:43008\n\t" \
  "v_smfmac_f32_16x16x64_bf16 v[76:79], a[112:115], v[126:133], v154 abid:1\n\t" \
  "ds_read_b128 v[138:141], v184 offset:43008\n\t" \
  "v_smfmac_f32_16x16x64_bf16 v[80:83], a[116:119], v[126:133], v155\n\t" \
  "s_waitcnt lgkmcnt(2)\n\t" \
  "ds_read_b64 v[228:229], v248 offset:0\n\t" \
  "v_smfmac_f32_16x16x64_bf16 v[60:63], v[236:239], v[220:227], v182 abid:1\n\t" \
  "ds_read_b64 v[230:231], v249 offset:0\n\t" \
  "v_smfmac_f32_16x16x64_bf16 v[84:87], a[120:123], v[126:133], v155 abid:1\n\t" \
  "buffer_load_dwordx4 a[184:187], v187, s[12:15], s48 offen offset:2048 sc1\n\t" \
  "ds_read_b64 v[232:233], v250 offset:0\n\t" \
  "v_smfmac_f32_16x16x64_bf16 v[88:91], a[124:127], v[126:133], v156\n\t" \
  "ds_read_b64 v[234:235], v251 offset:0\n\t" \
  "v_smfmac_f32_16x16x64_bf16 v[92:95], a[128:131], v[126:133], v156 abid:1\n\t" \
  "ds_read_b64 v[220:221], v248 offset:18432\n\t" \
  "v_smfmac_f32_16x16x64_bf16 v[96:99], a[132:135], v[126:133], v157\n\t" \
  "buffer_load_dwordx4 a[188:191], v187, s[12:15], s48 offen offset:3072 sc1\n\t" \
  "ds_read_b64 v[222:223], v249 offset:18432\n\t" \
  "v_smfmac_f32_16x16x64_bf16 v[100:103], a[136:139], v[126:133], v157 abid:1\n\t" \
  "s_waitcnt lgkmcnt(2)\n\t" \
  "ds_read_b64 v[224:225], v250 offset:18432\n\t" \
  "v_smfmac_f32_16x16x64_bf16 v[28:31], v[240:243], v[228:235], v182 abid:1\n\t" \
  "ds_read_b64 v[226:227], v251 offset:18432\n\t" \
  "v_add_u32 v236, s44, v187\n\t" \
  "v_smfmac_f32_16x16x64_bf16 v[104:107], a[140:143], v[126:133], v179 abid:1\n\t" \
  "ds_read_b128 v[236:239], v236 offset:8192\n\t" \
  "v_smfmac_f32_16x16x64_bf16 v[36:39], a[108:111], v[134:141], v154\n\t" \
  "s_add_u32 s48, s46, 49152\n\t" \
  "buffer_load_dwordx4 a[192:195], v187, s[12:15], s48 offen offset:0 sc1\n\t" \
  "ds_read_b128 v[126:129], v183 offset:8192\n\t" \
  "v_smfmac_f32_16x16x64_bf16 v[40:43], a[112:115], v[134:141], v154 abid:1\n\t" \
  "s_waitcnt lgkmcnt(2)\n\t" \
  "ds_read_b128 v[130:133], v184 offset:8192\n\t" \
  "v_smfmac_f32_16x16x64_bf16 v[100:103], v[240:243], v[220:227], v182 abid:1\n\t" \
  "ds_read_b64 v[228:229], v248 offset:36864\n\t" \
  "v_add_u32_sdwa v248, 0, v124 dst_sel:DWORD dst_unused:UNUSED_PAD src0_sel:DWORD src1_sel:WORD_0\n\t" \
  "v_smfmac_f32_16x16x64_bf16 v[44:47], a[116:119], v[134:141], v155\n\t" \
  "ds_read_b64 v[230:231], v249 offset:36864\n\t" \
  "v_add_u32_sdwa v249, 0, v124 dst_sel:DWORD dst_unused:UNUSED_PAD src0_sel:DWORD src1_sel:WORD_1\n\t" \
  "v_smfmac_f32_16x16x64_bf16 v[48:51], a[120:123], v[134:141], v155 abid:1\n\t" \
  "buffer_load_dwordx4 a[196:199], v187, s[12:15], s48 offen offset:1024 sc1\n\t" \
  "ds_read_b64 v[232:233], v250 offset:36864\n\t" \
  "v_add_u32_sdwa v250, 0, v125 dst_sel:DWORD dst_unused:UNUSED_PAD src0_sel:DWORD src1_sel:WORD_0\n\t" \
  "v_smfmac_f32_16x16x64_bf16 v[52:55], a[124:127], v[134:141], v156\n\t" \
  "ds_read_b64 v[234:235], v251 offset:36864\n\t" \
  "v_add_u32_sdwa v251, 0, v125 dst_sel:DWORD dst_unused:UNUSED_PAD src0_sel:DWORD src1_sel:WORD_1\n\t" \
  "v_smfmac_f32_16x16x64_bf16 v[56:59], a[128:131], v[134:141], v156 abid:1\n\t" \
  "ds_read_b64 v[220:221], v248 offset:0\n\t" \
  "v_smfmac_f32_16x16x64_bf16 v[60:63], a[132:135], v[134:141], v157\n\t" \
  "buffer_load_dwordx4 a[200:203], v187, s[12:15], s48 offen offset:2048 sc1\n\t" \
  "ds_read_b64 v[222:223], v249 offset:0\n\t" \
  "v_smfmac_f32_16x16x64_bf16 v[64:67], a[136:139], v[134:141], v157 abid:1\n\t" \
  "s_waitcnt lgkmcnt(2)\n\t" \
  "ds_read_b64 v[224:225], v250 offset:0\n\t" \
  "v_smfmac_f32_16x16x64_bf16 v[64:67], v[240:243], v[228:235], v182 abid:1\n\t" \
  "ds_read_b64 v[226:227], v251 offset:0\n\t" \
  "v_smfmac_f32_16x16x64_bf16 v[68:71], a[140:143], v[134:141], v179 abid:1\n\t" \
  "s_waitcnt vmcnt(15)\n\t" \
  "ds_read_b64 v[228:229], v248 offset:18432\n\t" \
  "v_smfmac_f32_16x16x64_bf16 v[0:3], a[144:147], v[126:133], v158\n\t" \
  "s_waitcnt vmcnt(14)\n\t" \
  "buffer_load_dwordx4 a[204:207], v187, s[12:15], s48 offen offset:3072 sc1\n\t" \
  "ds_read_b128 v[134:137], v183 offset:26624\n\t" \
  "v_smfmac_f32_16x16x64_bf16 v[4:7], a[148:151], v[126:133], v158 abid:1\n\t" \
  "s_add_u32 s48, s46, 53248\n\t" \
  "s_waitcnt lgkmcnt(2)\n\t" \
  "buffer_load_dwordx4 a[208:211], v187, s[12:15], s48 offen offset:0 sc1\n\t" \
  "ds_read_b128 v[138:141], v184 offset:26624\n\t" \
  "v_smfmac_f32_16x16x64_bf16 v[32:35], v[236:239], v[220:227], v182 abid:1\n\t" \
  "s_waitcnt vmcnt(15)\n\t" \
  "ds_read_b64 v[230:231], v249 offset:18432\n\t" \
  "v_mov_b64 v[240:241], 0\n\t" \
  "v_mov_b64 v[242:243], 0\n\t" \
  "v_smfmac_f32_16x16x64_bf16 v[8:11], a[152:155], v[126:133], v159\n\t" \
  "s_waitcnt vmcnt(14)\n\t" \
  "s_mov_b32 exec_hi, 0\n\t" \
  "v_add_u32 v240, s71, v187\n\t" \
  "ds_read_b128 v[240:243], v240\n\t" \
  "s_mov_b32 exec_hi, -1\n\t" \
  "v_smfmac_f32_16x16x64_bf16 v[12:15], a[156:159], v[126:133], v159 abid:1\n\t" \
  "s_waitcnt vmcnt(13)\n\t" \
  "buffer_load_dwordx4 a[212:215], v187, s[12:15], s48 offen offset:1024 sc1\n\t" \
  "ds_read_b64 v[232:233], v250 offset:18432\n\t" \
  "v_smfmac_f32_16x16x64_bf16 v[16:19], a[160:163], v[126:133], v160\n\t" \
  "s_waitcnt vmcnt(13)\n\t" \
  "ds_read_b64 v[234:235], v251 offset:18432\n\t" \
  "v_smfmac_f32_16x16x64_bf16 v[20:23], a[164:167], v[126:133], v160 abid:1\n\t" \
  "s_waitcnt vmcnt(12)\n\t" \
  "ds_read_b64 v[220:221], v248 offset:36864\n\t" \
  "v_add_u32_sdwa v248, 0, v244 dst_sel:DWORD dst_unused:UNUSED_PAD src0_sel:DWORD src1_sel:WORD_0\n\t" \
  "v_smfmac_f32_16x16x64_bf16 v[24:27], a[168:171], v[126:133], v161\n\t" \
  "s_waitcnt vmcnt(11)\n\t" \
  "buffer_load_dwordx4 v[166:169], v187, s[12:15], s47 offen offset:3072 sc1\n\t" \
  "ds_read_b64 v[222:223], v249 offset:36864\n\t" \
  "v_add_u32_sdwa v249, 0, v244 dst_sel:DWORD dst_unused:UNUSED_PAD src0_sel:DWORD src1_sel:WORD_1\n\t" \
  "v_smfmac_f32_16x16x64_bf16 v[28:31], a[172:175], v[126:133], v161 abid:1\n\t" \
  "s_waitcnt lgkmcnt(2)\n\t" \
  "ds_read_b64 v[224:225], v250 offset:36864\n\t" \
  "v_add_u32_sdwa v250, 0, v244 dst_sel:DWORD dst_unused:UNUSED_PAD src0_sel:DWORD src1_sel:WORD_0\n\t" \
  "v_smfmac_f32_16x16x64_bf16 v[104:107], v[236:239], v[228:235], v182 abid:1\n\t" \
  "s_waitcnt vmcnt(11)\n\t" \
  "ds_read_b64 v[226:227], v251 offset:36864\n\t" \
  "v_add_u32_sdwa v251, 0, v244 dst_sel:DWORD dst_unused:UNUSED_PAD src0_sel:DWORD src1_sel:WORD_1\n\t" \
  "v_smfmac_f32_16x16x64_bf16 v[32:35], a[176:179], v[126:133], v180\n\t" \
  "ds_read_b64 v[228:229], v248 offset:0\n\t" \
  "v_smfmac_f32_16x16x64_bf16 v[72:75], a[144:147], v[134:141], v158\n\t" \
  "buffer_load_dwordx4 a[216:219], v187, s[12:15], s48 offen offset:2048 sc1\n\t" \
  "ds_read_b128 v[126:129], v183 offset:45056\n\t" \
  "v_smfmac_f32_16x16x64_bf16 v[76:79], a[148:151], v[134:141], v158 abid:1\n\t" \
  "s_waitcnt lgkmcnt(2)\n\t" \
  "ds_read_b128 v[130:133], v184 offset:45056\n\t" \
  "v_smfmac_f32_16x16x64_bf16 v[68:71], v[236:239], v[220:227], v182 abid:1\n\t" \
  "ds_read_b64 v[230:231], v249 offset:0\n\t" \
  "v_smfmac_f32_16x16x64_bf16 v[80:83], a[152:155], v[134:141], v159\n\t" \
  "ds_read_b64 v[232:233], v250 offset:0\n\t" \
  "v_smfmac_f32_16x16x64_bf16 v[84:87], a[156:159], v[134:141], v159 abid:1\n\t" \
  "buffer_load_dwordx4 a[220:223], v187, s[12:15], s48 offen offset:3072 sc1\n\t" \
  "ds_read_b64 v[234:235], v251 offset:0\n\t" \
  "v_smfmac_f32_16x16x64_bf16 v[88:91], a[160:163], v[134:141], v160\n\t" \
  "ds_read_b64 v[220:221], v248 offset:18432\n\t" \
  "v_smfmac_f32_16x16x64_bf16 v[92:95], a[164:167], v[134:141], v160 abid:1\n\t" \
  "ds_read_b64 v[222:223], v249 offset:18432\n\t" \
  "v_smfmac_f32_16x16x64_bf16 v[96:99], a[168:171], v[134:141], v161\n\t" \
  "s_add_u32 s48, s46, 57344\n\t" \
  "s_waitcnt lgkmcnt(2)\n\t" \
  "buffer_load_dwordx4 a[224:227], v187, s[12:15], s48 offen offset:0 sc1\n\t" \
  "ds_read_b64 v[224:225], v250 offset:18432\n\t" \
  "s_set_gpr_idx_on s6, gpr_idx(DST)\n\t" \
  "v_smfmac_f32_16x16x64_bf16 v[0:3], v[240:243], v[228:235], v182 abid:1\n\t" \
  "s_set_gpr_idx_off\n\t" \
  "ds_read_b64 v[226:227], v251 offset:18432\n\t" \
  "v_add_u32 v236, s60, v187\n\t" \
  "v_smfmac_f32_16x16x64_bf16 v[100:103], a[172:175], v[134:141], v161 abid:1\n\t" \
  "ds_write_b128 v236, v[240:243]\n\t" \
  "v_smfmac_f32_16x16x64_bf16 v[104:107], a[176:179], v[134:141], v180\n\t" \
  "ds_read_b64 v[228:229], v248 offset:36864\n\t" \
  "v_smfmac_f32_16x16x64_bf16 v[36:39], a[144:147], v[126:133], v158\n\t" \
  "s_waitcnt lgkmcnt(2)\n\t" \
  "buffer_load_dwordx4 a[228:231], v187, s[12:15], s48 offen offset:1024 sc1\n\t" \
  "ds_read_b128 v[134:137], v183 offset:10240\n\t" \
  "s_set_gpr_idx_on s6, gpr_idx(DST)\n\t" \
  "v_smfmac_f32_16x16x64_bf16 v[72:75], v[240:243], v[220:227], v182 abid:1\n\t" \
  "s_set_gpr_idx_off\n\t" \
  "ds_read_b128 v[138:141], v184 offset:10240\n\t" \
  "v_smfmac_f32_16x16x64_bf16 v[40:43], a[148:151], v[126:133], v158 abid:1\n\t" \
  "ds_read_b64 v[230:231], v249 offset:36864\n\t" \
  "v_smfmac_f32_16x16x64_bf16 v[44:47], a[152:155], v[126:133], v159\n\t" \
  "ds_read_b64 v[232:233], v250 offset:36864\n\t" \
  "v_smfmac_f32_16x16x64_bf16 v[48:51], a[156:159], v[126:133], v159 abid:1\n\t" \
  "buffer_load_dwordx4 a[232:235], v187, s[12:15], s48 offen offset:2048 sc1\n\t" \
  "ds_read_b64 v[234:235], v251 offset:36864\n\t" \
  "v_smfmac_f32_16x16x64_bf16 v[52:55], a[160:163], v[126:133], v160\n\t" \
  "v_smfmac_f32_16x16x64_bf16 v[56:59], a[164:167], v[126:133], v160 abid:1\n\t" \
  "v_smfmac_f32_16x16x64_bf16 v[60:63], a[168:171], v[126:133], v161\n\t" \
  "s_waitcnt lgkmcnt(0)\n\t" \
  "buffer_load_dwordx4 a[236:239], v187, s[12:15], s48 offen offset:3072 sc1\n\t" \
  "s_set_gpr_idx_on s6, gpr_idx(DST)\n\t" \
  "v_smfmac_f32_16x16x64_bf16 v[36:39], v[240:243], v[228:235], v182 abid:1\n\t" \
  "s_set_gpr_idx_off\n\t" \
  "s_cmp_gt_u32 s61, 1\n\t" \
  "s_cbranch_scc1 .Lmf%=\n\t" \
  ".Lmg%=:\n\t" \
  "s_and_b32 s25, s62, 0x1ff\n\t" \
  "s_cbranch_scc1 .Lsl%=\n\t" \
  ".Lsh%=:\n\t" \
  "v_smfmac_f32_16x16x64_bf16 v[64:67], a[172:175], v[126:133], v161 abid:1\n\t" \
  "s_sub_u32 s56, s72, s56\n\t" \
  "v_smfmac_f32_16x16x64_bf16 v[68:71], a[176:179], v[126:133], v180\n\t" \
  "s_max_i32 s56, s56, s29\n\t" \
  "s_waitcnt vmcnt(15)\n\t" \
  "v_smfmac_f32_16x16x64_bf16 v[0:3], a[180:183], v[134:141], v162\n\t" \
  "s_add_u32 s48, s46, 61440\n\t" \
  "s_waitcnt vmcnt(14)\n\t" \
  "buffer_load_dwordx4 a[240:243], v187, s[12:15], s48 offen offset:0 sc1\n\t" \
  "ds_read_b128 v[126:129], v183 offset:28672\n\t" \
  "v_smfmac_f32_16x16x64_bf16 v[4:7], a[184:187], v[134:141], v162 abid:1\n\t" \
  "s_sub_u32 s56, s72, s56\n\t" \
  "s_waitcnt vmcnt(14)\n\t" \
  "buffer_load_dwordx4 a[244:247], v187, s[12:15], s48 offen offset:1024 sc1\n\t" \
  "ds_read_b128 v[130:133], v184 offset:28672\n\t" \
  "v_smfmac_f32_16x16x64_bf16 v[8:11], a[188:191], v[134:141], v163\n\t" \
  "s_mov_b32 s29, 55296\n\t" \
  "s_waitcnt vmcnt(14)\n\t" \
  "v_smfmac_f32_16x16x64_bf16 v[12:15], a[192:195], v[134:141], v163 abid:1\n\t" \
  "s_add_u32 s52, s52, s56\n\t" \
  "s_waitcnt vmcnt(13)\n\t" \
  "buffer_load_dwordx4 a[248:251], v187, s[12:15], s48 offen offset:2048 sc1\n\t" \
  "v_smfmac_f32_16x16x64_bf16 v[16:19], a[196:199], v[134:141], v164\n\t" \
  "s_and_b32 s27, s64, 7\n\t" \
  "s_waitcnt vmcnt(13)\n\t" \
  "v_smfmac_f32_16x16x64_bf16 v[20:23], a[200:203], v[134:141], v164 abid:1\n\t" \
  "s_lshl2_add_u32 s66, s27, s68\n\t" \
  "s_waitcnt vmcnt(12)\n\t" \
  "v_smfmac_f32_16x16x64_bf16 v[24:27], a[204:207], v[134:141], v165\n\t" \
  "s_add_u32 s47, s47, 4096\n\t" \
  "s_waitcnt vmcnt(11)\n\t" \
  "buffer_load_dwordx4 v[170:173], v187, s[12:15], s47 offen offset:0 sc1\n\t" \
  "v_smfmac_f32_16x16x64_bf16 v[28:31], a[208:211], v[134:141], v165 abid:1\n\t" \
  "s_and_b32 s27, s64, 8\n\t" \
  "s_waitcnt vmcnt(11)\n\t" \
  "v_smfmac_f32_16x16x64_bf16 v[32:35], a[212:215], v[134:141], v180 abid:1\n\t" \
  "s_mul_i32 s27, s27, 176\n\t" \
  "s_waitcnt lgkmcnt(0)\n\t" \
  "v_smfmac_f32_16x16x64_bf16 v[72:75], a[180:183], v[126:133], v162\n\t" \
  "s_add_u32 s66, s66, s27\n\t" \
  "buffer_load_dwordx4 a[252:255], v187, s[12:15], s48 offen offset:3072 sc1\n\t" \
  "ds_read_b128 v[134:137], v183 offset:47104\n\t" \
  "v_smfmac_f32_16x16x64_bf16 v[76:79], a[184:187], v[126:133], v162 abid:1\n\t" \
  "s_lshr_b64 s[64:65], s[64:65], 4\n\t" \
  "ds_read_b128 v[138:141], v184 offset:47104\n\t" \
  "v_smfmac_f32_16x16x64_bf16 v[80:83], a[188:191], v[126:133], v163\n\t" \
  "v_smfmac_f32_16x16x64_bf16 v[84:87], a[192:195], v[126:133], v163 abid:1\n\t" \
  "s_add_u32 s48, s46, 65536\n\t" \
  "buffer_load_dwordx4 v[188:191], v187, s[12:15], s48 offen offset:0 sc1\n\t" \
  "v_smfmac_f32_16x16x64_bf16 v[88:91], a[196:199], v[126:133], v164\n\t" \
  "v_smfmac_f32_16x16x64_bf16 v[92:95], a[200:203], v[126:133], v164 abid:1\n\t" \
  "v_smfmac_f32_16x16x64_bf16 v[96:99], a[204:207], v[126:133], v165\n\t" \
  "buffer_load_dwordx4 v[192:195], v187, s[12:15], s48 offen offset:1024 sc1\n\t" \
  "v_smfmac_f32_16x16x64_bf16 v[100:103], a[208:211], v[126:133], v165 abid:1\n\t" \
  "v_smfmac_f32_16x16x64_bf16 v[104:107], a[212:215], v[126:133], v180 abid:1\n\t" \
  "s_waitcnt lgkmcnt(0)\n\t" \
  "v_smfmac_f32_16x16x64_bf16 v[36:39], a[180:183], v[134:141], v162\n\t" \
  "buffer_load_dwordx4 v[196:199], v187, s[12:15], s48 offen offset:2048 sc1\n\t" \
  "ds_read_b128 v[126:129], v183 offset:12288\n\t" \
  "v_smfmac_f32_16x16x64_bf16 v[40:43], a[184:187], v[134:141], v162 abid:1\n\t" \
  "ds_read_b128 v[130:133], v184 offset:12288\n\t" \
  "v_smfmac_f32_16x16x64_bf16 v[44:47], a[188:191], v[134:141], v163\n\t" \
  "v_smfmac_f32_16x16x64_bf16 v[48:51], a[192:195], v[134:141], v163 abid:1\n\t" \
  "buffer_load_dwordx4 v[200:203], v187, s[12:15], s48 offen offset:3072 sc1\n\t" \
  "v_smfmac_f32_16x16x64_bf16 v[52:55], a[196:199], v[134:141], v164\n\t" \
  "v_smfmac_f32_16x16x64_bf16 v[56:59], a[200:203], v[134:141], v164 abid:1\n\t" \
  "v_smfmac_f32_16x16x64_bf16 v[60:63], a[204:207], v[134:141], v165\n\t" \
  "s_add_u32 s48, s46, 69632\n\t" \
  "buffer_load_dwordx4 v[204:207], v187, s[12:15], s48 offen offset:0 sc1\n\t" \
  "v_smfmac_f32_16x16x64_bf16 v[64:67], a[208:211], v[134:141], v165 abid:1\n\t" \
  "v_smfmac_f32_16x16x64_bf16 v[68:71], a[212:215], v[134:141], v180 abid:1\n\t" \
  "s_waitcnt vmcnt(15) lgkmcnt(0)\n\t" \
  "v_smfmac_f32_16x16x64_bf16 v[0:3], a[216:219], v[126:133], v166\n\t" \
  "s_waitcnt vmcnt(14)\n\t" \
  "buffer_load_dwordx4 v[208:211], v187, s[12:15], s48 offen offset:1024 sc1\n\t" \
  "ds_read_b128 v[134:137], v183 offset:30720\n\t" \
  "v_smfmac_f32_16x16x64_bf16 v[4:7], a[220:223], v[126:133], v166 abid:1\n\t" \
  "s_waitcnt vmcnt(14)\n\t" \
  "buffer_load_dwordx4 v[212:215], v187, s[12:15], s48 offen offset:2048 sc1\n\t" \
  "ds_read_b128 v[138:141], v184 offset:30720\n\t" \
  "v_smfmac_f32_16x16x64_bf16 v[8:11], a[224:227], v[126:133], v167\n\t" \
  "s_waitcnt vmcnt(14)\n\t" \
  "v_smfmac_f32_16x16x64_bf16 v[12:15], a[228:231], v[126:133], v167 abid:1\n\t" \
  "s_waitcnt vmcnt(13)\n\t" \
  "buffer_load_dwordx4 v[216:219], v187, s[12:15], s48 offen offset:3072 sc1\n\t" \
  "v_smfmac_f32_16x16x64_bf16 v[16:19], a[232:235], v[126:133], v168\n\t" \
  "s_waitcnt vmcnt(13)\n\t" \
  "v_smfmac_f32_16x16x64_bf16 v[20:23], a[236:239], v[126:133], v168 abid:1\n\t" \
  "s_waitcnt vmcnt(12)\n\t" \
  "v_smfmac_f32_16x16x64_bf16 v[24:27], a[240:243], v[126:133], v169\n\t" \
  "s_waitcnt vmcnt(11)\n\t" \
  "buffer_load_dwordx4 v[174:177], v187, s[12:15], s47 offen offset:1024 sc1\n\t" \
  "v_smfmac_f32_16x16x64_bf16 v[28:31], a[244:247], v[126:133], v169 abid:1\n\t" \
  "s_waitcnt vmcnt(11)\n\t" \
  "v_smfmac_f32_16x16x64_bf16 v[32:35], a[248:251], v[126:133], v181\n\t" \
  "s_waitcnt lgkmcnt(0)\n\t" \
  "v_smfmac_f32_16x16x64_bf16 v[72:75], a[216:219], v[134:141], v166\n\t" \
  "s_add_u32 s48, s46, 81920\n\t" \
  "buffer_load_dwordx4 v[252:255], v187, s[12:15], s48 offen offset:0 sc1\n\t" \
  "ds_read_b128 v[126:129], v183 offset:49152\n\t" \
  "v_smfmac_f32_16x16x64_bf16 v[76:79], a[220:223], v[134:141], v166 abid:1\n\t" \
  "ds_read_b128 v[130:133], v184 offset:49152\n\t" \
  "v_smfmac_f32_16x16x64_bf16 v[80:83], a[224:227], v[134:141], v167\n\t" \
  "v_smfmac_f32_16x16x64_bf16 v[84:87], a[228:231], v[134:141], v167 abid:1\n\t" \
  "s_add_u32 s48, s46, 73728\n\t" \
  "buffer_load_dwordx4 v[220:223], v187, s[12:15], s48 offen offset:0 sc1\n\t" \
  "v_smfmac_f32_16x16x64_bf16 v[88:91], a[232:235], v[134:141], v168\n\t" \
  "v_smfmac_f32_16x16x64_bf16 v[92:95], a[236:239], v[134:141], v168 abid:1\n\t" \
  "v_smfmac_f32_16x16x64_bf16 v[96:99], a[240:243], v[134:141], v169\n\t" \
  "buffer_load_dwordx4 v[224:227], v187, s[12:15], s48 offen offset:1024 sc1\n\t" \
  "v_smfmac_f32_16x16x64_bf16 v[100:103], a[244:247], v[134:141], v169 abid:1\n\t" \
  "v_smfmac_f32_16x16x64_bf16 v[104:107], a[248:251], v[134:141], v181\n\t" \
  "s_waitcnt lgkmcnt(0)\n\t" \
  "v_smfmac_f32_16x16x64_bf16 v[36:39], a[216:219], v[126:133], v166\n\t" \
  "buffer_load_dwordx4 v[228:231], v187, s[12:15], s48 offen offset:2048 sc1\n\t" \
  "ds_read_b128 v[134:137], v183 offset:14336\n\t" \
  "v_smfmac_f32_16x16x64_bf16 v[40:43], a[220:223], v[126:133], v166 abid:1\n\t" \
  "ds_read_b128 v[138:141], v184 offset:14336\n\t" \
  "v_smfmac_f32_16x16x64_bf16 v[44:47], a[224:227], v[126:133], v167\n\t" \
  "v_smfmac_f32_16x16x64_bf16 v[48:51], a[228:231], v[126:133], v167 abid:1\n\t" \
  "buffer_load_dwordx4 v[232:235], v187, s[12:15], s48 offen offset:3072 sc1\n\t" \
  "v_smfmac_f32_16x16x64_bf16 v[52:55], a[232:235], v[126:133], v168\n\t" \
  "v_smfmac_f32_16x16x64_bf16 v[56:59], a[236:239], v[126:133], v168 abid:1\n\t" \
  "v_smfmac_f32_16x16x64_bf16 v[60:63], a[240:243], v[126:133], v169\n\t" \
  "s_add_u32 s48, s46, 77824\n\t" \
  "buffer_load_dwordx4 v[236:239], v187, s[12:15], s48 offen offset:0 sc1\n\t" \
  "v_smfmac_f32_16x16x64_bf16 v[64:67], a[244:247], v[126:133], v169 abid:1\n\t" \
  "v_smfmac_f32_16x16x64_bf16 v[68:71], a[248:251], v[126:133], v181\n\t" \
  "s_waitcnt vmcnt(15) lgkmcnt(0)\n\t" \
  "v_smfmac_f32_16x16x64_bf16 v[0:3], a[252:255], v[134:141], v170\n\t" \
  "s_waitcnt vmcnt(14)\n\t" \
  "buffer_load_dwordx4 v[240:243], v187, s[12:15], s48 offen offset:1024 sc1\n\t" \
  "ds_read_b128 v[126:129], v183 offset:32768\n\t" \
  "v_smfmac_f32_16x16x64_bf16 v[4:7], v[188:191], v[134:141], v170 abid:1\n\t" \
  "s_waitcnt vmcnt(14)\n\t" \
  "buffer_load_dwordx4 v[244:247], v187, s[12:15], s48 offen offset:2048 sc1\n\t" \
  "ds_read_b128 v[130:133], v184 offset:32768\n\t" \
  "v_smfmac_f32_16x16x64_bf16 v[8:11], v[192:195], v[134:141], v171\n\t" \
  "s_waitcnt vmcnt(14)\n\t" \
  "buffer_load_dwordx4 v[248:251], v187, s[12:15], s48 offen offset:3072 sc1\n\t" \
  "v_smfmac_f32_16x16x64_bf16 v[12:15], v[196:199], v[134:141], v171 abid:1\n\t" \
  "s_waitcnt vmcnt(14)\n\t" \
  "v_smfmac_f32_16x16x64_bf16 v[16:19], v[200:203], v[134:141], v172\n\t" \
  "s_waitcnt vmcnt(13)\n\t" \
  "v_smfmac_f32_16x16x64_bf16 v[20:23], v[204:207], v[134:141], v172 abid:1\n\t" \
  "s_waitcnt vmcnt(12)\n\t" \
  "v_smfmac_f32_16x16x64_bf16 v[24:27], v[208:211], v[134:141], v173\n\t" \
  "s_barrier\n\t" \
  "s_add_u32 m0, s33, 0\n\t" \
  "s_waitcnt vmcnt(11)\n\t" \
  "buffer_load_dwordx4 v186, s[16:19], s34 offen offset:0 nt lds\n\t" \
  "v_smfmac_f32_16x16x64_bf16 v[28:31], v[212:215], v[134:141], v173 abid:1\n\t" \
  "s_add_u32 m0, s33, 1920\n\t" \
  "s_waitcnt vmcnt(11)\n\t" \
  "buffer_load_dwordx4 v186, s[16:19], s34 offen offset:128 nt lds\n\t" \
  "v_smfmac_f32_16x16x64_bf16 v[32:35], v[216:219], v[134:141], v181 abid:1\n\t" \
  "s_add_u32 m0, s33, 3840\n\t" \
  "s_waitcnt lgkmcnt(0)\n\t" \
  "buffer_load_dwordx4 v186, s[16:19], s34 offen offset:256 nt lds\n\t" \
  "v_smfmac_f32_16x16x64_bf16 v[72:75], a[252:255], v[126:133], v170\n\t" \
  "ds_read_b128 v[134:137], v183 offset:16384\n\t" \
  "v_smfmac_f32_16x16x64_bf16 v[76:79], v[188:191], v[126:133], v170 abid:1\n\t" \
  "ds_read_b128 v[138:141], v184 offset:16384\n\t" \
  "v_smfmac_f32_16x16x64_bf16 v[80:83], v[192:195], v[126:133], v171\n\t" \
  "v_smfmac_f32_16x16x64_bf16 v[84:87], v[196:199], v[126:133], v171 abid:1\n\t" \
  "v_smfmac_f32_16x16x64_bf16 v[88:91], v[200:203], v[126:133], v172\n\t" \
  "v_smfmac_f32_16x16x64_bf16 v[92:95], v[204:207], v[126:133], v172 abid:1\n\t" \
  "v_smfmac_f32_16x16x64_bf16 v[96:99], v[208:211], v[126:133], v173\n\t" \
  "v_smfmac_f32_16x16x64_bf16 v[100:103], v[212:215], v[126:133], v173 abid:1\n\t" \
  "v_smfmac_f32_16x16x64_bf16 v[104:107], v[216:219], v[126:133], v181 abid:1\n\t" \
  "ds_read_b128 v[126:129], v183 offset:34816\n\t" \
  "ds_read_b128 v[130:133], v184 offset:34816\n\t" \
  "s_waitcnt vmcnt(11) lgkmcnt(2)\n\t" \
  "v_smfmac_f32_16x16x64_bf16 v[32:35], v[252:255], v[134:141], v182\n\t" \
  "s_add_u32 m0, s33, 5760\n\t" \
  "s_nop 0\n\t" \
  "buffer_load_dwordx4 v186, s[16:19], s34 offen offset:384 nt lds\n\t" \
  "s_waitcnt vmcnt(11)\n\t" \
  "v_smfmac_f32_16x16x64_bf16 v[0:3], v[220:223], v[134:141], v174\n\t" \
  "s_add_u32 m0, s33, 7680\n\t" \
  "s_bitcmp1_b32 s24, 0\n\t" \
  "s_cmov_b32 m0, 0x30000\n\t" \
  "s_cselect_b32 s25, 0x40000000, s34\n\t" \
  "buffer_load_dwordx4 v186, s[16:19], s25 offen offset:512 nt lds\n\t" \
  "s_add_u32 s31, s31, 18432\n\t" \
  "s_sub_u32 s26, s31, 129024\n\t" \
  "s_min_u32 s31, s31, s26\n\t" \
  "s_add_u32 s33, s31, s32\n\t" \
  "s_add_u32 s34, s34, 4718592\n\t" \
  "s_waitcnt lgkmcnt(0)\n\t" \
  "v_smfmac_f32_16x16x64_bf16 v[104:107], v[252:255], v[126:133], v182\n\t" \
  "v_smfmac_f32_16x16x64_bf16 v[72:75], v[220:223], v[126:133], v174\n\t" \
  "s_barrier\n\t" \
  "s_mov_b32 s37, 36864\n\t" \
  "s_movk_i32 s30, 18432\n\t" \
  "s_mov_b32 s38, 0\n\t" \
  "s_waitcnt vmcnt(11)\n\t" \
  "v_smfmac_f32_16x16x64_bf16 v[4:7], v[224:227], v[134:141], v174 abid:1\n\t" \
  "s_mov_b32 s43, 0\n\t" \
  "s_mov_b32 s40, s35\n\t" \
  "s_add_u32 s46, s72, 0x4000\n\t" \
  "s_cmp_eq_u32 s55, 0\n\t" \
  "s_cselect_b32 s48, 7, 6\n\t" \
  "v_cvt_pk_bf16_f32 v32, v32, v33\n\t" \
  "v_cvt_pk_bf16_f32 v33, v34, v35\n\t" \
  "v_xor_b32 v34, s41, v183\n\t" \
  "v_smfmac_f32_16x16x64_bf16 v[76:79], v[224:227], v[126:133], v174 abid:1\n\t" \
  "ds_write_b64 v34, v[32:33] offset:16384\n\t" \
  "v_cvt_pk_bf16_f32 v104, v104, v105\n\t" \
  "v_cvt_pk_bf16_f32 v105, v106, v107\n\t" \
  "v_cvt_pk_bf16_f32 v0, v0, v1\n\t" \
  "v_cvt_pk_bf16_f32 v1, v2, v3\n\t" \
  "s_waitcnt vmcnt(10)\n\t" \
  "v_smfmac_f32_16x16x64_bf16 v[8:11], v[228:231], v[134:141], v175\n\t" \
  "ds_write_b64 v34, v[104:105] offset:34816\n\t" \
  "v_cvt_pk_bf16_f32 v2, v4, v5\n\t" \
  "v_cvt_pk_bf16_f32 v3, v6, v7\n\t" \
  "v_smfmac_f32_16x16x64_bf16 v[80:83], v[228:231], v[126:133], v175\n\t" \
  "ds_write_b128 v185, v[0:3] offset:0\n\t" \
  "v_xor_b32 v106, 64, v185\n\t" \
  "v_add_u32 v107, s44, v187\n\t" \
  "v_lshrrev_b32 v35, 4, v187\n\t" \
  "v_and_b32 v35, 48, v35\n\t" \
  "s_waitcnt vmcnt(9)\n\t" \
  "v_smfmac_f32_16x16x64_bf16 v[12:15], v[232:235], v[134:141], v175 abid:1\n\t" \
  "ds_read_b128 v[0:3], v183 offset:53248\n\t" \
  "ds_read_b128 v[4:7], v184 offset:53248\n\t" \
  "v_xor_b32 v34, v35, v187\n\t" \
  "v_and_b32 v34, 0xf0, v34\n\t" \
  "v_smfmac_f32_16x16x64_bf16 v[84:87], v[232:235], v[126:133], v175 abid:1\n\t" \
  "s_add_u32 m0, s33, 0\n\t" \
  "s_nop 0\n\t" \
  "buffer_load_dwordx4 v186, s[16:19], s34 offen offset:0 nt lds\n\t" \
  "s_movk_i32 s27, 72\n\t" \
  "v_mad_u32_u24 v34, v35, s27, v34\n\t" \
  "v_or_b32 v35, s45, v35\n\t" \
  "v_cvt_pk_bf16_f32 v8, v8, v9\n\t" \
  "v_cvt_pk_bf16_f32 v9, v10, v11\n\t" \
  "s_waitcnt vmcnt(9)\n\t" \
  "v_smfmac_f32_16x16x64_bf16 v[16:19], v[236:239], v[134:141], v176\n\t" \
  "v_cvt_pk_bf16_f32 v10, v12, v13\n\t" \
  "v_cvt_pk_bf16_f32 v11, v14, v15\n\t" \
  "v_smfmac_f32_16x16x64_bf16 v[88:91], v[236:239], v[126:133], v176\n\t" \
  "ds_write_b128 v106, v[8:11] offset:0\n\t" \
  "v_cvt_pk_bf16_f32 v72, v72, v73\n\t" \
  "v_cvt_pk_bf16_f32 v73, v74, v75\n\t" \
  "v_cvt_pk_bf16_f32 v74, v76, v77\n\t" \
  "v_cvt_pk_bf16_f32 v75, v78, v79\n\t" \
  "s_waitcnt vmcnt(8)\n\t" \
  "v_smfmac_f32_16x16x64_bf16 v[20:23], v[240:243], v[134:141], v176 abid:1\n\t" \
  "ds_read_b128 v[8:11], v183 offset:51200\n\t" \
  "ds_read_b128 v[12:15], v184 offset:51200\n\t" \
  "v_smfmac_f32_16x16x64_bf16 v[92:95], v[240:243], v[126:133], v176 abid:1\n\t" \
  "ds_write_b128 v185, v[72:75] offset:18432\n\t" \
  "v_cvt_pk_bf16_f32 v16, v16, v17\n\t" \
  "v_cvt_pk_bf16_f32 v17, v18, v19\n\t" \
  "v_add_u32 v183, 55296, v183\n\t" \
  "v_add_u32 v184, 55296, v184\n\t" \
  "s_waitcnt vmcnt(7)\n\t" \
  "v_smfmac_f32_16x16x64_bf16 v[24:27], v[244:247], v[134:141], v177\n\t" \
  "s_add_u32 m0, s33, 1920\n\t" \
  "s_nop 0\n\t" \
  "buffer_load_dwordx4 v186, s[16:19], s34 offen offset:128 nt lds\n\t" \
  "v_cvt_pk_bf16_f32 v18, v20, v21\n\t" \
  "v_cvt_pk_bf16_f32 v19, v22, v23\n\t" \
  "v_smfmac_f32_16x16x64_bf16 v[96:99], v[244:247], v[126:133], v177\n\t" \
  "ds_write_b128 v185, v[16:19] offset:128\n\t" \
  "v_cvt_pk_bf16_f32 v80, v80, v81\n\t" \
  "v_cvt_pk_bf16_f32 v81, v82, v83\n\t" \
  "v_cvt_pk_bf16_f32 v82, v84, v85\n\t" \
  "v_cvt_pk_bf16_f32 v83, v86, v87\n\t" \
  "s_waitcnt vmcnt(7)\n\t" \
  "v_smfmac_f32_16x16x64_bf16 v[28:31], v[248:251], v[134:141], v177 abid:1\n\t" \
  "ds_write_b128 v106, v[80:83] offset:18432\n\t" \
  "v_smfmac_f32_16x16x64_bf16 v[100:103], v[248:251], v[126:133], v177 abid:1\n\t" \
  "v_lshrrev_b32 v32, 2, v187\n\t" \
  "s_cmp_gt_u32 s61, 2\n\t" \
  "v_add_u32 v32, s60, v32\n\t" \
  "s_cbranch_scc1 .Ln4%=\n\t" \
  ".Ln3%=:\n\t" \
  "ds_read2st64_b32 v[32:33], v32 offset0:4 offset1:9\n\t" \
  "v_cvt_pk_bf16_f32 v88, v88, v89\n\t" \
  "v_cvt_pk_bf16_f32 v89, v90, v91\n\t" \
  "s_waitcnt lgkmcnt(4)\n\t" \
  "v_smfmac_f32_16x16x64_bf16 v[36:39], a[252:255], v[8:15], v170\n\t" \
  "s_lshl_b32 s28, %[sBID], 3\n\t" \
  "s_memrealtime s[72:73]\n\t" \
  "ds_read_b128 v[72:75], v107 offset:0\n\t" \
  "v_cvt_pk_bf16_f32 v90, v92, v93\n\t" \
  "v_cvt_pk_bf16_f32 v91, v94, v95\n\t" \
  "v_smfmac_f32_16x16x64_bf16 v[40:43], v[188:191], v[8:15], v170 abid:1\n\t" \
  "ds_write_b128 v185, v[88:91] offset:18560\n\t" \
  "v_cvt_pk_bf16_f32 v24, v24, v25\n\t" \
  "v_cvt_pk_bf16_f32 v25, v26, v27\n\t" \
  "v_smfmac_f32_16x16x64_bf16 v[44:47], v[192:195], v[8:15], v171\n\t" \
  "s_cmp_lg_u32 s24, 1\n\t" \
  "s_cmov_b32 s28, 0x40000000\n\t" \
  "ds_read_b128 v[76:79], v107 offset:1024\n\t" \
  "v_cvt_pk_bf16_f32 v26, v28, v29\n\t" \
  "v_cvt_pk_bf16_f32 v27, v30, v31\n\t" \
  "v_smfmac_f32_16x16x64_bf16 v[48:51], v[196:199], v[8:15], v171 abid:1\n\t" \
  "ds_write_b128 v106, v[24:27] offset:128\n\t" \
  "v_cvt_pk_bf16_f32 v96, v96, v97\n\t" \
  "v_cvt_pk_bf16_f32 v97, v98, v99\n\t" \
  "v_smfmac_f32_16x16x64_bf16 v[52:55], v[200:203], v[8:15], v172\n\t" \
  "ds_read_b128 v[80:83], v107 offset:2048\n\t" \
  "v_cvt_pk_bf16_f32 v98, v100, v101\n\t" \
  "v_cvt_pk_bf16_f32 v99, v102, v103\n\t" \
  "v_smfmac_f32_16x16x64_bf16 v[56:59], v[204:207], v[8:15], v172 abid:1\n\t" \
  "ds_write_b128 v106, v[96:99] offset:18560\n\t" \
  "s_add_u32 m0, s33, 3840\n\t" \
  "s_nop 0\n\t" \
  "buffer_load_dwordx4 v186, s[16:19], s34 offen offset:256 nt lds\n\t" \
  "v_mov_b32 v126, s57\n\t" \
  "v_mov_b32 v127, s58\n\t" \
  "v_smfmac_f32_16x16x64_bf16 v[60:63], v[208:211], v[8:15], v173\n\t" \
  "ds_read_b128 v[84:87], v107 offset:3072\n\t" \
  "ds_read_b128 v[88:91], v107 offset:4096\n\t" \
  "s_add_u32 m0, s33, 5760\n\t" \
  "s_nop 0\n\t" \
  "buffer_load_dwordx4 v186, s[16:19], s34 offen offset:384 nt lds\n\t" \
  "v_smfmac_f32_16x16x64_bf16 v[64:67], v[212:215], v[8:15], v173 abid:1\n\t" \
  "ds_read_b128 v[92:95], v107 offset:5120\n\t" \
  "ds_read_b128 v[96:99], v107 offset:6144\n\t" \
  "s_add_u32 m0, s33, 7680\n\t" \
  "s_bitcmp1_b32 s24, 0\n\t" \
  "s_cmov_b32 m0, 0x30000\n\t" \
  "s_cselect_b32 s25, 0x40000000, s34\n\t" \
  "buffer_load_dwordx4 v186, s[16:19], s25 offen offset:512 nt lds\n\t" \
  "v_smfmac_f32_16x16x64_bf16 v[68:71], v[216:219], v[8:15], v181 abid:1\n\t" \
  "ds_read_b128 v[100:103], v107 offset:7168\n\t" \
  "ds_read_b128 v[104:107], v107 offset:8192\n\t" \
  "s_mov_b64 exec, 1\n\t" \
  "buffer_store_dwordx2 v[126:127], off, s[12:15], s28 offset:256 sc1\n\t" \
  "s_mov_b64 exec, -1\n\t" \
  "s_add_u32 s31, s31, 18432\n\t" \
  "s_sub_u32 s26, s31, 129024\n\t" \
  "s_min_u32 s31, s31, s26\n\t" \
  "s_add_u32 s33, s31, s32\n\t" \
  "s_add_u32 s34, s34, 4718592\n\t" \
  "s_or_b32 s25, s62, s63\n\t" \
  "s_cbranch_scc1 .Lq2%=\n\t" \
  ".Lq3%=:\n\t" \
  "s_setpc_b64 s[66:67]\n\t" \
  ".Lhop0%=:\n\t" \
  "s_cbranch_execz .Lhop4%=\n\t" \
  ".Lfin%=:\n\t" \
  "s_waitcnt lgkmcnt(0)\n\t" \
  "s_barrier\n\t" \
  "v_smfmac_f32_16x16x64_bf16 v[68:71], v[252:255], v[0:7], v182\n\t" \
  "s_and_b32 s25, s32, 1024\n\t" \
  "v_add_u32 v134, s25, v187\n\t" \
  "v_smfmac_f32_16x16x64_bf16 v[36:39], v[220:223], v[0:7], v174\n\t" \
  "v_add_u32 v24, s43, v134\n\t" \
  "ds_read_b128 v[24:27], v24 offset:16384\n\t" \
  ".Lhop1%=:\n\t" \
  "s_cbranch_execz .Lhop5%=\n\t" \
  "s_add_u32 s43, s43, 18432\n\t" \
  "s_sub_u32 s42, s43, 129024\n\t" \
  "s_min_u32 s43, s43, s42\n\t" \
  "v_smfmac_f32_16x16x64_bf16 v[40:43], v[224:227], v[0:7], v174 abid:1\n\t" \
  "v_add_u32 v28, s43, v134\n\t" \
  "ds_read_b128 v[28:31], v28 offset:16384\n\t" \
  "s_add_u32 s43, s43, 18432\n\t" \
  "s_sub_u32 s42, s43, 129024\n\t" \
  ".Lhop2%=:\n\t" \
  "s_cbranch_execz .Lhop6%=\n\t" \
  "s_min_u32 s43, s43, s42\n\t" \
  "v_smfmac_f32_16x16x64_bf16 v[44:47], v[228:231], v[0:7], v175\n\t" \
  "s_bitcmp0_b32 s24, 0\n\t" \
  "s_cmov_b32 s40, 0x40000000\n\t" \
  "v_xor_b32 v127, s41, v183\n\t" \
  "v_subrev_u32 v127, s30, v127\n\t" \
  "v_smfmac_f32_16x16x64_bf16 v[48:51], v[232:235], v[0:7], v175 abid:1\n\t" \
  "v_cvt_pk_bf16_f32 v68, v68, v69\n\t" \
  ".Lhop3%=:\n\t" \
  "s_cbranch_execz .Lhop7%=\n\t" \
  "v_cvt_pk_bf16_f32 v69, v70, v71\n\t" \
  "ds_write_b64 v127, v[68:69] offset:16384\n\t" \
  "v_add_u32 v126, s37, v185\n\t" \
  "v_xor_b32 v128, 64, v126\n\t" \
  "v_smfmac_f32_16x16x64_bf16 v[52:55], v[236:239], v[0:7], v176\n\t" \
  "v_smfmac_f32_16x16x64_bf16 v[56:59], v[240:243], v[0:7], v176 abid:1\n\t" \
  "v_cvt_pk_bf16_f32 v36, v36, v37\n\t" \
  ".Lhop4%=:\n\t" \
  "s_cbranch_execz .Lhop8%=\n\t" \
  "v_cvt_pk_bf16_f32 v37, v38, v39\n\t" \
  "v_cvt_pk_bf16_f32 v38, v40, v41\n\t" \
  "v_cvt_pk_bf16_f32 v39, v42, v43\n\t" \
  "ds_write_b128 v126, v[36:39] offset:0\n\t" \
  "v_smfmac_f32_16x16x64_bf16 v[60:63], v[244:247], v[0:7], v177\n\t" \
  "v_smfmac_f32_16x16x64_bf16 v[64:67], v[248:251], v[0:7], v177 abid:1\n\t" \
  "v_cvt_pk_bf16_f32 v44, v44, v45\n\t" \
  ".Lhop5%=:\n\t" \
  "s_cbranch_execz .Lhop9%=\n\t" \
  "v_cvt_pk_bf16_f32 v45, v46, v47\n\t" \
  "v_cvt_pk_bf16_f32 v46, v48, v49\n\t" \
  "v_cvt_pk_bf16_f32 v47, v50, v51\n\t" \
  "ds_write_b128 v128, v[44:47] offset:0\n\t" \
  "s_waitcnt lgkmcnt(4)\n\t" \
  "buffer_store_dwordx4 v[24:27], v186, s[20:23], s40 offen offset:384 sc1\n\t" \
  "s_add_u32 s38, s38, s49\n\t" \
  "s_add_u32 s40, s40, 4718592\n\t" \
  ".Lhop6%=:\n\t" \
  "s_cbranch_execz .Lhop10%=\n\t" \
  "v_cvt_pk_bf16_f32 v52, v52, v53\n\t" \
  "v_cvt_pk_bf16_f32 v53, v54, v55\n\t" \
  "v_cvt_pk_bf16_f32 v54, v56, v57\n\t" \
  "v_cvt_pk_bf16_f32 v55, v58, v59\n\t" \
  "ds_write_b128 v126, v[52:55] offset:128\n\t" \
  "v_add_u32 v135, s38, v187\n\t" \
  "v_xad_u32 v136, v187, 64, s38\n\t" \
  ".Lhop7%=:\n\t" \
  "s_cbranch_execz .Lhe%=\n\t" \
  "v_cvt_pk_bf16_f32 v60, v60, v61\n\t" \
  "v_cvt_pk_bf16_f32 v61, v62, v63\n\t" \
  "v_cvt_pk_bf16_f32 v62, v64, v65\n\t" \
  "v_cvt_pk_bf16_f32 v63, v66, v67\n\t" \
  "ds_write_b128 v128, v[60:63] offset:128\n\t" \
  "ds_read_b128 v[8:11], v135 offset:0\n\t" \
  "ds_read_b128 v[12:15], v136 offset:1024\n\t" \
  ".Lhop8%=:\n\t" \
  "s_cbranch_execz .Lmf%=\n\t" \
  "ds_read_b128 v[16:19], v135 offset:2048\n\t" \
  "ds_read_b128 v[20:23], v136 offset:3072\n\t" \
  "s_waitcnt lgkmcnt(9)\n\t" \
  "buffer_store_dwordx4 v[28:31], v186, s[20:23], s40 offen offset:384 sc1\n\t" \
  "s_add_u32 s40, s40, 4718592\n\t" \
  "v_add_u32 v24, s43, v134\n\t" \
  "s_add_u32 s36, s35, s39\n\t" \
  "s_waitcnt lgkmcnt(3)\n\t" \
  "buffer_store_dwordx4 v[8:11], v34, s[20:23], s36 offen sc1\n\t" \
  ".Lhop9%=:\n\t" \
  "s_cbranch_execz .Lhopy%=\n\t" \
  "s_barrier\n\t" \
  "ds_read_b128 v[24:27], v24 offset:16384\n\t" \
  "s_add_u32 s36, s36, 4608\n\t" \
  "s_waitcnt lgkmcnt(3)\n\t" \
  "buffer_store_dwordx4 v[12:15], v34, s[20:23], s36 offen sc1\n\t" \
  "s_add_u32 s36, s36, 4608\n\t" \
  "s_waitcnt lgkmcnt(2)\n\t" \
  "buffer_store_dwordx4 v[16:19], v34, s[20:23], s36 offen sc1\n\t" \
  "s_add_u32 s36, s36, 4608\n\t" \
  ".Lhop10%=:\n\t" \
  "s_cbranch_execz .Lhopy%=\n\t" \
  "s_waitcnt lgkmcnt(1)\n\t" \
  "buffer_store_dwordx4 v[20:23], v34, s[20:23], s36 offen sc1\n\t" \
  "s_waitcnt lgkmcnt(0)\n\t" \
  "buffer_store_dwordx4 v[24:27], v186, s[20:23], s40 offen offset:384 sc1\n\t" \
  "s_cmp_lg_u32 s63, 0\n\t" \
  "s_cbranch_scc1 .Lez%=\n\t" \
  ".Ley%=:\n\t" \
  "s_endpgm\n\t" \
  ".Lrc%=:\n\t" \
  "s_lshr_b32 s26, s55, 1\n\t" \
  "s_and_b32 s52, s55, 1\n\t" \
  "s_lshl2_add_u32 s52, s52, s24\n\t" \
  "s_cmp_eq_u32 s55, 8\n\t" \
  "s_cmov_b32 s26, s24\n\t" \
  "s_cmov_b32 s52, 8\n\t" \
  "s_mul_i32 s54, s26, 3\n\t" \
  "s_add_u32 s54, s54, s56\n\t" \
  "s_mul_i32 s51, s54, 9\n\t" \
  "s_add_u32 s51, s51, s52\n\t" \
  "s_lshl_b32 s51, s51, 7\n\t" \
  "s_add_u32 s51, s51, 4096\n\t" \
  "v_bfe_u32 v163, v0, 3, 3\n\t" \
  "s_movk_i32 s26, 1088\n\t" \
  "v_and_b32 v158, 4, v0\n\t" \
  "s_add_u32 s27, s41, 131584\n\t" \
  "v_lshl_add_u32 v158, v158, 7, s27\n\t" \
  "v_mad_u32_u24 v163, v163, s26, v158\n\t" \
  "v_and_b32 v158, 3, v0\n\t" \
  "v_lshl_add_u32 v163, v158, 4, v163\n\t" \
  "v_mov_b32 v160, 0\n\t" \
  "s_mul_i32 s27, s56, 6144\n\t" \
  "v_and_b32 v159, 60, v0\n\t" \
  "v_lshl_add_u32 v164, v158, 9, s27\n\t" \
  "v_lshl_add_u32 v164, v159, 1, v164\n\t" \
  "s_mul_i32 s28, s24, 9216\n\t" \
  "s_add_u32 s28, s28, 92160\n\t" \
  "v_lshl_add_u32 v165, v158, 6, s28\n\t" \
  "v_and_b32 v159, 48, v0\n\t" \
  "v_lshl_add_u32 v165, v159, 4, v165\n\t" \
  "v_and_b32 v159, 12, v0\n\t" \
  "v_add_u32 v165, v165, v159\n\t" \
  "v_add_u32 v153, s28, v187\n\t" \
  "s_add_u32 s28, s28, 3072\n\t" \
  "s_bitcmp0_b32 s24, 0\n\t" \
  "s_waitcnt vmcnt(6)\n\t" \
  "s_cbranch_scc1 .Lr1%=\n\t" \
  "s_waitcnt vmcnt(5)\n\t" \
  ".Lr1%=:\n\t" \
  "s_barrier\n\t" \
  "ds_read_b64 v[72:73], v163 offset:0\n\t" \
  "ds_read_b64 v[74:75], v163 offset:128\n\t" \
  "ds_read_b64 v[76:77], v163 offset:256\n\t" \
  "ds_read_b64 v[78:79], v163 offset:384\n\t" \
  "ds_read_b64 v[80:81], v163 offset:8704\n\t" \
  "ds_read_b64 v[82:83], v163 offset:8832\n\t" \
  "ds_read_b64 v[84:85], v163 offset:8960\n\t" \
  "ds_read_b64 v[86:87], v163 offset:9088\n\t" \
  "ds_read_b64 v[88:89], v163 offset:17408\n\t" \
  "ds_read_b64 v[90:91], v163 offset:17536\n\t" \
  "ds_read_b64 v[92:93], v163 offset:17664\n\t" \
  "ds_read_b64 v[94:95], v163 offset:17792\n\t" \
  "v_readfirstlane_b32 s9, v142\n\t" \
  "v_readfirstlane_b32 s58, v143\n\t" \
  "s_mul_i32 s57, s9, 0x9E3779B1\n\t" \
  "s_add_u32 s57, s57, 0x632BE5AB\n\t" \
  "s_and_b32 s57, s57, 0xffffff00\n\t" \
  "s_mul_i32 s70, s9, 0x85EBCA6B\n\t" \
  "s_xor_b32 s58, s58, s70\n\t" \
  "s_add_u32 s58, s58, 0x3C6EF372\n\t" \
  "s_mov_b32 s27, 0x80008000\n\t" \
  "s_waitcnt lgkmcnt(8)\n\t" \
  "v_and_b32 v167, s25, v72\n\t" \
  "v_and_b32 v175, s25, v73\n\t" \
  "v_and_b32 v168, s25, v74\n\t" \
  "v_and_b32 v176, s25, v75\n\t" \
  "v_and_b32 v169, s25, v76\n\t" \
  "v_and_b32 v177, s25, v77\n\t" \
  "v_and_b32 v170, s25, v78\n\t" \
  "v_and_b32 v178, s25, v79\n\t" \
  "v_pk_max_u16 v171, v167, v168\n\t" \
  "v_pk_max_u16 v179, v175, v176\n\t" \
  "v_pk_max_u16 v172, v169, v170\n\t" \
  "v_pk_max_u16 v180, v177, v178\n\t" \
  "v_pk_min_u16 v171, v171, v172\n\t" \
  "v_pk_min_u16 v179, v179, v180\n\t" \
  "v_pk_min_u16 v172, v167, v168\n\t" \
  "v_pk_min_u16 v180, v175, v176\n\t" \
  "v_pk_max_u16 v171, v171, v172\n\t" \
  "v_pk_max_u16 v179, v179, v180\n\t" \
  "v_pk_min_u16 v172, v169, v170\n\t" \
  "v_pk_min_u16 v180, v177, v178\n\t" \
  "v_pk_max_u16 v171, v171, v172\n\t" \
  "v_pk_max_u16 v179, v179, v180\n\t" \
  "v_pk_add_u16 v172, v168, -1 op_sel_hi:[1,0]\n\t" \
  "v_pk_add_u16 v180, v176, -1 op_sel_hi:[1,0]\n\t" \
  "v_pk_add_u16 v173, v169, -1 op_sel_hi:[1,0]\n\t" \
  "v_pk_add_u16 v181, v177, -1 op_sel_hi:[1,0]\n\t" \
  "v_pk_sub_i16 v167, v167, v171\n\t" \
  "v_pk_sub_i16 v175, v175, v179\n\t" \
  "v_pk_sub_i16 v168, v168, v171\n\t" \
  "v_pk_sub_i16 v176, v176, v179\n\t" \
  "v_pk_sub_i16 v169, v169, v171\n\t" \
  "v_pk_sub_i16 v177, v177, v179\n\t" \
  "v_pk_sub_i16 v170, v170, v171\n\t" \
  "v_pk_sub_i16 v178, v178, v179\n\t" \
  "v_and_or_b32 v171, v169, v170, v167\n\t" \
  "v_and_or_b32 v179, v177, v178, v175\n\t" \
  "v_and_or_b32 v174, v167, v168, v170\n\t" \
  "v_and_or_b32 v182, v175, v176, v178\n\t" \
  "v_or3_b32 v171, v171, v168, v172\n\t" \
  "v_or3_b32 v179, v179, v176, v180\n\t" \
  "v_or3_b32 v174, v174, v169, v173\n\t" \
  "v_or3_b32 v182, v182, v177, v181\n\t" \
  "v_lshrrev_b32 v160, 1, v160\n\t" \
  "v_bitop3_b32 v160, v160, v171, s27 bitop3:0xf2\n\t" \
  "v_lshrrev_b32 v160, 1, v160\n\t" \
  "v_bitop3_b32 v160, v160, v179, s27 bitop3:0xf2\n\t" \
  "v_lshrrev_b32 v160, 1, v160\n\t" \
  "v_bitop3_b32 v160, v160, v174, s27 bitop3:0xf2\n\t" \
  "v_lshrrev_b32 v160, 1, v160\n\t" \
  "v_bitop3_b32 v160, v160, v182, s27 bitop3:0xf2\n\t" \
  "s_waitcnt lgkmcnt(4)\n\t" \
  "v_and_b32 v167, s25, v80\n\t" \
  "v_and_b32 v175, s25, v81\n\t" \
  "v_and_b32 v168, s25, v82\n\t" \
  "v_and_b32 v176, s25, v83\n\t" \
  "v_and_b32 v169, s25, v84\n\t" \
  "v_and_b32 v177, s25, v85\n\t" \
  "v_and_b32 v170, s25, v86\n\t" \
  "v_and_b32 v178, s25, v87\n\t" \
  "v_pk_max_u16 v171, v167, v168\n\t" \
  "v_pk_max_u16 v179, v175, v176\n\t" \
  "v_pk_max_u16 v172, v169, v170\n\t" \
  "v_pk_max_u16 v180, v177, v178\n\t" \
  "v_pk_min_u16 v171, v171, v172\n\t" \
  "v_pk_min_u16 v179, v179, v180\n\t" \
  "v_pk_min_u16 v172, v167, v168\n\t" \
  "v_pk_min_u16 v180, v175, v176\n\t" \
  "v_pk_max_u16 v171, v171, v172\n\t" \
  "v_pk_max_u16 v179, v179, v180\n\t" \
  "v_pk_min_u16 v172, v169, v170\n\t" \
  "v_pk_min_u16 v180, v177, v178\n\t" \
  "v_pk_max_u16 v171, v171, v172\n\t" \
  "v_pk_max_u16 v179, v179, v180\n\t" \
  "v_pk_add_u16 v172, v168, -1 op_sel_hi:[1,0]\n\t" \
  "v_pk_add_u16 v180, v176, -1 op_sel_hi:[1,0]\n\t" \
  "v_pk_add_u16 v173, v169, -1 op_sel_hi:[1,0]\n\t" \
  "v_pk_add_u16 v181, v177, -1 op_sel_hi:[1,0]\n\t" \
  "v_pk_sub_i16 v167, v167, v171\n\t" \
  "v_pk_sub_i16 v175, v175, v179\n\t" \
  "v_pk_sub_i16 v168, v168, v171\n\t" \
  "v_pk_sub_i16 v176, v176, v179\n\t" \
  "v_pk_sub_i16 v169, v169, v171\n\t" \
  "v_pk_sub_i16 v177, v177, v179\n\t" \
  "v_pk_sub_i16 v170, v170, v171\n\t" \
  "v_pk_sub_i16 v178, v178, v179\n\t" \
  "v_and_or_b32 v171, v169, v170, v167\n\t" \
  "v_and_or_b32 v179, v177, v178, v175\n\t" \
  "v_and_or_b32 v174, v167, v168, v170\n\t" \
  "v_and_or_b32 v182, v175, v176, v178\n\t" \
  "v_or3_b32 v171, v171, v168, v172\n\t" \
  "v_or3_b32 v179, v179, v176, v180\n\t" \
  "v_or3_b32 v174, v174, v169, v173\n\t" \
  "v_or3_b32 v182, v182, v177, v181\n\t" \
  "v_lshrrev_b32 v160, 1, v160\n\t" \
  "v_bitop3_b32 v160, v160, v171, s27 bitop3:0xf2\n\t" \
  "v_lshrrev_b32 v160, 1, v160\n\t" \
  "v_bitop3_b32 v160, v160, v179, s27 bitop3:0xf2\n\t" \
  "v_lshrrev_b32 v160, 1, v160\n\t" \
  "v_bitop3_b32 v160, v160, v174, s27 bitop3:0xf2\n\t" \
  "v_lshrrev_b32 v160, 1, v160\n\t" \
  "v_bitop3_b32 v160, v160, v182, s27 bitop3:0xf2\n\t" \
  "s_waitcnt lgkmcnt(0)\n\t" \
  "v_and_b32 v167, s25, v88\n\t" \
  "v_and_b32 v175, s25, v89\n\t" \
  "v_and_b32 v168, s25, v90\n\t" \
  "v_and_b32 v176, s25, v91\n\t" \
  "v_and_b32 v169, s25, v92\n\t" \
  "v_and_b32 v177, s25, v93\n\t" \
  "v_and_b32 v170, s25, v94\n\t" \
  "v_and_b32 v178, s25, v95\n\t" \
  "v_pk_max_u16 v171, v167, v168\n\t" \
  "v_pk_max_u16 v179, v175, v176\n\t" \
  "v_pk_max_u16 v172, v169, v170\n\t" \
  "v_pk_max_u16 v180, v177, v178\n\t" \
  "v_pk_min_u16 v171, v171, v172\n\t" \
  "v_pk_min_u16 v179, v179, v180\n\t" \
  "v_pk_min_u16 v172, v167, v168\n\t" \
  "v_pk_min_u16 v180, v175, v176\n\t" \
  "v_pk_max_u16 v171, v171, v172\n\t" \
  "v_pk_max_u16 v179, v179, v180\n\t" \
  "v_pk_min_u16 v172, v169, v170\n\t" \
  "v_pk_min_u16 v180, v177, v178\n\t" \
  "v_pk_max_u16 v171, v171, v172\n\t" \
  "v_pk_max_u16 v179, v179, v180\n\t" \
  "v_pk_add_u16 v172, v168, -1 op_sel_hi:[1,0]\n\t" \
  "v_pk_add_u16 v180, v176, -1 op_sel_hi:[1,0]\n\t" \
  "v_pk_add_u16 v173, v169, -1 op_sel_hi:[1,0]\n\t" \
  "v_pk_add_u16 v181, v177, -1 op_sel_hi:[1,0]\n\t" \
  "v_pk_sub_i16 v167, v167, v171\n\t" \
  "v_pk_sub_i16 v175, v175, v179\n\t" \
  "v_pk_sub_i16 v168, v168, v171\n\t" \
  "v_pk_sub_i16 v176, v176, v179\n\t" \
  "v_pk_sub_i16 v169, v169, v171\n\t" \
  "v_pk_sub_i16 v177, v177, v179\n\t" \
  "v_pk_sub_i16 v170, v170, v171\n\t" \
  "v_pk_sub_i16 v178, v178, v179\n\t" \
  "v_and_or_b32 v171, v169, v170, v167\n\t" \
  "v_and_or_b32 v179, v177, v178, v175\n\t" \
  "v_and_or_b32 v174, v167, v168, v170\n\t" \
  "v_and_or_b32 v182, v175, v176, v178\n\t" \
  "v_or3_b32 v171, v171, v168, v172\n\t" \
  "v_or3_b32 v179, v179, v176, v180\n\t" \
  "v_or3_b32 v174, v174, v169, v173\n\t" \
  "v_or3_b32 v182, v182, v177, v181\n\t" \
  "v_lshrrev_b32 v160, 1, v160\n\t" \
  "v_bitop3_b32 v160, v160, v171, s27 bitop3:0xf2\n\t" \
  "v_lshrrev_b32 v160, 1, v160\n\t" \
  "v_bitop3_b32 v160, v160, v179, s27 bitop3:0xf2\n\t" \
  "v_lshrrev_b32 v160, 1, v160\n\t" \
  "v_bitop3_b32 v160, v160, v174, s27 bitop3:0xf2\n\t" \
  "v_lshrrev_b32 v160, 1, v160\n\t" \
  "v_bitop3_b32 v160, v160, v182, s27 bitop3:0xf2\n\t" \
  "v_lshrrev_b32 v160, 4, v160\n\t" \
  "v_bcnt_u32_b32 v161, v160, 0\n\t" \
  "v_mov_b32 v162, v161\n\t" \
  "s_nop 1\n\t" \
  "v_add_u32_dpp v162, v162, v162 row_shr:1 row_mask:0xf bank_mask:0xf bound_ctrl:0\n\t" \
  "s_nop 1\n\t" \
  "v_add_u32_dpp v162, v162, v162 row_shr:2 row_mask:0xf bank_mask:0xf bound_ctrl:0\n\t" \
  "s_nop 1\n\t" \
  "v_add_u32_dpp v162, v162, v162 row_shr:4 row_mask:0xf bank_mask:0xf bound_ctrl:0\n\t" \
  "s_nop 1\n\t" \
  "v_add_u32_dpp v162, v162, v162 row_shr:8 row_mask:0xf bank_mask:0xf bound_ctrl:0\n\t" \
  "s_nop 1\n\t" \
  "v_add_u32_dpp v162, v162, v162 row_bcast:15 row_mask:0xa bank_mask:0xf\n\t" \
  "s_nop 1\n\t" \
  "v_add_u32_dpp v162, v162, v162 row_bcast:31 row_mask:0xc bank_mask:0xf\n\t" \
  "s_mul_i32 s25, s54, 27\n\t" \
  "v_readlane_b32 s27, v162, 63\n\t" \
  "v_sub_u32 v162, v162, v161\n\t" \
  "s_movk_i32 s70, 8704\n\t" \
  "v_cmpx_ne_u32 vcc, 0, v160\n\t" \
  "s_cbranch_execz .Lr3%=\n\t" \
  ".Lr2%=:\n\t" \
  "v_ffbl_b32 v154, v160\n\t" \
  "v_add_u32 v158, -1, v160\n\t" \
  "v_and_b32 v160, v160, v158\n\t" \
  "v_lshrrev_b32 v155, 4, v154\n\t" \
  "v_and_b32 v157, 1, v154\n\t" \
  "v_lshl_add_u32 v157, v157, 1, v155\n\t" \
  "v_bfe_u32 v155, v154, 2, 2\n\t" \
  "v_and_b32 v156, 2, v154\n\t" \
  "v_mad_u32_u24 v158, v155, s70, v163\n\t" \
  "v_lshl_add_u32 v158, v156, 6, v158\n\t" \
  "v_lshl_add_u32 v158, v157, 1, v158\n\t" \
  "ds_read_u16 v166, v158 offset:128\n\t" \
  "v_lshl_add_u32 v158, v155, 11, v164\n\t" \
  "v_lshl_or_b32 v158, v157, 7, v158\n\t" \
  "v_or_b32 v158, v158, v156\n\t" \
  "v_lshl_add_u32 v159, v155, 10, v165\n\t" \
  "v_lshl_add_u32 v159, v157, 4, v159\n\t" \
  "v_add_u32 v159, v159, v156\n\t" \
  "v_min_u32 v154, 14, v162\n\t" \
  "v_lshl_add_u32 v154, v154, 2, s28\n\t" \
  "v_add_u32 v162, 1, v162\n\t" \
  "s_waitcnt lgkmcnt(0)\n\t" \
  "ds_write_b16 v159, v166\n\t" \
  "v_lshl_or_b32 v158, v166, 16, v158\n\t" \
  "ds_write_b32 v154, v158\n\t" \
  "v_cmpx_ne_u32 vcc, 0, v160\n\t" \
  "s_cbranch_execnz .Lr2%=\n\t" \
  ".Lr3%=:\n\t" \
  "s_mov_b64 exec, -1\n\t" \
  "v_lshrrev_b32 v158, 1, v187\n\t" \
  "v_add_u32 v158, s28, v158\n\t" \
  "ds_read_b64 v[154:155], v158\n\t" \
  "ds_read_b128 v[72:75], v153 offset:0\n\t" \
  "ds_read_b128 v[76:79], v153 offset:1024\n\t" \
  "ds_read_b128 v[80:83], v153 offset:2048\n\t" \
  "s_min_u32 s27, s27, 63\n\t" \
  "s_or_b32 s27, s27, s57\n\t" \
  "v_mov_b32 v156, s27\n\t" \
  "v_mov_b32 v157, s58\n\t" \
  "s_waitcnt lgkmcnt(3)\n\t" \
  "s_mov_b64 exec, 0xff\n\t" \
  "buffer_store_dwordx4 v[154:157], v187, s[12:15], s51 offen sc1\n\t" \
  "s_mov_b64 exec, -1\n\t" \
  "s_add_u32 s25, s25, s52\n\t" \
  "s_lshl_b32 s25, s25, 10\n\t" \
  "ds_write_b128 v153, v[204:207] offset:0\n\t" \
  "ds_write_b128 v153, v[204:207] offset:1024\n\t" \
  "ds_write_b128 v153, v[204:207] offset:2048\n\t" \
  "ds_write_b128 v153, v[204:207] offset:3072\n\t" \
  "s_add_u32 s25, s25, 408576\n\t" \
  "s_waitcnt lgkmcnt(6)\n\t" \
  "buffer_store_dwordx4 v[72:75], v187, s[12:15], s25 offen sc1\n\t" \
  "s_add_u32 s25, s25, 9216\n\t" \
  "s_waitcnt lgkmcnt(5)\n\t" \
  "buffer_store_dwordx4 v[76:79], v187, s[12:15], s25 offen sc1\n\t" \
  "s_add_u32 s25, s25, 9216\n\t" \
  "s_waitcnt lgkmcnt(4)\n\t" \
  "buffer_store_dwordx4 v[80:83], v187, s[12:15], s25 offen sc1\n\t" \
  "s_mov_b32 s61, 1\n\t" \
  "s_call_b64 s[74:75], .Lxs%=\n\t" \
  "s_branch .Le6%=\n\t" \
  ".Lp5%=:\n\t" \
  "s_lshl_b32 s25, s25, 3\n\t" \
  "v_mov_b32 v108, s57\n\t" \
  "v_mov_b32 v109, s58\n\t" \
  "s_mov_b64 exec, 1\n\t" \
  "buffer_store_dwordx2 v[108:109], off, s[12:15], s25 offset:2304 sc1\n\t" \
  "s_mov_b64 exec, -1\n\t" \
  "s_branch .Lp6%=\n\t" \
  ".Lrr%=:\n\t" \
  "s_sub_u32 s50, s50, 1\n\t" \
  "s_cbranch_scc1 .Lrg%=\n\t" \
  "s_sleep 2\n\t" \
  "buffer_load_dwordx4 v[188:191], v203, s[12:15], s36 offen offset:0 sc1\n\t" \
  "buffer_load_dwordx4 v[192:195], v203, s[12:15], s36 offen offset:1152 sc1\n\t" \
  "buffer_load_dwordx4 v[196:199], v203, s[12:15], s36 offen offset:2304 sc1\n\t" \
  "s_waitcnt vmcnt(0)\n\t" \
  "s_branch .Lrk%=\n\t" \
  ".Lss%=:\n\t" \
  "s_movk_i32 s50, 0x7fff\n\t" \
  "v_cmp_lt_u32 vcc, 16, v206\n\t" \
  "s_or_b64 exec, s[10:11], vcc\n\t" \
  "s_mov_b64 s[10:11], exec\n\t" \
  "v_mov_b32 v248, 0\n\t" \
  "v_mov_b32 v249, 0\n\t" \
  "v_mov_b32 v250, 0\n\t" \
  "s_mov_b64 exec, -1\n\t" \
  "s_mov_b64 s[4:5], 0\n\t" \
  "s_mov_b32 s61, 0\n\t" \
  "s_movk_i32 s25, 56\n\t" \
  ".Lsc%=:\n\t" \
  "s_bitcmp1_b64 s[10:11], s25\n\t" \
  "s_addc_u32 s62, s62, s62\n\t" \
  "s_sub_u32 s25, s25, 7\n\t" \
  "s_cbranch_scc0 .Lsc%=\n\t" \
  "v_lshrrev_b32 v204, 1, v187\n\t" \
  "v_min_u32 v204, 0xd0, v204\n\t" \
  ".Lsp%=:\n\t" \
  "buffer_load_dwordx2 v[206:207], v204, s[12:15], 0 offen offset:2304 sc1\n\t" \
  "s_waitcnt vmcnt(0)\n\t" \
  "v_xor_b32 v206, s57, v206\n\t" \
  "v_bitop3_b32 v206, v206, s58, v207 bitop3:0xf6\n\t" \
  "v_cmp_ne_u32 vcc, 0, v206\n\t" \
  "s_cbranch_vccz .Lsg%=\n\t" \
  "s_sleep 4\n\t" \
  "s_sub_u32 s50, s50, 1\n\t" \
  "s_cbranch_scc0 .Lsp%=\n\t" \
  "s_branch .Lsg%=\n\t" \
  ".Ln4%=:\n\t" \
  "ds_read_b32 v35, v32 offset:3584\n\t" \
  "s_branch .Ln3%=\n\t" \
  ".Lmf%=:\n\t" \
  "s_cbranch_execz .Lmw1%=\n\t" \
  "s_mov_b32 s27, 1\n\t" \
  ".Lml%=:\n\t" \
  "s_ff1_i32_b64 s25, s[4:5]\n\t" \
  "s_bitset0_b64 s[4:5], s25\n\t" \
  "s_max_i32 s25, s25, 0\n\t" \
  "s_mul_i32 s26, s25, 37\n\t" \
  "s_lshr_b32 s26, s26, 8\n\t" \
  "s_mul_i32 s70, s26, -3\n\t" \
  "s_add_u32 s70, s70, s25\n\t" \
  "s_mul_i32 s71, s26, -5\n\t" \
  "s_add_u32 s71, s71, s25\n\t" \
  "s_lshl_b32 s71, s71, 9\n\t" \
  "s_add_u32 s71, s71, s44\n\t" \
  "s_sub_u32 s71, s71, 36864\n\t" \
  "v_lshl_add_u32 v244, s70, 4, v246\n\t" \
  "ds_read_b32 v244, v244 offset:32\n\t" \
  "s_mov_b32 exec_hi, 0\n\t" \
  "v_add_u32 v240, s71, v187\n\t" \
  "ds_read_b128 v[240:243], v240\n\t" \
  "s_mov_b32 exec_hi, -1\n\t" \
  "s_lshl_b32 s26, s26, 2\n\t" \
  ".Lmw1%=:\n\t" \
  "s_cbranch_execz .Lmw2%=\n\t" \
  "s_cmp_eq_u32 s27, 1\n\t" \
  "s_cmov_b32 s7, s26\n\t" \
  "s_cmp_eq_u32 s27, 2\n\t" \
  "s_cmov_b32 s8, s26\n\t" \
  "v_add_u32 v236, 1280, v236\n\t" \
  "v_add_u32 v245, 1280, v245\n\t" \
  "s_waitcnt lgkmcnt(1)\n\t" \
  "v_bitop3_b32 v244, v244, s43, v247 bitop3:0x6a\n\t" \
  "v_add_u32_sdwa v248, 0, v244 dst_sel:DWORD dst_unused:UNUSED_PAD src0_sel:DWORD src1_sel:WORD_0\n\t" \
  "v_add_u32_sdwa v249, 0, v244 dst_sel:DWORD dst_unused:UNUSED_PAD src0_sel:DWORD src1_sel:WORD_1\n\t" \
  ".Lmw2%=:\n\t" \
  "s_cbranch_execz .Lmw3%=\n\t" \
  "ds_read_b64 v[220:221], v248 offset:0\n\t" \
  "ds_read_b64 v[222:223], v249 offset:0\n\t" \
  "ds_read_b64 v[224:225], v248 offset:0\n\t" \
  "ds_read_b64 v[226:227], v249 offset:0\n\t" \
  "ds_read_b64 v[228:229], v248 offset:18432\n\t" \
  "ds_read_b64 v[230:231], v249 offset:18432\n\t" \
  "ds_read_b64 v[232:233], v248 offset:18432\n\t" \
  "ds_read_b64 v[234:235], v249 offset:18432\n\t" \
  "ds_read_b64 v[212:213], v248 offset:36864\n\t" \
  "ds_read_b64 v[214:215], v249 offset:36864\n\t" \
  "ds_read_b64 v[216:217], v248 offset:36864\n\t" \
  "ds_read_b64 v[218:219], v249 offset:36864\n\t" \
  ".Lmw3%=:\n\t" \
  "s_cbranch_execz .Lhopy%=\n\t" \
  "ds_write_b32 v245, v244 offset:1024\n\t" \
  "s_waitcnt lgkmcnt(1)\n\t" \
  "ds_write_b128 v236, v[240:243]\n\t" \
  "s_set_gpr_idx_on s26, gpr_idx(DST)\n\t" \
  "v_smfmac_f32_16x16x64_bf16 v[0:3], v[240:243], v[220:227], v182 abid:1\n\t" \
  "v_smfmac_f32_16x16x64_bf16 v[72:75], v[240:243], v[228:235], v182 abid:1\n\t" \
  "v_smfmac_f32_16x16x64_bf16 v[36:39], v[240:243], v[212:219], v182 abid:1\n\t" \
  "s_set_gpr_idx_off\n\t" \
  "s_add_u32 s27, s27, 1\n\t" \
  "s_cmp_lt_u32 s27, 4\n\t" \
  "s_cbranch_scc1 .Lmn%=\n\t" \
  "s_lshl_b32 s55, s55, 8\n\t" \
  "s_or_b32 s55, s55, s26\n\t" \
  "s_bitset1_b32 s62, 16\n\t" \
  ".Lmn%=:\n\t" \
  "s_cmp_lt_u32 s27, s61\n\t" \
  "s_cbranch_scc1 .Lml%=\n\t" \
  "s_branch .Lmg%=\n\t" \
  ".Lsl%=:\n\t" \
  "s_mov_b32 s36, 0\n\t" \
  "s_mov_b32 s11, 0\n\t" \
  ".Lsa%=:\n\t" \
  "s_waitcnt lgkmcnt(0)\n\t" \
  "s_mov_b32 s25, 0\n\t" \
  ".Lsaj%=:\n\t" \
  "s_bitcmp0_b32 s62, s25\n\t" \
  "s_cbranch_scc1 .Lsan%=\n\t" \
  "s_mul_i32 s26, s24, 81\n\t" \
  "s_add_u32 s26, s26, s25\n\t" \
  "s_lshl_b32 s26, s26, 10\n\t" \
  "s_add_u32 s26, s26, 408576\n\t" \
  "v_add_u32 v228, s36, v183\n\t" \
  "v_add_u32 v229, s36, v184\n\t" \
  "s_lshl2_add_u32 s28, s25, s11\n\t" \
  "s_movk_i32 s27, 9\n\t" \
  ".Lsak%=:\n\t" \
  "buffer_load_dwordx4 v[240:243], v187, s[12:15], s26 offen sc1\n\t" \
  "ds_read_b128 v[220:223], v228\n\t" \
  "ds_read_b128 v[224:227], v229\n\t" \
  "v_add_u32 v228, 0x800, v228\n\t" \
  "v_add_u32 v229, 0x800, v229\n\t" \
  "s_add_u32 s26, s26, 9216\n\t" \
  "s_waitcnt vmcnt(0) lgkmcnt(0)\n\t" \
  "s_set_gpr_idx_on s28, gpr_idx(DST)\n\t" \
  "v_smfmac_f32_16x16x64_bf16 v[0:3], v[240:243], v[220:227], v182 abid:1\n\t" \
  "s_set_gpr_idx_off\n\t" \
  "s_sub_u32 s27, s27, 1\n\t" \
  "s_cmp_lg_u32 s27, 0\n\t" \
  "s_cbranch_scc1 .Lsak%=\n\t" \
  ".Lsan%=:\n\t" \
  "s_add_u32 s25, s25, 1\n\t" \
  "s_cmp_lt_u32 s25, 9\n\t" \
  "s_cbranch_scc1 .Lsaj%=\n\t" \
  "s_nop 7\n\t" \
  "s_cmp_eq_u32 s11, 36\n\t" \
  "s_cbranch_scc1 .Lsh%=\n\t" \
  "s_add_u32 s36, s36, 18432\n\t" \
  "s_cmp_eq_u32 s11, 0\n\t" \
  "s_cselect_b32 s11, 72, 36\n\t" \
  "s_branch .Lsa%=\n\t" \
  ".Lq2%=:\n\t" \
  "s_cmp_lg_u32 s62, 0\n\t" \
  "s_cbranch_scc1 .Lq4%=\n\t" \
  "s_mov_b32 s64, 0xc9ccc9cb\n\t" \
  "s_mov_b32 s65, 0x44cccc9\n\t" \
  "s_mov_b32 s55, 44\n\t" \
  "s_mov_b32 s48, 7\n\t" \
  "s_branch .Lq3%=\n\t" \
  ".Lq4%=:\n\t" \
  "s_mov_b32 s48, 7\n\t" \
  "s_mov_b32 s4, 0xcccccccb\n\t" \
  "s_mov_b32 s5, 0x44cc\n\t" \
  "s_mov_b32 s10, s68\n\t" \
  "s_mov_b64 s[64:65], 0\n\t" \
  "s_getpc_b64 s[68:69]\n\t" \
  ".Lq5%=:\n\t" \
  "s_add_u32 s68, s68, .Lhs%=-.Lq5%=\n\t" \
  "s_branch .Lq3%=\n\t" \
  ".Lhs%=:\n\t" \
  "s_bitcmp1_b32 s62, 16\n\t" \
  "s_cbranch_scc1 .Lhm%=\n\t" \
  "s_sub_u32 s36, 0, s30\n\t" \
  "s_mov_b32 s11, 36\n\t" \
  ".Lsb%=:\n\t" \
  "s_waitcnt lgkmcnt(0)\n\t" \
  "s_mov_b32 s25, 0\n\t" \
  ".Lsbj%=:\n\t" \
  "s_bitcmp0_b32 s62, s25\n\t" \
  "s_cbranch_scc1 .Lsbn%=\n\t" \
  "s_mul_i32 s26, s24, 81\n\t" \
  "s_add_u32 s26, s26, s25\n\t" \
  "s_lshl_b32 s26, s26, 10\n\t" \
  "s_add_u32 s26, s26, 408576\n\t" \
  "v_add_u32 v16, s36, v183\n\t" \
  "v_add_u32 v17, s36, v184\n\t" \
  "s_lshl2_add_u32 s28, s25, s11\n\t" \
  "s_movk_i32 s27, 9\n\t" \
  ".Lsbk%=:\n\t" \
  "buffer_load_dwordx4 v[28:31], v187, s[12:15], s26 offen sc1\n\t" \
  "ds_read_b128 v[8:11], v16\n\t" \
  "ds_read_b128 v[12:15], v17\n\t" \
  "v_add_u32 v16, 0x800, v16\n\t" \
  "v_add_u32 v17, 0x800, v17\n\t" \
  "s_add_u32 s26, s26, 9216\n\t" \
  "s_waitcnt vmcnt(0) lgkmcnt(0)\n\t" \
  "s_set_gpr_idx_on s28, gpr_idx(DST)\n\t" \
  "v_smfmac_f32_16x16x64_bf16 v[0:3], v[28:31], v[8:15], v182 abid:1\n\t" \
  "s_set_gpr_idx_off\n\t" \
  "s_sub_u32 s27, s27, 1\n\t" \
  "s_cmp_lg_u32 s27, 0\n\t" \
  "s_cbranch_scc1 .Lsbk%=\n\t" \
  ".Lsbn%=:\n\t" \
  "s_add_u32 s25, s25, 1\n\t" \
  "s_cmp_lt_u32 s25, 9\n\t" \
  "s_cbranch_scc1 .Lsbj%=\n\t" \
  "s_nop 7\n\t" \
  ".Lht%=:\n\t" \
  "s_and_b32 s27, s4, 7\n\t" \
  "s_lshl2_add_u32 s66, s27, s10\n\t" \
  "s_and_b32 s27, s4, 8\n\t" \
  "s_mul_i32 s27, s27, 176\n\t" \
  "s_add_u32 s66, s66, s27\n\t" \
  "s_lshr_b64 s[4:5], s[4:5], 4\n\t" \
  "s_setpc_b64 s[66:67]\n\t" \
  ".Lhm%=:\n\t" \
  "s_mov_b32 s25, s61\n\t" \
  "s_mov_b32 s26, s55\n\t" \
  ".Lhn%=:\n\t" \
  "s_sub_u32 s25, s25, 1\n\t" \
  "s_mul_i32 s27, s25, 1280\n\t" \
  "s_add_u32 s27, s27, s60\n\t" \
  "v_lshrrev_b32 v16, 2, v187\n\t" \
  "v_add_u32 v16, s27, v16\n\t" \
  "v_add_u32 v28, s27, v187\n\t" \
  "ds_read_b32 v16, v16 offset:1024\n\t" \
  "ds_read_b128 v[28:31], v28\n\t" \
  "s_and_b32 s27, s26, 0xff\n\t" \
  "s_lshr_b32 s26, s26, 8\n\t" \
  "s_waitcnt lgkmcnt(1)\n\t" \
  "v_add_u32_sdwa v8, s37, v16 dst_sel:DWORD dst_unused:UNUSED_PAD src0_sel:DWORD src1_sel:WORD_0\n\t" \
  "v_add_u32_sdwa v10, s37, v16 dst_sel:DWORD dst_unused:UNUSED_PAD src0_sel:DWORD src1_sel:WORD_1\n\t" \
  "ds_read_b64 v[8:9], v8 offset:0\n\t" \
  "ds_read_b64 v[10:11], v10 offset:0\n\t" \
  "s_waitcnt lgkmcnt(0)\n\t" \
  "s_set_gpr_idx_on s27, gpr_idx(DST)\n\t" \
  "v_smfmac_f32_16x16x64_bf16 v[36:39], v[28:31], v[8:15], v182 abid:1\n\t" \
  "s_set_gpr_idx_off\n\t" \
  "s_cmp_gt_u32 s25, 3\n\t" \
  "s_cbranch_scc1 .Lhn%=\n\t" \
  "s_branch .Lht%=\n\t" \
  ".Lhe%=:\n\t" \
  "s_cbranch_execz .Lhh%=\n\t" \
  "s_and_b32 s61, s55, 7\n\t" \
  "s_lshr_b32 s55, s55, 3\n\t" \
  "s_cmp_lg_u32 s61, 0\n\t" \
  "s_cbranch_scc1 .Lh0%=\n\t" \
  "v_lshlrev_b32 v16, 1, v187\n\t" \
  "s_mov_b32 m0, 131584\n\t" \
  "s_mov_b32 s69, s67\n\t" \
  "global_load_lds_dword v16, s[68:69]\n\t" \
  ".Lh3%=:\n\t" \
  "s_and_b32 s27, s64, 7\n\t" \
  "s_lshl2_add_u32 s66, s27, s68\n\t" \
  "s_and_b32 s27, s64, 8\n\t" \
  "s_mul_i32 s27, s27, 176\n\t" \
  "s_add_u32 s66, s66, s27\n\t" \
  "s_lshr_b64 s[64:65], s[64:65], 4\n\t" \
  "s_setpc_b64 s[66:67]\n\t" \
  ".Lhh%=:\n\t" \
  "s_cbranch_execz .Lhi%=\n\t" \
  ".Lh0%=:\n\t" \
  "s_cmp_lg_u32 s61, 1\n\t" \
  "s_cbranch_scc1 .Lh1%=\n\t" \
  "s_call_b64 s[74:75], .Lxs%=\n\t" \
  "s_branch .Lh3%=\n\t" \
  ".Lh1%=:\n\t" \
  "s_cmp_lg_u32 s61, 2\n\t" \
  "s_cbranch_scc1 .Lh5%=\n\t" \
  "s_add_u32 s25, s38, s49\n\t" \
  "v_add_u32 v24, s25, v187\n\t" \
  "v_xad_u32 v25, v187, 64, s25\n\t" \
  "ds_read_b128 v[8:11], v24 offset:0\n\t" \
  "ds_read_b128 v[12:15], v25 offset:1024\n\t" \
  "ds_read_b128 v[16:19], v24 offset:2048\n\t" \
  "ds_read_b128 v[20:23], v25 offset:3072\n\t" \
  ".Lhi%=:\n\t" \
  "s_cbranch_execz .Lhj%=\n\t" \
  "s_add_u32 s38, s38, 18432\n\t" \
  "s_sub_u32 s44, s38, 129024\n\t" \
  "s_min_u32 s38, s38, s44\n\t" \
  "s_add_u32 s36, s35, s39\n\t" \
  "s_waitcnt lgkmcnt(3)\n\t" \
  "buffer_store_dwordx4 v[8:11], v34, s[20:23], s36 offen sc1\n\t" \
  "s_add_u32 s36, s36, 4608\n\t" \
  "s_waitcnt lgkmcnt(2)\n\t" \
  "buffer_store_dwordx4 v[12:15], v34, s[20:23], s36 offen sc1\n\t" \
  "s_add_u32 s36, s36, 4608\n\t" \
  "s_waitcnt lgkmcnt(1)\n\t" \
  "buffer_store_dwordx4 v[16:19], v34, s[20:23], s36 offen sc1\n\t" \
  "s_add_u32 s36, s36, 4608\n\t" \
  "s_waitcnt lgkmcnt(0)\n\t" \
  "buffer_store_dwordx4 v[20:23], v34, s[20:23], s36 offen sc1\n\t" \
  ".Lhj%=:\n\t" \
  "s_cbranch_execz .Lhopy%=\n\t" \
  "s_add_u32 s35, s35, 4718592\n\t" \
  "s_branch .Lh3%=\n\t" \
  ".Lh5%=:\n\t" \
  "s_cmp_eq_u32 s61, 5\n\t" \
  "s_cbranch_scc1 .Lh2%=\n\t" \
  "s_mov_b32 m0, 132352\n\t" \
  "s_nop 0\n\t" \
  "buffer_load_dwordx4 v187, s[12:15], 0 offen offset:256 sc1 lds\n\t" \
  "buffer_load_dwordx4 v187, s[12:15], 0 offen offset:1280 sc1 lds\n\t" \
  "s_branch .Lh3%=\n\t" \
  ".Lh2%=:\n\t" \
  "v_add_u32 v16, 132608, v187\n\t" \
  "ds_read_b128 v[8:11], v16\n\t" \
  "ds_read_b128 v[12:15], v16 offset:1024\n\t" \
  "s_waitcnt lgkmcnt(0)\n\t" \
  "v_xor_b32 v8, s57, v8\n\t" \
  "v_bitop3_b32 v8, v8, s58, v9 bitop3:0xf6\n\t" \
  "v_bitop3_b32 v8, v8, s57, v10 bitop3:0xf6\n\t" \
  "v_bitop3_b32 v8, v8, s58, v11 bitop3:0xf6\n\t" \
  "v_bitop3_b32 v8, v8, s57, v12 bitop3:0xf6\n\t" \
  "v_bitop3_b32 v8, v8, s58, v13 bitop3:0xf6\n\t" \
  "v_bitop3_b32 v8, v8, s57, v14 bitop3:0xf6\n\t" \
  "v_bitop3_b32 v8, v8, s58, v15 bitop3:0xf6\n\t" \
  "v_cmp_ne_u32 vcc, 0, v8\n\t" \
  "s_add_u32 s25, s9, 1\n\t" \
  "v_mov_b32 v8, s25\n\t" \
  "s_cbranch_vccnz .Lh4%=\n\t" \
  "s_mov_b64 exec, 1\n\t" \
  "buffer_store_dword v8, off, s[12:15], 0 sc1\n\t" \
  "s_mov_b64 exec, -1\n\t" \
  "s_mov_b32 s63, 0\n\t" \
  ".Lh4%=:\n\t" \
  "s_branch .Lh3%=\n\t" \
  ".Lez%=:\n\t" \
  "s_mov_b32 s26, 0x7fff\n\t" \
  ".Lex%=:\n\t" \
  "buffer_load_dwordx4 v[126:129], v187, s[12:15], 0 offen offset:256 sc1\n\t" \
  "buffer_load_dwordx4 v[130:133], v187, s[12:15], 0 offen offset:1280 sc1\n\t" \
  "s_waitcnt vmcnt(0)\n\t" \
  "v_xor_b32 v126, s57, v126\n\t" \
  "v_bitop3_b32 v126, v126, s58, v127 bitop3:0xf6\n\t" \
  "v_bitop3_b32 v126, v126, s57, v128 bitop3:0xf6\n\t" \
  "v_bitop3_b32 v126, v126, s58, v129 bitop3:0xf6\n\t" \
  "v_bitop3_b32 v126, v126, s57, v130 bitop3:0xf6\n\t" \
  "v_bitop3_b32 v126, v126, s58, v131 bitop3:0xf6\n\t" \
  "v_bitop3_b32 v126, v126, s57, v132 bitop3:0xf6\n\t" \
  "v_bitop3_b32 v126, v126, s58, v133 bitop3:0xf6\n\t" \
  "v_cmp_ne_u32 vcc, 0, v126\n\t" \
  "s_add_u32 s25, s9, 1\n\t" \
  "v_mov_b32 v126, s25\n\t" \
  "s_cbranch_vccnz .Lew%=\n\t" \
  "s_mov_b64 exec, 1\n\t" \
  "buffer_store_dword v126, off, s[12:15], 0 sc1\n\t" \
  "s_mov_b64 exec, -1\n\t" \
  "s_mov_b32 s63, 0\n\t" \
  ".Lew%=:\n\t" \
  "s_cmp_eq_u32 s63, 0\n\t" \
  "s_cbranch_scc1 .Ley%=\n\t" \
  "s_sleep 8\n\t" \
  "s_sub_u32 s26, s26, 1\n\t" \
  "s_cbranch_scc0 .Lex%=\n\t" \
  "s_branch .Ley%=\n\t" \
  ".p2align 6\n\t" \
  ".Lvar%=:\n\t" \
  ".fill (.Lvar%=+0-.)/4, 4, 0xbf800000\n\t" \
  "s_branch .Lfin%=\n\t" \
  "s_branch .Lhe%=\n\t" \
  "s_waitcnt vmcnt(6)\n\t" \
  "s_waitcnt vmcnt(10)\n\t" \
  "s_waitcnt vmcnt(12)\n\t" \
  "s_waitcnt vmcnt(17)\n\t" \
  "s_waitcnt vmcnt(21) lgkmcnt(0)\n\t" \
  "s_barrier\n\t" \
  "v_smfmac_f32_16x16x64_bf16 v[36:39], v[220:223], v[0:7], v174\n\t" \
  "ds_read_b128 v[126:129], v183 offset:0\n\t" \
  "v_add_u32 v24, s37, v185\n\t" \
  "v_smfmac_f32_16x16x64_bf16 v[40:43], v[224:227], v[0:7], v174 abid:1\n\t" \
  "ds_read_b128 v[130:133], v184 offset:0\n\t" \
  "v_xor_b32 v25, s41, v183\n\t" \
  "v_smfmac_f32_16x16x64_bf16 v[44:47], v[228:231], v[0:7], v175\n\t" \
  "ds_read_b128 v[134:137], v183 offset:2048\n\t" \
  "v_subrev_u32 v25, s30, v25\n\t" \
  "v_smfmac_f32_16x16x64_bf16 v[48:51], v[232:235], v[0:7], v175 abid:1\n\t" \
  "s_add_u32 s30, s29, 18432\n\t" \
  "ds_read_b128 v[138:141], v184 offset:2048\n\t" \
  "v_cvt_pk_bf16_f32 v8, v36, v37\n\t" \
  "v_cvt_pk_bf16_f32 v9, v38, v39\n\t" \
  "v_smfmac_f32_16x16x64_bf16 v[52:55], v[236:239], v[0:7], v176\n\t" \
  "s_sub_u32 s25, s30, 129024\n\t" \
  "ds_read_b128 v[36:39], v35 offset:0\n\t" \
  "v_cvt_pk_bf16_f32 v10, v40, v41\n\t" \
  "v_cvt_pk_bf16_f32 v11, v42, v43\n\t" \
  "v_smfmac_f32_16x16x64_bf16 v[56:59], v[240:243], v[0:7], v176 abid:1\n\t" \
  "s_min_u32 s30, s30, s25\n\t" \
  "ds_read_b128 v[40:43], v35 offset:64\n\t" \
  "v_cvt_pk_bf16_f32 v16, v44, v45\n\t" \
  "v_cvt_pk_bf16_f32 v17, v46, v47\n\t" \
  "v_smfmac_f32_16x16x64_bf16 v[60:63], v[244:247], v[0:7], v177\n\t" \
  "s_sub_u32 s30, s30, s29\n\t" \
  "ds_read_b128 v[44:47], v35 offset:128\n\t" \
  "v_cvt_pk_bf16_f32 v18, v48, v49\n\t" \
  "v_cvt_pk_bf16_f32 v19, v50, v51\n\t" \
  "v_smfmac_f32_16x16x64_bf16 v[64:67], v[248:251], v[0:7], v177 abid:1\n\t" \
  "s_mov_b32 s37, s29\n\t" \
  "ds_read_b128 v[48:51], v35 offset:192\n\t" \
  "v_cvt_pk_bf16_f32 v12, v52, v53\n\t" \
  "v_cvt_pk_bf16_f32 v13, v54, v55\n\t" \
  "v_smfmac_f32_16x16x64_bf16 v[68:71], v[252:255], v[0:7], v182\n\t" \
  "s_waitcnt lgkmcnt(3)\n\t" \
  "ds_read_b128 v[52:55], v35 offset:256\n\t" \
  "v_cvt_pk_bf16_f32 v14, v56, v57\n\t" \
  "v_cvt_pk_bf16_f32 v15, v58, v59\n\t" \
  "v_smfmac_f32_16x16x64_bf16 v[36:39], a[0:3], v[126:133], v142\n\t" \
  "s_waitcnt lgkmcnt(3)\n\t" \
  "ds_read_b128 v[56:59], v35 offset:320\n\t" \
  "v_cvt_pk_bf16_f32 v20, v60, v61\n\t" \
  "v_cvt_pk_bf16_f32 v21, v62, v63\n\t" \
  "v_smfmac_f32_16x16x64_bf16 v[40:43], a[4:7], v[126:133], v142 abid:1\n\t" \
  "s_waitcnt lgkmcnt(3)\n\t" \
  "ds_read_b128 v[60:63], v35 offset:384\n\t" \
  "v_cvt_pk_bf16_f32 v22, v64, v65\n\t" \
  "v_cvt_pk_bf16_f32 v23, v66, v67\n\t" \
  "v_smfmac_f32_16x16x64_bf16 v[44:47], a[8:11], v[126:133], v143\n\t" \
  "s_waitcnt lgkmcnt(3)\n\t" \
  "ds_read_b128 v[64:67], v35 offset:448\n\t" \
  "v_cvt_pk_bf16_f32 v26, v68, v69\n\t" \
  "v_cvt_pk_bf16_f32 v27, v70, v71\n\t" \
  "v_smfmac_f32_16x16x64_bf16 v[48:51], a[12:15], v[126:133], v143 abid:1\n\t" \
  "s_waitcnt lgkmcnt(3)\n\t" \
  "ds_read_b128 v[68:71], v35 offset:512\n\t" \
  "v_add_u32_sdwa v0, s29, v108 dst_sel:DWORD dst_unused:UNUSED_PAD src0_sel:DWORD src1_sel:WORD_0\n\t" \
  "v_smfmac_f32_16x16x64_bf16 v[52:55], a[16:19], v[126:133], v144\n\t" \
  "s_waitcnt lgkmcnt(3)\n\t" \
  "s_bfe_i32 s47, s48, 0x10000\n\t" \
  "ds_write_b128 v24, v[8:11] offset:0\n\t" \
  "v_add_u32_sdwa v2, s29, v108 dst_sel:DWORD dst_unused:UNUSED_PAD src0_sel:DWORD src1_sel:WORD_1\n\t" \
  "v_add_u32 v28, s38, v187\n\t" \
  "v_smfmac_f32_16x16x64_bf16 v[56:59], a[20:23], v[126:133], v144 abid:1\n\t" \
  "s_waitcnt lgkmcnt(3)\n\t" \
  "s_lshr_b32 s48, s48, 1\n\t" \
  "ds_read_b128 v[28:31], v28 offset:0\n\t" \
  "v_add_u32_sdwa v4, s29, v109 dst_sel:DWORD dst_unused:UNUSED_PAD src0_sel:DWORD src1_sel:WORD_0\n\t" \
  "v_smfmac_f32_16x16x64_bf16 v[60:63], a[24:27], v[126:133], v145\n\t" \
  "s_waitcnt lgkmcnt(2)\n\t" \
  "s_add_u32 s36, s35, s39\n\t" \
  "ds_read_b64 v[0:1], v0 offset:0\n\t" \
  "v_add_u32_sdwa v6, s29, v109 dst_sel:DWORD dst_unused:UNUSED_PAD src0_sel:DWORD src1_sel:WORD_1\n\t" \
  "v_smfmac_f32_16x16x64_bf16 v[64:67], a[28:31], v[126:133], v145 abid:1\n\t" \
  "s_waitcnt lgkmcnt(2)\n\t" \
  "s_andn2_b32 s44, 0x40000000, s47\n\t" \
  "v_smfmac_f32_16x16x64_bf16 v[68:71], a[32:35], v[126:133], v178\n\t" \
  "s_or_b32 s36, s36, s44\n\t" \
  "ds_write_b128 v24, v[12:15] offset:128\n\t" \
  "v_xor_b32 v24, 64, v24\n\t" \
  "v_smfmac_f32_16x16x64_bf16 v[36:39], a[36:39], v[134:141], v146\n\t" \
  "s_waitcnt lgkmcnt(2)\n\t" \
  "buffer_store_dwordx4 v[28:31], v34, s[20:23], s36 offen sc1\n\t" \
  "v_add_u32_sdwa v8, s29, v110 dst_sel:DWORD dst_unused:UNUSED_PAD src0_sel:DWORD src1_sel:WORD_0\n\t" \
  "v_smfmac_f32_16x16x64_bf16 v[40:43], a[40:43], v[134:141], v146 abid:1\n\t" \
  "s_and_b32 s44, s47, 4718592\n\t" \
  "ds_read_b128 v[126:129], v183 offset:4096\n\t" \
  "v_add_u32_sdwa v10, s29, v110 dst_sel:DWORD dst_unused:UNUSED_PAD src0_sel:DWORD src1_sel:WORD_1\n\t" \
  "v_xad_u32 v28, v187, 64, s38\n\t" \
  "v_smfmac_f32_16x16x64_bf16 v[44:47], a[44:47], v[134:141], v147\n\t" \
  "s_add_u32 s35, s35, s44\n\t" \
  "ds_read_b128 v[28:31], v28 offset:1024\n\t" \
  "v_add_u32_sdwa v12, s29, v111 dst_sel:DWORD dst_unused:UNUSED_PAD src0_sel:DWORD src1_sel:WORD_0\n\t" \
  "v_smfmac_f32_16x16x64_bf16 v[48:51], a[48:51], v[134:141], v147 abid:1\n\t" \
  "s_add_u32 s36, s36, 4608\n\t" \
  "ds_write_b128 v24, v[16:19] offset:0\n\t" \
  "v_add_u32_sdwa v14, s29, v111 dst_sel:DWORD dst_unused:UNUSED_PAD src0_sel:DWORD src1_sel:WORD_1\n\t" \
  "v_smfmac_f32_16x16x64_bf16 v[52:55], a[52:55], v[134:141], v148\n\t" \
  "v_smfmac_f32_16x16x64_bf16 v[56:59], a[56:59], v[134:141], v148 abid:1\n\t" \
  "ds_read_b128 v[130:133], v184 offset:4096\n\t" \
  "v_smfmac_f32_16x16x64_bf16 v[60:63], a[60:63], v[134:141], v149\n\t" \
  "ds_read_b64 v[2:3], v2 offset:0\n\t" \
  "s_waitcnt lgkmcnt(3)\n\t" \
  "buffer_store_dwordx4 v[28:31], v34, s[20:23], s36 offen sc1\n\t" \
  "v_smfmac_f32_16x16x64_bf16 v[64:67], a[64:67], v[134:141], v149 abid:1\n\t" \
  "s_and_b32 s44, s47, 18432\n\t" \
  "ds_write_b128 v24, v[20:23] offset:128\n\t" \
  "v_add_u32_sdwa v16, s29, v112 dst_sel:DWORD dst_unused:UNUSED_PAD src0_sel:DWORD src1_sel:WORD_0\n\t" \
  "v_add_u32 v28, s38, v187\n\t" \
  "v_smfmac_f32_16x16x64_bf16 v[68:71], a[68:71], v[134:141], v178 abid:1\n\t" \
  "s_waitcnt lgkmcnt(2)\n\t" \
  "ds_read_b128 v[134:137], v183 offset:6144\n\t" \
  "ds_read_b128 v[28:31], v28 offset:2048\n\t" \
  "v_add_u32_sdwa v18, s29, v112 dst_sel:DWORD dst_unused:UNUSED_PAD src0_sel:DWORD src1_sel:WORD_1\n\t" \
  "v_smfmac_f32_16x16x64_bf16 v[36:39], a[72:75], v[126:133], v150\n\t" \
  "s_add_u32 s36, s36, 4608\n\t" \
  "ds_read_b128 v[138:141], v184 offset:6144\n\t" \
  "v_add_u32_sdwa v20, s29, v113 dst_sel:DWORD dst_unused:UNUSED_PAD src0_sel:DWORD src1_sel:WORD_0\n\t" \
  "v_smfmac_f32_16x16x64_bf16 v[40:43], a[76:79], v[126:133], v150 abid:1\n\t" \
  "ds_read_b64 v[4:5], v4 offset:0\n\t" \
  "v_add_u32_sdwa v22, s29, v113 dst_sel:DWORD dst_unused:UNUSED_PAD src0_sel:DWORD src1_sel:WORD_1\n\t" \
  "v_smfmac_f32_16x16x64_bf16 v[44:47], a[80:83], v[126:133], v151\n\t" \
  "s_and_b32 s27, s64, 7\n\t" \
  "ds_write_b64 v25, v[26:27] offset:16384\n\t" \
  "v_smfmac_f32_16x16x64_bf16 v[48:51], a[84:87], v[126:133], v151 abid:1\n\t" \
  "s_lshl2_add_u32 s66, s27, s68\n\t" \
  "ds_read_b64 v[6:7], v6 offset:0\n\t" \
  "s_waitcnt lgkmcnt(4)\n\t" \
  "buffer_store_dwordx4 v[28:31], v34, s[20:23], s36 offen sc1\n\t" \
  "v_smfmac_f32_16x16x64_bf16 v[52:55], a[88:91], v[126:133], v152\n\t" \
  "s_and_b32 s27, s64, 8\n\t" \
  "ds_read_b64 v[8:9], v8 offset:0\n\t" \
  "v_xad_u32 v28, v187, 64, s38\n\t" \
  "v_smfmac_f32_16x16x64_bf16 v[56:59], a[92:95], v[126:133], v152 abid:1\n\t" \
  "s_mul_i32 s27, s27, 176\n\t" \
  "s_add_u32 s38, s38, s44\n\t" \
  "ds_read_b64 v[10:11], v10 offset:0\n\t" \
  "ds_read_b128 v[28:31], v28 offset:3072\n\t" \
  "v_smfmac_f32_16x16x64_bf16 v[60:63], a[96:99], v[126:133], v153\n\t" \
  "s_add_u32 s66, s66, s27\n\t" \
  "s_add_u32 s36, s36, 4608\n\t" \
  "ds_read_b64 v[12:13], v12 offset:0\n\t" \
  "v_smfmac_f32_16x16x64_bf16 v[64:67], a[100:103], v[126:133], v153 abid:1\n\t" \
  "s_waitcnt lgkmcnt(3)\n\t" \
  "s_sub_u32 s44, s38, 129024\n\t" \
  "ds_read_b64 v[14:15], v14 offset:0\n\t" \
  "v_smfmac_f32_16x16x64_bf16 v[36:39], v[72:75], v[0:7], v182 abid:1\n\t" \
  "s_lshr_b64 s[64:65], s[64:65], 4\n\t" \
  "s_min_u32 s38, s38, s44\n\t" \
  "ds_read_b64 v[16:17], v16 offset:0\n\t" \
  "v_add_u32_sdwa v0, s29, v114 dst_sel:DWORD dst_unused:UNUSED_PAD src0_sel:DWORD src1_sel:WORD_0\n\t" \
  "v_smfmac_f32_16x16x64_bf16 v[68:71], a[104:107], v[126:133], v179\n\t" \
  "ds_read_b128 v[126:129], v183 offset:8192\n\t" \
  "s_waitcnt lgkmcnt(4)\n\t" \
  "buffer_store_dwordx4 v[28:31], v34, s[20:23], s36 offen sc1\n\t" \
  "v_add_u32_sdwa v2, s29, v114 dst_sel:DWORD dst_unused:UNUSED_PAD src0_sel:DWORD src1_sel:WORD_1\n\t" \
  "v_smfmac_f32_16x16x64_bf16 v[36:39], a[108:111], v[134:141], v154\n\t" \
  "ds_read_b128 v[130:133], v184 offset:8192\n\t" \
  "v_add_u32_sdwa v4, s29, v115 dst_sel:DWORD dst_unused:UNUSED_PAD src0_sel:DWORD src1_sel:WORD_0\n\t" \
  "v_smfmac_f32_16x16x64_bf16 v[40:43], a[112:115], v[134:141], v154 abid:1\n\t" \
  "s_branch .Ljn01%=\n\t" \
  ".fill (.Lvar%=+1404-.)/4, 4, 0xbf800000\n\t" \
  "s_nop 0\n\t" \
  "s_branch .Lfin%=\n\t" \
  "s_branch .Lhe%=\n\t" \
  "s_waitcnt vmcnt(6)\n\t" \
  "s_waitcnt vmcnt(10)\n\t" \
  "s_waitcnt vmcnt(12)\n\t" \
  "s_waitcnt vmcnt(17)\n\t" \
  "s_waitcnt vmcnt(21) lgkmcnt(0)\n\t" \
  "s_barrier\n\t" \
  "v_smfmac_f32_16x16x64_bf16 v[36:39], v[220:223], v[0:7], v174\n\t" \
  "ds_read_b128 v[126:129], v183 offset:0\n\t" \
  "v_add_u32 v24, s37, v185\n\t" \
  "v_smfmac_f32_16x16x64_bf16 v[40:43], v[224:227], v[0:7], v174 abid:1\n\t" \
  "ds_read_b128 v[130:133], v184 offset:0\n\t" \
  "v_xor_b32 v25, s41, v183\n\t" \
  "v_smfmac_f32_16x16x64_bf16 v[44:47], v[228:231], v[0:7], v175\n\t" \
  "ds_read_b128 v[134:137], v183 offset:2048\n\t" \
  "v_subrev_u32 v25, s30, v25\n\t" \
  "v_smfmac_f32_16x16x64_bf16 v[48:51], v[232:235], v[0:7], v175 abid:1\n\t" \
  "s_add_u32 s30, s29, 18432\n\t" \
  "ds_read_b128 v[138:141], v184 offset:2048\n\t" \
  "v_cvt_pk_bf16_f32 v8, v36, v37\n\t" \
  "v_cvt_pk_bf16_f32 v9, v38, v39\n\t" \
  "v_smfmac_f32_16x16x64_bf16 v[52:55], v[236:239], v[0:7], v176\n\t" \
  "s_sub_u32 s25, s30, 129024\n\t" \
  "ds_read_b128 v[36:39], v35 offset:0\n\t" \
  "v_cvt_pk_bf16_f32 v10, v40, v41\n\t" \
  "v_cvt_pk_bf16_f32 v11, v42, v43\n\t" \
  "v_smfmac_f32_16x16x64_bf16 v[56:59], v[240:243], v[0:7], v176 abid:1\n\t" \
  "s_min_u32 s30, s30, s25\n\t" \
  "ds_read_b128 v[40:43], v35 offset:64\n\t" \
  "v_cvt_pk_bf16_f32 v16, v44, v45\n\t" \
  "v_cvt_pk_bf16_f32 v17, v46, v47\n\t" \
  "v_smfmac_f32_16x16x64_bf16 v[60:63], v[244:247], v[0:7], v177\n\t" \
  "s_sub_u32 s30, s30, s29\n\t" \
  "ds_read_b128 v[44:47], v35 offset:128\n\t" \
  "v_cvt_pk_bf16_f32 v18, v48, v49\n\t" \
  "v_cvt_pk_bf16_f32 v19, v50, v51\n\t" \
  "v_smfmac_f32_16x16x64_bf16 v[64:67], v[248:251], v[0:7], v177 abid:1\n\t" \
  "s_mov_b32 s37, s29\n\t" \
  "ds_read_b128 v[48:51], v35 offset:192\n\t" \
  "v_cvt_pk_bf16_f32 v12, v52, v53\n\t" \
  "v_cvt_pk_bf16_f32 v13, v54, v55\n\t" \
  "v_smfmac_f32_16x16x64_bf16 v[68:71], v[252:255], v[0:7], v182\n\t" \
  "s_waitcnt lgkmcnt(3)\n\t" \
  "ds_read_b128 v[52:55], v35 offset:256\n\t" \
  "v_cvt_pk_bf16_f32 v14, v56, v57\n\t" \
  "v_cvt_pk_bf16_f32 v15, v58, v59\n\t" \
  "v_smfmac_f32_16x16x64_bf16 v[36:39], a[0:3], v[126:133], v142\n\t" \
  "s_waitcnt lgkmcnt(3)\n\t" \
  "ds_read_b128 v[56:59], v35 offset:320\n\t" \
  "v_cvt_pk_bf16_f32 v20, v60, v61\n\t" \
  "v_cvt_pk_bf16_f32 v21, v62, v63\n\t" \
  "v_smfmac_f32_16x16x64_bf16 v[40:43], a[4:7], v[126:133], v142 abid:1\n\t" \
  "s_waitcnt lgkmcnt(3)\n\t" \
  "ds_read_b128 v[60:63], v35 offset:384\n\t" \
  "v_cvt_pk_bf16_f32 v22, v64, v65\n\t" \
  "v_cvt_pk_bf16_f32 v23, v66, v67\n\t" \
  "v_smfmac_f32_16x16x64_bf16 v[44:47], a[8:11], v[126:133], v143\n\t" \
  "s_waitcnt lgkmcnt(3)\n\t" \
  "ds_read_b128 v[64:67], v35 offset:448\n\t" \
  "v_cvt_pk_bf16_f32 v26, v68, v69\n\t" \
  "v_cvt_pk_bf16_f32 v27, v70, v71\n\t" \
  "v_smfmac_f32_16x16x64_bf16 v[48:51], a[12:15], v[126:133], v143 abid:1\n\t" \
  "s_waitcnt lgkmcnt(3)\n\t" \
  "ds_read_b128 v[68:71], v35 offset:512\n\t" \
  "v_add_u32_sdwa v0, s29, v108 dst_sel:DWORD dst_unused:UNUSED_PAD src0_sel:DWORD src1_sel:WORD_0\n\t" \
  "v_smfmac_f32_16x16x64_bf16 v[52:55], a[16:19], v[126:133], v144\n\t" \
  "s_add_u32 m0, s33, 0\n\t" \
  "s_waitcnt lgkmcnt(3)\n\t" \
  "ds_write_b128 v24, v[8:11] offset:0\n\t" \
  "v_add_u32_sdwa v2, s29, v108 dst_sel:DWORD dst_unused:UNUSED_PAD src0_sel:DWORD src1_sel:WORD_1\n\t" \
  "v_smfmac_f32_16x16x64_bf16 v[56:59], a[20:23], v[126:133], v144 abid:1\n\t" \
  "s_waitcnt lgkmcnt(3)\n\t" \
  "buffer_load_dwordx4 v186, s[16:19], s34 offen offset:0 nt lds\n\t" \
  "v_add_u32_sdwa v4, s29, v109 dst_sel:DWORD dst_unused:UNUSED_PAD src0_sel:DWORD src1_sel:WORD_0\n\t" \
  "v_smfmac_f32_16x16x64_bf16 v[60:63], a[24:27], v[126:133], v145\n\t" \
  "s_add_u32 m0, s33, 1920\n\t" \
  "s_waitcnt lgkmcnt(2)\n\t" \
  "ds_read_b64 v[0:1], v0 offset:0\n\t" \
  "v_add_u32_sdwa v6, s29, v109 dst_sel:DWORD dst_unused:UNUSED_PAD src0_sel:DWORD src1_sel:WORD_1\n\t" \
  "v_smfmac_f32_16x16x64_bf16 v[64:67], a[28:31], v[126:133], v145 abid:1\n\t" \
  "s_waitcnt lgkmcnt(2)\n\t" \
  "buffer_load_dwordx4 v186, s[16:19], s34 offen offset:128 nt lds\n\t" \
  "v_smfmac_f32_16x16x64_bf16 v[68:71], a[32:35], v[126:133], v178\n\t" \
  "s_add_u32 m0, s33, 3840\n\t" \
  "ds_write_b128 v24, v[12:15] offset:128\n\t" \
  "v_xor_b32 v24, 64, v24\n\t" \
  "v_smfmac_f32_16x16x64_bf16 v[36:39], a[36:39], v[134:141], v146\n\t" \
  "buffer_load_dwordx4 v186, s[16:19], s34 offen offset:256 nt lds\n\t" \
  "v_add_u32_sdwa v8, s29, v110 dst_sel:DWORD dst_unused:UNUSED_PAD src0_sel:DWORD src1_sel:WORD_0\n\t" \
  "v_smfmac_f32_16x16x64_bf16 v[40:43], a[40:43], v[134:141], v146 abid:1\n\t" \
  "s_add_u32 m0, s33, 5760\n\t" \
  "ds_read_b128 v[126:129], v183 offset:4096\n\t" \
  "v_add_u32_sdwa v10, s29, v110 dst_sel:DWORD dst_unused:UNUSED_PAD src0_sel:DWORD src1_sel:WORD_1\n\t" \
  "v_smfmac_f32_16x16x64_bf16 v[44:47], a[44:47], v[134:141], v147\n\t" \
  "buffer_load_dwordx4 v186, s[16:19], s34 offen offset:384 nt lds\n\t" \
  "v_add_u32_sdwa v12, s29, v111 dst_sel:DWORD dst_unused:UNUSED_PAD src0_sel:DWORD src1_sel:WORD_0\n\t" \
  "v_smfmac_f32_16x16x64_bf16 v[48:51], a[48:51], v[134:141], v147 abid:1\n\t" \
  "s_add_u32 m0, s33, 7680\n\t" \
  "ds_write_b128 v24, v[16:19] offset:0\n\t" \
  "v_add_u32_sdwa v14, s29, v111 dst_sel:DWORD dst_unused:UNUSED_PAD src0_sel:DWORD src1_sel:WORD_1\n\t" \
  "v_smfmac_f32_16x16x64_bf16 v[52:55], a[52:55], v[134:141], v148\n\t" \
  "buffer_load_dwordx4 v186, s[16:19], s34 offen offset:512 nt lds\n\t" \
  "v_smfmac_f32_16x16x64_bf16 v[56:59], a[56:59], v[134:141], v148 abid:1\n\t" \
  "s_add_u32 s31, s31, 18432\n\t" \
  "ds_read_b128 v[130:133], v184 offset:4096\n\t" \
  "v_smfmac_f32_16x16x64_bf16 v[60:63], a[60:63], v[134:141], v149\n\t" \
  "s_sub_u32 s26, s31, 129024\n\t" \
  "ds_read_b64 v[2:3], v2 offset:0\n\t" \
  "v_smfmac_f32_16x16x64_bf16 v[64:67], a[64:67], v[134:141], v149 abid:1\n\t" \
  "s_min_u32 s31, s31, s26\n\t" \
  "ds_write_b128 v24, v[20:23] offset:128\n\t" \
  "v_add_u32_sdwa v16, s29, v112 dst_sel:DWORD dst_unused:UNUSED_PAD src0_sel:DWORD src1_sel:WORD_0\n\t" \
  "v_smfmac_f32_16x16x64_bf16 v[68:71], a[68:71], v[134:141], v178 abid:1\n\t" \
  "s_waitcnt lgkmcnt(2)\n\t" \
  "ds_read_b128 v[134:137], v183 offset:6144\n\t" \
  "v_add_u32_sdwa v18, s29, v112 dst_sel:DWORD dst_unused:UNUSED_PAD src0_sel:DWORD src1_sel:WORD_1\n\t" \
  "v_smfmac_f32_16x16x64_bf16 v[36:39], a[72:75], v[126:133], v150\n\t" \
  "s_add_u32 s33, s31, s32\n\t" \
  "ds_read_b128 v[138:141], v184 offset:6144\n\t" \
  "v_add_u32_sdwa v20, s29, v113 dst_sel:DWORD dst_unused:UNUSED_PAD src0_sel:DWORD src1_sel:WORD_0\n\t" \
  "v_smfmac_f32_16x16x64_bf16 v[40:43], a[76:79], v[126:133], v150 abid:1\n\t" \
  "s_add_u32 s34, s34, 4718592\n\t" \
  "ds_read_b64 v[4:5], v4 offset:0\n\t" \
  "v_add_u32_sdwa v22, s29, v113 dst_sel:DWORD dst_unused:UNUSED_PAD src0_sel:DWORD src1_sel:WORD_1\n\t" \
  "v_smfmac_f32_16x16x64_bf16 v[44:47], a[80:83], v[126:133], v151\n\t" \
  "s_and_b32 s27, s64, 7\n\t" \
  "ds_write_b64 v25, v[26:27] offset:16384\n\t" \
  "v_smfmac_f32_16x16x64_bf16 v[48:51], a[84:87], v[126:133], v151 abid:1\n\t" \
  "s_lshl2_add_u32 s66, s27, s68\n\t" \
  "ds_read_b64 v[6:7], v6 offset:0\n\t" \
  "v_smfmac_f32_16x16x64_bf16 v[52:55], a[88:91], v[126:133], v152\n\t" \
  "s_and_b32 s27, s64, 8\n\t" \
  "ds_read_b64 v[8:9], v8 offset:0\n\t" \
  "v_smfmac_f32_16x16x64_bf16 v[56:59], a[92:95], v[126:133], v152 abid:1\n\t" \
  "s_mul_i32 s27, s27, 176\n\t" \
  "ds_read_b64 v[10:11], v10 offset:0\n\t" \
  "v_smfmac_f32_16x16x64_bf16 v[60:63], a[96:99], v[126:133], v153\n\t" \
  "s_add_u32 s66, s66, s27\n\t" \
  "ds_read_b64 v[12:13], v12 offset:0\n\t" \
  "v_smfmac_f32_16x16x64_bf16 v[64:67], a[100:103], v[126:133], v153 abid:1\n\t" \
  "s_waitcnt lgkmcnt(3)\n\t" \
  "ds_read_b64 v[14:15], v14 offset:0\n\t" \
  "v_smfmac_f32_16x16x64_bf16 v[36:39], v[72:75], v[0:7], v182 abid:1\n\t" \
  "s_lshr_b64 s[64:65], s[64:65], 4\n\t" \
  "ds_read_b64 v[16:17], v16 offset:0\n\t" \
  "v_add_u32_sdwa v0, s29, v114 dst_sel:DWORD dst_unused:UNUSED_PAD src0_sel:DWORD src1_sel:WORD_0\n\t" \
  "v_smfmac_f32_16x16x64_bf16 v[68:71], a[104:107], v[126:133], v179\n\t" \
  "ds_read_b128 v[126:129], v183 offset:8192\n\t" \
  "v_add_u32_sdwa v2, s29, v114 dst_sel:DWORD dst_unused:UNUSED_PAD src0_sel:DWORD src1_sel:WORD_1\n\t" \
  "v_smfmac_f32_16x16x64_bf16 v[36:39], a[108:111], v[134:141], v154\n\t" \
  "ds_read_b128 v[130:133], v184 offset:8192\n\t" \
  "v_add_u32_sdwa v4, s29, v115 dst_sel:DWORD dst_unused:UNUSED_PAD src0_sel:DWORD src1_sel:WORD_0\n\t" \
  "v_smfmac_f32_16x16x64_bf16 v[40:43], a[112:115], v[134:141], v154 abid:1\n\t" \
  ".Ljn01%=:\n\t" \
  "s_waitcnt lgkmcnt(3)\n\t" \
  "ds_read_b64 v[18:19], v18 offset:0\n\t" \
  "v_add_u32_sdwa v6, s29, v115 dst_sel:DWORD dst_unused:UNUSED_PAD src0_sel:DWORD src1_sel:WORD_1\n\t" \
  "v_smfmac_f32_16x16x64_bf16 v[40:43], v[76:79], v[8:15], v182 abid:1\n\t" \
  "ds_read_b64 v[20:21], v20 offset:0\n\t" \
  "v_add_u32_sdwa v8, s29, v32 dst_sel:DWORD dst_unused:UNUSED_PAD src0_sel:DWORD src1_sel:WORD_0\n\t" \
  "v_smfmac_f32_16x16x64_bf16 v[44:47], a[116:119], v[134:141], v155\n\t" \
  "ds_read_b64 v[22:23], v22 offset:0\n\t" \
  "v_add_u32_sdwa v10, s29, v32 dst_sel:DWORD dst_unused:UNUSED_PAD src0_sel:DWORD src1_sel:WORD_1\n\t" \
  "v_smfmac_f32_16x16x64_bf16 v[48:51], a[120:123], v[134:141], v155 abid:1\n\t" \
  "ds_read_b64 v[0:1], v0 offset:0\n\t" \
  "v_add_u32 v28, s60, v187\n\t" \
  "v_smfmac_f32_16x16x64_bf16 v[52:55], a[124:127], v[134:141], v156\n\t" \
  "ds_read_b64 v[2:3], v2 offset:0\n\t" \
  "v_smfmac_f32_16x16x64_bf16 v[56:59], a[128:131], v[134:141], v156 abid:1\n\t" \
  "ds_read_b64 v[4:5], v4 offset:0\n\t" \
  "v_smfmac_f32_16x16x64_bf16 v[60:63], a[132:135], v[134:141], v157\n\t" \
  "s_waitcnt lgkmcnt(3)\n\t" \
  "ds_read_b64 v[6:7], v6 offset:0\n\t" \
  "v_smfmac_f32_16x16x64_bf16 v[44:47], v[80:83], v[16:23], v182 abid:1\n\t" \
  "ds_read_b64 v[8:9], v8 offset:0\n\t" \
  "v_add_u32 v24, s38, v187\n\t" \
  "v_smfmac_f32_16x16x64_bf16 v[64:67], a[136:139], v[134:141], v157 abid:1\n\t" \
  "ds_read_b128 v[24:27], v24 offset:0\n\t" \
  "v_add_u32_sdwa v16, s29, v116 dst_sel:DWORD dst_unused:UNUSED_PAD src0_sel:DWORD src1_sel:WORD_0\n\t" \
  "v_smfmac_f32_16x16x64_bf16 v[68:71], a[140:143], v[134:141], v179 abid:1\n\t" \
  "ds_read_b128 v[134:137], v183 offset:10240\n\t" \
  "v_add_u32_sdwa v18, s29, v116 dst_sel:DWORD dst_unused:UNUSED_PAD src0_sel:DWORD src1_sel:WORD_1\n\t" \
  "v_smfmac_f32_16x16x64_bf16 v[36:39], a[144:147], v[126:133], v158\n\t" \
  "s_waitcnt lgkmcnt(3)\n\t" \
  "ds_read_b128 v[138:141], v184 offset:10240\n\t" \
  "v_add_u32_sdwa v20, s29, v117 dst_sel:DWORD dst_unused:UNUSED_PAD src0_sel:DWORD src1_sel:WORD_0\n\t" \
  "v_smfmac_f32_16x16x64_bf16 v[48:51], v[84:87], v[0:7], v182 abid:1\n\t" \
  "s_add_u32 s36, s35, s39\n\t" \
  "ds_read_b64 v[10:11], v10 offset:0\n\t" \
  "v_add_u32_sdwa v22, s29, v117 dst_sel:DWORD dst_unused:UNUSED_PAD src0_sel:DWORD src1_sel:WORD_1\n\t" \
  "v_smfmac_f32_16x16x64_bf16 v[40:43], a[148:151], v[126:133], v158 abid:1\n\t" \
  "s_waitcnt lgkmcnt(3)\n\t" \
  "buffer_store_dwordx4 v[24:27], v34, s[20:23], s36 offen sc1\n\t" \
  "v_add_u32_sdwa v0, s29, v118 dst_sel:DWORD dst_unused:UNUSED_PAD src0_sel:DWORD src1_sel:WORD_0\n\t" \
  "v_smfmac_f32_16x16x64_bf16 v[44:47], a[152:155], v[126:133], v159\n\t" \
  "ds_read_b128 v[28:31], v28 offset:0\n\t" \
  "v_xad_u32 v24, v187, 64, s38\n\t" \
  "v_smfmac_f32_16x16x64_bf16 v[48:51], a[156:159], v[126:133], v159 abid:1\n\t" \
  "ds_read_b128 v[24:27], v24 offset:1024\n\t" \
  "v_add_u32_sdwa v2, s29, v118 dst_sel:DWORD dst_unused:UNUSED_PAD src0_sel:DWORD src1_sel:WORD_1\n\t" \
  "v_smfmac_f32_16x16x64_bf16 v[52:55], a[160:163], v[126:133], v160\n\t" \
  "ds_read_b64 v[16:17], v16 offset:0\n\t" \
  "v_add_u32_sdwa v4, s29, v119 dst_sel:DWORD dst_unused:UNUSED_PAD src0_sel:DWORD src1_sel:WORD_0\n\t" \
  "v_smfmac_f32_16x16x64_bf16 v[56:59], a[164:167], v[126:133], v160 abid:1\n\t" \
  "ds_read_b64 v[18:19], v18 offset:0\n\t" \
  "v_add_u32_sdwa v6, s29, v119 dst_sel:DWORD dst_unused:UNUSED_PAD src0_sel:DWORD src1_sel:WORD_1\n\t" \
  "v_smfmac_f32_16x16x64_bf16 v[60:63], a[168:171], v[126:133], v161\n\t" \
  "s_add_u32 s36, s36, 4608\n\t" \
  "s_waitcnt lgkmcnt(3)\n\t" \
  "ds_read_b64 v[20:21], v20 offset:0\n\t" \
  "s_set_gpr_idx_on s6, gpr_idx(DST)\n\t" \
  "v_smfmac_f32_16x16x64_bf16 v[36:39], v[28:31], v[8:15], v182 abid:1\n\t" \
  "s_set_gpr_idx_off\n\t" \
  "s_waitcnt lgkmcnt(3)\n\t" \
  "buffer_store_dwordx4 v[24:27], v34, s[20:23], s36 offen sc1\n\t" \
  "v_add_u32_sdwa v8, s29, v120 dst_sel:DWORD dst_unused:UNUSED_PAD src0_sel:DWORD src1_sel:WORD_0\n\t" \
  "v_smfmac_f32_16x16x64_bf16 v[64:67], a[172:175], v[126:133], v161 abid:1\n\t" \
  "ds_read_b64 v[22:23], v22 offset:0\n\t" \
  "v_add_u32 v24, s38, v187\n\t" \
  "v_smfmac_f32_16x16x64_bf16 v[68:71], a[176:179], v[126:133], v180\n\t" \
  "ds_read_b128 v[24:27], v24 offset:2048\n\t" \
  "v_add_u32_sdwa v10, s29, v120 dst_sel:DWORD dst_unused:UNUSED_PAD src0_sel:DWORD src1_sel:WORD_1\n\t" \
  "v_smfmac_f32_16x16x64_bf16 v[36:39], a[180:183], v[134:141], v162\n\t" \
  "ds_read_b128 v[126:129], v183 offset:12288\n\t" \
  "v_add_u32_sdwa v12, s29, v121 dst_sel:DWORD dst_unused:UNUSED_PAD src0_sel:DWORD src1_sel:WORD_0\n\t" \
  "v_smfmac_f32_16x16x64_bf16 v[40:43], a[184:187], v[134:141], v162 abid:1\n\t" \
  "ds_read_b128 v[130:133], v184 offset:12288\n\t" \
  "v_add_u32_sdwa v14, s29, v121 dst_sel:DWORD dst_unused:UNUSED_PAD src0_sel:DWORD src1_sel:WORD_1\n\t" \
  "v_smfmac_f32_16x16x64_bf16 v[44:47], a[188:191], v[134:141], v163\n\t" \
  "s_add_u32 s36, s36, 4608\n\t" \
  "s_waitcnt lgkmcnt(3)\n\t" \
  "ds_read_b64 v[0:1], v0 offset:0\n\t" \
  "v_smfmac_f32_16x16x64_bf16 v[52:55], v[88:91], v[16:23], v182 abid:1\n\t" \
  "s_waitcnt lgkmcnt(3)\n\t" \
  "buffer_store_dwordx4 v[24:27], v34, s[20:23], s36 offen sc1\n\t" \
  "v_add_u32_sdwa v16, s29, v122 dst_sel:DWORD dst_unused:UNUSED_PAD src0_sel:DWORD src1_sel:WORD_0\n\t" \
  "v_smfmac_f32_16x16x64_bf16 v[48:51], a[192:195], v[134:141], v163 abid:1\n\t" \
  "ds_read_b64 v[2:3], v2 offset:0\n\t" \
  "v_xad_u32 v24, v187, 64, s38\n\t" \
  "v_smfmac_f32_16x16x64_bf16 v[52:55], a[196:199], v[134:141], v164\n\t" \
  "s_add_u32 s38, s38, 18432\n\t" \
  "ds_read_b128 v[24:27], v24 offset:3072\n\t" \
  "v_add_u32_sdwa v18, s29, v122 dst_sel:DWORD dst_unused:UNUSED_PAD src0_sel:DWORD src1_sel:WORD_1\n\t" \
  "v_smfmac_f32_16x16x64_bf16 v[56:59], a[200:203], v[134:141], v164 abid:1\n\t" \
  "s_sub_u32 s44, s38, 129024\n\t" \
  "ds_read_b64 v[4:5], v4 offset:0\n\t" \
  "v_add_u32_sdwa v20, s29, v123 dst_sel:DWORD dst_unused:UNUSED_PAD src0_sel:DWORD src1_sel:WORD_0\n\t" \
  "v_smfmac_f32_16x16x64_bf16 v[60:63], a[204:207], v[134:141], v165\n\t" \
  "s_min_u32 s38, s38, s44\n\t" \
  "ds_read_b64 v[6:7], v6 offset:0\n\t" \
  "v_add_u32_sdwa v22, s29, v123 dst_sel:DWORD dst_unused:UNUSED_PAD src0_sel:DWORD src1_sel:WORD_1\n\t" \
  "v_smfmac_f32_16x16x64_bf16 v[64:67], a[208:211], v[134:141], v165 abid:1\n\t" \
  "s_add_u32 s36, s36, 4608\n\t" \
  "ds_read_b64 v[8:9], v8 offset:0\n\t" \
  "v_smfmac_f32_16x16x64_bf16 v[68:71], a[212:215], v[134:141], v180 abid:1\n\t" \
  "s_waitcnt lgkmcnt(3)\n\t" \
  "buffer_store_dwordx4 v[24:27], v34, s[20:23], s36 offen sc1\n\t" \
  "v_smfmac_f32_16x16x64_bf16 v[36:39], a[216:219], v[126:133], v166\n\t" \
  "s_add_u32 s35, s35, 4718592\n\t" \
  "ds_read_b128 v[134:137], v183 offset:14336\n\t" \
  "v_smfmac_f32_16x16x64_bf16 v[40:43], a[220:223], v[126:133], v166 abid:1\n\t" \
  "s_waitcnt lgkmcnt(2)\n\t" \
  "ds_read_b128 v[138:141], v184 offset:14336\n\t" \
  "v_smfmac_f32_16x16x64_bf16 v[56:59], v[92:95], v[0:7], v182 abid:1\n\t" \
  "ds_read_b64 v[10:11], v10 offset:0\n\t" \
  "v_smfmac_f32_16x16x64_bf16 v[44:47], a[224:227], v[126:133], v167\n\t" \
  "ds_read_b64 v[12:13], v12 offset:0\n\t" \
  "v_smfmac_f32_16x16x64_bf16 v[48:51], a[228:231], v[126:133], v167 abid:1\n\t" \
  "ds_read_b64 v[14:15], v14 offset:0\n\t" \
  "v_smfmac_f32_16x16x64_bf16 v[52:55], a[232:235], v[126:133], v168\n\t" \
  "ds_read_b64 v[16:17], v16 offset:0\n\t" \
  "v_smfmac_f32_16x16x64_bf16 v[56:59], a[236:239], v[126:133], v168 abid:1\n\t" \
  "ds_read_b64 v[18:19], v18 offset:0\n\t" \
  "v_smfmac_f32_16x16x64_bf16 v[60:63], a[240:243], v[126:133], v169\n\t" \
  "ds_read_b64 v[20:21], v20 offset:0\n\t" \
  "v_smfmac_f32_16x16x64_bf16 v[64:67], a[244:247], v[126:133], v169 abid:1\n\t" \
  "s_waitcnt lgkmcnt(3)\n\t" \
  "ds_read_b64 v[22:23], v22 offset:0\n\t" \
  "v_smfmac_f32_16x16x64_bf16 v[60:63], v[96:99], v[8:15], v182 abid:1\n\t" \
  "v_add_u32_sdwa v8, s29, v124 dst_sel:DWORD dst_unused:UNUSED_PAD src0_sel:DWORD src1_sel:WORD_0\n\t" \
  "v_smfmac_f32_16x16x64_bf16 v[68:71], a[248:251], v[126:133], v181\n\t" \
  "ds_read_b128 v[0:3], v183 offset:16384\n\t" \
  "v_add_u32_sdwa v10, s29, v124 dst_sel:DWORD dst_unused:UNUSED_PAD src0_sel:DWORD src1_sel:WORD_1\n\t" \
  "v_smfmac_f32_16x16x64_bf16 v[36:39], a[252:255], v[134:141], v170\n\t" \
  "ds_read_b128 v[4:7], v184 offset:16384\n\t" \
  "v_add_u32_sdwa v12, s29, v125 dst_sel:DWORD dst_unused:UNUSED_PAD src0_sel:DWORD src1_sel:WORD_0\n\t" \
  "v_smfmac_f32_16x16x64_bf16 v[40:43], v[188:191], v[134:141], v170 abid:1\n\t" \
  "s_waitcnt lgkmcnt(2)\n\t" \
  "ds_read_b64 v[8:9], v8 offset:0\n\t" \
  "v_add_u32_sdwa v14, s29, v125 dst_sel:DWORD dst_unused:UNUSED_PAD src0_sel:DWORD src1_sel:WORD_1\n\t" \
  "v_smfmac_f32_16x16x64_bf16 v[64:67], v[100:103], v[16:23], v182 abid:1\n\t" \
  "s_add_u32 s29, s29, s30\n\t" \
  "ds_read_b64 v[10:11], v10 offset:0\n\t" \
  "v_add_u32 v183, s30, v183\n\t" \
  "v_smfmac_f32_16x16x64_bf16 v[44:47], v[192:195], v[134:141], v171\n\t" \
  "ds_read_b64 v[12:13], v12 offset:0\n\t" \
  "v_add_u32 v184, s30, v184\n\t" \
  "v_smfmac_f32_16x16x64_bf16 v[48:51], v[196:199], v[134:141], v171 abid:1\n\t" \
  "ds_read_b64 v[14:15], v14 offset:0\n\t" \
  "v_smfmac_f32_16x16x64_bf16 v[52:55], v[200:203], v[134:141], v172\n\t" \
  "v_smfmac_f32_16x16x64_bf16 v[56:59], v[204:207], v[134:141], v172 abid:1\n\t" \
  "v_smfmac_f32_16x16x64_bf16 v[60:63], v[208:211], v[134:141], v173\n\t" \
  "v_smfmac_f32_16x16x64_bf16 v[64:67], v[212:215], v[134:141], v173 abid:1\n\t" \
  "s_waitcnt lgkmcnt(0)\n\t" \
  "v_smfmac_f32_16x16x64_bf16 v[68:71], v[104:107], v[8:15], v182 abid:1\n\t" \
  "v_smfmac_f32_16x16x64_bf16 v[68:71], v[216:219], v[134:141], v181 abid:1\n\t" \
  "s_setpc_b64 s[66:67]\n\t" \
  ".fill (.Lvar%=+4096-.)/4, 4, 0xbf800000\n\t" \
  "s_branch .Lfin%=\n\t" \
  "s_branch .Lhe%=\n\t" \
  "s_waitcnt vmcnt(6)\n\t" \
  "s_waitcnt vmcnt(10)\n\t" \
  "s_waitcnt vmcnt(12)\n\t" \
  "s_waitcnt vmcnt(17)\n\t" \
  "s_waitcnt vmcnt(21) lgkmcnt(0)\n\t" \
  "s_barrier\n\t" \
  "v_smfmac_f32_16x16x64_bf16 v[36:39], v[220:223], v[0:7], v174\n\t" \
  "ds_read_b128 v[126:129], v183 offset:0\n\t" \
  "v_add_u32 v24, s37, v185\n\t" \
  "v_smfmac_f32_16x16x64_bf16 v[40:43], v[224:227], v[0:7], v174 abid:1\n\t" \
  "ds_read_b128 v[130:133], v184 offset:0\n\t" \
  "v_xor_b32 v25, s41, v183\n\t" \
  "v_smfmac_f32_16x16x64_bf16 v[44:47], v[228:231], v[0:7], v175\n\t" \
  "ds_read_b128 v[134:137], v183 offset:2048\n\t" \
  "v_subrev_u32 v25, s30, v25\n\t" \
  "v_smfmac_f32_16x16x64_bf16 v[48:51], v[232:235], v[0:7], v175 abid:1\n\t" \
  "s_add_u32 s30, s29, 18432\n\t" \
  "ds_read_b128 v[138:141], v184 offset:2048\n\t" \
  "v_cvt_pk_bf16_f32 v8, v36, v37\n\t" \
  "v_cvt_pk_bf16_f32 v9, v38, v39\n\t" \
  "v_smfmac_f32_16x16x64_bf16 v[52:55], v[236:239], v[0:7], v176\n\t" \
  "s_sub_u32 s25, s30, 129024\n\t" \
  "ds_read_b128 v[36:39], v35 offset:0\n\t" \
  "v_cvt_pk_bf16_f32 v10, v40, v41\n\t" \
  "v_cvt_pk_bf16_f32 v11, v42, v43\n\t" \
  "v_smfmac_f32_16x16x64_bf16 v[56:59], v[240:243], v[0:7], v176 abid:1\n\t" \
  "s_min_u32 s30, s30, s25\n\t" \
  "ds_read_b128 v[40:43], v35 offset:64\n\t" \
  "v_cvt_pk_bf16_f32 v16, v44, v45\n\t" \
  "v_cvt_pk_bf16_f32 v17, v46, v47\n\t" \
  "v_smfmac_f32_16x16x64_bf16 v[60:63], v[244:247], v[0:7], v177\n\t" \
  "s_sub_u32 s30, s30, s29\n\t" \
  "ds_read_b128 v[44:47], v35 offset:128\n\t" \
  "v_cvt_pk_bf16_f32 v18, v48, v49\n\t" \
  "v_cvt_pk_bf16_f32 v19, v50, v51\n\t" \
  "v_smfmac_f32_16x16x64_bf16 v[64:67], v[248:251], v[0:7], v177 abid:1\n\t" \
  "s_mov_b32 s37, s29\n\t" \
  "ds_read_b128 v[48:51], v35 offset:192\n\t" \
  "v_cvt_pk_bf16_f32 v12, v52, v53\n\t" \
  "v_cvt_pk_bf16_f32 v13, v54, v55\n\t" \
  "v_smfmac_f32_16x16x64_bf16 v[68:71], v[252:255], v[0:7], v182\n\t" \
  "s_waitcnt lgkmcnt(3)\n\t" \
  "ds_read_b128 v[52:55], v35 offset:256\n\t" \
  "v_cvt_pk_bf16_f32 v14, v56, v57\n\t" \
  "v_cvt_pk_bf16_f32 v15, v58, v59\n\t" \
  "v_smfmac_f32_16x16x64_bf16 v[36:39], a[0:3], v[126:133], v142\n\t" \
  "s_waitcnt lgkmcnt(3)\n\t" \
  "ds_read_b128 v[56:59], v35 offset:320\n\t" \
  "v_cvt_pk_bf16_f32 v20, v60, v61\n\t" \
  "v_cvt_pk_bf16_f32 v21, v62, v63\n\t" \
  "v_smfmac_f32_16x16x64_bf16 v[40:43], a[4:7], v[126:133], v142 abid:1\n\t" \
  "s_waitcnt lgkmcnt(3)\n\t" \
  "ds_read_b128 v[60:63], v35 offset:384\n\t" \
  "v_cvt_pk_bf16_f32 v22, v64, v65\n\t" \
  "v_cvt_pk_bf16_f32 v23, v66, v67\n\t" \
  "v_smfmac_f32_16x16x64_bf16 v[44:47], a[8:11], v[126:133], v143\n\t" \
  "s_waitcnt lgkmcnt(3)\n\t" \
  "ds_read_b128 v[64:67], v35 offset:448\n\t" \
  "v_cvt_pk_bf16_f32 v26, v68, v69\n\t" \
  "v_cvt_pk_bf16_f32 v27, v70, v71\n\t" \
  "v_smfmac_f32_16x16x64_bf16 v[48:51], a[12:15], v[126:133], v143 abid:1\n\t" \
  "s_waitcnt lgkmcnt(3)\n\t" \
  "ds_read_b128 v[68:71], v35 offset:512\n\t" \
  "v_add_u32_sdwa v0, s29, v108 dst_sel:DWORD dst_unused:UNUSED_PAD src0_sel:DWORD src1_sel:WORD_0\n\t" \
  "v_smfmac_f32_16x16x64_bf16 v[52:55], a[16:19], v[126:133], v144\n\t" \
  "s_waitcnt lgkmcnt(3)\n\t" \
  "s_bfe_i32 s47, s48, 0x10000\n\t" \
  "ds_write_b128 v24, v[8:11] offset:0\n\t" \
  "v_add_u32_sdwa v2, s29, v108 dst_sel:DWORD dst_unused:UNUSED_PAD src0_sel:DWORD src1_sel:WORD_1\n\t" \
  "v_add_u32 v28, s38, v187\n\t" \
  "v_smfmac_f32_16x16x64_bf16 v[56:59], a[20:23], v[126:133], v144 abid:1\n\t" \
  "s_waitcnt lgkmcnt(3)\n\t" \
  "s_lshr_b32 s48, s48, 1\n\t" \
  "ds_read_b128 v[28:31], v28 offset:0\n\t" \
  "v_add_u32_sdwa v4, s29, v109 dst_sel:DWORD dst_unused:UNUSED_PAD src0_sel:DWORD src1_sel:WORD_0\n\t" \
  "v_smfmac_f32_16x16x64_bf16 v[60:63], a[24:27], v[126:133], v145\n\t" \
  "s_waitcnt lgkmcnt(2)\n\t" \
  "s_add_u32 s36, s35, s39\n\t" \
  "ds_read_b64 v[0:1], v0 offset:0\n\t" \
  "v_add_u32_sdwa v6, s29, v109 dst_sel:DWORD dst_unused:UNUSED_PAD src0_sel:DWORD src1_sel:WORD_1\n\t" \
  "v_smfmac_f32_16x16x64_bf16 v[64:67], a[28:31], v[126:133], v145 abid:1\n\t" \
  "s_waitcnt lgkmcnt(2)\n\t" \
  "s_andn2_b32 s44, 0x40000000, s47\n\t" \
  "v_smfmac_f32_16x16x64_bf16 v[68:71], a[32:35], v[126:133], v178\n\t" \
  "s_or_b32 s36, s36, s44\n\t" \
  "ds_write_b128 v24, v[12:15] offset:128\n\t" \
  "v_xor_b32 v24, 64, v24\n\t" \
  "v_smfmac_f32_16x16x64_bf16 v[36:39], a[36:39], v[134:141], v146\n\t" \
  "s_waitcnt lgkmcnt(2)\n\t" \
  "buffer_store_dwordx4 v[28:31], v34, s[20:23], s36 offen sc1\n\t" \
  "v_add_u32_sdwa v8, s29, v110 dst_sel:DWORD dst_unused:UNUSED_PAD src0_sel:DWORD src1_sel:WORD_0\n\t" \
  "v_smfmac_f32_16x16x64_bf16 v[40:43], a[40:43], v[134:141], v146 abid:1\n\t" \
  "s_and_b32 s44, s47, 4718592\n\t" \
  "ds_read_b128 v[126:129], v183 offset:4096\n\t" \
  "v_add_u32_sdwa v10, s29, v110 dst_sel:DWORD dst_unused:UNUSED_PAD src0_sel:DWORD src1_sel:WORD_1\n\t" \
  "v_xad_u32 v28, v187, 64, s38\n\t" \
  "v_smfmac_f32_16x16x64_bf16 v[44:47], a[44:47], v[134:141], v147\n\t" \
  "s_add_u32 s35, s35, s44\n\t" \
  "ds_read_b128 v[28:31], v28 offset:1024\n\t" \
  "v_add_u32_sdwa v12, s29, v111 dst_sel:DWORD dst_unused:UNUSED_PAD src0_sel:DWORD src1_sel:WORD_0\n\t" \
  "v_smfmac_f32_16x16x64_bf16 v[48:51], a[48:51], v[134:141], v147 abid:1\n\t" \
  "s_add_u32 s36, s36, 4608\n\t" \
  "ds_write_b128 v24, v[16:19] offset:0\n\t" \
  "v_add_u32_sdwa v14, s29, v111 dst_sel:DWORD dst_unused:UNUSED_PAD src0_sel:DWORD src1_sel:WORD_1\n\t" \
  "v_smfmac_f32_16x16x64_bf16 v[52:55], a[52:55], v[134:141], v148\n\t" \
  "v_smfmac_f32_16x16x64_bf16 v[56:59], a[56:59], v[134:141], v148 abid:1\n\t" \
  "ds_read_b128 v[130:133], v184 offset:4096\n\t" \
  "v_smfmac_f32_16x16x64_bf16 v[60:63], a[60:63], v[134:141], v149\n\t" \
  "ds_read_b64 v[2:3], v2 offset:0\n\t" \
  "s_waitcnt lgkmcnt(3)\n\t" \
  "buffer_store_dwordx4 v[28:31], v34, s[20:23], s36 offen sc1\n\t" \
  "v_smfmac_f32_16x16x64_bf16 v[64:67], a[64:67], v[134:141], v149 abid:1\n\t" \
  "s_and_b32 s44, s47, 18432\n\t" \
  "ds_write_b128 v24, v[20:23] offset:128\n\t" \
  "v_add_u32_sdwa v16, s29, v112 dst_sel:DWORD dst_unused:UNUSED_PAD src0_sel:DWORD src1_sel:WORD_0\n\t" \
  "v_add_u32 v28, s38, v187\n\t" \
  "v_smfmac_f32_16x16x64_bf16 v[68:71], a[68:71], v[134:141], v178 abid:1\n\t" \
  "s_waitcnt lgkmcnt(2)\n\t" \
  "ds_read_b128 v[134:137], v183 offset:6144\n\t" \
  "ds_read_b128 v[28:31], v28 offset:2048\n\t" \
  "v_add_u32_sdwa v18, s29, v112 dst_sel:DWORD dst_unused:UNUSED_PAD src0_sel:DWORD src1_sel:WORD_1\n\t" \
  "v_smfmac_f32_16x16x64_bf16 v[36:39], a[72:75], v[126:133], v150\n\t" \
  "s_add_u32 s36, s36, 4608\n\t" \
  "ds_read_b128 v[138:141], v184 offset:6144\n\t" \
  "v_add_u32_sdwa v20, s29, v113 dst_sel:DWORD dst_unused:UNUSED_PAD src0_sel:DWORD src1_sel:WORD_0\n\t" \
  "v_smfmac_f32_16x16x64_bf16 v[40:43], a[76:79], v[126:133], v150 abid:1\n\t" \
  "ds_read_b64 v[4:5], v4 offset:0\n\t" \
  "v_add_u32_sdwa v22, s29, v113 dst_sel:DWORD dst_unused:UNUSED_PAD src0_sel:DWORD src1_sel:WORD_1\n\t" \
  "v_smfmac_f32_16x16x64_bf16 v[44:47], a[80:83], v[126:133], v151\n\t" \
  "s_and_b32 s27, s64, 7\n\t" \
  "ds_write_b64 v25, v[26:27] offset:16384\n\t" \
  "v_smfmac_f32_16x16x64_bf16 v[48:51], a[84:87], v[126:133], v151 abid:1\n\t" \
  "s_lshl2_add_u32 s66, s27, s68\n\t" \
  "ds_read_b64 v[6:7], v6 offset:0\n\t" \
  "ds_read_b64 v[8:9], v8 offset:0\n\t" \
  "s_waitcnt lgkmcnt(5)\n\t" \
  "buffer_store_dwordx4 v[28:31], v34, s[20:23], s36 offen sc1\n\t" \
  "v_smfmac_f32_16x16x64_bf16 v[52:55], a[88:91], v[126:133], v152\n\t" \
  "s_and_b32 s27, s64, 8\n\t" \
  "ds_read_b64 v[10:11], v10 offset:0\n\t" \
  "v_xad_u32 v28, v187, 64, s38\n\t" \
  "v_smfmac_f32_16x16x64_bf16 v[56:59], a[92:95], v[126:133], v152 abid:1\n\t" \
  "s_mul_i32 s27, s27, 176\n\t" \
  "s_add_u32 s38, s38, s44\n\t" \
  "ds_read_b64 v[12:13], v12 offset:0\n\t" \
  "ds_read_b128 v[28:31], v28 offset:3072\n\t" \
  "v_smfmac_f32_16x16x64_bf16 v[60:63], a[96:99], v[126:133], v153\n\t" \
  "s_add_u32 s66, s66, s27\n\t" \
  "s_add_u32 s36, s36, 4608\n\t" \
  "ds_read_b64 v[14:15], v14 offset:0\n\t" \
  "v_smfmac_f32_16x16x64_bf16 v[64:67], a[100:103], v[126:133], v153 abid:1\n\t" \
  "s_waitcnt lgkmcnt(4)\n\t" \
  "s_sub_u32 s44, s38, 129024\n\t" \
  "ds_read_b64 v[16:17], v16 offset:0\n\t" \
  "v_smfmac_f32_16x16x64_bf16 v[36:39], v[72:75], v[0:7], v182 abid:1\n\t" \
  "s_lshr_b64 s[64:65], s[64:65], 4\n\t" \
  "s_min_u32 s38, s38, s44\n\t" \
  "ds_read_b64 v[18:19], v18 offset:0\n\t" \
  "v_add_u32_sdwa v0, s29, v114 dst_sel:DWORD dst_unused:UNUSED_PAD src0_sel:DWORD src1_sel:WORD_0\n\t" \
  "v_smfmac_f32_16x16x64_bf16 v[68:71], a[104:107], v[126:133], v179\n\t" \
  "ds_read_b128 v[126:129], v183 offset:8192\n\t" \
  "s_waitcnt lgkmcnt(4)\n\t" \
  "buffer_store_dwordx4 v[28:31], v34, s[20:23], s36 offen sc1\n\t" \
  "v_add_u32_sdwa v2, s29, v114 dst_sel:DWORD dst_unused:UNUSED_PAD src0_sel:DWORD src1_sel:WORD_1\n\t" \
  "v_smfmac_f32_16x16x64_bf16 v[36:39], a[108:111], v[134:141], v154\n\t" \
  "s_waitcnt lgkmcnt(3)\n\t" \
  "ds_read_b128 v[130:133], v184 offset:8192\n\t" \
  "v_add_u32_sdwa v4, s29, v115 dst_sel:DWORD dst_unused:UNUSED_PAD src0_sel:DWORD src1_sel:WORD_0\n\t" \
  "v_smfmac_f32_16x16x64_bf16 v[40:43], v[76:79], v[8:15], v182 abid:1\n\t" \
  "s_branch .Ljn02%=\n\t" \
  ".fill (.Lvar%=+5500-.)/4, 4, 0xbf800000\n\t" \
  "s_nop 0\n\t" \
  "s_branch .Lfin%=\n\t" \
  "s_branch .Lhe%=\n\t" \
  "s_waitcnt vmcnt(6)\n\t" \
  "s_waitcnt vmcnt(10)\n\t" \
  "s_waitcnt vmcnt(12)\n\t" \
  "s_waitcnt vmcnt(17)\n\t" \
  "s_waitcnt vmcnt(21) lgkmcnt(0)\n\t" \
  "s_barrier\n\t" \
  "v_smfmac_f32_16x16x64_bf16 v[36:39], v[220:223], v[0:7], v174\n\t" \
  "ds_read_b128 v[126:129], v183 offset:0\n\t" \
  "v_add_u32 v24, s37, v185\n\t" \
  "v_smfmac_f32_16x16x64_bf16 v[40:43], v[224:227], v[0:7], v174 abid:1\n\t" \
  "ds_read_b128 v[130:133], v184 offset:0\n\t" \
  "v_xor_b32 v25, s41, v183\n\t" \
  "v_smfmac_f32_16x16x64_bf16 v[44:47], v[228:231], v[0:7], v175\n\t" \
  "ds_read_b128 v[134:137], v183 offset:2048\n\t" \
  "v_subrev_u32 v25, s30, v25\n\t" \
  "v_smfmac_f32_16x16x64_bf16 v[48:51], v[232:235], v[0:7], v175 abid:1\n\t" \
  "s_add_u32 s30, s29, 18432\n\t" \
  "ds_read_b128 v[138:141], v184 offset:2048\n\t" \
  "v_cvt_pk_bf16_f32 v8, v36, v37\n\t" \
  "v_cvt_pk_bf16_f32 v9, v38, v39\n\t" \
  "v_smfmac_f32_16x16x64_bf16 v[52:55], v[236:239], v[0:7], v176\n\t" \
  "s_sub_u32 s25, s30, 129024\n\t" \
  "ds_read_b128 v[36:39], v35 offset:0\n\t" \
  "v_cvt_pk_bf16_f32 v10, v40, v41\n\t" \
  "v_cvt_pk_bf16_f32 v11, v42, v43\n\t" \
  "v_smfmac_f32_16x16x64_bf16 v[56:59], v[240:243], v[0:7], v176 abid:1\n\t" \
  "s_min_u32 s30, s30, s25\n\t" \
  "ds_read_b128 v[40:43], v35 offset:64\n\t" \
  "v_cvt_pk_bf16_f32 v16, v44, v45\n\t" \
  "v_cvt_pk_bf16_f32 v17, v46, v47\n\t" \
  "v_smfmac_f32_16x16x64_bf16 v[60:63], v[244:247], v[0:7], v177\n\t" \
  "s_sub_u32 s30, s30, s29\n\t" \
  "ds_read_b128 v[44:47], v35 offset:128\n\t" \
  "v_cvt_pk_bf16_f32 v18, v48, v49\n\t" \
  "v_cvt_pk_bf16_f32 v19, v50, v51\n\t" \
  "v_smfmac_f32_16x16x64_bf16 v[64:67], v[248:251], v[0:7], v177 abid:1\n\t" \
  "s_mov_b32 s37, s29\n\t" \
  "ds_read_b128 v[48:51], v35 offset:192\n\t" \
  "v_cvt_pk_bf16_f32 v12, v52, v53\n\t" \
  "v_cvt_pk_bf16_f32 v13, v54, v55\n\t" \
  "v_smfmac_f32_16x16x64_bf16 v[68:71], v[252:255], v[0:7], v182\n\t" \
  "s_waitcnt lgkmcnt(3)\n\t" \
  "ds_read_b128 v[52:55], v35 offset:256\n\t" \
  "v_cvt_pk_bf16_f32 v14, v56, v57\n\t" \
  "v_cvt_pk_bf16_f32 v15, v58, v59\n\t" \
  "v_smfmac_f32_16x16x64_bf16 v[36:39], a[0:3], v[126:133], v142\n\t" \
  "s_waitcnt lgkmcnt(3)\n\t" \
  "ds_read_b128 v[56:59], v35 offset:320\n\t" \
  "v_cvt_pk_bf16_f32 v20, v60, v61\n\t" \
  "v_cvt_pk_bf16_f32 v21, v62, v63\n\t" \
  "v_smfmac_f32_16x16x64_bf16 v[40:43], a[4:7], v[126:133], v142 abid:1\n\t" \
  "s_waitcnt lgkmcnt(3)\n\t" \
  "ds_read_b128 v[60:63], v35 offset:384\n\t" \
  "v_cvt_pk_bf16_f32 v22, v64, v65\n\t" \
  "v_cvt_pk_bf16_f32 v23, v66, v67\n\t" \
  "v_smfmac_f32_16x16x64_bf16 v[44:47], a[8:11], v[126:133], v143\n\t" \
  "s_waitcnt lgkmcnt(3)\n\t" \
  "ds_read_b128 v[64:67], v35 offset:448\n\t" \
  "v_cvt_pk_bf16_f32 v26, v68, v69\n\t" \
  "v_cvt_pk_bf16_f32 v27, v70, v71\n\t" \
  "v_smfmac_f32_16x16x64_bf16 v[48:51], a[12:15], v[126:133], v143 abid:1\n\t" \
  "s_waitcnt lgkmcnt(3)\n\t" \
  "ds_read_b128 v[68:71], v35 offset:512\n\t" \
  "v_add_u32_sdwa v0, s29, v108 dst_sel:DWORD dst_unused:UNUSED_PAD src0_sel:DWORD src1_sel:WORD_0\n\t" \
  "v_smfmac_f32_16x16x64_bf16 v[52:55], a[16:19], v[126:133], v144\n\t" \
  "s_add_u32 m0, s33, 0\n\t" \
  "s_waitcnt lgkmcnt(3)\n\t" \
  "ds_write_b128 v24, v[8:11] offset:0\n\t" \
  "v_add_u32_sdwa v2, s29, v108 dst_sel:DWORD dst_unused:UNUSED_PAD src0_sel:DWORD src1_sel:WORD_1\n\t" \
  "v_smfmac_f32_16x16x64_bf16 v[56:59], a[20:23], v[126:133], v144 abid:1\n\t" \
  "s_waitcnt lgkmcnt(3)\n\t" \
  "buffer_load_dwordx4 v186, s[16:19], s34 offen offset:0 nt lds\n\t" \
  "v_add_u32_sdwa v4, s29, v109 dst_sel:DWORD dst_unused:UNUSED_PAD src0_sel:DWORD src1_sel:WORD_0\n\t" \
  "v_smfmac_f32_16x16x64_bf16 v[60:63], a[24:27], v[126:133], v145\n\t" \
  "s_add_u32 m0, s33, 1920\n\t" \
  "s_waitcnt lgkmcnt(2)\n\t" \
  "ds_read_b64 v[0:1], v0 offset:0\n\t" \
  "v_add_u32_sdwa v6, s29, v109 dst_sel:DWORD dst_unused:UNUSED_PAD src0_sel:DWORD src1_sel:WORD_1\n\t" \
  "v_smfmac_f32_16x16x64_bf16 v[64:67], a[28:31], v[126:133], v145 abid:1\n\t" \
  "s_waitcnt lgkmcnt(2)\n\t" \
  "buffer_load_dwordx4 v186, s[16:19], s34 offen offset:128 nt lds\n\t" \
  "v_smfmac_f32_16x16x64_bf16 v[68:71], a[32:35], v[126:133], v178\n\t" \
  "s_add_u32 m0, s33, 3840\n\t" \
  "ds_write_b128 v24, v[12:15] offset:128\n\t" \
  "v_xor_b32 v24, 64, v24\n\t" \
  "v_smfmac_f32_16x16x64_bf16 v[36:39], a[36:39], v[134:141], v146\n\t" \
  "buffer_load_dwordx4 v186, s[16:19], s34 offen offset:256 nt lds\n\t" \
  "v_add_u32_sdwa v8, s29, v110 dst_sel:DWORD dst_unused:UNUSED_PAD src0_sel:DWORD src1_sel:WORD_0\n\t" \
  "v_smfmac_f32_16x16x64_bf16 v[40:43], a[40:43], v[134:141], v146 abid:1\n\t" \
  "s_add_u32 m0, s33, 5760\n\t" \
  "ds_read_b128 v[126:129], v183 offset:4096\n\t" \
  "v_add_u32_sdwa v10, s29, v110 dst_sel:DWORD dst_unused:UNUSED_PAD src0_sel:DWORD src1_sel:WORD_1\n\t" \
  "v_smfmac_f32_16x16x64_bf16 v[44:47], a[44:47], v[134:141], v147\n\t" \
  "buffer_load_dwordx4 v186, s[16:19], s34 offen offset:384 nt lds\n\t" \
  "v_add_u32_sdwa v12, s29, v111 dst_sel:DWORD dst_unused:UNUSED_PAD src0_sel:DWORD src1_sel:WORD_0\n\t" \
  "v_smfmac_f32_16x16x64_bf16 v[48:51], a[48:51], v[134:141], v147 abid:1\n\t" \
  "s_add_u32 m0, s33, 7680\n\t" \
  "ds_write_b128 v24, v[16:19] offset:0\n\t" \
  "v_add_u32_sdwa v14, s29, v111 dst_sel:DWORD dst_unused:UNUSED_PAD src0_sel:DWORD src1_sel:WORD_1\n\t" \
  "v_smfmac_f32_16x16x64_bf16 v[52:55], a[52:55], v[134:141], v148\n\t" \
  "buffer_load_dwordx4 v186, s[16:19], s34 offen offset:512 nt lds\n\t" \
  "v_smfmac_f32_16x16x64_bf16 v[56:59], a[56:59], v[134:141], v148 abid:1\n\t" \
  "s_add_u32 s31, s31, 18432\n\t" \
  "ds_read_b128 v[130:133], v184 offset:4096\n\t" \
  "v_smfmac_f32_16x16x64_bf16 v[60:63], a[60:63], v[134:141], v149\n\t" \
  "s_sub_u32 s26, s31, 129024\n\t" \
  "ds_read_b64 v[2:3], v2 offset:0\n\t" \
  "v_smfmac_f32_16x16x64_bf16 v[64:67], a[64:67], v[134:141], v149 abid:1\n\t" \
  "s_min_u32 s31, s31, s26\n\t" \
  "ds_write_b128 v24, v[20:23] offset:128\n\t" \
  "v_add_u32_sdwa v16, s29, v112 dst_sel:DWORD dst_unused:UNUSED_PAD src0_sel:DWORD src1_sel:WORD_0\n\t" \
  "v_smfmac_f32_16x16x64_bf16 v[68:71], a[68:71], v[134:141], v178 abid:1\n\t" \
  "s_waitcnt lgkmcnt(2)\n\t" \
  "ds_read_b128 v[134:137], v183 offset:6144\n\t" \
  "v_add_u32_sdwa v18, s29, v112 dst_sel:DWORD dst_unused:UNUSED_PAD src0_sel:DWORD src1_sel:WORD_1\n\t" \
  "v_smfmac_f32_16x16x64_bf16 v[36:39], a[72:75], v[126:133], v150\n\t" \
  "s_add_u32 s33, s31, s32\n\t" \
  "ds_read_b128 v[138:141], v184 offset:6144\n\t" \
  "v_add_u32_sdwa v20, s29, v113 dst_sel:DWORD dst_unused:UNUSED_PAD src0_sel:DWORD src1_sel:WORD_0\n\t" \
  "v_smfmac_f32_16x16x64_bf16 v[40:43], a[76:79], v[126:133], v150 abid:1\n\t" \
  "s_add_u32 s34, s34, 4718592\n\t" \
  "ds_read_b64 v[4:5], v4 offset:0\n\t" \
  "v_add_u32_sdwa v22, s29, v113 dst_sel:DWORD dst_unused:UNUSED_PAD src0_sel:DWORD src1_sel:WORD_1\n\t" \
  "v_smfmac_f32_16x16x64_bf16 v[44:47], a[80:83], v[126:133], v151\n\t" \
  "s_and_b32 s27, s64, 7\n\t" \
  "ds_write_b64 v25, v[26:27] offset:16384\n\t" \
  "v_smfmac_f32_16x16x64_bf16 v[48:51], a[84:87], v[126:133], v151 abid:1\n\t" \
  "s_lshl2_add_u32 s66, s27, s68\n\t" \
  "ds_read_b64 v[6:7], v6 offset:0\n\t" \
  "ds_read_b64 v[8:9], v8 offset:0\n\t" \
  "v_smfmac_f32_16x16x64_bf16 v[52:55], a[88:91], v[126:133], v152\n\t" \
  "s_and_b32 s27, s64, 8\n\t" \
  "ds_read_b64 v[10:11], v10 offset:0\n\t" \
  "v_smfmac_f32_16x16x64_bf16 v[56:59], a[92:95], v[126:133], v152 abid:1\n\t" \
  "s_mul_i32 s27, s27, 176\n\t" \
  "ds_read_b64 v[12:13], v12 offset:0\n\t" \
  "v_smfmac_f32_16x16x64_bf16 v[60:63], a[96:99], v[126:133], v153\n\t" \
  "s_add_u32 s66, s66, s27\n\t" \
  "ds_read_b64 v[14:15], v14 offset:0\n\t" \
  "v_smfmac_f32_16x16x64_bf16 v[64:67], a[100:103], v[126:133], v153 abid:1\n\t" \
  "s_waitcnt lgkmcnt(4)\n\t" \
  "ds_read_b64 v[16:17], v16 offset:0\n\t" \
  "v_smfmac_f32_16x16x64_bf16 v[36:39], v[72:75], v[0:7], v182 abid:1\n\t" \
  "s_lshr_b64 s[64:65], s[64:65], 4\n\t" \
  "ds_read_b64 v[18:19], v18 offset:0\n\t" \
  "v_add_u32_sdwa v0, s29, v114 dst_sel:DWORD dst_unused:UNUSED_PAD src0_sel:DWORD src1_sel:WORD_0\n\t" \
  "v_smfmac_f32_16x16x64_bf16 v[68:71], a[104:107], v[126:133], v179\n\t" \
  "ds_read_b128 v[126:129], v183 offset:8192\n\t" \
  "v_add_u32_sdwa v2, s29, v114 dst_sel:DWORD dst_unused:UNUSED_PAD src0_sel:DWORD src1_sel:WORD_1\n\t" \
  "v_smfmac_f32_16x16x64_bf16 v[36:39], a[108:111], v[134:141], v154\n\t" \
  "s_waitcnt lgkmcnt(3)\n\t" \
  "ds_read_b128 v[130:133], v184 offset:8192\n\t" \
  "v_add_u32_sdwa v4, s29, v115 dst_sel:DWORD dst_unused:UNUSED_PAD src0_sel:DWORD src1_sel:WORD_0\n\t" \
  "v_smfmac_f32_16x16x64_bf16 v[40:43], v[76:79], v[8:15], v182 abid:1\n\t" \
  ".Ljn02%=:\n\t" \
  "ds_read_b64 v[20:21], v20 offset:0\n\t" \
  "v_add_u32_sdwa v6, s29, v115 dst_sel:DWORD dst_unused:UNUSED_PAD src0_sel:DWORD src1_sel:WORD_1\n\t" \
  "v_smfmac_f32_16x16x64_bf16 v[40:43], a[112:115], v[134:141], v154 abid:1\n\t" \
  "ds_read_b64 v[22:23], v22 offset:0\n\t" \
  "v_add_u32_sdwa v8, s29, v32 dst_sel:DWORD dst_unused:UNUSED_PAD src0_sel:DWORD src1_sel:WORD_0\n\t" \
  "v_smfmac_f32_16x16x64_bf16 v[44:47], a[116:119], v[134:141], v155\n\t" \
  "ds_read_b64 v[0:1], v0 offset:0\n\t" \
  "v_add_u32_sdwa v10, s29, v32 dst_sel:DWORD dst_unused:UNUSED_PAD src0_sel:DWORD src1_sel:WORD_1\n\t" \
  "v_smfmac_f32_16x16x64_bf16 v[48:51], a[120:123], v[134:141], v155 abid:1\n\t" \
  "ds_read_b64 v[2:3], v2 offset:0\n\t" \
  "v_add_u32 v28, s60, v187\n\t" \
  "v_smfmac_f32_16x16x64_bf16 v[52:55], a[124:127], v[134:141], v156\n\t" \
  "ds_read_b64 v[4:5], v4 offset:0\n\t" \
  "v_smfmac_f32_16x16x64_bf16 v[56:59], a[128:131], v[134:141], v156 abid:1\n\t" \
  "s_waitcnt lgkmcnt(3)\n\t" \
  "ds_read_b64 v[6:7], v6 offset:0\n\t" \
  "v_smfmac_f32_16x16x64_bf16 v[44:47], v[80:83], v[16:23], v182 abid:1\n\t" \
  "ds_read_b64 v[8:9], v8 offset:0\n\t" \
  "v_add_u32_sdwa v16, s29, v116 dst_sel:DWORD dst_unused:UNUSED_PAD src0_sel:DWORD src1_sel:WORD_0\n\t" \
  "v_smfmac_f32_16x16x64_bf16 v[60:63], a[132:135], v[134:141], v157\n\t" \
  "ds_read_b64 v[10:11], v10 offset:0\n\t" \
  "v_add_u32 v24, s38, v187\n\t" \
  "v_smfmac_f32_16x16x64_bf16 v[64:67], a[136:139], v[134:141], v157 abid:1\n\t" \
  "ds_read_b128 v[24:27], v24 offset:0\n\t" \
  "v_add_u32_sdwa v18, s29, v116 dst_sel:DWORD dst_unused:UNUSED_PAD src0_sel:DWORD src1_sel:WORD_1\n\t" \
  "v_smfmac_f32_16x16x64_bf16 v[68:71], a[140:143], v[134:141], v179 abid:1\n\t" \
  "s_waitcnt lgkmcnt(3)\n\t" \
  "ds_read_b128 v[134:137], v183 offset:10240\n\t" \
  "v_add_u32_sdwa v20, s29, v117 dst_sel:DWORD dst_unused:UNUSED_PAD src0_sel:DWORD src1_sel:WORD_0\n\t" \
  "v_smfmac_f32_16x16x64_bf16 v[48:51], v[84:87], v[0:7], v182 abid:1\n\t" \
  "ds_read_b128 v[138:141], v184 offset:10240\n\t" \
  "v_add_u32_sdwa v22, s29, v117 dst_sel:DWORD dst_unused:UNUSED_PAD src0_sel:DWORD src1_sel:WORD_1\n\t" \
  "v_smfmac_f32_16x16x64_bf16 v[36:39], a[144:147], v[126:133], v158\n\t" \
  "s_add_u32 s36, s35, s39\n\t" \
  "ds_read_b128 v[28:31], v28 offset:0\n\t" \
  "v_add_u32_sdwa v0, s29, v118 dst_sel:DWORD dst_unused:UNUSED_PAD src0_sel:DWORD src1_sel:WORD_0\n\t" \
  "v_smfmac_f32_16x16x64_bf16 v[40:43], a[148:151], v[126:133], v158 abid:1\n\t" \
  "s_waitcnt lgkmcnt(3)\n\t" \
  "buffer_store_dwordx4 v[24:27], v34, s[20:23], s36 offen sc1\n\t" \
  "v_add_u32_sdwa v2, s29, v118 dst_sel:DWORD dst_unused:UNUSED_PAD src0_sel:DWORD src1_sel:WORD_1\n\t" \
  "v_smfmac_f32_16x16x64_bf16 v[44:47], a[152:155], v[126:133], v159\n\t" \
  "ds_read_b64 v[16:17], v16 offset:0\n\t" \
  "v_xad_u32 v24, v187, 64, s38\n\t" \
  "v_smfmac_f32_16x16x64_bf16 v[48:51], a[156:159], v[126:133], v159 abid:1\n\t" \
  "ds_read_b128 v[24:27], v24 offset:1024\n\t" \
  "v_add_u32_sdwa v4, s29, v119 dst_sel:DWORD dst_unused:UNUSED_PAD src0_sel:DWORD src1_sel:WORD_0\n\t" \
  "v_smfmac_f32_16x16x64_bf16 v[52:55], a[160:163], v[126:133], v160\n\t" \
  "s_waitcnt lgkmcnt(2)\n\t" \
  "ds_read_b64 v[18:19], v18 offset:0\n\t" \
  "v_add_u32_sdwa v6, s29, v119 dst_sel:DWORD dst_unused:UNUSED_PAD src0_sel:DWORD src1_sel:WORD_1\n\t" \
  "s_set_gpr_idx_on s6, gpr_idx(DST)\n\t" \
  "v_smfmac_f32_16x16x64_bf16 v[36:39], v[28:31], v[8:15], v182 abid:1\n\t" \
  "s_set_gpr_idx_off\n\t" \
  "ds_read_b64 v[20:21], v20 offset:0\n\t" \
  "v_add_u32_sdwa v8, s29, v33 dst_sel:DWORD dst_unused:UNUSED_PAD src0_sel:DWORD src1_sel:WORD_0\n\t" \
  "v_smfmac_f32_16x16x64_bf16 v[56:59], a[164:167], v[126:133], v160 abid:1\n\t" \
  "s_add_u32 s36, s36, 4608\n\t" \
  "ds_read_b64 v[22:23], v22 offset:0\n\t" \
  "v_add_u32_sdwa v10, s29, v33 dst_sel:DWORD dst_unused:UNUSED_PAD src0_sel:DWORD src1_sel:WORD_1\n\t" \
  "v_smfmac_f32_16x16x64_bf16 v[60:63], a[168:171], v[126:133], v161\n\t" \
  "s_waitcnt lgkmcnt(3)\n\t" \
  "buffer_store_dwordx4 v[24:27], v34, s[20:23], s36 offen sc1\n\t" \
  "v_add_u32 v28, s60, v187\n\t" \
  "v_smfmac_f32_16x16x64_bf16 v[64:67], a[172:175], v[126:133], v161 abid:1\n\t" \
  "ds_read_b64 v[0:1], v0 offset:0\n\t" \
  "v_add_u32 v24, s38, v187\n\t" \
  "v_smfmac_f32_16x16x64_bf16 v[68:71], a[176:179], v[126:133], v180\n\t" \
  "ds_read_b128 v[24:27], v24 offset:2048\n\t" \
  "v_smfmac_f32_16x16x64_bf16 v[36:39], a[180:183], v[134:141], v162\n\t" \
  "s_waitcnt lgkmcnt(2)\n\t" \
  "ds_read_b128 v[126:129], v183 offset:12288\n\t" \
  "v_smfmac_f32_16x16x64_bf16 v[52:55], v[88:91], v[16:23], v182 abid:1\n\t" \
  "ds_read_b128 v[130:133], v184 offset:12288\n\t" \
  "v_add_u32_sdwa v16, s29, v120 dst_sel:DWORD dst_unused:UNUSED_PAD src0_sel:DWORD src1_sel:WORD_0\n\t" \
  "v_smfmac_f32_16x16x64_bf16 v[40:43], a[184:187], v[134:141], v162 abid:1\n\t" \
  "s_add_u32 s36, s36, 4608\n\t" \
  "ds_read_b64 v[2:3], v2 offset:0\n\t" \
  "v_add_u32_sdwa v18, s29, v120 dst_sel:DWORD dst_unused:UNUSED_PAD src0_sel:DWORD src1_sel:WORD_1\n\t" \
  "v_smfmac_f32_16x16x64_bf16 v[44:47], a[188:191], v[134:141], v163\n\t" \
  "s_waitcnt lgkmcnt(3)\n\t" \
  "buffer_store_dwordx4 v[24:27], v34, s[20:23], s36 offen sc1\n\t" \
  "v_add_u32_sdwa v20, s29, v121 dst_sel:DWORD dst_unused:UNUSED_PAD src0_sel:DWORD src1_sel:WORD_0\n\t" \
  "v_smfmac_f32_16x16x64_bf16 v[48:51], a[192:195], v[134:141], v163 abid:1\n\t" \
  "ds_read_b64 v[4:5], v4 offset:0\n\t" \
  "v_xad_u32 v24, v187, 64, s38\n\t" \
  "v_smfmac_f32_16x16x64_bf16 v[52:55], a[196:199], v[134:141], v164\n\t" \
  "s_add_u32 s38, s38, 18432\n\t" \
  "ds_read_b128 v[24:27], v24 offset:3072\n\t" \
  "v_add_u32_sdwa v22, s29, v121 dst_sel:DWORD dst_unused:UNUSED_PAD src0_sel:DWORD src1_sel:WORD_1\n\t" \
  "v_smfmac_f32_16x16x64_bf16 v[56:59], a[200:203], v[134:141], v164 abid:1\n\t" \
  "s_sub_u32 s44, s38, 129024\n\t" \
  "ds_read_b64 v[6:7], v6 offset:0\n\t" \
  "v_smfmac_f32_16x16x64_bf16 v[60:63], a[204:207], v[134:141], v165\n\t" \
  "s_min_u32 s38, s38, s44\n\t" \
  "ds_read_b64 v[8:9], v8 offset:0\n\t" \
  "v_smfmac_f32_16x16x64_bf16 v[64:67], a[208:211], v[134:141], v165 abid:1\n\t" \
  "s_add_u32 s36, s36, 4608\n\t" \
  "ds_read_b64 v[10:11], v10 offset:0\n\t" \
  "v_smfmac_f32_16x16x64_bf16 v[68:71], a[212:215], v[134:141], v180 abid:1\n\t" \
  "s_waitcnt lgkmcnt(3)\n\t" \
  "buffer_store_dwordx4 v[24:27], v34, s[20:23], s36 offen sc1\n\t" \
  "v_smfmac_f32_16x16x64_bf16 v[36:39], a[216:219], v[126:133], v166\n\t" \
  "s_waitcnt lgkmcnt(2)\n\t" \
  "ds_read_b128 v[134:137], v183 offset:14336\n\t" \
  "v_smfmac_f32_16x16x64_bf16 v[56:59], v[92:95], v[0:7], v182 abid:1\n\t" \
  "s_add_u32 s35, s35, 4718592\n\t" \
  "ds_read_b128 v[138:141], v184 offset:14336\n\t" \
  "v_smfmac_f32_16x16x64_bf16 v[40:43], a[220:223], v[126:133], v166 abid:1\n\t" \
  "ds_read_b128 v[28:31], v28 offset:1280\n\t" \
  "v_smfmac_f32_16x16x64_bf16 v[44:47], a[224:227], v[126:133], v167\n\t" \
  "ds_read_b64 v[16:17], v16 offset:0\n\t" \
  "v_smfmac_f32_16x16x64_bf16 v[48:51], a[228:231], v[126:133], v167 abid:1\n\t" \
  "ds_read_b64 v[18:19], v18 offset:0\n\t" \
  "v_smfmac_f32_16x16x64_bf16 v[52:55], a[232:235], v[126:133], v168\n\t" \
  "ds_read_b64 v[20:21], v20 offset:0\n\t" \
  "v_smfmac_f32_16x16x64_bf16 v[56:59], a[236:239], v[126:133], v168 abid:1\n\t" \
  "s_waitcnt lgkmcnt(3)\n\t" \
  "ds_read_b64 v[22:23], v22 offset:0\n\t" \
  "s_set_gpr_idx_on s7, gpr_idx(DST)\n\t" \
  "v_smfmac_f32_16x16x64_bf16 v[36:39], v[28:31], v[8:15], v182 abid:1\n\t" \
  "s_set_gpr_idx_off\n\t" \
  "v_add_u32_sdwa v8, s29, v122 dst_sel:DWORD dst_unused:UNUSED_PAD src0_sel:DWORD src1_sel:WORD_0\n\t" \
  "v_smfmac_f32_16x16x64_bf16 v[60:63], a[240:243], v[126:133], v169\n\t" \
  "ds_read_b64 v[8:9], v8 offset:0\n\t" \
  "v_add_u32_sdwa v10, s29, v122 dst_sel:DWORD dst_unused:UNUSED_PAD src0_sel:DWORD src1_sel:WORD_1\n\t" \
  "v_smfmac_f32_16x16x64_bf16 v[64:67], a[244:247], v[126:133], v169 abid:1\n\t" \
  "ds_read_b64 v[10:11], v10 offset:0\n\t" \
  "v_add_u32_sdwa v12, s29, v123 dst_sel:DWORD dst_unused:UNUSED_PAD src0_sel:DWORD src1_sel:WORD_0\n\t" \
  "v_smfmac_f32_16x16x64_bf16 v[68:71], a[248:251], v[126:133], v181\n\t" \
  "s_waitcnt lgkmcnt(2)\n\t" \
  "ds_read_b128 v[0:3], v183 offset:16384\n\t" \
  "ds_read_b64 v[12:13], v12 offset:0\n\t" \
  "v_add_u32_sdwa v14, s29, v123 dst_sel:DWORD dst_unused:UNUSED_PAD src0_sel:DWORD src1_sel:WORD_1\n\t" \
  "v_smfmac_f32_16x16x64_bf16 v[60:63], v[96:99], v[16:23], v182 abid:1\n\t" \
  "ds_read_b128 v[4:7], v184 offset:16384\n\t" \
  "v_add_u32_sdwa v16, s29, v124 dst_sel:DWORD dst_unused:UNUSED_PAD src0_sel:DWORD src1_sel:WORD_0\n\t" \
  "v_smfmac_f32_16x16x64_bf16 v[36:39], a[252:255], v[134:141], v170\n\t" \
  "ds_read_b64 v[14:15], v14 offset:0\n\t" \
  "v_add_u32_sdwa v18, s29, v124 dst_sel:DWORD dst_unused:UNUSED_PAD src0_sel:DWORD src1_sel:WORD_1\n\t" \
  "v_smfmac_f32_16x16x64_bf16 v[40:43], v[188:191], v[134:141], v170 abid:1\n\t" \
  "ds_read_b64 v[16:17], v16 offset:0\n\t" \
  "v_add_u32_sdwa v20, s29, v125 dst_sel:DWORD dst_unused:UNUSED_PAD src0_sel:DWORD src1_sel:WORD_0\n\t" \
  "v_smfmac_f32_16x16x64_bf16 v[44:47], v[192:195], v[134:141], v171\n\t" \
  "ds_read_b64 v[18:19], v18 offset:0\n\t" \
  "v_add_u32_sdwa v22, s29, v125 dst_sel:DWORD dst_unused:UNUSED_PAD src0_sel:DWORD src1_sel:WORD_1\n\t" \
  "v_smfmac_f32_16x16x64_bf16 v[48:51], v[196:199], v[134:141], v171 abid:1\n\t" \
  "s_add_u32 s29, s29, s30\n\t" \
  "ds_read_b64 v[20:21], v20 offset:0\n\t" \
  "v_add_u32 v183, s30, v183\n\t" \
  "v_smfmac_f32_16x16x64_bf16 v[52:55], v[200:203], v[134:141], v172\n\t" \
  "s_waitcnt lgkmcnt(3)\n\t" \
  "ds_read_b64 v[22:23], v22 offset:0\n\t" \
  "v_add_u32 v184, s30, v184\n\t" \
  "v_smfmac_f32_16x16x64_bf16 v[64:67], v[100:103], v[8:15], v182 abid:1\n\t" \
  "v_smfmac_f32_16x16x64_bf16 v[56:59], v[204:207], v[134:141], v172 abid:1\n\t" \
  "v_smfmac_f32_16x16x64_bf16 v[60:63], v[208:211], v[134:141], v173\n\t" \
  "v_smfmac_f32_16x16x64_bf16 v[64:67], v[212:215], v[134:141], v173 abid:1\n\t" \
  "s_waitcnt lgkmcnt(0)\n\t" \
  "v_smfmac_f32_16x16x64_bf16 v[68:71], v[104:107], v[16:23], v182 abid:1\n\t" \
  "v_smfmac_f32_16x16x64_bf16 v[68:71], v[216:219], v[134:141], v181 abid:1\n\t" \
  "s_setpc_b64 s[66:67]\n\t" \
  ".fill (.Lvar%=+8192-.)/4, 4, 0xbf800000\n\t" \
  "s_branch .Lfin%=\n\t" \
  "s_branch .Lhe%=\n\t" \
  "s_waitcnt vmcnt(6)\n\t" \
  "s_waitcnt vmcnt(10)\n\t" \
  "s_waitcnt vmcnt(12)\n\t" \
  "s_waitcnt vmcnt(17)\n\t" \
  "s_waitcnt vmcnt(21) lgkmcnt(0)\n\t" \
  "s_barrier\n\t" \
  "v_smfmac_f32_16x16x64_bf16 v[36:39], v[220:223], v[0:7], v174\n\t" \
  "ds_read_b128 v[126:129], v183 offset:0\n\t" \
  "v_add_u32 v24, s37, v185\n\t" \
  "v_lshrrev_b32 v28, 4, v187\n\t" \
  "v_smfmac_f32_16x16x64_bf16 v[40:43], v[224:227], v[0:7], v174 abid:1\n\t" \
  "ds_read_b128 v[130:133], v184 offset:0\n\t" \
  "v_xor_b32 v25, s41, v183\n\t" \
  "v_and_or_b32 v28, v28, 48, s45\n\t" \
  "v_smfmac_f32_16x16x64_bf16 v[44:47], v[228:231], v[0:7], v175\n\t" \
  "ds_read_b128 v[134:137], v183 offset:2048\n\t" \
  "v_subrev_u32 v25, s30, v25\n\t" \
  "v_smfmac_f32_16x16x64_bf16 v[48:51], v[232:235], v[0:7], v175 abid:1\n\t" \
  "s_add_u32 s30, s29, 18432\n\t" \
  "ds_read_b128 v[138:141], v184 offset:2048\n\t" \
  "v_cvt_pk_bf16_f32 v8, v36, v37\n\t" \
  "v_cvt_pk_bf16_f32 v9, v38, v39\n\t" \
  "v_smfmac_f32_16x16x64_bf16 v[52:55], v[236:239], v[0:7], v176\n\t" \
  "s_sub_u32 s25, s30, 129024\n\t" \
  "ds_read_b128 v[36:39], v28 offset:0\n\t" \
  "v_cvt_pk_bf16_f32 v10, v40, v41\n\t" \
  "v_cvt_pk_bf16_f32 v11, v42, v43\n\t" \
  "v_smfmac_f32_16x16x64_bf16 v[56:59], v[240:243], v[0:7], v176 abid:1\n\t" \
  "s_min_u32 s30, s30, s25\n\t" \
  "ds_read_b128 v[40:43], v28 offset:64\n\t" \
  "v_cvt_pk_bf16_f32 v16, v44, v45\n\t" \
  "v_cvt_pk_bf16_f32 v17, v46, v47\n\t" \
  "v_smfmac_f32_16x16x64_bf16 v[60:63], v[244:247], v[0:7], v177\n\t" \
  "s_sub_u32 s30, s30, s29\n\t" \
  "ds_read_b128 v[44:47], v28 offset:128\n\t" \
  "v_cvt_pk_bf16_f32 v18, v48, v49\n\t" \
  "v_cvt_pk_bf16_f32 v19, v50, v51\n\t" \
  "v_smfmac_f32_16x16x64_bf16 v[64:67], v[248:251], v[0:7], v177 abid:1\n\t" \
  "s_mov_b32 s37, s29\n\t" \
  "ds_read_b128 v[48:51], v28 offset:192\n\t" \
  "v_cvt_pk_bf16_f32 v12, v52, v53\n\t" \
  "v_cvt_pk_bf16_f32 v13, v54, v55\n\t" \
  "v_smfmac_f32_16x16x64_bf16 v[68:71], v[252:255], v[0:7], v182\n\t" \
  "s_waitcnt lgkmcnt(3)\n\t" \
  "ds_read_b128 v[52:55], v28 offset:256\n\t" \
  "v_cvt_pk_bf16_f32 v14, v56, v57\n\t" \
  "v_cvt_pk_bf16_f32 v15, v58, v59\n\t" \
  "v_smfmac_f32_16x16x64_bf16 v[36:39], a[0:3], v[126:133], v142\n\t" \
  "s_waitcnt lgkmcnt(3)\n\t" \
  "ds_read_b128 v[56:59], v28 offset:320\n\t" \
  "v_cvt_pk_bf16_f32 v20, v60, v61\n\t" \
  "v_cvt_pk_bf16_f32 v21, v62, v63\n\t" \
  "v_smfmac_f32_16x16x64_bf16 v[40:43], a[4:7], v[126:133], v142 abid:1\n\t" \
  "s_waitcnt lgkmcnt(3)\n\t" \
  "ds_read_b128 v[60:63], v28 offset:384\n\t" \
  "v_cvt_pk_bf16_f32 v22, v64, v65\n\t" \
  "v_cvt_pk_bf16_f32 v23, v66, v67\n\t" \
  "v_smfmac_f32_16x16x64_bf16 v[44:47], a[8:11], v[126:133], v143\n\t" \
  "s_waitcnt lgkmcnt(3)\n\t" \
  "ds_read_b128 v[64:67], v28 offset:448\n\t" \
  "v_cvt_pk_bf16_f32 v26, v68, v69\n\t" \
  "v_cvt_pk_bf16_f32 v27, v70, v71\n\t" \
  "v_smfmac_f32_16x16x64_bf16 v[48:51], a[12:15], v[126:133], v143 abid:1\n\t" \
  "s_waitcnt lgkmcnt(3)\n\t" \
  "ds_read_b128 v[68:71], v28 offset:512\n\t" \
  "v_add_u32_sdwa v0, s29, v108 dst_sel:DWORD dst_unused:UNUSED_PAD src0_sel:DWORD src1_sel:WORD_0\n\t" \
  "v_smfmac_f32_16x16x64_bf16 v[52:55], a[16:19], v[126:133], v144\n\t" \
  "s_waitcnt lgkmcnt(3)\n\t" \
  "s_bfe_i32 s47, s48, 0x10000\n\t" \
  "ds_write_b128 v24, v[8:11] offset:0\n\t" \
  "v_add_u32_sdwa v2, s29, v108 dst_sel:DWORD dst_unused:UNUSED_PAD src0_sel:DWORD src1_sel:WORD_1\n\t" \
  "v_add_u32 v28, s38, v187\n\t" \
  "v_smfmac_f32_16x16x64_bf16 v[56:59], a[20:23], v[126:133], v144 abid:1\n\t" \
  "s_waitcnt lgkmcnt(3)\n\t" \
  "s_lshr_b32 s48, s48, 1\n\t" \
  "ds_read_b128 v[28:31], v28 offset:0\n\t" \
  "v_add_u32_sdwa v4, s29, v109 dst_sel:DWORD dst_unused:UNUSED_PAD src0_sel:DWORD src1_sel:WORD_0\n\t" \
  "v_smfmac_f32_16x16x64_bf16 v[60:63], a[24:27], v[126:133], v145\n\t" \
  "s_waitcnt lgkmcnt(2)\n\t" \
  "s_add_u32 s36, s35, s39\n\t" \
  "ds_read_b64 v[0:1], v0 offset:0\n\t" \
  "v_add_u32_sdwa v6, s29, v109 dst_sel:DWORD dst_unused:UNUSED_PAD src0_sel:DWORD src1_sel:WORD_1\n\t" \
  "v_smfmac_f32_16x16x64_bf16 v[64:67], a[28:31], v[126:133], v145 abid:1\n\t" \
  "s_waitcnt lgkmcnt(2)\n\t" \
  "s_andn2_b32 s44, 0x40000000, s47\n\t" \
  "v_smfmac_f32_16x16x64_bf16 v[68:71], a[32:35], v[126:133], v178\n\t" \
  "s_or_b32 s36, s36, s44\n\t" \
  "ds_write_b128 v24, v[12:15] offset:128\n\t" \
  "v_xor_b32 v24, 64, v24\n\t" \
  "v_smfmac_f32_16x16x64_bf16 v[36:39], a[36:39], v[134:141], v146\n\t" \
  "s_waitcnt lgkmcnt(2)\n\t" \
  "buffer_store_dwordx4 v[28:31], v34, s[20:23], s36 offen sc1\n\t" \
  "v_add_u32_sdwa v8, s29, v110 dst_sel:DWORD dst_unused:UNUSED_PAD src0_sel:DWORD src1_sel:WORD_0\n\t" \
  "v_smfmac_f32_16x16x64_bf16 v[40:43], a[40:43], v[134:141], v146 abid:1\n\t" \
  "s_and_b32 s44, s47, 4718592\n\t" \
  "ds_read_b128 v[126:129], v183 offset:4096\n\t" \
  "v_add_u32_sdwa v10, s29, v110 dst_sel:DWORD dst_unused:UNUSED_PAD src0_sel:DWORD src1_sel:WORD_1\n\t" \
  "v_xad_u32 v28, v187, 64, s38\n\t" \
  "v_smfmac_f32_16x16x64_bf16 v[44:47], a[44:47], v[134:141], v147\n\t" \
  "s_add_u32 s35, s35, s44\n\t" \
  "ds_read_b128 v[28:31], v28 offset:1024\n\t" \
  "v_add_u32_sdwa v12, s29, v111 dst_sel:DWORD dst_unused:UNUSED_PAD src0_sel:DWORD src1_sel:WORD_0\n\t" \
  "v_smfmac_f32_16x16x64_bf16 v[48:51], a[48:51], v[134:141], v147 abid:1\n\t" \
  "s_add_u32 s36, s36, 4608\n\t" \
  "ds_write_b128 v24, v[16:19] offset:0\n\t" \
  "v_add_u32_sdwa v14, s29, v111 dst_sel:DWORD dst_unused:UNUSED_PAD src0_sel:DWORD src1_sel:WORD_1\n\t" \
  "v_smfmac_f32_16x16x64_bf16 v[52:55], a[52:55], v[134:141], v148\n\t" \
  "v_smfmac_f32_16x16x64_bf16 v[56:59], a[56:59], v[134:141], v148 abid:1\n\t" \
  "ds_read_b128 v[130:133], v184 offset:4096\n\t" \
  "v_smfmac_f32_16x16x64_bf16 v[60:63], a[60:63], v[134:141], v149\n\t" \
  "ds_read_b64 v[2:3], v2 offset:0\n\t" \
  "s_waitcnt lgkmcnt(3)\n\t" \
  "buffer_store_dwordx4 v[28:31], v34, s[20:23], s36 offen sc1\n\t" \
  "v_smfmac_f32_16x16x64_bf16 v[64:67], a[64:67], v[134:141], v149 abid:1\n\t" \
  "s_and_b32 s44, s47, 18432\n\t" \
  "ds_write_b128 v24, v[20:23] offset:128\n\t" \
  "v_add_u32_sdwa v16, s29, v112 dst_sel:DWORD dst_unused:UNUSED_PAD src0_sel:DWORD src1_sel:WORD_0\n\t" \
  "v_add_u32 v28, s38, v187\n\t" \
  "v_smfmac_f32_16x16x64_bf16 v[68:71], a[68:71], v[134:141], v178 abid:1\n\t" \
  "s_waitcnt lgkmcnt(2)\n\t" \
  "ds_read_b128 v[134:137], v183 offset:6144\n\t" \
  "ds_read_b128 v[28:31], v28 offset:2048\n\t" \
  "v_add_u32_sdwa v18, s29, v112 dst_sel:DWORD dst_unused:UNUSED_PAD src0_sel:DWORD src1_sel:WORD_1\n\t" \
  "v_smfmac_f32_16x16x64_bf16 v[36:39], a[72:75], v[126:133], v150\n\t" \
  "s_add_u32 s36, s36, 4608\n\t" \
  "ds_read_b128 v[138:141], v184 offset:6144\n\t" \
  "v_add_u32_sdwa v20, s29, v113 dst_sel:DWORD dst_unused:UNUSED_PAD src0_sel:DWORD src1_sel:WORD_0\n\t" \
  "v_smfmac_f32_16x16x64_bf16 v[40:43], a[76:79], v[126:133], v150 abid:1\n\t" \
  "ds_read_b64 v[4:5], v4 offset:0\n\t" \
  "v_add_u32_sdwa v22, s29, v113 dst_sel:DWORD dst_unused:UNUSED_PAD src0_sel:DWORD src1_sel:WORD_1\n\t" \
  "v_smfmac_f32_16x16x64_bf16 v[44:47], a[80:83], v[126:133], v151\n\t" \
  "s_and_b32 s27, s64, 7\n\t" \
  "ds_write_b64 v25, v[26:27] offset:16384\n\t" \
  "v_smfmac_f32_16x16x64_bf16 v[48:51], a[84:87], v[126:133], v151 abid:1\n\t" \
  "s_lshl2_add_u32 s66, s27, s68\n\t" \
  "ds_read_b64 v[6:7], v6 offset:0\n\t" \
  "ds_read_b64 v[8:9], v8 offset:0\n\t" \
  "s_waitcnt lgkmcnt(5)\n\t" \
  "buffer_store_dwordx4 v[28:31], v34, s[20:23], s36 offen sc1\n\t" \
  "v_smfmac_f32_16x16x64_bf16 v[52:55], a[88:91], v[126:133], v152\n\t" \
  "s_and_b32 s27, s64, 8\n\t" \
  "ds_read_b64 v[10:11], v10 offset:0\n\t" \
  "ds_read_b64 v[12:13], v12 offset:0\n\t" \
  "v_xad_u32 v28, v187, 64, s38\n\t" \
  "v_smfmac_f32_16x16x64_bf16 v[56:59], a[92:95], v[126:133], v152 abid:1\n\t" \
  "s_mul_i32 s27, s27, 176\n\t" \
  "s_add_u32 s38, s38, s44\n\t" \
  "ds_read_b64 v[14:15], v14 offset:0\n\t" \
  "ds_read_b64 v[16:17], v16 offset:0\n\t" \
  "ds_read_b128 v[28:31], v28 offset:3072\n\t" \
  "v_smfmac_f32_16x16x64_bf16 v[60:63], a[96:99], v[126:133], v153\n\t" \
  "s_add_u32 s66, s66, s27\n\t" \
  "s_add_u32 s36, s36, 4608\n\t" \
  "ds_read_b64 v[18:19], v18 offset:0\n\t" \
  "v_smfmac_f32_16x16x64_bf16 v[64:67], a[100:103], v[126:133], v153 abid:1\n\t" \
  "s_waitcnt lgkmcnt(6)\n\t" \
  "s_sub_u32 s44, s38, 129024\n\t" \
  "ds_read_b64 v[20:21], v20 offset:0\n\t" \
  "v_smfmac_f32_16x16x64_bf16 v[36:39], v[72:75], v[0:7], v182 abid:1\n\t" \
  "s_lshr_b64 s[64:65], s[64:65], 4\n\t" \
  "s_min_u32 s38, s38, s44\n\t" \
  "ds_read_b64 v[22:23], v22 offset:0\n\t" \
  "v_add_u32_sdwa v0, s29, v114 dst_sel:DWORD dst_unused:UNUSED_PAD src0_sel:DWORD src1_sel:WORD_0\n\t" \
  "v_smfmac_f32_16x16x64_bf16 v[68:71], a[104:107], v[126:133], v179\n\t" \
  "s_waitcnt lgkmcnt(4)\n\t" \
  "ds_read_b128 v[126:129], v183 offset:8192\n\t" \
  "s_waitcnt lgkmcnt(4)\n\t" \
  "buffer_store_dwordx4 v[28:31], v34, s[20:23], s36 offen sc1\n\t" \
  "v_add_u32_sdwa v2, s29, v114 dst_sel:DWORD dst_unused:UNUSED_PAD src0_sel:DWORD src1_sel:WORD_1\n\t" \
  "v_smfmac_f32_16x16x64_bf16 v[40:43], v[76:79], v[8:15], v182 abid:1\n\t" \
  "ds_read_b128 v[130:133], v184 offset:8192\n\t" \
  "v_add_u32_sdwa v4, s29, v115 dst_sel:DWORD dst_unused:UNUSED_PAD src0_sel:DWORD src1_sel:WORD_0\n\t" \
  "v_smfmac_f32_16x16x64_bf16 v[36:39], a[108:111], v[134:141], v154\n\t" \
  "s_branch .Ljn03%=\n\t" \
  ".fill (.Lvar%=+9596-.)/4, 4, 0xbf800000\n\t" \
  "s_nop 0\n\t" \
  "s_branch .Lfin%=\n\t" \
  "s_branch .Lhe%=\n\t" \
  "s_waitcnt vmcnt(6)\n\t" \
  "s_waitcnt vmcnt(10)\n\t" \
  "s_waitcnt vmcnt(12)\n\t" \
  "s_waitcnt vmcnt(17)\n\t" \
  "s_waitcnt vmcnt(21) lgkmcnt(0)\n\t" \
  "s_barrier\n\t" \
  "v_smfmac_f32_16x16x64_bf16 v[36:39], v[220:223], v[0:7], v174\n\t" \
  "ds_read_b128 v[126:129], v183 offset:0\n\t" \
  "v_add_u32 v24, s37, v185\n\t" \
  "v_lshrrev_b32 v28, 4, v187\n\t" \
  "v_smfmac_f32_16x16x64_bf16 v[40:43], v[224:227], v[0:7], v174 abid:1\n\t" \
  "ds_read_b128 v[130:133], v184 offset:0\n\t" \
  "v_xor_b32 v25, s41, v183\n\t" \
  "v_and_or_b32 v28, v28, 48, s45\n\t" \
  "v_smfmac_f32_16x16x64_bf16 v[44:47], v[228:231], v[0:7], v175\n\t" \
  "ds_read_b128 v[134:137], v183 offset:2048\n\t" \
  "v_subrev_u32 v25, s30, v25\n\t" \
  "v_smfmac_f32_16x16x64_bf16 v[48:51], v[232:235], v[0:7], v175 abid:1\n\t" \
  "s_add_u32 s30, s29, 18432\n\t" \
  "ds_read_b128 v[138:141], v184 offset:2048\n\t" \
  "v_cvt_pk_bf16_f32 v8, v36, v37\n\t" \
  "v_cvt_pk_bf16_f32 v9, v38, v39\n\t" \
  "v_smfmac_f32_16x16x64_bf16 v[52:55], v[236:239], v[0:7], v176\n\t" \
  "s_sub_u32 s25, s30, 129024\n\t" \
  "ds_read_b128 v[36:39], v28 offset:0\n\t" \
  "v_cvt_pk_bf16_f32 v10, v40, v41\n\t" \
  "v_cvt_pk_bf16_f32 v11, v42, v43\n\t" \
  "v_smfmac_f32_16x16x64_bf16 v[56:59], v[240:243], v[0:7], v176 abid:1\n\t" \
  "s_min_u32 s30, s30, s25\n\t" \
  "ds_read_b128 v[40:43], v28 offset:64\n\t" \
  "v_cvt_pk_bf16_f32 v16, v44, v45\n\t" \
  "v_cvt_pk_bf16_f32 v17, v46, v47\n\t" \
  "v_smfmac_f32_16x16x64_bf16 v[60:63], v[244:247], v[0:7], v177\n\t" \
  "s_sub_u32 s30, s30, s29\n\t" \
  "ds_read_b128 v[44:47], v28 offset:128\n\t" \
  "v_cvt_pk_bf16_f32 v18, v48, v49\n\t" \
  "v_cvt_pk_bf16_f32 v19, v50, v51\n\t" \
  "v_smfmac_f32_16x16x64_bf16 v[64:67], v[248:251], v[0:7], v177 abid:1\n\t" \
  "s_mov_b32 s37, s29\n\t" \
  "ds_read_b128 v[48:51], v28 offset:192\n\t" \
  "v_cvt_pk_bf16_f32 v12, v52, v53\n\t" \
  "v_cvt_pk_bf16_f32 v13, v54, v55\n\t" \
  "v_smfmac_f32_16x16x64_bf16 v[68:71], v[252:255], v[0:7], v182\n\t" \
  "s_waitcnt lgkmcnt(3)\n\t" \
  "ds_read_b128 v[52:55], v28 offset:256\n\t" \
  "v_cvt_pk_bf16_f32 v14, v56, v57\n\t" \
  "v_cvt_pk_bf16_f32 v15, v58, v59\n\t" \
  "v_smfmac_f32_16x16x64_bf16 v[36:39], a[0:3], v[126:133], v142\n\t" \
  "s_waitcnt lgkmcnt(3)\n\t" \
  "ds_read_b128 v[56:59], v28 offset:320\n\t" \
  "v_cvt_pk_bf16_f32 v20, v60, v61\n\t" \
  "v_cvt_pk_bf16_f32 v21, v62, v63\n\t" \
  "v_smfmac_f32_16x16x64_bf16 v[40:43], a[4:7], v[126:133], v142 abid:1\n\t" \
  "s_waitcnt lgkmcnt(3)\n\t" \
  "ds_read_b128 v[60:63], v28 offset:384\n\t" \
  "v_cvt_pk_bf16_f32 v22, v64, v65\n\t" \
  "v_cvt_pk_bf16_f32 v23, v66, v67\n\t" \
  "v_smfmac_f32_16x16x64_bf16 v[44:47], a[8:11], v[126:133], v143\n\t" \
  "s_waitcnt lgkmcnt(3)\n\t" \
  "ds_read_b128 v[64:67], v28 offset:448\n\t" \
  "v_cvt_pk_bf16_f32 v26, v68, v69\n\t" \
  "v_cvt_pk_bf16_f32 v27, v70, v71\n\t" \
  "v_smfmac_f32_16x16x64_bf16 v[48:51], a[12:15], v[126:133], v143 abid:1\n\t" \
  "s_waitcnt lgkmcnt(3)\n\t" \
  "ds_read_b128 v[68:71], v28 offset:512\n\t" \
  "v_add_u32_sdwa v0, s29, v108 dst_sel:DWORD dst_unused:UNUSED_PAD src0_sel:DWORD src1_sel:WORD_0\n\t" \
  "v_smfmac_f32_16x16x64_bf16 v[52:55], a[16:19], v[126:133], v144\n\t" \
  "s_add_u32 m0, s33, 0\n\t" \
  "s_waitcnt lgkmcnt(3)\n\t" \
  "ds_write_b128 v24, v[8:11] offset:0\n\t" \
  "v_add_u32_sdwa v2, s29, v108 dst_sel:DWORD dst_unused:UNUSED_PAD src0_sel:DWORD src1_sel:WORD_1\n\t" \
  "v_smfmac_f32_16x16x64_bf16 v[56:59], a[20:23], v[126:133], v144 abid:1\n\t" \
  "s_waitcnt lgkmcnt(3)\n\t" \
  "buffer_load_dwordx4 v186, s[16:19], s34 offen offset:0 nt lds\n\t" \
  "v_add_u32_sdwa v4, s29, v109 dst_sel:DWORD dst_unused:UNUSED_PAD src0_sel:DWORD src1_sel:WORD_0\n\t" \
  "v_smfmac_f32_16x16x64_bf16 v[60:63], a[24:27], v[126:133], v145\n\t" \
  "s_add_u32 m0, s33, 1920\n\t" \
  "s_waitcnt lgkmcnt(2)\n\t" \
  "ds_read_b64 v[0:1], v0 offset:0\n\t" \
  "v_add_u32_sdwa v6, s29, v109 dst_sel:DWORD dst_unused:UNUSED_PAD src0_sel:DWORD src1_sel:WORD_1\n\t" \
  "v_smfmac_f32_16x16x64_bf16 v[64:67], a[28:31], v[126:133], v145 abid:1\n\t" \
  "s_waitcnt lgkmcnt(2)\n\t" \
  "buffer_load_dwordx4 v186, s[16:19], s34 offen offset:128 nt lds\n\t" \
  "v_smfmac_f32_16x16x64_bf16 v[68:71], a[32:35], v[126:133], v178\n\t" \
  "s_add_u32 m0, s33, 3840\n\t" \
  "ds_write_b128 v24, v[12:15] offset:128\n\t" \
  "v_xor_b32 v24, 64, v24\n\t" \
  "v_smfmac_f32_16x16x64_bf16 v[36:39], a[36:39], v[134:141], v146\n\t" \
  "buffer_load_dwordx4 v186, s[16:19], s34 offen offset:256 nt lds\n\t" \
  "v_add_u32_sdwa v8, s29, v110 dst_sel:DWORD dst_unused:UNUSED_PAD src0_sel:DWORD src1_sel:WORD_0\n\t" \
  "v_smfmac_f32_16x16x64_bf16 v[40:43], a[40:43], v[134:141], v146 abid:1\n\t" \
  "s_add_u32 m0, s33, 5760\n\t" \
  "ds_read_b128 v[126:129], v183 offset:4096\n\t" \
  "v_add_u32_sdwa v10, s29, v110 dst_sel:DWORD dst_unused:UNUSED_PAD src0_sel:DWORD src1_sel:WORD_1\n\t" \
  "v_smfmac_f32_16x16x64_bf16 v[44:47], a[44:47], v[134:141], v147\n\t" \
  "buffer_load_dwordx4 v186, s[16:19], s34 offen offset:384 nt lds\n\t" \
  "v_add_u32_sdwa v12, s29, v111 dst_sel:DWORD dst_unused:UNUSED_PAD src0_sel:DWORD src1_sel:WORD_0\n\t" \
  "v_smfmac_f32_16x16x64_bf16 v[48:51], a[48:51], v[134:141], v147 abid:1\n\t" \
  "s_add_u32 m0, s33, 7680\n\t" \
  "ds_write_b128 v24, v[16:19] offset:0\n\t" \
  "v_add_u32_sdwa v14, s29, v111 dst_sel:DWORD dst_unused:UNUSED_PAD src0_sel:DWORD src1_sel:WORD_1\n\t" \
  "v_smfmac_f32_16x16x64_bf16 v[52:55], a[52:55], v[134:141], v148\n\t" \
  "buffer_load_dwordx4 v186, s[16:19], s34 offen offset:512 nt lds\n\t" \
  "v_smfmac_f32_16x16x64_bf16 v[56:59], a[56:59], v[134:141], v148 abid:1\n\t" \
  "s_add_u32 s31, s31, 18432\n\t" \
  "ds_read_b128 v[130:133], v184 offset:4096\n\t" \
  "v_smfmac_f32_16x16x64_bf16 v[60:63], a[60:63], v[134:141], v149\n\t" \
  "s_sub_u32 s26, s31, 129024\n\t" \
  "ds_read_b64 v[2:3], v2 offset:0\n\t" \
  "v_smfmac_f32_16x16x64_bf16 v[64:67], a[64:67], v[134:141], v149 abid:1\n\t" \
  "s_min_u32 s31, s31, s26\n\t" \
  "ds_write_b128 v24, v[20:23] offset:128\n\t" \
  "v_add_u32_sdwa v16, s29, v112 dst_sel:DWORD dst_unused:UNUSED_PAD src0_sel:DWORD src1_sel:WORD_0\n\t" \
  "v_smfmac_f32_16x16x64_bf16 v[68:71], a[68:71], v[134:141], v178 abid:1\n\t" \
  "s_waitcnt lgkmcnt(2)\n\t" \
  "ds_read_b128 v[134:137], v183 offset:6144\n\t" \
  "v_add_u32_sdwa v18, s29, v112 dst_sel:DWORD dst_unused:UNUSED_PAD src0_sel:DWORD src1_sel:WORD_1\n\t" \
  "v_smfmac_f32_16x16x64_bf16 v[36:39], a[72:75], v[126:133], v150\n\t" \
  "s_add_u32 s33, s31, s32\n\t" \
  "ds_read_b128 v[138:141], v184 offset:6144\n\t" \
  "v_add_u32_sdwa v20, s29, v113 dst_sel:DWORD dst_unused:UNUSED_PAD src0_sel:DWORD src1_sel:WORD_0\n\t" \
  "v_smfmac_f32_16x16x64_bf16 v[40:43], a[76:79], v[126:133], v150 abid:1\n\t" \
  "s_add_u32 s34, s34, 4718592\n\t" \
  "ds_read_b64 v[4:5], v4 offset:0\n\t" \
  "v_add_u32_sdwa v22, s29, v113 dst_sel:DWORD dst_unused:UNUSED_PAD src0_sel:DWORD src1_sel:WORD_1\n\t" \
  "v_smfmac_f32_16x16x64_bf16 v[44:47], a[80:83], v[126:133], v151\n\t" \
  "s_and_b32 s27, s64, 7\n\t" \
  "ds_write_b64 v25, v[26:27] offset:16384\n\t" \
  "v_smfmac_f32_16x16x64_bf16 v[48:51], a[84:87], v[126:133], v151 abid:1\n\t" \
  "s_lshl2_add_u32 s66, s27, s68\n\t" \
  "ds_read_b64 v[6:7], v6 offset:0\n\t" \
  "ds_read_b64 v[8:9], v8 offset:0\n\t" \
  "v_smfmac_f32_16x16x64_bf16 v[52:55], a[88:91], v[126:133], v152\n\t" \
  "s_and_b32 s27, s64, 8\n\t" \
  "ds_read_b64 v[10:11], v10 offset:0\n\t" \
  "ds_read_b64 v[12:13], v12 offset:0\n\t" \
  "v_smfmac_f32_16x16x64_bf16 v[56:59], a[92:95], v[126:133], v152 abid:1\n\t" \
  "s_mul_i32 s27, s27, 176\n\t" \
  "ds_read_b64 v[14:15], v14 offset:0\n\t" \
  "ds_read_b64 v[16:17], v16 offset:0\n\t" \
  "v_smfmac_f32_16x16x64_bf16 v[60:63], a[96:99], v[126:133], v153\n\t" \
  "s_add_u32 s66, s66, s27\n\t" \
  "ds_read_b64 v[18:19], v18 offset:0\n\t" \
  "v_smfmac_f32_16x16x64_bf16 v[64:67], a[100:103], v[126:133], v153 abid:1\n\t" \
  "s_waitcnt lgkmcnt(6)\n\t" \
  "ds_read_b64 v[20:21], v20 offset:0\n\t" \
  "v_smfmac_f32_16x16x64_bf16 v[36:39], v[72:75], v[0:7], v182 abid:1\n\t" \
  "s_lshr_b64 s[64:65], s[64:65], 4\n\t" \
  "ds_read_b64 v[22:23], v22 offset:0\n\t" \
  "v_add_u32_sdwa v0, s29, v114 dst_sel:DWORD dst_unused:UNUSED_PAD src0_sel:DWORD src1_sel:WORD_0\n\t" \
  "v_smfmac_f32_16x16x64_bf16 v[68:71], a[104:107], v[126:133], v179\n\t" \
  "s_waitcnt lgkmcnt(4)\n\t" \
  "ds_read_b128 v[126:129], v183 offset:8192\n\t" \
  "v_add_u32_sdwa v2, s29, v114 dst_sel:DWORD dst_unused:UNUSED_PAD src0_sel:DWORD src1_sel:WORD_1\n\t" \
  "v_smfmac_f32_16x16x64_bf16 v[40:43], v[76:79], v[8:15], v182 abid:1\n\t" \
  "ds_read_b128 v[130:133], v184 offset:8192\n\t" \
  "v_add_u32_sdwa v4, s29, v115 dst_sel:DWORD dst_unused:UNUSED_PAD src0_sel:DWORD src1_sel:WORD_0\n\t" \
  "v_smfmac_f32_16x16x64_bf16 v[36:39], a[108:111], v[134:141], v154\n\t" \
  ".Ljn03%=:\n\t" \
  "ds_read_b64 v[0:1], v0 offset:0\n\t" \
  "v_add_u32_sdwa v6, s29, v115 dst_sel:DWORD dst_unused:UNUSED_PAD src0_sel:DWORD src1_sel:WORD_1\n\t" \
  "v_smfmac_f32_16x16x64_bf16 v[40:43], a[112:115], v[134:141], v154 abid:1\n\t" \
  "s_waitcnt lgkmcnt(3)\n\t" \
  "ds_read_b64 v[2:3], v2 offset:0\n\t" \
  "v_add_u32_sdwa v8, s29, v32 dst_sel:DWORD dst_unused:UNUSED_PAD src0_sel:DWORD src1_sel:WORD_0\n\t" \
  "v_smfmac_f32_16x16x64_bf16 v[44:47], v[80:83], v[16:23], v182 abid:1\n\t" \
  "ds_read_b64 v[4:5], v4 offset:0\n\t" \
  "v_add_u32_sdwa v10, s29, v32 dst_sel:DWORD dst_unused:UNUSED_PAD src0_sel:DWORD src1_sel:WORD_1\n\t" \
  "v_smfmac_f32_16x16x64_bf16 v[44:47], a[116:119], v[134:141], v155\n\t" \
  "ds_read_b64 v[6:7], v6 offset:0\n\t" \
  "v_add_u32 v28, s60, v187\n\t" \
  "v_smfmac_f32_16x16x64_bf16 v[48:51], a[120:123], v[134:141], v155 abid:1\n\t" \
  "ds_read_b64 v[8:9], v8 offset:0\n\t" \
  "v_add_u32_sdwa v16, s29, v116 dst_sel:DWORD dst_unused:UNUSED_PAD src0_sel:DWORD src1_sel:WORD_0\n\t" \
  "v_smfmac_f32_16x16x64_bf16 v[52:55], a[124:127], v[134:141], v156\n\t" \
  "ds_read_b64 v[10:11], v10 offset:0\n\t" \
  "v_add_u32_sdwa v18, s29, v116 dst_sel:DWORD dst_unused:UNUSED_PAD src0_sel:DWORD src1_sel:WORD_1\n\t" \
  "v_smfmac_f32_16x16x64_bf16 v[56:59], a[128:131], v[134:141], v156 abid:1\n\t" \
  "ds_read_b128 v[28:31], v28 offset:0\n\t" \
  "v_add_u32_sdwa v20, s29, v117 dst_sel:DWORD dst_unused:UNUSED_PAD src0_sel:DWORD src1_sel:WORD_0\n\t" \
  "v_smfmac_f32_16x16x64_bf16 v[60:63], a[132:135], v[134:141], v157\n\t" \
  "s_waitcnt lgkmcnt(3)\n\t" \
  "ds_read_b64 v[16:17], v16 offset:0\n\t" \
  "v_add_u32 v24, s38, v187\n\t" \
  "v_smfmac_f32_16x16x64_bf16 v[48:51], v[84:87], v[0:7], v182 abid:1\n\t" \
  "ds_read_b128 v[24:27], v24 offset:0\n\t" \
  "v_add_u32_sdwa v22, s29, v117 dst_sel:DWORD dst_unused:UNUSED_PAD src0_sel:DWORD src1_sel:WORD_1\n\t" \
  "v_smfmac_f32_16x16x64_bf16 v[64:67], a[136:139], v[134:141], v157 abid:1\n\t" \
  "ds_read_b64 v[18:19], v18 offset:0\n\t" \
  "v_add_u32_sdwa v0, s29, v118 dst_sel:DWORD dst_unused:UNUSED_PAD src0_sel:DWORD src1_sel:WORD_0\n\t" \
  "v_smfmac_f32_16x16x64_bf16 v[68:71], a[140:143], v[134:141], v179 abid:1\n\t" \
  "s_waitcnt lgkmcnt(3)\n\t" \
  "ds_read_b128 v[134:137], v183 offset:10240\n\t" \
  "v_add_u32_sdwa v2, s29, v118 dst_sel:DWORD dst_unused:UNUSED_PAD src0_sel:DWORD src1_sel:WORD_1\n\t" \
  "s_set_gpr_idx_on s6, gpr_idx(DST)\n\t" \
  "v_smfmac_f32_16x16x64_bf16 v[36:39], v[28:31], v[8:15], v182 abid:1\n\t" \
  "s_set_gpr_idx_off\n\t" \
  "s_add_u32 s36, s35, s39\n\t" \
  "ds_read_b128 v[138:141], v184 offset:10240\n\t" \
  "v_add_u32_sdwa v4, s29, v119 dst_sel:DWORD dst_unused:UNUSED_PAD src0_sel:DWORD src1_sel:WORD_0\n\t" \
  "v_smfmac_f32_16x16x64_bf16 v[36:39], a[144:147], v[126:133], v158\n\t" \
  "s_waitcnt lgkmcnt(3)\n\t" \
  "buffer_store_dwordx4 v[24:27], v34, s[20:23], s36 offen sc1\n\t" \
  "v_add_u32_sdwa v6, s29, v119 dst_sel:DWORD dst_unused:UNUSED_PAD src0_sel:DWORD src1_sel:WORD_1\n\t" \
  "v_smfmac_f32_16x16x64_bf16 v[40:43], a[148:151], v[126:133], v158 abid:1\n\t" \
  "ds_read_b64 v[20:21], v20 offset:0\n\t" \
  "v_xad_u32 v24, v187, 64, s38\n\t" \
  "v_smfmac_f32_16x16x64_bf16 v[44:47], a[152:155], v[126:133], v159\n\t" \
  "ds_read_b128 v[24:27], v24 offset:1024\n\t" \
  "v_add_u32_sdwa v8, s29, v33 dst_sel:DWORD dst_unused:UNUSED_PAD src0_sel:DWORD src1_sel:WORD_0\n\t" \
  "v_smfmac_f32_16x16x64_bf16 v[48:51], a[156:159], v[126:133], v159 abid:1\n\t" \
  "ds_read_b64 v[22:23], v22 offset:0\n\t" \
  "v_add_u32_sdwa v10, s29, v33 dst_sel:DWORD dst_unused:UNUSED_PAD src0_sel:DWORD src1_sel:WORD_1\n\t" \
  "v_smfmac_f32_16x16x64_bf16 v[52:55], a[160:163], v[126:133], v160\n\t" \
  "ds_read_b64 v[0:1], v0 offset:0\n\t" \
  "v_add_u32 v28, s60, v187\n\t" \
  "v_smfmac_f32_16x16x64_bf16 v[56:59], a[164:167], v[126:133], v160 abid:1\n\t" \
  "s_add_u32 s36, s36, 4608\n\t" \
  "ds_read_b64 v[2:3], v2 offset:0\n\t" \
  "v_smfmac_f32_16x16x64_bf16 v[60:63], a[168:171], v[126:133], v161\n\t" \
  "s_waitcnt lgkmcnt(3)\n\t" \
  "buffer_store_dwordx4 v[24:27], v34, s[20:23], s36 offen sc1\n\t" \
  "v_smfmac_f32_16x16x64_bf16 v[64:67], a[172:175], v[126:133], v161 abid:1\n\t" \
  "s_waitcnt lgkmcnt(2)\n\t" \
  "ds_read_b64 v[4:5], v4 offset:0\n\t" \
  "v_add_u32 v24, s38, v187\n\t" \
  "v_smfmac_f32_16x16x64_bf16 v[52:55], v[88:91], v[16:23], v182 abid:1\n\t" \
  "ds_read_b128 v[24:27], v24 offset:2048\n\t" \
  "v_add_u32_sdwa v16, s29, v120 dst_sel:DWORD dst_unused:UNUSED_PAD src0_sel:DWORD src1_sel:WORD_0\n\t" \
  "v_smfmac_f32_16x16x64_bf16 v[68:71], a[176:179], v[126:133], v180\n\t" \
  "ds_read_b128 v[126:129], v183 offset:12288\n\t" \
  "v_add_u32_sdwa v18, s29, v120 dst_sel:DWORD dst_unused:UNUSED_PAD src0_sel:DWORD src1_sel:WORD_1\n\t" \
  "v_smfmac_f32_16x16x64_bf16 v[36:39], a[180:183], v[134:141], v162\n\t" \
  "ds_read_b128 v[130:133], v184 offset:12288\n\t" \
  "v_add_u32_sdwa v20, s29, v121 dst_sel:DWORD dst_unused:UNUSED_PAD src0_sel:DWORD src1_sel:WORD_0\n\t" \
  "v_smfmac_f32_16x16x64_bf16 v[40:43], a[184:187], v[134:141], v162 abid:1\n\t" \
  "s_add_u32 s36, s36, 4608\n\t" \
  "ds_read_b64 v[6:7], v6 offset:0\n\t" \
  "v_add_u32_sdwa v22, s29, v121 dst_sel:DWORD dst_unused:UNUSED_PAD src0_sel:DWORD src1_sel:WORD_1\n\t" \
  "v_smfmac_f32_16x16x64_bf16 v[44:47], a[188:191], v[134:141], v163\n\t" \
  "s_waitcnt lgkmcnt(3)\n\t" \
  "buffer_store_dwordx4 v[24:27], v34, s[20:23], s36 offen sc1\n\t" \
  "v_smfmac_f32_16x16x64_bf16 v[48:51], a[192:195], v[134:141], v163 abid:1\n\t" \
  "ds_read_b64 v[8:9], v8 offset:0\n\t" \
  "v_xad_u32 v24, v187, 64, s38\n\t" \
  "v_smfmac_f32_16x16x64_bf16 v[52:55], a[196:199], v[134:141], v164\n\t" \
  "s_add_u32 s38, s38, 18432\n\t" \
  "ds_read_b128 v[24:27], v24 offset:3072\n\t" \
  "v_smfmac_f32_16x16x64_bf16 v[56:59], a[200:203], v[134:141], v164 abid:1\n\t" \
  "s_waitcnt lgkmcnt(2)\n\t" \
  "ds_read_b64 v[10:11], v10 offset:0\n\t" \
  "v_smfmac_f32_16x16x64_bf16 v[56:59], v[92:95], v[0:7], v182 abid:1\n\t" \
  "s_sub_u32 s44, s38, 129024\n\t" \
  "ds_read_b128 v[28:31], v28 offset:1280\n\t" \
  "v_smfmac_f32_16x16x64_bf16 v[60:63], a[204:207], v[134:141], v165\n\t" \
  "s_add_u32 s36, s36, 4608\n\t" \
  "ds_read_b64 v[16:17], v16 offset:0\n\t" \
  "v_smfmac_f32_16x16x64_bf16 v[64:67], a[208:211], v[134:141], v165 abid:1\n\t" \
  "s_waitcnt lgkmcnt(3)\n\t" \
  "buffer_store_dwordx4 v[24:27], v34, s[20:23], s36 offen sc1\n\t" \
  "v_smfmac_f32_16x16x64_bf16 v[68:71], a[212:215], v[134:141], v180 abid:1\n\t" \
  "s_min_u32 s38, s38, s44\n\t" \
  "ds_read_b128 v[134:137], v183 offset:14336\n\t" \
  "v_smfmac_f32_16x16x64_bf16 v[36:39], a[216:219], v[126:133], v166\n\t" \
  "s_waitcnt lgkmcnt(2)\n\t" \
  "ds_read_b128 v[138:141], v184 offset:14336\n\t" \
  "s_set_gpr_idx_on s7, gpr_idx(DST)\n\t" \
  "v_smfmac_f32_16x16x64_bf16 v[36:39], v[28:31], v[8:15], v182 abid:1\n\t" \
  "s_set_gpr_idx_off\n\t" \
  "ds_read_b64 v[18:19], v18 offset:0\n\t" \
  "v_add_u32_sdwa v8, s29, v122 dst_sel:DWORD dst_unused:UNUSED_PAD src0_sel:DWORD src1_sel:WORD_0\n\t" \
  "v_smfmac_f32_16x16x64_bf16 v[40:43], a[220:223], v[126:133], v166 abid:1\n\t" \
  "s_add_u32 s35, s35, 4718592\n\t" \
  "ds_read_b64 v[20:21], v20 offset:0\n\t" \
  "v_add_u32_sdwa v10, s29, v122 dst_sel:DWORD dst_unused:UNUSED_PAD src0_sel:DWORD src1_sel:WORD_1\n\t" \
  "v_smfmac_f32_16x16x64_bf16 v[44:47], a[224:227], v[126:133], v167\n\t" \
  "ds_read_b64 v[22:23], v22 offset:0\n\t" \
  "v_add_u32_sdwa v12, s29, v123 dst_sel:DWORD dst_unused:UNUSED_PAD src0_sel:DWORD src1_sel:WORD_0\n\t" \
  "v_smfmac_f32_16x16x64_bf16 v[48:51], a[228:231], v[126:133], v167 abid:1\n\t" \
  "ds_read_b64 v[8:9], v8 offset:0\n\t" \
  "v_add_u32_sdwa v14, s29, v123 dst_sel:DWORD dst_unused:UNUSED_PAD src0_sel:DWORD src1_sel:WORD_1\n\t" \
  "v_smfmac_f32_16x16x64_bf16 v[52:55], a[232:235], v[126:133], v168\n\t" \
  "ds_read_b64 v[10:11], v10 offset:0\n\t" \
  "v_smfmac_f32_16x16x64_bf16 v[56:59], a[236:239], v[126:133], v168 abid:1\n\t" \
  "ds_read_b64 v[12:13], v12 offset:0\n\t" \
  "v_smfmac_f32_16x16x64_bf16 v[60:63], a[240:243], v[126:133], v169\n\t" \
  "s_waitcnt lgkmcnt(3)\n\t" \
  "ds_read_b64 v[14:15], v14 offset:0\n\t" \
  "v_smfmac_f32_16x16x64_bf16 v[60:63], v[96:99], v[16:23], v182 abid:1\n\t" \
  "v_add_u32_sdwa v16, s29, v35 dst_sel:DWORD dst_unused:UNUSED_PAD src0_sel:DWORD src1_sel:WORD_0\n\t" \
  "v_smfmac_f32_16x16x64_bf16 v[64:67], a[244:247], v[126:133], v169 abid:1\n\t" \
  "ds_read_b64 v[16:17], v16 offset:0\n\t" \
  "v_add_u32_sdwa v18, s29, v35 dst_sel:DWORD dst_unused:UNUSED_PAD src0_sel:DWORD src1_sel:WORD_1\n\t" \
  "v_smfmac_f32_16x16x64_bf16 v[68:71], a[248:251], v[126:133], v181\n\t" \
  "ds_read_b128 v[0:3], v183 offset:16384\n\t" \
  "ds_read_b64 v[18:19], v18 offset:0\n\t" \
  "v_add_u32 v28, s60, v187\n\t" \
  "v_smfmac_f32_16x16x64_bf16 v[36:39], a[252:255], v[134:141], v170\n\t" \
  "s_waitcnt lgkmcnt(3)\n\t" \
  "ds_read_b128 v[4:7], v184 offset:16384\n\t" \
  "v_add_u32 v183, s30, v183\n\t" \
  "v_smfmac_f32_16x16x64_bf16 v[64:67], v[100:103], v[8:15], v182 abid:1\n\t" \
  "ds_read_b128 v[28:31], v28 offset:2560\n\t" \
  "v_add_u32_sdwa v8, s29, v124 dst_sel:DWORD dst_unused:UNUSED_PAD src0_sel:DWORD src1_sel:WORD_0\n\t" \
  "v_smfmac_f32_16x16x64_bf16 v[40:43], v[188:191], v[134:141], v170 abid:1\n\t" \
  "ds_read_b64 v[8:9], v8 offset:0\n\t" \
  "v_add_u32_sdwa v10, s29, v124 dst_sel:DWORD dst_unused:UNUSED_PAD src0_sel:DWORD src1_sel:WORD_1\n\t" \
  "v_smfmac_f32_16x16x64_bf16 v[44:47], v[192:195], v[134:141], v171\n\t" \
  "ds_read_b64 v[10:11], v10 offset:0\n\t" \
  "v_add_u32_sdwa v12, s29, v125 dst_sel:DWORD dst_unused:UNUSED_PAD src0_sel:DWORD src1_sel:WORD_0\n\t" \
  "v_smfmac_f32_16x16x64_bf16 v[48:51], v[196:199], v[134:141], v171 abid:1\n\t" \
  "ds_read_b64 v[12:13], v12 offset:0\n\t" \
  "v_add_u32_sdwa v14, s29, v125 dst_sel:DWORD dst_unused:UNUSED_PAD src0_sel:DWORD src1_sel:WORD_1\n\t" \
  "v_smfmac_f32_16x16x64_bf16 v[52:55], v[200:203], v[134:141], v172\n\t" \
  "s_waitcnt lgkmcnt(3)\n\t" \
  "ds_read_b64 v[14:15], v14 offset:0\n\t" \
  "v_add_u32 v184, s30, v184\n\t" \
  "s_set_gpr_idx_on s8, gpr_idx(DST)\n\t" \
  "v_smfmac_f32_16x16x64_bf16 v[36:39], v[28:31], v[16:23], v182 abid:1\n\t" \
  "s_set_gpr_idx_off\n\t" \
  "v_smfmac_f32_16x16x64_bf16 v[56:59], v[204:207], v[134:141], v172 abid:1\n\t" \
  "s_add_u32 s29, s29, s30\n\t" \
  "v_smfmac_f32_16x16x64_bf16 v[60:63], v[208:211], v[134:141], v173\n\t" \
  "v_smfmac_f32_16x16x64_bf16 v[64:67], v[212:215], v[134:141], v173 abid:1\n\t" \
  "s_waitcnt lgkmcnt(0)\n\t" \
  "v_smfmac_f32_16x16x64_bf16 v[68:71], v[104:107], v[8:15], v182 abid:1\n\t" \
  "v_smfmac_f32_16x16x64_bf16 v[68:71], v[216:219], v[134:141], v181 abid:1\n\t" \
  "s_setpc_b64 s[66:67]\n\t" \
  ".fill (.Lvar%=+12288-.)/4, 4, 0xbf800000\n\t" \
  "s_branch .Lfin%=\n\t" \
  "s_branch .Lhe%=\n\t" \
  "s_waitcnt vmcnt(6)\n\t" \
  "s_waitcnt vmcnt(10)\n\t" \
  "s_waitcnt vmcnt(12)\n\t" \
  "s_waitcnt vmcnt(17)\n\t" \
  "s_waitcnt vmcnt(21) lgkmcnt(0)\n\t" \
  "s_cmp_lt_u32 s72, s52\n\t" \
  "s_cbranch_scc1 .Lns11%=\n\t" \
  ".Lng11%=:\n\t" \
  "s_barrier\n\t" \
  "v_smfmac_f32_16x16x64_bf16 v[36:39], v[220:223], v[0:7], v174\n\t" \
  "ds_read_b128 v[126:129], v183 offset:0\n\t" \
  "v_add_u32 v24, s37, v185\n\t" \
  "v_smfmac_f32_16x16x64_bf16 v[40:43], v[224:227], v[0:7], v174 abid:1\n\t" \
  "s_movk_i32 s53, 1952\n\t" \
  "ds_read_b128 v[130:133], v184 offset:0\n\t" \
  "v_xor_b32 v25, s41, v183\n\t" \
  "v_smfmac_f32_16x16x64_bf16 v[44:47], v[228:231], v[0:7], v175\n\t" \
  "s_mov_b32 s51, s53\n\t" \
  "ds_read_b128 v[134:137], v183 offset:2048\n\t" \
  "v_subrev_u32 v25, s30, v25\n\t" \
  "v_smfmac_f32_16x16x64_bf16 v[48:51], v[232:235], v[0:7], v175 abid:1\n\t" \
  "s_add_u32 s30, s29, 18432\n\t" \
  "ds_read_b128 v[138:141], v184 offset:2048\n\t" \
  "v_cvt_pk_bf16_f32 v8, v36, v37\n\t" \
  "v_cvt_pk_bf16_f32 v9, v38, v39\n\t" \
  "v_smfmac_f32_16x16x64_bf16 v[52:55], v[236:239], v[0:7], v176\n\t" \
  "s_sub_u32 s25, s30, 129024\n\t" \
  "ds_read_b128 v[36:39], v35 offset:0\n\t" \
  "v_cvt_pk_bf16_f32 v10, v40, v41\n\t" \
  "v_cvt_pk_bf16_f32 v11, v42, v43\n\t" \
  "v_smfmac_f32_16x16x64_bf16 v[56:59], v[240:243], v[0:7], v176 abid:1\n\t" \
  "s_min_u32 s30, s30, s25\n\t" \
  "ds_read_b128 v[40:43], v35 offset:64\n\t" \
  "v_cvt_pk_bf16_f32 v16, v44, v45\n\t" \
  "v_cvt_pk_bf16_f32 v17, v46, v47\n\t" \
  "v_smfmac_f32_16x16x64_bf16 v[60:63], v[244:247], v[0:7], v177\n\t" \
  "s_sub_u32 s30, s30, s29\n\t" \
  "ds_read_b128 v[44:47], v35 offset:128\n\t" \
  "v_cvt_pk_bf16_f32 v18, v48, v49\n\t" \
  "v_cvt_pk_bf16_f32 v19, v50, v51\n\t" \
  "v_smfmac_f32_16x16x64_bf16 v[64:67], v[248:251], v[0:7], v177 abid:1\n\t" \
  "s_mov_b32 s37, s29\n\t" \
  "ds_read_b128 v[48:51], v35 offset:192\n\t" \
  "v_cvt_pk_bf16_f32 v12, v52, v53\n\t" \
  "v_cvt_pk_bf16_f32 v13, v54, v55\n\t" \
  "v_smfmac_f32_16x16x64_bf16 v[68:71], v[252:255], v[0:7], v182\n\t" \
  "s_waitcnt lgkmcnt(3)\n\t" \
  "ds_read_b128 v[52:55], v35 offset:256\n\t" \
  "v_cvt_pk_bf16_f32 v14, v56, v57\n\t" \
  "v_cvt_pk_bf16_f32 v15, v58, v59\n\t" \
  "v_smfmac_f32_16x16x64_bf16 v[36:39], a[0:3], v[126:133], v142\n\t" \
  "s_waitcnt lgkmcnt(3)\n\t" \
  "ds_read_b128 v[56:59], v35 offset:320\n\t" \
  "v_cvt_pk_bf16_f32 v20, v60, v61\n\t" \
  "v_cvt_pk_bf16_f32 v21, v62, v63\n\t" \
  "v_smfmac_f32_16x16x64_bf16 v[40:43], a[4:7], v[126:133], v142 abid:1\n\t" \
  "s_waitcnt lgkmcnt(3)\n\t" \
  "ds_read_b128 v[60:63], v35 offset:384\n\t" \
  "v_cvt_pk_bf16_f32 v22, v64, v65\n\t" \
  "v_cvt_pk_bf16_f32 v23, v66, v67\n\t" \
  "v_smfmac_f32_16x16x64_bf16 v[44:47], a[8:11], v[126:133], v143\n\t" \
  "s_waitcnt lgkmcnt(3)\n\t" \
  "ds_read_b128 v[64:67], v35 offset:448\n\t" \
  "v_cvt_pk_bf16_f32 v26, v68, v69\n\t" \
  "v_cvt_pk_bf16_f32 v27, v70, v71\n\t" \
  "v_smfmac_f32_16x16x64_bf16 v[48:51], a[12:15], v[126:133], v143 abid:1\n\t" \
  "s_waitcnt lgkmcnt(3)\n\t" \
  "ds_read_b128 v[68:71], v35 offset:512\n\t" \
  "v_add_u32_sdwa v0, s29, v108 dst_sel:DWORD dst_unused:UNUSED_PAD src0_sel:DWORD src1_sel:WORD_0\n\t" \
  "v_smfmac_f32_16x16x64_bf16 v[52:55], a[16:19], v[126:133], v144\n\t" \
  "s_waitcnt lgkmcnt(3)\n\t" \
  "ds_read_b64 v[0:1], v0 offset:0\n\t" \
  "v_add_u32_sdwa v2, s29, v108 dst_sel:DWORD dst_unused:UNUSED_PAD src0_sel:DWORD src1_sel:WORD_1\n\t" \
  "v_smfmac_f32_16x16x64_bf16 v[56:59], a[20:23], v[126:133], v144 abid:1\n\t" \
  "s_waitcnt lgkmcnt(3)\n\t" \
  "s_bfe_i32 s47, s48, 0x10000\n\t" \
  "ds_write_b128 v24, v[8:11] offset:0\n\t" \
  "v_add_u32_sdwa v4, s29, v109 dst_sel:DWORD dst_unused:UNUSED_PAD src0_sel:DWORD src1_sel:WORD_0\n\t" \
  "v_add_u32 v28, s38, v187\n\t" \
  "v_smfmac_f32_16x16x64_bf16 v[60:63], a[24:27], v[126:133], v145\n\t" \
  "s_waitcnt lgkmcnt(3)\n\t" \
  "s_lshr_b32 s48, s48, 1\n\t" \
  "ds_read_b128 v[28:31], v28 offset:4096\n\t" \
  "v_add_u32_sdwa v6, s29, v109 dst_sel:DWORD dst_unused:UNUSED_PAD src0_sel:DWORD src1_sel:WORD_1\n\t" \
  "v_smfmac_f32_16x16x64_bf16 v[64:67], a[28:31], v[126:133], v145 abid:1\n\t" \
  "s_waitcnt lgkmcnt(2)\n\t" \
  "s_add_u32 s36, s35, s39\n\t" \
  "ds_read_b64 v[2:3], v2 offset:0\n\t" \
  "v_smfmac_f32_16x16x64_bf16 v[68:71], a[32:35], v[126:133], v178\n\t" \
  "s_andn2_b32 s44, 0x40000000, s47\n\t" \
  "v_smfmac_f32_16x16x64_bf16 v[36:39], a[36:39], v[134:141], v146\n\t" \
  "s_or_b32 s36, s36, s44\n\t" \
  "ds_write_b128 v24, v[12:15] offset:128\n\t" \
  "v_xor_b32 v24, 64, v24\n\t" \
  "v_smfmac_f32_16x16x64_bf16 v[40:43], a[40:43], v[134:141], v146 abid:1\n\t" \
  "s_waitcnt lgkmcnt(2)\n\t" \
  "buffer_store_dwordx4 v[28:31], v34, s[20:23], s36 offen sc1\n\t" \
  "v_add_u32_sdwa v8, s29, v110 dst_sel:DWORD dst_unused:UNUSED_PAD src0_sel:DWORD src1_sel:WORD_0\n\t" \
  "v_smfmac_f32_16x16x64_bf16 v[44:47], a[44:47], v[134:141], v147\n\t" \
  "s_and_b32 s44, s47, 4718592\n\t" \
  "ds_read_b128 v[126:129], v183 offset:4096\n\t" \
  "v_add_u32_sdwa v10, s29, v110 dst_sel:DWORD dst_unused:UNUSED_PAD src0_sel:DWORD src1_sel:WORD_1\n\t" \
  "v_xad_u32 v28, v187, 64, s38\n\t" \
  "v_smfmac_f32_16x16x64_bf16 v[48:51], a[48:51], v[134:141], v147 abid:1\n\t" \
  "s_add_u32 s35, s35, s44\n\t" \
  "ds_read_b128 v[28:31], v28 offset:5120\n\t" \
  "v_add_u32_sdwa v12, s29, v111 dst_sel:DWORD dst_unused:UNUSED_PAD src0_sel:DWORD src1_sel:WORD_0\n\t" \
  "v_smfmac_f32_16x16x64_bf16 v[52:55], a[52:55], v[134:141], v148\n\t" \
  "s_add_u32 s36, s36, 4608\n\t" \
  "ds_write_b128 v24, v[16:19] offset:0\n\t" \
  "v_add_u32_sdwa v14, s29, v111 dst_sel:DWORD dst_unused:UNUSED_PAD src0_sel:DWORD src1_sel:WORD_1\n\t" \
  "v_smfmac_f32_16x16x64_bf16 v[56:59], a[56:59], v[134:141], v148 abid:1\n\t" \
  "ds_read_b128 v[130:133], v184 offset:4096\n\t" \
  "v_smfmac_f32_16x16x64_bf16 v[60:63], a[60:63], v[134:141], v149\n\t" \
  "ds_read_b64 v[4:5], v4 offset:0\n\t" \
  "v_smfmac_f32_16x16x64_bf16 v[64:67], a[64:67], v[134:141], v149 abid:1\n\t" \
  "ds_read_b64 v[6:7], v6 offset:0\n\t" \
  "s_waitcnt lgkmcnt(4)\n\t" \
  "buffer_store_dwordx4 v[28:31], v34, s[20:23], s36 offen sc1\n\t" \
  "v_smfmac_f32_16x16x64_bf16 v[68:71], a[68:71], v[134:141], v178 abid:1\n\t" \
  "s_waitcnt lgkmcnt(2)\n\t" \
  "s_and_b32 s44, s47, 18432\n\t" \
  "ds_write_b128 v24, v[20:23] offset:128\n\t" \
  "v_add_u32_sdwa v16, s29, v112 dst_sel:DWORD dst_unused:UNUSED_PAD src0_sel:DWORD src1_sel:WORD_0\n\t" \
  "v_add_u32 v28, s38, v187\n\t" \
  "v_smfmac_f32_16x16x64_bf16 v[36:39], a[72:75], v[126:133], v150\n\t" \
  "ds_read_b128 v[134:137], v183 offset:6144\n\t" \
  "ds_read_b128 v[28:31], v28 offset:6144\n\t" \
  "v_add_u32_sdwa v18, s29, v112 dst_sel:DWORD dst_unused:UNUSED_PAD src0_sel:DWORD src1_sel:WORD_1\n\t" \
  "v_smfmac_f32_16x16x64_bf16 v[40:43], a[76:79], v[126:133], v150 abid:1\n\t" \
  "s_and_b32 s27, s64, 7\n\t" \
  "s_add_u32 s36, s36, 4608\n\t" \
  "ds_read_b128 v[138:141], v184 offset:6144\n\t" \
  "v_add_u32_sdwa v20, s29, v113 dst_sel:DWORD dst_unused:UNUSED_PAD src0_sel:DWORD src1_sel:WORD_0\n\t" \
  "v_smfmac_f32_16x16x64_bf16 v[44:47], a[80:83], v[126:133], v151\n\t" \
  "s_waitcnt lgkmcnt(3)\n\t" \
  "ds_read_b64 v[8:9], v8 offset:0\n\t" \
  "v_add_u32_sdwa v22, s29, v113 dst_sel:DWORD dst_unused:UNUSED_PAD src0_sel:DWORD src1_sel:WORD_1\n\t" \
  "v_smfmac_f32_16x16x64_bf16 v[36:39], v[72:75], v[0:7], v182 abid:1\n\t" \
  "s_lshl2_add_u32 s66, s27, s68\n\t" \
  "ds_write_b64 v25, v[26:27] offset:16384\n\t" \
  "v_add_u32_sdwa v0, s29, v114 dst_sel:DWORD dst_unused:UNUSED_PAD src0_sel:DWORD src1_sel:WORD_0\n\t" \
  "v_smfmac_f32_16x16x64_bf16 v[48:51], a[84:87], v[126:133], v151 abid:1\n\t" \
  "s_and_b32 s27, s64, 8\n\t" \
  "ds_read_b64 v[10:11], v10 offset:0\n\t" \
  "s_waitcnt lgkmcnt(4)\n\t" \
  "buffer_store_dwordx4 v[28:31], v34, s[20:23], s36 offen sc1\n\t" \
  "v_add_u32_sdwa v2, s29, v114 dst_sel:DWORD dst_unused:UNUSED_PAD src0_sel:DWORD src1_sel:WORD_1\n\t" \
  "v_smfmac_f32_16x16x64_bf16 v[52:55], a[88:91], v[126:133], v152\n\t" \
  "s_mul_i32 s27, s27, 176\n\t" \
  "ds_read_b64 v[12:13], v12 offset:0\n\t" \
  "v_add_u32_sdwa v4, s29, v115 dst_sel:DWORD dst_unused:UNUSED_PAD src0_sel:DWORD src1_sel:WORD_0\n\t" \
  "v_xad_u32 v28, v187, 64, s38\n\t" \
  "v_smfmac_f32_16x16x64_bf16 v[56:59], a[92:95], v[126:133], v152 abid:1\n\t" \
  "s_add_u32 s66, s66, s27\n\t" \
  "s_add_u32 s38, s38, s44\n\t" \
  "ds_read_b64 v[14:15], v14 offset:0\n\t" \
  "ds_read_b128 v[28:31], v28 offset:7168\n\t" \
  "v_add_u32_sdwa v6, s29, v115 dst_sel:DWORD dst_unused:UNUSED_PAD src0_sel:DWORD src1_sel:WORD_1\n\t" \
  "v_smfmac_f32_16x16x64_bf16 v[60:63], a[96:99], v[126:133], v153\n\t" \
  "s_lshr_b64 s[64:65], s[64:65], 4\n\t" \
  "s_add_u32 s36, s36, 4608\n\t" \
  "ds_read_b64 v[16:17], v16 offset:0\n\t" \
  "v_smfmac_f32_16x16x64_bf16 v[64:67], a[100:103], v[126:133], v153 abid:1\n\t" \
  "s_sub_u32 s28, s72, s46\n\t" \
  "s_sub_u32 s44, s38, 129024\n\t" \
  "ds_read_b64 v[18:19], v18 offset:0\n\t" \
  "v_smfmac_f32_16x16x64_bf16 v[68:71], a[104:107], v[126:133], v179\n\t" \
  "s_waitcnt lgkmcnt(7)\n\t" \
  "s_min_u32 s38, s38, s44\n\t" \
  "ds_read_b128 v[126:129], v183 offset:8192\n\t" \
  "v_smfmac_f32_16x16x64_bf16 v[36:39], a[108:111], v[134:141], v154\n\t" \
  "s_waitcnt lgkmcnt(3)\n\t" \
  "ds_read_b128 v[130:133], v184 offset:8192\n\t" \
  "s_waitcnt lgkmcnt(4)\n\t" \
  "buffer_store_dwordx4 v[28:31], v34, s[20:23], s36 offen sc1\n\t" \
  "v_smfmac_f32_16x16x64_bf16 v[40:43], v[76:79], v[8:15], v182 abid:1\n\t" \
  "s_add_u32 s46, s72, 185\n\t" \
  "ds_read_b64 v[20:21], v20 offset:0\n\t" \
  "v_add_u32_sdwa v8, s29, v32 dst_sel:DWORD dst_unused:UNUSED_PAD src0_sel:DWORD src1_sel:WORD_0\n\t" \
  "v_smfmac_f32_16x16x64_bf16 v[40:43], a[112:115], v[134:141], v154 abid:1\n\t" \
  "s_branch .Ljn11%=\n\t" \
  ".Lns11%=:\n\t" \
  "s_memrealtime s[72:73]\n\t" \
  "s_waitcnt lgkmcnt(0)\n\t" \
  "s_sub_i32 s28, s72, s52\n\t" \
  "s_cmp_lt_i32 s28, 0\n\t" \
  "s_cbranch_scc1 .Lns11%=\n\t" \
  "s_branch .Lng11%=\n\t" \
  ".fill (.Lvar%=+13692-.)/4, 4, 0xbf800000\n\t" \
  "s_nop 0\n\t" \
  "s_branch .Lfin%=\n\t" \
  "s_branch .Lhe%=\n\t" \
  "s_waitcnt vmcnt(6)\n\t" \
  "s_waitcnt vmcnt(10)\n\t" \
  "s_waitcnt vmcnt(12)\n\t" \
  "s_waitcnt vmcnt(17)\n\t" \
  "s_waitcnt vmcnt(21) lgkmcnt(0)\n\t" \
  "s_cmp_lt_u32 s72, s52\n\t" \
  "s_cbranch_scc1 .Lls11%=\n\t" \
  ".Llg11%=:\n\t" \
  "s_barrier\n\t" \
  "v_smfmac_f32_16x16x64_bf16 v[36:39], v[220:223], v[0:7], v174\n\t" \
  "ds_read_b128 v[126:129], v183 offset:0\n\t" \
  "v_add_u32 v24, s37, v185\n\t" \
  "v_smfmac_f32_16x16x64_bf16 v[40:43], v[224:227], v[0:7], v174 abid:1\n\t" \
  "ds_read_b128 v[130:133], v184 offset:0\n\t" \
  "v_xor_b32 v25, s41, v183\n\t" \
  "v_smfmac_f32_16x16x64_bf16 v[44:47], v[228:231], v[0:7], v175\n\t" \
  "ds_read_b128 v[134:137], v183 offset:2048\n\t" \
  "v_subrev_u32 v25, s30, v25\n\t" \
  "v_smfmac_f32_16x16x64_bf16 v[48:51], v[232:235], v[0:7], v175 abid:1\n\t" \
  "s_add_u32 s30, s29, 18432\n\t" \
  "ds_read_b128 v[138:141], v184 offset:2048\n\t" \
  "v_cvt_pk_bf16_f32 v8, v36, v37\n\t" \
  "v_cvt_pk_bf16_f32 v9, v38, v39\n\t" \
  "v_smfmac_f32_16x16x64_bf16 v[52:55], v[236:239], v[0:7], v176\n\t" \
  "s_sub_u32 s25, s30, 129024\n\t" \
  "ds_read_b128 v[36:39], v35 offset:0\n\t" \
  "v_cvt_pk_bf16_f32 v10, v40, v41\n\t" \
  "v_cvt_pk_bf16_f32 v11, v42, v43\n\t" \
  "v_smfmac_f32_16x16x64_bf16 v[56:59], v[240:243], v[0:7], v176 abid:1\n\t" \
  "s_min_u32 s30, s30, s25\n\t" \
  "ds_read_b128 v[40:43], v35 offset:64\n\t" \
  "v_cvt_pk_bf16_f32 v16, v44, v45\n\t" \
  "v_cvt_pk_bf16_f32 v17, v46, v47\n\t" \
  "v_smfmac_f32_16x16x64_bf16 v[60:63], v[244:247], v[0:7], v177\n\t" \
  "s_sub_u32 s30, s30, s29\n\t" \
  "ds_read_b128 v[44:47], v35 offset:128\n\t" \
  "v_cvt_pk_bf16_f32 v18, v48, v49\n\t" \
  "v_cvt_pk_bf16_f32 v19, v50, v51\n\t" \
  "v_smfmac_f32_16x16x64_bf16 v[64:67], v[248:251], v[0:7], v177 abid:1\n\t" \
  "s_mov_b32 s37, s29\n\t" \
  "ds_read_b128 v[48:51], v35 offset:192\n\t" \
  "v_cvt_pk_bf16_f32 v12, v52, v53\n\t" \
  "v_cvt_pk_bf16_f32 v13, v54, v55\n\t" \
  "v_smfmac_f32_16x16x64_bf16 v[68:71], v[252:255], v[0:7], v182\n\t" \
  "s_waitcnt lgkmcnt(3)\n\t" \
  "ds_read_b128 v[52:55], v35 offset:256\n\t" \
  "v_cvt_pk_bf16_f32 v14, v56, v57\n\t" \
  "v_cvt_pk_bf16_f32 v15, v58, v59\n\t" \
  "v_smfmac_f32_16x16x64_bf16 v[36:39], a[0:3], v[126:133], v142\n\t" \
  "s_waitcnt lgkmcnt(3)\n\t" \
  "ds_read_b128 v[56:59], v35 offset:320\n\t" \
  "v_cvt_pk_bf16_f32 v20, v60, v61\n\t" \
  "v_cvt_pk_bf16_f32 v21, v62, v63\n\t" \
  "v_smfmac_f32_16x16x64_bf16 v[40:43], a[4:7], v[126:133], v142 abid:1\n\t" \
  "s_waitcnt lgkmcnt(3)\n\t" \
  "ds_read_b128 v[60:63], v35 offset:384\n\t" \
  "v_cvt_pk_bf16_f32 v22, v64, v65\n\t" \
  "v_cvt_pk_bf16_f32 v23, v66, v67\n\t" \
  "v_smfmac_f32_16x16x64_bf16 v[44:47], a[8:11], v[126:133], v143\n\t" \
  "s_waitcnt lgkmcnt(3)\n\t" \
  "ds_read_b128 v[64:67], v35 offset:448\n\t" \
  "v_cvt_pk_bf16_f32 v26, v68, v69\n\t" \
  "v_cvt_pk_bf16_f32 v27, v70, v71\n\t" \
  "v_smfmac_f32_16x16x64_bf16 v[48:51], a[12:15], v[126:133], v143 abid:1\n\t" \
  "s_waitcnt lgkmcnt(3)\n\t" \
  "ds_read_b128 v[68:71], v35 offset:512\n\t" \
  "v_add_u32_sdwa v0, s29, v108 dst_sel:DWORD dst_unused:UNUSED_PAD src0_sel:DWORD src1_sel:WORD_0\n\t" \
  "v_smfmac_f32_16x16x64_bf16 v[52:55], a[16:19], v[126:133], v144\n\t" \
  "s_waitcnt lgkmcnt(3)\n\t" \
  "ds_read_b64 v[0:1], v0 offset:0\n\t" \
  "v_add_u32_sdwa v2, s29, v108 dst_sel:DWORD dst_unused:UNUSED_PAD src0_sel:DWORD src1_sel:WORD_1\n\t" \
  "v_smfmac_f32_16x16x64_bf16 v[56:59], a[20:23], v[126:133], v144 abid:1\n\t" \
  "s_add_u32 m0, s33, 0\n\t" \
  "s_waitcnt lgkmcnt(3)\n\t" \
  "ds_write_b128 v24, v[8:11] offset:0\n\t" \
  "v_add_u32_sdwa v4, s29, v109 dst_sel:DWORD dst_unused:UNUSED_PAD src0_sel:DWORD src1_sel:WORD_0\n\t" \
  "v_smfmac_f32_16x16x64_bf16 v[60:63], a[24:27], v[126:133], v145\n\t" \
  "s_waitcnt lgkmcnt(3)\n\t" \
  "buffer_load_dwordx4 v186, s[16:19], s34 offen offset:0 nt lds\n\t" \
  "v_add_u32_sdwa v6, s29, v109 dst_sel:DWORD dst_unused:UNUSED_PAD src0_sel:DWORD src1_sel:WORD_1\n\t" \
  "v_smfmac_f32_16x16x64_bf16 v[64:67], a[28:31], v[126:133], v145 abid:1\n\t" \
  "s_add_u32 m0, s33, 1920\n\t" \
  "s_waitcnt lgkmcnt(2)\n\t" \
  "ds_read_b64 v[2:3], v2 offset:0\n\t" \
  "v_smfmac_f32_16x16x64_bf16 v[68:71], a[32:35], v[126:133], v178\n\t" \
  "buffer_load_dwordx4 v186, s[16:19], s34 offen offset:128 nt lds\n\t" \
  "v_smfmac_f32_16x16x64_bf16 v[36:39], a[36:39], v[134:141], v146\n\t" \
  "s_add_u32 m0, s33, 3840\n\t" \
  "ds_write_b128 v24, v[12:15] offset:128\n\t" \
  "v_xor_b32 v24, 64, v24\n\t" \
  "v_smfmac_f32_16x16x64_bf16 v[40:43], a[40:43], v[134:141], v146 abid:1\n\t" \
  "buffer_load_dwordx4 v186, s[16:19], s34 offen offset:256 nt lds\n\t" \
  "v_add_u32_sdwa v8, s29, v110 dst_sel:DWORD dst_unused:UNUSED_PAD src0_sel:DWORD src1_sel:WORD_0\n\t" \
  "v_smfmac_f32_16x16x64_bf16 v[44:47], a[44:47], v[134:141], v147\n\t" \
  "s_add_u32 m0, s33, 5760\n\t" \
  "ds_read_b128 v[126:129], v183 offset:4096\n\t" \
  "v_add_u32_sdwa v10, s29, v110 dst_sel:DWORD dst_unused:UNUSED_PAD src0_sel:DWORD src1_sel:WORD_1\n\t" \
  "v_smfmac_f32_16x16x64_bf16 v[48:51], a[48:51], v[134:141], v147 abid:1\n\t" \
  "buffer_load_dwordx4 v186, s[16:19], s34 offen offset:384 nt lds\n\t" \
  "v_add_u32_sdwa v12, s29, v111 dst_sel:DWORD dst_unused:UNUSED_PAD src0_sel:DWORD src1_sel:WORD_0\n\t" \
  "v_smfmac_f32_16x16x64_bf16 v[52:55], a[52:55], v[134:141], v148\n\t" \
  "s_add_u32 s31, s31, 18432\n\t" \
  "ds_write_b128 v24, v[16:19] offset:0\n\t" \
  "v_add_u32_sdwa v14, s29, v111 dst_sel:DWORD dst_unused:UNUSED_PAD src0_sel:DWORD src1_sel:WORD_1\n\t" \
  "v_smfmac_f32_16x16x64_bf16 v[56:59], a[56:59], v[134:141], v148 abid:1\n\t" \
  "s_sub_u32 s26, s31, 129024\n\t" \
  "ds_read_b128 v[130:133], v184 offset:4096\n\t" \
  "v_smfmac_f32_16x16x64_bf16 v[60:63], a[60:63], v[134:141], v149\n\t" \
  "s_min_u32 s31, s31, s26\n\t" \
  "ds_read_b64 v[4:5], v4 offset:0\n\t" \
  "v_smfmac_f32_16x16x64_bf16 v[64:67], a[64:67], v[134:141], v149 abid:1\n\t" \
  "s_add_u32 s33, s31, s32\n\t" \
  "ds_read_b64 v[6:7], v6 offset:0\n\t" \
  "v_smfmac_f32_16x16x64_bf16 v[68:71], a[68:71], v[134:141], v178 abid:1\n\t" \
  "s_waitcnt lgkmcnt(2)\n\t" \
  "ds_write_b128 v24, v[20:23] offset:128\n\t" \
  "v_add_u32_sdwa v16, s29, v112 dst_sel:DWORD dst_unused:UNUSED_PAD src0_sel:DWORD src1_sel:WORD_0\n\t" \
  "v_smfmac_f32_16x16x64_bf16 v[36:39], a[72:75], v[126:133], v150\n\t" \
  "s_add_u32 s34, s34, 4718592\n\t" \
  "ds_read_b128 v[134:137], v183 offset:6144\n\t" \
  "v_add_u32_sdwa v18, s29, v112 dst_sel:DWORD dst_unused:UNUSED_PAD src0_sel:DWORD src1_sel:WORD_1\n\t" \
  "v_smfmac_f32_16x16x64_bf16 v[40:43], a[76:79], v[126:133], v150 abid:1\n\t" \
  "s_and_b32 s27, s64, 7\n\t" \
  "ds_read_b128 v[138:141], v184 offset:6144\n\t" \
  "v_add_u32_sdwa v20, s29, v113 dst_sel:DWORD dst_unused:UNUSED_PAD src0_sel:DWORD src1_sel:WORD_0\n\t" \
  "v_smfmac_f32_16x16x64_bf16 v[44:47], a[80:83], v[126:133], v151\n\t" \
  "s_waitcnt lgkmcnt(3)\n\t" \
  "ds_read_b64 v[8:9], v8 offset:0\n\t" \
  "v_add_u32_sdwa v22, s29, v113 dst_sel:DWORD dst_unused:UNUSED_PAD src0_sel:DWORD src1_sel:WORD_1\n\t" \
  "v_smfmac_f32_16x16x64_bf16 v[36:39], v[72:75], v[0:7], v182 abid:1\n\t" \
  "s_lshl2_add_u32 s66, s27, s68\n\t" \
  "ds_write_b64 v25, v[26:27] offset:16384\n\t" \
  "v_add_u32_sdwa v0, s29, v114 dst_sel:DWORD dst_unused:UNUSED_PAD src0_sel:DWORD src1_sel:WORD_0\n\t" \
  "v_smfmac_f32_16x16x64_bf16 v[48:51], a[84:87], v[126:133], v151 abid:1\n\t" \
  "s_and_b32 s27, s64, 8\n\t" \
  "ds_read_b64 v[10:11], v10 offset:0\n\t" \
  "v_add_u32_sdwa v2, s29, v114 dst_sel:DWORD dst_unused:UNUSED_PAD src0_sel:DWORD src1_sel:WORD_1\n\t" \
  "v_smfmac_f32_16x16x64_bf16 v[52:55], a[88:91], v[126:133], v152\n\t" \
  "s_mul_i32 s27, s27, 176\n\t" \
  "ds_read_b64 v[12:13], v12 offset:0\n\t" \
  "v_add_u32_sdwa v4, s29, v115 dst_sel:DWORD dst_unused:UNUSED_PAD src0_sel:DWORD src1_sel:WORD_0\n\t" \
  "v_smfmac_f32_16x16x64_bf16 v[56:59], a[92:95], v[126:133], v152 abid:1\n\t" \
  "s_add_u32 s66, s66, s27\n\t" \
  "ds_read_b64 v[14:15], v14 offset:0\n\t" \
  "v_add_u32_sdwa v6, s29, v115 dst_sel:DWORD dst_unused:UNUSED_PAD src0_sel:DWORD src1_sel:WORD_1\n\t" \
  "v_smfmac_f32_16x16x64_bf16 v[60:63], a[96:99], v[126:133], v153\n\t" \
  "s_lshr_b64 s[64:65], s[64:65], 4\n\t" \
  "ds_read_b64 v[16:17], v16 offset:0\n\t" \
  "v_smfmac_f32_16x16x64_bf16 v[64:67], a[100:103], v[126:133], v153 abid:1\n\t" \
  "s_sub_u32 s28, s72, s46\n\t" \
  "ds_read_b64 v[18:19], v18 offset:0\n\t" \
  "v_smfmac_f32_16x16x64_bf16 v[68:71], a[104:107], v[126:133], v179\n\t" \
  "s_waitcnt lgkmcnt(7)\n\t" \
  "ds_read_b128 v[126:129], v183 offset:8192\n\t" \
  "v_smfmac_f32_16x16x64_bf16 v[36:39], a[108:111], v[134:141], v154\n\t" \
  "s_waitcnt lgkmcnt(3)\n\t" \
  "ds_read_b128 v[130:133], v184 offset:8192\n\t" \
  "v_smfmac_f32_16x16x64_bf16 v[40:43], v[76:79], v[8:15], v182 abid:1\n\t" \
  "s_add_u32 s46, s72, 185\n\t" \
  "ds_read_b64 v[20:21], v20 offset:0\n\t" \
  "v_add_u32_sdwa v8, s29, v32 dst_sel:DWORD dst_unused:UNUSED_PAD src0_sel:DWORD src1_sel:WORD_0\n\t" \
  "v_smfmac_f32_16x16x64_bf16 v[40:43], a[112:115], v[134:141], v154 abid:1\n\t" \
  ".Ljn11%=:\n\t" \
  "s_max_i32 s28, s28, 0\n\t" \
  "ds_read_b64 v[22:23], v22 offset:0\n\t" \
  "v_add_u32_sdwa v10, s29, v32 dst_sel:DWORD dst_unused:UNUSED_PAD src0_sel:DWORD src1_sel:WORD_1\n\t" \
  "v_smfmac_f32_16x16x64_bf16 v[44:47], a[116:119], v[134:141], v155\n\t" \
  "s_min_u32 s28, s28, 6\n\t" \
  "ds_read_b64 v[0:1], v0 offset:0\n\t" \
  "v_add_u32 v28, s60, v187\n\t" \
  "v_smfmac_f32_16x16x64_bf16 v[48:51], a[120:123], v[134:141], v155 abid:1\n\t" \
  "s_lshl_b32 s28, s28, 6\n\t" \
  "ds_read_b64 v[2:3], v2 offset:0\n\t" \
  "v_smfmac_f32_16x16x64_bf16 v[52:55], a[124:127], v[134:141], v156\n\t" \
  "s_add_u32 s28, s28, s53\n\t" \
  "ds_read_b64 v[4:5], v4 offset:0\n\t" \
  "v_smfmac_f32_16x16x64_bf16 v[56:59], a[128:131], v[134:141], v156 abid:1\n\t" \
  "s_waitcnt lgkmcnt(3)\n\t" \
  "ds_read_b64 v[6:7], v6 offset:0\n\t" \
  "v_smfmac_f32_16x16x64_bf16 v[44:47], v[80:83], v[16:23], v182 abid:1\n\t" \
  "s_max_u32 s51, s51, s28\n\t" \
  "ds_read_b64 v[8:9], v8 offset:0\n\t" \
  "v_add_u32_sdwa v16, s29, v116 dst_sel:DWORD dst_unused:UNUSED_PAD src0_sel:DWORD src1_sel:WORD_0\n\t" \
  "v_smfmac_f32_16x16x64_bf16 v[60:63], a[132:135], v[134:141], v157\n\t" \
  "s_add_u32 s54, s54, s51\n\t" \
  "ds_read_b64 v[10:11], v10 offset:0\n\t" \
  "v_add_u32_sdwa v18, s29, v116 dst_sel:DWORD dst_unused:UNUSED_PAD src0_sel:DWORD src1_sel:WORD_1\n\t" \
  "v_smfmac_f32_16x16x64_bf16 v[64:67], a[136:139], v[134:141], v157 abid:1\n\t" \
  "s_lshr_b32 s52, s54, 4\n\t" \
  "ds_read_b128 v[28:31], v28 offset:0\n\t" \
  "v_add_u32 v24, s38, v187\n\t" \
  "v_smfmac_f32_16x16x64_bf16 v[68:71], a[140:143], v[134:141], v179 abid:1\n\t" \
  "s_waitcnt lgkmcnt(3)\n\t" \
  "ds_read_b128 v[24:27], v24 offset:4096\n\t" \
  "v_add_u32_sdwa v20, s29, v117 dst_sel:DWORD dst_unused:UNUSED_PAD src0_sel:DWORD src1_sel:WORD_0\n\t" \
  "v_smfmac_f32_16x16x64_bf16 v[48:51], v[84:87], v[0:7], v182 abid:1\n\t" \
  "s_add_u32 s52, s52, s56\n\t" \
  "ds_read_b128 v[134:137], v183 offset:10240\n\t" \
  "v_add_u32_sdwa v22, s29, v117 dst_sel:DWORD dst_unused:UNUSED_PAD src0_sel:DWORD src1_sel:WORD_1\n\t" \
  "v_smfmac_f32_16x16x64_bf16 v[36:39], a[144:147], v[126:133], v158\n\t" \
  "s_lshr_b32 s28, s51, 4\n\t" \
  "ds_read_b128 v[138:141], v184 offset:10240\n\t" \
  "v_add_u32_sdwa v0, s29, v118 dst_sel:DWORD dst_unused:UNUSED_PAD src0_sel:DWORD src1_sel:WORD_0\n\t" \
  "v_smfmac_f32_16x16x64_bf16 v[40:43], a[148:151], v[126:133], v158 abid:1\n\t" \
  "s_add_u32 s36, s35, s39\n\t" \
  "s_waitcnt lgkmcnt(3)\n\t" \
  "ds_read_b64 v[16:17], v16 offset:0\n\t" \
  "v_add_u32_sdwa v2, s29, v118 dst_sel:DWORD dst_unused:UNUSED_PAD src0_sel:DWORD src1_sel:WORD_1\n\t" \
  "s_set_gpr_idx_on s6, gpr_idx(DST)\n\t" \
  "v_smfmac_f32_16x16x64_bf16 v[36:39], v[28:31], v[8:15], v182 abid:1\n\t" \
  "s_set_gpr_idx_off\n\t" \
  "s_waitcnt lgkmcnt(3)\n\t" \
  "buffer_store_dwordx4 v[24:27], v34, s[20:23], s36 offen sc1\n\t" \
  "v_add_u32_sdwa v4, s29, v119 dst_sel:DWORD dst_unused:UNUSED_PAD src0_sel:DWORD src1_sel:WORD_0\n\t" \
  "v_smfmac_f32_16x16x64_bf16 v[44:47], a[152:155], v[126:133], v159\n\t" \
  "s_add_u32 s28, s28, s72\n\t" \
  "ds_read_b64 v[18:19], v18 offset:0\n\t" \
  "v_xad_u32 v24, v187, 64, s38\n\t" \
  "v_smfmac_f32_16x16x64_bf16 v[48:51], a[156:159], v[126:133], v159 abid:1\n\t" \
  "s_sub_u32 s28, s28, s101\n\t" \
  "ds_read_b128 v[24:27], v24 offset:5120\n\t" \
  "v_add_u32_sdwa v6, s29, v119 dst_sel:DWORD dst_unused:UNUSED_PAD src0_sel:DWORD src1_sel:WORD_1\n\t" \
  "v_smfmac_f32_16x16x64_bf16 v[52:55], a[160:163], v[126:133], v160\n\t" \
  "s_max_u32 s52, s52, s28\n\t" \
  "ds_read_b64 v[20:21], v20 offset:0\n\t" \
  "v_add_u32_sdwa v8, s29, v120 dst_sel:DWORD dst_unused:UNUSED_PAD src0_sel:DWORD src1_sel:WORD_0\n\t" \
  "v_smfmac_f32_16x16x64_bf16 v[56:59], a[164:167], v[126:133], v160 abid:1\n\t" \
  "s_sub_u32 s51, s51, 96\n\t" \
  "ds_read_b64 v[22:23], v22 offset:0\n\t" \
  "v_add_u32_sdwa v10, s29, v120 dst_sel:DWORD dst_unused:UNUSED_PAD src0_sel:DWORD src1_sel:WORD_1\n\t" \
  "v_smfmac_f32_16x16x64_bf16 v[60:63], a[168:171], v[126:133], v161\n\t" \
  "s_add_u32 s36, s36, 4608\n\t" \
  "ds_read_b64 v[0:1], v0 offset:0\n\t" \
  "v_add_u32_sdwa v12, s29, v121 dst_sel:DWORD dst_unused:UNUSED_PAD src0_sel:DWORD src1_sel:WORD_0\n\t" \
  "v_smfmac_f32_16x16x64_bf16 v[64:67], a[172:175], v[126:133], v161 abid:1\n\t" \
  "s_waitcnt lgkmcnt(3)\n\t" \
  "buffer_store_dwordx4 v[24:27], v34, s[20:23], s36 offen sc1\n\t" \
  "v_add_u32_sdwa v14, s29, v121 dst_sel:DWORD dst_unused:UNUSED_PAD src0_sel:DWORD src1_sel:WORD_1\n\t" \
  "v_smfmac_f32_16x16x64_bf16 v[68:71], a[176:179], v[126:133], v180\n\t" \
  "s_max_u32 s51, s51, s53\n\t" \
  "ds_read_b128 v[126:129], v183 offset:12288\n\t" \
  "v_add_u32 v24, s38, v187\n\t" \
  "v_smfmac_f32_16x16x64_bf16 v[36:39], a[180:183], v[134:141], v162\n\t" \
  "s_waitcnt lgkmcnt(2)\n\t" \
  "ds_read_b128 v[24:27], v24 offset:6144\n\t" \
  "v_smfmac_f32_16x16x64_bf16 v[52:55], v[88:91], v[16:23], v182 abid:1\n\t" \
  "ds_read_b128 v[130:133], v184 offset:12288\n\t" \
  "v_add_u32_sdwa v16, s29, v122 dst_sel:DWORD dst_unused:UNUSED_PAD src0_sel:DWORD src1_sel:WORD_0\n\t" \
  "v_smfmac_f32_16x16x64_bf16 v[40:43], a[184:187], v[134:141], v162 abid:1\n\t" \
  "ds_read_b64 v[2:3], v2 offset:0\n\t" \
  "v_add_u32_sdwa v18, s29, v122 dst_sel:DWORD dst_unused:UNUSED_PAD src0_sel:DWORD src1_sel:WORD_1\n\t" \
  "v_smfmac_f32_16x16x64_bf16 v[44:47], a[188:191], v[134:141], v163\n\t" \
  "s_add_u32 s36, s36, 4608\n\t" \
  "ds_read_b64 v[4:5], v4 offset:0\n\t" \
  "v_add_u32_sdwa v20, s29, v123 dst_sel:DWORD dst_unused:UNUSED_PAD src0_sel:DWORD src1_sel:WORD_0\n\t" \
  "v_smfmac_f32_16x16x64_bf16 v[48:51], a[192:195], v[134:141], v163 abid:1\n\t" \
  "s_waitcnt lgkmcnt(3)\n\t" \
  "buffer_store_dwordx4 v[24:27], v34, s[20:23], s36 offen sc1\n\t" \
  "v_add_u32_sdwa v22, s29, v123 dst_sel:DWORD dst_unused:UNUSED_PAD src0_sel:DWORD src1_sel:WORD_1\n\t" \
  "v_smfmac_f32_16x16x64_bf16 v[52:55], a[196:199], v[134:141], v164\n\t" \
  "ds_read_b64 v[6:7], v6 offset:0\n\t" \
  "v_xad_u32 v24, v187, 64, s38\n\t" \
  "v_smfmac_f32_16x16x64_bf16 v[56:59], a[200:203], v[134:141], v164 abid:1\n\t" \
  "s_add_u32 s38, s38, 18432\n\t" \
  "ds_read_b128 v[24:27], v24 offset:7168\n\t" \
  "v_smfmac_f32_16x16x64_bf16 v[60:63], a[204:207], v[134:141], v165\n\t" \
  "s_sub_u32 s44, s38, 129024\n\t" \
  "ds_read_b64 v[8:9], v8 offset:0\n\t" \
  "v_smfmac_f32_16x16x64_bf16 v[64:67], a[208:211], v[134:141], v165 abid:1\n\t" \
  "s_min_u32 s38, s38, s44\n\t" \
  "ds_read_b64 v[10:11], v10 offset:0\n\t" \
  "v_smfmac_f32_16x16x64_bf16 v[68:71], a[212:215], v[134:141], v180 abid:1\n\t" \
  "s_add_u32 s36, s36, 4608\n\t" \
  "s_waitcnt lgkmcnt(3)\n\t" \
  "ds_read_b128 v[134:137], v183 offset:14336\n\t" \
  "v_smfmac_f32_16x16x64_bf16 v[56:59], v[92:95], v[0:7], v182 abid:1\n\t" \
  "s_waitcnt lgkmcnt(3)\n\t" \
  "buffer_store_dwordx4 v[24:27], v34, s[20:23], s36 offen sc1\n\t" \
  "v_smfmac_f32_16x16x64_bf16 v[36:39], a[216:219], v[126:133], v166\n\t" \
  "s_add_u32 s35, s35, 4718592\n\t" \
  "ds_read_b128 v[138:141], v184 offset:14336\n\t" \
  "v_add_u32 v24, s43, v187\n\t" \
  "v_smfmac_f32_16x16x64_bf16 v[40:43], a[220:223], v[126:133], v166 abid:1\n\t" \
  "ds_read_b128 v[24:27], v24 offset:16384\n\t" \
  "v_smfmac_f32_16x16x64_bf16 v[44:47], a[224:227], v[126:133], v167\n\t" \
  "ds_read_b64 v[12:13], v12 offset:0\n\t" \
  "v_smfmac_f32_16x16x64_bf16 v[48:51], a[228:231], v[126:133], v167 abid:1\n\t" \
  "ds_read_b64 v[14:15], v14 offset:0\n\t" \
  "v_smfmac_f32_16x16x64_bf16 v[52:55], a[232:235], v[126:133], v168\n\t" \
  "ds_read_b64 v[16:17], v16 offset:0\n\t" \
  "v_smfmac_f32_16x16x64_bf16 v[56:59], a[236:239], v[126:133], v168 abid:1\n\t" \
  "s_waitcnt lgkmcnt(3)\n\t" \
  "buffer_store_dwordx4 v[24:27], v186, s[20:23], s40 offen offset:384 sc1\n\t" \
  "v_smfmac_f32_16x16x64_bf16 v[60:63], a[240:243], v[126:133], v169\n\t" \
  "s_add_u32 s43, s43, 18432\n\t" \
  "ds_read_b64 v[18:19], v18 offset:0\n\t" \
  "v_smfmac_f32_16x16x64_bf16 v[64:67], a[244:247], v[126:133], v169 abid:1\n\t" \
  "s_waitcnt lgkmcnt(2)\n\t" \
  "ds_read_b64 v[20:21], v20 offset:0\n\t" \
  "v_smfmac_f32_16x16x64_bf16 v[60:63], v[96:99], v[8:15], v182 abid:1\n\t" \
  "s_sub_u32 s42, s43, 129024\n\t" \
  "ds_read_b64 v[22:23], v22 offset:0\n\t" \
  "v_add_u32_sdwa v8, s29, v124 dst_sel:DWORD dst_unused:UNUSED_PAD src0_sel:DWORD src1_sel:WORD_0\n\t" \
  "v_smfmac_f32_16x16x64_bf16 v[68:71], a[248:251], v[126:133], v181\n\t" \
  "s_min_u32 s43, s43, s42\n\t" \
  "ds_read_b128 v[0:3], v183 offset:16384\n\t" \
  "v_add_u32_sdwa v10, s29, v124 dst_sel:DWORD dst_unused:UNUSED_PAD src0_sel:DWORD src1_sel:WORD_1\n\t" \
  "v_smfmac_f32_16x16x64_bf16 v[36:39], a[252:255], v[134:141], v170\n\t" \
  "s_add_u32 s40, s40, 4718592\n\t" \
  "ds_read_b128 v[4:7], v184 offset:16384\n\t" \
  "v_add_u32_sdwa v12, s29, v125 dst_sel:DWORD dst_unused:UNUSED_PAD src0_sel:DWORD src1_sel:WORD_0\n\t" \
  "v_smfmac_f32_16x16x64_bf16 v[40:43], v[188:191], v[134:141], v170 abid:1\n\t" \
  "ds_read_b64 v[8:9], v8 offset:0\n\t" \
  "v_add_u32_sdwa v14, s29, v125 dst_sel:DWORD dst_unused:UNUSED_PAD src0_sel:DWORD src1_sel:WORD_1\n\t" \
  "v_smfmac_f32_16x16x64_bf16 v[44:47], v[192:195], v[134:141], v171\n\t" \
  "s_waitcnt lgkmcnt(3)\n\t" \
  "ds_read_b64 v[10:11], v10 offset:0\n\t" \
  "v_add_u32 v183, s30, v183\n\t" \
  "v_smfmac_f32_16x16x64_bf16 v[64:67], v[100:103], v[16:23], v182 abid:1\n\t" \
  "s_add_u32 s29, s29, s30\n\t" \
  "ds_read_b64 v[12:13], v12 offset:0\n\t" \
  "v_add_u32 v184, s30, v184\n\t" \
  "v_smfmac_f32_16x16x64_bf16 v[48:51], v[196:199], v[134:141], v171 abid:1\n\t" \
  "ds_read_b64 v[14:15], v14 offset:0\n\t" \
  "v_smfmac_f32_16x16x64_bf16 v[52:55], v[200:203], v[134:141], v172\n\t" \
  "s_memrealtime s[72:73]\n\t" \
  "v_smfmac_f32_16x16x64_bf16 v[56:59], v[204:207], v[134:141], v172 abid:1\n\t" \
  "v_smfmac_f32_16x16x64_bf16 v[60:63], v[208:211], v[134:141], v173\n\t" \
  "v_smfmac_f32_16x16x64_bf16 v[64:67], v[212:215], v[134:141], v173 abid:1\n\t" \
  "s_waitcnt lgkmcnt(0)\n\t" \
  "v_smfmac_f32_16x16x64_bf16 v[68:71], v[104:107], v[8:15], v182 abid:1\n\t" \
  "v_smfmac_f32_16x16x64_bf16 v[68:71], v[216:219], v[134:141], v181 abid:1\n\t" \
  "s_setpc_b64 s[66:67]\n\t" \
  ".Lls11%=:\n\t" \
  "s_memrealtime s[72:73]\n\t" \
  "s_waitcnt lgkmcnt(0)\n\t" \
  "s_sub_i32 s28, s72, s52\n\t" \
  "s_cmp_lt_i32 s28, 0\n\t" \
  "s_cbranch_scc1 .Lls11%=\n\t" \
  "s_branch .Llg11%=\n\t" \
  ".fill (.Lvar%=+16384-.)/4, 4, 0xbf800000\n\t" \
  "s_branch .Lfin%=\n\t" \
  "s_branch .Lhe%=\n\t" \
  "s_waitcnt vmcnt(6)\n\t" \
  "s_waitcnt vmcnt(10)\n\t" \
  "s_waitcnt vmcnt(12)\n\t" \
  "s_waitcnt vmcnt(17)\n\t" \
  "s_waitcnt vmcnt(21) lgkmcnt(0)\n\t" \
  "s_cmp_lt_u32 s72, s52\n\t" \
  "s_cbranch_scc1 .Lns12%=\n\t" \
  ".Lng12%=:\n\t" \
  "s_barrier\n\t" \
  "v_smfmac_f32_16x16x64_bf16 v[36:39], v[220:223], v[0:7], v174\n\t" \
  "ds_read_b128 v[126:129], v183 offset:0\n\t" \
  "v_add_u32 v24, s37, v185\n\t" \
  "v_smfmac_f32_16x16x64_bf16 v[40:43], v[224:227], v[0:7], v174 abid:1\n\t" \
  "s_movk_i32 s53, 1952\n\t" \
  "ds_read_b128 v[130:133], v184 offset:0\n\t" \
  "v_xor_b32 v25, s41, v183\n\t" \
  "v_smfmac_f32_16x16x64_bf16 v[44:47], v[228:231], v[0:7], v175\n\t" \
  "s_mov_b32 s51, s53\n\t" \
  "ds_read_b128 v[134:137], v183 offset:2048\n\t" \
  "v_subrev_u32 v25, s30, v25\n\t" \
  "v_smfmac_f32_16x16x64_bf16 v[48:51], v[232:235], v[0:7], v175 abid:1\n\t" \
  "s_add_u32 s30, s29, 18432\n\t" \
  "ds_read_b128 v[138:141], v184 offset:2048\n\t" \
  "v_cvt_pk_bf16_f32 v8, v36, v37\n\t" \
  "v_cvt_pk_bf16_f32 v9, v38, v39\n\t" \
  "v_smfmac_f32_16x16x64_bf16 v[52:55], v[236:239], v[0:7], v176\n\t" \
  "s_sub_u32 s25, s30, 129024\n\t" \
  "ds_read_b128 v[36:39], v35 offset:0\n\t" \
  "v_cvt_pk_bf16_f32 v10, v40, v41\n\t" \
  "v_cvt_pk_bf16_f32 v11, v42, v43\n\t" \
  "v_smfmac_f32_16x16x64_bf16 v[56:59], v[240:243], v[0:7], v176 abid:1\n\t" \
  "s_min_u32 s30, s30, s25\n\t" \
  "ds_read_b128 v[40:43], v35 offset:64\n\t" \
  "v_cvt_pk_bf16_f32 v16, v44, v45\n\t" \
  "v_cvt_pk_bf16_f32 v17, v46, v47\n\t" \
  "v_smfmac_f32_16x16x64_bf16 v[60:63], v[244:247], v[0:7], v177\n\t" \
  "s_sub_u32 s30, s30, s29\n\t" \
  "ds_read_b128 v[44:47], v35 offset:128\n\t" \
  "v_cvt_pk_bf16_f32 v18, v48, v49\n\t" \
  "v_cvt_pk_bf16_f32 v19, v50, v51\n\t" \
  "v_smfmac_f32_16x16x64_bf16 v[64:67], v[248:251], v[0:7], v177 abid:1\n\t" \
  "s_mov_b32 s37, s29\n\t" \
  "ds_read_b128 v[48:51], v35 offset:192\n\t" \
  "v_cvt_pk_bf16_f32 v12, v52, v53\n\t" \
  "v_cvt_pk_bf16_f32 v13, v54, v55\n\t" \
  "v_smfmac_f32_16x16x64_bf16 v[68:71], v[252:255], v[0:7], v182\n\t" \
  "s_waitcnt lgkmcnt(3)\n\t" \
  "ds_read_b128 v[52:55], v35 offset:256\n\t" \
  "v_cvt_pk_bf16_f32 v14, v56, v57\n\t" \
  "v_cvt_pk_bf16_f32 v15, v58, v59\n\t" \
  "v_smfmac_f32_16x16x64_bf16 v[36:39], a[0:3], v[126:133], v142\n\t" \
  "s_waitcnt lgkmcnt(3)\n\t" \
  "ds_read_b128 v[56:59], v35 offset:320\n\t" \
  "v_cvt_pk_bf16_f32 v20, v60, v61\n\t" \
  "v_cvt_pk_bf16_f32 v21, v62, v63\n\t" \
  "v_smfmac_f32_16x16x64_bf16 v[40:43], a[4:7], v[126:133], v142 abid:1\n\t" \
  "s_waitcnt lgkmcnt(3)\n\t" \
  "ds_read_b128 v[60:63], v35 offset:384\n\t" \
  "v_cvt_pk_bf16_f32 v22, v64, v65\n\t" \
  "v_cvt_pk_bf16_f32 v23, v66, v67\n\t" \
  "v_smfmac_f32_16x16x64_bf16 v[44:47], a[8:11], v[126:133], v143\n\t" \
  "s_waitcnt lgkmcnt(3)\n\t" \
  "ds_read_b128 v[64:67], v35 offset:448\n\t" \
  "v_cvt_pk_bf16_f32 v26, v68, v69\n\t" \
  "v_cvt_pk_bf16_f32 v27, v70, v71\n\t" \
  "v_smfmac_f32_16x16x64_bf16 v[48:51], a[12:15], v[126:133], v143 abid:1\n\t" \
  "s_waitcnt lgkmcnt(3)\n\t" \
  "ds_read_b128 v[68:71], v35 offset:512\n\t" \
  "v_add_u32_sdwa v0, s29, v108 dst_sel:DWORD dst_unused:UNUSED_PAD src0_sel:DWORD src1_sel:WORD_0\n\t" \
  "v_smfmac_f32_16x16x64_bf16 v[52:55], a[16:19], v[126:133], v144\n\t" \
  "s_waitcnt lgkmcnt(3)\n\t" \
  "ds_read_b64 v[0:1], v0 offset:0\n\t" \
  "v_add_u32_sdwa v2, s29, v108 dst_sel:DWORD dst_unused:UNUSED_PAD src0_sel:DWORD src1_sel:WORD_1\n\t" \
  "v_smfmac_f32_16x16x64_bf16 v[56:59], a[20:23], v[126:133], v144 abid:1\n\t" \
  "s_waitcnt lgkmcnt(3)\n\t" \
  "s_bfe_i32 s47, s48, 0x10000\n\t" \
  "ds_write_b128 v24, v[8:11] offset:0\n\t" \
  "v_add_u32_sdwa v4, s29, v109 dst_sel:DWORD dst_unused:UNUSED_PAD src0_sel:DWORD src1_sel:WORD_0\n\t" \
  "v_add_u32 v28, s38, v187\n\t" \
  "v_smfmac_f32_16x16x64_bf16 v[60:63], a[24:27], v[126:133], v145\n\t" \
  "s_waitcnt lgkmcnt(3)\n\t" \
  "s_lshr_b32 s48, s48, 1\n\t" \
  "ds_read_b128 v[28:31], v28 offset:4096\n\t" \
  "v_add_u32_sdwa v6, s29, v109 dst_sel:DWORD dst_unused:UNUSED_PAD src0_sel:DWORD src1_sel:WORD_1\n\t" \
  "v_smfmac_f32_16x16x64_bf16 v[64:67], a[28:31], v[126:133], v145 abid:1\n\t" \
  "s_waitcnt lgkmcnt(2)\n\t" \
  "s_add_u32 s36, s35, s39\n\t" \
  "ds_read_b64 v[2:3], v2 offset:0\n\t" \
  "v_smfmac_f32_16x16x64_bf16 v[68:71], a[32:35], v[126:133], v178\n\t" \
  "s_andn2_b32 s44, 0x40000000, s47\n\t" \
  "v_smfmac_f32_16x16x64_bf16 v[36:39], a[36:39], v[134:141], v146\n\t" \
  "s_or_b32 s36, s36, s44\n\t" \
  "ds_write_b128 v24, v[12:15] offset:128\n\t" \
  "v_xor_b32 v24, 64, v24\n\t" \
  "v_smfmac_f32_16x16x64_bf16 v[40:43], a[40:43], v[134:141], v146 abid:1\n\t" \
  "s_waitcnt lgkmcnt(2)\n\t" \
  "buffer_store_dwordx4 v[28:31], v34, s[20:23], s36 offen sc1\n\t" \
  "v_add_u32_sdwa v8, s29, v110 dst_sel:DWORD dst_unused:UNUSED_PAD src0_sel:DWORD src1_sel:WORD_0\n\t" \
  "v_smfmac_f32_16x16x64_bf16 v[44:47], a[44:47], v[134:141], v147\n\t" \
  "s_and_b32 s44, s47, 4718592\n\t" \
  "ds_read_b128 v[126:129], v183 offset:4096\n\t" \
  "v_add_u32_sdwa v10, s29, v110 dst_sel:DWORD dst_unused:UNUSED_PAD src0_sel:DWORD src1_sel:WORD_1\n\t" \
  "v_xad_u32 v28, v187, 64, s38\n\t" \
  "v_smfmac_f32_16x16x64_bf16 v[48:51], a[48:51], v[134:141], v147 abid:1\n\t" \
  "s_add_u32 s35, s35, s44\n\t" \
  "ds_read_b128 v[28:31], v28 offset:5120\n\t" \
  "v_add_u32_sdwa v12, s29, v111 dst_sel:DWORD dst_unused:UNUSED_PAD src0_sel:DWORD src1_sel:WORD_0\n\t" \
  "v_smfmac_f32_16x16x64_bf16 v[52:55], a[52:55], v[134:141], v148\n\t" \
  "s_add_u32 s36, s36, 4608\n\t" \
  "ds_write_b128 v24, v[16:19] offset:0\n\t" \
  "v_add_u32_sdwa v14, s29, v111 dst_sel:DWORD dst_unused:UNUSED_PAD src0_sel:DWORD src1_sel:WORD_1\n\t" \
  "v_smfmac_f32_16x16x64_bf16 v[56:59], a[56:59], v[134:141], v148 abid:1\n\t" \
  "ds_read_b128 v[130:133], v184 offset:4096\n\t" \
  "v_smfmac_f32_16x16x64_bf16 v[60:63], a[60:63], v[134:141], v149\n\t" \
  "ds_read_b64 v[4:5], v4 offset:0\n\t" \
  "v_smfmac_f32_16x16x64_bf16 v[64:67], a[64:67], v[134:141], v149 abid:1\n\t" \
  "ds_read_b64 v[6:7], v6 offset:0\n\t" \
  "s_waitcnt lgkmcnt(4)\n\t" \
  "buffer_store_dwordx4 v[28:31], v34, s[20:23], s36 offen sc1\n\t" \
  "v_smfmac_f32_16x16x64_bf16 v[68:71], a[68:71], v[134:141], v178 abid:1\n\t" \
  "s_waitcnt lgkmcnt(2)\n\t" \
  "s_and_b32 s44, s47, 18432\n\t" \
  "ds_write_b128 v24, v[20:23] offset:128\n\t" \
  "v_add_u32_sdwa v16, s29, v112 dst_sel:DWORD dst_unused:UNUSED_PAD src0_sel:DWORD src1_sel:WORD_0\n\t" \
  "v_add_u32 v28, s38, v187\n\t" \
  "v_smfmac_f32_16x16x64_bf16 v[36:39], a[72:75], v[126:133], v150\n\t" \
  "ds_read_b128 v[134:137], v183 offset:6144\n\t" \
  "ds_read_b128 v[28:31], v28 offset:6144\n\t" \
  "v_add_u32_sdwa v18, s29, v112 dst_sel:DWORD dst_unused:UNUSED_PAD src0_sel:DWORD src1_sel:WORD_1\n\t" \
  "v_smfmac_f32_16x16x64_bf16 v[40:43], a[76:79], v[126:133], v150 abid:1\n\t" \
  "s_and_b32 s27, s64, 7\n\t" \
  "s_add_u32 s36, s36, 4608\n\t" \
  "ds_read_b128 v[138:141], v184 offset:6144\n\t" \
  "v_add_u32_sdwa v20, s29, v113 dst_sel:DWORD dst_unused:UNUSED_PAD src0_sel:DWORD src1_sel:WORD_0\n\t" \
  "v_smfmac_f32_16x16x64_bf16 v[44:47], a[80:83], v[126:133], v151\n\t" \
  "s_waitcnt lgkmcnt(3)\n\t" \
  "ds_read_b64 v[8:9], v8 offset:0\n\t" \
  "v_add_u32_sdwa v22, s29, v113 dst_sel:DWORD dst_unused:UNUSED_PAD src0_sel:DWORD src1_sel:WORD_1\n\t" \
  "v_smfmac_f32_16x16x64_bf16 v[36:39], v[72:75], v[0:7], v182 abid:1\n\t" \
  "s_lshl2_add_u32 s66, s27, s68\n\t" \
  "ds_write_b64 v25, v[26:27] offset:16384\n\t" \
  "v_add_u32_sdwa v0, s29, v114 dst_sel:DWORD dst_unused:UNUSED_PAD src0_sel:DWORD src1_sel:WORD_0\n\t" \
  "v_smfmac_f32_16x16x64_bf16 v[48:51], a[84:87], v[126:133], v151 abid:1\n\t" \
  "s_and_b32 s27, s64, 8\n\t" \
  "ds_read_b64 v[10:11], v10 offset:0\n\t" \
  "ds_read_b64 v[12:13], v12 offset:0\n\t" \
  "s_waitcnt lgkmcnt(5)\n\t" \
  "buffer_store_dwordx4 v[28:31], v34, s[20:23], s36 offen sc1\n\t" \
  "v_add_u32_sdwa v2, s29, v114 dst_sel:DWORD dst_unused:UNUSED_PAD src0_sel:DWORD src1_sel:WORD_1\n\t" \
  "v_smfmac_f32_16x16x64_bf16 v[52:55], a[88:91], v[126:133], v152\n\t" \
  "s_mul_i32 s27, s27, 176\n\t" \
  "ds_read_b64 v[14:15], v14 offset:0\n\t" \
  "ds_read_b64 v[16:17], v16 offset:0\n\t" \
  "v_add_u32_sdwa v4, s29, v115 dst_sel:DWORD dst_unused:UNUSED_PAD src0_sel:DWORD src1_sel:WORD_0\n\t" \
  "v_xad_u32 v28, v187, 64, s38\n\t" \
  "v_smfmac_f32_16x16x64_bf16 v[56:59], a[92:95], v[126:133], v152 abid:1\n\t" \
  "s_add_u32 s66, s66, s27\n\t" \
  "s_add_u32 s38, s38, s44\n\t" \
  "ds_read_b64 v[18:19], v18 offset:0\n\t" \
  "ds_read_b128 v[28:31], v28 offset:7168\n\t" \
  "v_add_u32_sdwa v6, s29, v115 dst_sel:DWORD dst_unused:UNUSED_PAD src0_sel:DWORD src1_sel:WORD_1\n\t" \
  "v_smfmac_f32_16x16x64_bf16 v[60:63], a[96:99], v[126:133], v153\n\t" \
  "s_lshr_b64 s[64:65], s[64:65], 4\n\t" \
  "s_add_u32 s36, s36, 4608\n\t" \
  "ds_read_b64 v[20:21], v20 offset:0\n\t" \
  "v_smfmac_f32_16x16x64_bf16 v[64:67], a[100:103], v[126:133], v153 abid:1\n\t" \
  "s_sub_u32 s28, s72, s46\n\t" \
  "s_sub_u32 s44, s38, 129024\n\t" \
  "ds_read_b64 v[22:23], v22 offset:0\n\t" \
  "v_smfmac_f32_16x16x64_bf16 v[68:71], a[104:107], v[126:133], v179\n\t" \
  "s_waitcnt lgkmcnt(4)\n\t" \
  "s_min_u32 s38, s38, s44\n\t" \
  "ds_read_b128 v[126:129], v183 offset:8192\n\t" \
  "v_smfmac_f32_16x16x64_bf16 v[40:43], v[76:79], v[8:15], v182 abid:1\n\t" \
  "s_add_u32 s46, s72, 185\n\t" \
  "ds_read_b128 v[130:133], v184 offset:8192\n\t" \
  "s_waitcnt lgkmcnt(4)\n\t" \
  "buffer_store_dwordx4 v[28:31], v34, s[20:23], s36 offen sc1\n\t" \
  "v_smfmac_f32_16x16x64_bf16 v[36:39], a[108:111], v[134:141], v154\n\t" \
  "s_max_i32 s28, s28, 0\n\t" \
  "ds_read_b64 v[0:1], v0 offset:0\n\t" \
  "v_add_u32_sdwa v8, s29, v32 dst_sel:DWORD dst_unused:UNUSED_PAD src0_sel:DWORD src1_sel:WORD_0\n\t" \
  "v_smfmac_f32_16x16x64_bf16 v[40:43], a[112:115], v[134:141], v154 abid:1\n\t" \
  "s_branch .Ljn12%=\n\t" \
  ".Lns12%=:\n\t" \
  "s_memrealtime s[72:73]\n\t" \
  "s_waitcnt lgkmcnt(0)\n\t" \
  "s_sub_i32 s28, s72, s52\n\t" \
  "s_cmp_lt_i32 s28, 0\n\t" \
  "s_cbranch_scc1 .Lns12%=\n\t" \
  "s_branch .Lng12%=\n\t" \
  ".fill (.Lvar%=+17788-.)/4, 4, 0xbf800000\n\t" \
  "s_nop 0\n\t" \
  "s_branch .Lfin%=\n\t" \
  "s_branch .Lhe%=\n\t" \
  "s_waitcnt vmcnt(6)\n\t" \
  "s_waitcnt vmcnt(10)\n\t" \
  "s_waitcnt vmcnt(12)\n\t" \
  "s_waitcnt vmcnt(17)\n\t" \
  "s_waitcnt vmcnt(21) lgkmcnt(0)\n\t" \
  "s_cmp_lt_u32 s72, s52\n\t" \
  "s_cbranch_scc1 .Lls12%=\n\t" \
  ".Llg12%=:\n\t" \
  "s_barrier\n\t" \
  "v_smfmac_f32_16x16x64_bf16 v[36:39], v[220:223], v[0:7], v174\n\t" \
  "ds_read_b128 v[126:129], v183 offset:0\n\t" \
  "v_add_u32 v24, s37, v185\n\t" \
  "v_smfmac_f32_16x16x64_bf16 v[40:43], v[224:227], v[0:7], v174 abid:1\n\t" \
  "ds_read_b128 v[130:133], v184 offset:0\n\t" \
  "v_xor_b32 v25, s41, v183\n\t" \
  "v_smfmac_f32_16x16x64_bf16 v[44:47], v[228:231], v[0:7], v175\n\t" \
  "ds_read_b128 v[134:137], v183 offset:2048\n\t" \
  "v_subrev_u32 v25, s30, v25\n\t" \
  "v_smfmac_f32_16x16x64_bf16 v[48:51], v[232:235], v[0:7], v175 abid:1\n\t" \
  "s_add_u32 s30, s29, 18432\n\t" \
  "ds_read_b128 v[138:141], v184 offset:2048\n\t" \
  "v_cvt_pk_bf16_f32 v8, v36, v37\n\t" \
  "v_cvt_pk_bf16_f32 v9, v38, v39\n\t" \
  "v_smfmac_f32_16x16x64_bf16 v[52:55], v[236:239], v[0:7], v176\n\t" \
  "s_sub_u32 s25, s30, 129024\n\t" \
  "ds_read_b128 v[36:39], v35 offset:0\n\t" \
  "v_cvt_pk_bf16_f32 v10, v40, v41\n\t" \
  "v_cvt_pk_bf16_f32 v11, v42, v43\n\t" \
  "v_smfmac_f32_16x16x64_bf16 v[56:59], v[240:243], v[0:7], v176 abid:1\n\t" \
  "s_min_u32 s30, s30, s25\n\t" \
  "ds_read_b128 v[40:43], v35 offset:64\n\t" \
  "v_cvt_pk_bf16_f32 v16, v44, v45\n\t" \
  "v_cvt_pk_bf16_f32 v17, v46, v47\n\t" \
  "v_smfmac_f32_16x16x64_bf16 v[60:63], v[244:247], v[0:7], v177\n\t" \
  "s_sub_u32 s30, s30, s29\n\t" \
  "ds_read_b128 v[44:47], v35 offset:128\n\t" \
  "v_cvt_pk_bf16_f32 v18, v48, v49\n\t" \
  "v_cvt_pk_bf16_f32 v19, v50, v51\n\t" \
  "v_smfmac_f32_16x16x64_bf16 v[64:67], v[248:251], v[0:7], v177 abid:1\n\t" \
  "s_mov_b32 s37, s29\n\t" \
  "ds_read_b128 v[48:51], v35 offset:192\n\t" \
  "v_cvt_pk_bf16_f32 v12, v52, v53\n\t" \
  "v_cvt_pk_bf16_f32 v13, v54, v55\n\t" \
  "v_smfmac_f32_16x16x64_bf16 v[68:71], v[252:255], v[0:7], v182\n\t" \
  "s_waitcnt lgkmcnt(3)\n\t" \
  "ds_read_b128 v[52:55], v35 offset:256\n\t" \
  "v_cvt_pk_bf16_f32 v14, v56, v57\n\t" \
  "v_cvt_pk_bf16_f32 v15, v58, v59\n\t" \
  "v_smfmac_f32_16x16x64_bf16 v[36:39], a[0:3], v[126:133], v142\n\t" \
  "s_waitcnt lgkmcnt(3)\n\t" \
  "ds_read_b128 v[56:59], v35 offset:320\n\t" \
  "v_cvt_pk_bf16_f32 v20, v60, v61\n\t" \
  "v_cvt_pk_bf16_f32 v21, v62, v63\n\t" \
  "v_smfmac_f32_16x16x64_bf16 v[40:43], a[4:7], v[126:133], v142 abid:1\n\t" \
  "s_waitcnt lgkmcnt(3)\n\t" \
  "ds_read_b128 v[60:63], v35 offset:384\n\t" \
  "v_cvt_pk_bf16_f32 v22, v64, v65\n\t" \
  "v_cvt_pk_bf16_f32 v23, v66, v67\n\t" \
  "v_smfmac_f32_16x16x64_bf16 v[44:47], a[8:11], v[126:133], v143\n\t" \
  "s_waitcnt lgkmcnt(3)\n\t" \
  "ds_read_b128 v[64:67], v35 offset:448\n\t" \
  "v_cvt_pk_bf16_f32 v26, v68, v69\n\t" \
  "v_cvt_pk_bf16_f32 v27, v70, v71\n\t" \
  "v_smfmac_f32_16x16x64_bf16 v[48:51], a[12:15], v[126:133], v143 abid:1\n\t" \
  "s_waitcnt lgkmcnt(3)\n\t" \
  "ds_read_b128 v[68:71], v35 offset:512\n\t" \
  "v_add_u32_sdwa v0, s29, v108 dst_sel:DWORD dst_unused:UNUSED_PAD src0_sel:DWORD src1_sel:WORD_0\n\t" \
  "v_smfmac_f32_16x16x64_bf16 v[52:55], a[16:19], v[126:133], v144\n\t" \
  "s_waitcnt lgkmcnt(3)\n\t" \
  "ds_read_b64 v[0:1], v0 offset:0\n\t" \
  "v_add_u32_sdwa v2, s29, v108 dst_sel:DWORD dst_unused:UNUSED_PAD src0_sel:DWORD src1_sel:WORD_1\n\t" \
  "v_smfmac_f32_16x16x64_bf16 v[56:59], a[20:23], v[126:133], v144 abid:1\n\t" \
  "s_add_u32 m0, s33, 0\n\t" \
  "s_waitcnt lgkmcnt(3)\n\t" \
  "ds_write_b128 v24, v[8:11] offset:0\n\t" \
  "v_add_u32_sdwa v4, s29, v109 dst_sel:DWORD dst_unused:UNUSED_PAD src0_sel:DWORD src1_sel:WORD_0\n\t" \
  "v_smfmac_f32_16x16x64_bf16 v[60:63], a[24:27], v[126:133], v145\n\t" \
  "s_waitcnt lgkmcnt(3)\n\t" \
  "buffer_load_dwordx4 v186, s[16:19], s34 offen offset:0 nt lds\n\t" \
  "v_add_u32_sdwa v6, s29, v109 dst_sel:DWORD dst_unused:UNUSED_PAD src0_sel:DWORD src1_sel:WORD_1\n\t" \
  "v_smfmac_f32_16x16x64_bf16 v[64:67], a[28:31], v[126:133], v145 abid:1\n\t" \
  "s_add_u32 m0, s33, 1920\n\t" \
  "s_waitcnt lgkmcnt(2)\n\t" \
  "ds_read_b64 v[2:3], v2 offset:0\n\t" \
  "v_smfmac_f32_16x16x64_bf16 v[68:71], a[32:35], v[126:133], v178\n\t" \
  "buffer_load_dwordx4 v186, s[16:19], s34 offen offset:128 nt lds\n\t" \
  "v_smfmac_f32_16x16x64_bf16 v[36:39], a[36:39], v[134:141], v146\n\t" \
  "s_add_u32 m0, s33, 3840\n\t" \
  "ds_write_b128 v24, v[12:15] offset:128\n\t" \
  "v_xor_b32 v24, 64, v24\n\t" \
  "v_smfmac_f32_16x16x64_bf16 v[40:43], a[40:43], v[134:141], v146 abid:1\n\t" \
  "buffer_load_dwordx4 v186, s[16:19], s34 offen offset:256 nt lds\n\t" \
  "v_add_u32_sdwa v8, s29, v110 dst_sel:DWORD dst_unused:UNUSED_PAD src0_sel:DWORD src1_sel:WORD_0\n\t" \
  "v_smfmac_f32_16x16x64_bf16 v[44:47], a[44:47], v[134:141], v147\n\t" \
  "s_add_u32 m0, s33, 5760\n\t" \
  "ds_read_b128 v[126:129], v183 offset:4096\n\t" \
  "v_add_u32_sdwa v10, s29, v110 dst_sel:DWORD dst_unused:UNUSED_PAD src0_sel:DWORD src1_sel:WORD_1\n\t" \
  "v_smfmac_f32_16x16x64_bf16 v[48:51], a[48:51], v[134:141], v147 abid:1\n\t" \
  "buffer_load_dwordx4 v186, s[16:19], s34 offen offset:384 nt lds\n\t" \
  "v_add_u32_sdwa v12, s29, v111 dst_sel:DWORD dst_unused:UNUSED_PAD src0_sel:DWORD src1_sel:WORD_0\n\t" \
  "v_smfmac_f32_16x16x64_bf16 v[52:55], a[52:55], v[134:141], v148\n\t" \
  "s_add_u32 s31, s31, 18432\n\t" \
  "ds_write_b128 v24, v[16:19] offset:0\n\t" \
  "v_add_u32_sdwa v14, s29, v111 dst_sel:DWORD dst_unused:UNUSED_PAD src0_sel:DWORD src1_sel:WORD_1\n\t" \
  "v_smfmac_f32_16x16x64_bf16 v[56:59], a[56:59], v[134:141], v148 abid:1\n\t" \
  "s_sub_u32 s26, s31, 129024\n\t" \
  "ds_read_b128 v[130:133], v184 offset:4096\n\t" \
  "v_smfmac_f32_16x16x64_bf16 v[60:63], a[60:63], v[134:141], v149\n\t" \
  "s_min_u32 s31, s31, s26\n\t" \
  "ds_read_b64 v[4:5], v4 offset:0\n\t" \
  "v_smfmac_f32_16x16x64_bf16 v[64:67], a[64:67], v[134:141], v149 abid:1\n\t" \
  "s_add_u32 s33, s31, s32\n\t" \
  "ds_read_b64 v[6:7], v6 offset:0\n\t" \
  "v_smfmac_f32_16x16x64_bf16 v[68:71], a[68:71], v[134:141], v178 abid:1\n\t" \
  "s_waitcnt lgkmcnt(2)\n\t" \
  "ds_write_b128 v24, v[20:23] offset:128\n\t" \
  "v_add_u32_sdwa v16, s29, v112 dst_sel:DWORD dst_unused:UNUSED_PAD src0_sel:DWORD src1_sel:WORD_0\n\t" \
  "v_smfmac_f32_16x16x64_bf16 v[36:39], a[72:75], v[126:133], v150\n\t" \
  "s_add_u32 s34, s34, 4718592\n\t" \
  "ds_read_b128 v[134:137], v183 offset:6144\n\t" \
  "v_add_u32_sdwa v18, s29, v112 dst_sel:DWORD dst_unused:UNUSED_PAD src0_sel:DWORD src1_sel:WORD_1\n\t" \
  "v_smfmac_f32_16x16x64_bf16 v[40:43], a[76:79], v[126:133], v150 abid:1\n\t" \
  "s_and_b32 s27, s64, 7\n\t" \
  "ds_read_b128 v[138:141], v184 offset:6144\n\t" \
  "v_add_u32_sdwa v20, s29, v113 dst_sel:DWORD dst_unused:UNUSED_PAD src0_sel:DWORD src1_sel:WORD_0\n\t" \
  "v_smfmac_f32_16x16x64_bf16 v[44:47], a[80:83], v[126:133], v151\n\t" \
  "s_waitcnt lgkmcnt(3)\n\t" \
  "ds_read_b64 v[8:9], v8 offset:0\n\t" \
  "v_add_u32_sdwa v22, s29, v113 dst_sel:DWORD dst_unused:UNUSED_PAD src0_sel:DWORD src1_sel:WORD_1\n\t" \
  "v_smfmac_f32_16x16x64_bf16 v[36:39], v[72:75], v[0:7], v182 abid:1\n\t" \
  "s_lshl2_add_u32 s66, s27, s68\n\t" \
  "ds_write_b64 v25, v[26:27] offset:16384\n\t" \
  "v_add_u32_sdwa v0, s29, v114 dst_sel:DWORD dst_unused:UNUSED_PAD src0_sel:DWORD src1_sel:WORD_0\n\t" \
  "v_smfmac_f32_16x16x64_bf16 v[48:51], a[84:87], v[126:133], v151 abid:1\n\t" \
  "s_and_b32 s27, s64, 8\n\t" \
  "ds_read_b64 v[10:11], v10 offset:0\n\t" \
  "ds_read_b64 v[12:13], v12 offset:0\n\t" \
  "v_add_u32_sdwa v2, s29, v114 dst_sel:DWORD dst_unused:UNUSED_PAD src0_sel:DWORD src1_sel:WORD_1\n\t" \
  "v_smfmac_f32_16x16x64_bf16 v[52:55], a[88:91], v[126:133], v152\n\t" \
  "s_mul_i32 s27, s27, 176\n\t" \
  "ds_read_b64 v[14:15], v14 offset:0\n\t" \
  "ds_read_b64 v[16:17], v16 offset:0\n\t" \
  "v_add_u32_sdwa v4, s29, v115 dst_sel:DWORD dst_unused:UNUSED_PAD src0_sel:DWORD src1_sel:WORD_0\n\t" \
  "v_smfmac_f32_16x16x64_bf16 v[56:59], a[92:95], v[126:133], v152 abid:1\n\t" \
  "s_add_u32 s66, s66, s27\n\t" \
  "ds_read_b64 v[18:19], v18 offset:0\n\t" \
  "v_add_u32_sdwa v6, s29, v115 dst_sel:DWORD dst_unused:UNUSED_PAD src0_sel:DWORD src1_sel:WORD_1\n\t" \
  "v_smfmac_f32_16x16x64_bf16 v[60:63], a[96:99], v[126:133], v153\n\t" \
  "s_lshr_b64 s[64:65], s[64:65], 4\n\t" \
  "ds_read_b64 v[20:21], v20 offset:0\n\t" \
  "v_smfmac_f32_16x16x64_bf16 v[64:67], a[100:103], v[126:133], v153 abid:1\n\t" \
  "s_sub_u32 s28, s72, s46\n\t" \
  "ds_read_b64 v[22:23], v22 offset:0\n\t" \
  "v_smfmac_f32_16x16x64_bf16 v[68:71], a[104:107], v[126:133], v179\n\t" \
  "s_waitcnt lgkmcnt(4)\n\t" \
  "ds_read_b128 v[126:129], v183 offset:8192\n\t" \
  "v_smfmac_f32_16x16x64_bf16 v[40:43], v[76:79], v[8:15], v182 abid:1\n\t" \
  "s_add_u32 s46, s72, 185\n\t" \
  "ds_read_b128 v[130:133], v184 offset:8192\n\t" \
  "v_smfmac_f32_16x16x64_bf16 v[36:39], a[108:111], v[134:141], v154\n\t" \
  "s_max_i32 s28, s28, 0\n\t" \
  "ds_read_b64 v[0:1], v0 offset:0\n\t" \
  "v_add_u32_sdwa v8, s29, v32 dst_sel:DWORD dst_unused:UNUSED_PAD src0_sel:DWORD src1_sel:WORD_0\n\t" \
  "v_smfmac_f32_16x16x64_bf16 v[40:43], a[112:115], v[134:141], v154 abid:1\n\t" \
  ".Ljn12%=:\n\t" \
  "s_waitcnt lgkmcnt(3)\n\t" \
  "ds_read_b64 v[2:3], v2 offset:0\n\t" \
  "v_add_u32_sdwa v10, s29, v32 dst_sel:DWORD dst_unused:UNUSED_PAD src0_sel:DWORD src1_sel:WORD_1\n\t" \
  "v_smfmac_f32_16x16x64_bf16 v[44:47], v[80:83], v[16:23], v182 abid:1\n\t" \
  "s_min_u32 s28, s28, 6\n\t" \
  "ds_read_b64 v[4:5], v4 offset:0\n\t" \
  "v_add_u32 v28, s60, v187\n\t" \
  "v_smfmac_f32_16x16x64_bf16 v[44:47], a[116:119], v[134:141], v155\n\t" \
  "s_lshl_b32 s28, s28, 6\n\t" \
  "ds_read_b64 v[6:7], v6 offset:0\n\t" \
  "v_add_u32_sdwa v16, s29, v116 dst_sel:DWORD dst_unused:UNUSED_PAD src0_sel:DWORD src1_sel:WORD_0\n\t" \
  "v_smfmac_f32_16x16x64_bf16 v[48:51], a[120:123], v[134:141], v155 abid:1\n\t" \
  "s_add_u32 s28, s28, s53\n\t" \
  "ds_read_b64 v[8:9], v8 offset:0\n\t" \
  "v_add_u32_sdwa v18, s29, v116 dst_sel:DWORD dst_unused:UNUSED_PAD src0_sel:DWORD src1_sel:WORD_1\n\t" \
  "v_smfmac_f32_16x16x64_bf16 v[52:55], a[124:127], v[134:141], v156\n\t" \
  "s_max_u32 s51, s51, s28\n\t" \
  "ds_read_b64 v[10:11], v10 offset:0\n\t" \
  "v_add_u32_sdwa v20, s29, v117 dst_sel:DWORD dst_unused:UNUSED_PAD src0_sel:DWORD src1_sel:WORD_0\n\t" \
  "v_smfmac_f32_16x16x64_bf16 v[56:59], a[128:131], v[134:141], v156 abid:1\n\t" \
  "s_add_u32 s54, s54, s51\n\t" \
  "ds_read_b128 v[28:31], v28 offset:0\n\t" \
  "v_add_u32_sdwa v22, s29, v117 dst_sel:DWORD dst_unused:UNUSED_PAD src0_sel:DWORD src1_sel:WORD_1\n\t" \
  "v_smfmac_f32_16x16x64_bf16 v[60:63], a[132:135], v[134:141], v157\n\t" \
  "s_waitcnt lgkmcnt(3)\n\t" \
  "ds_read_b64 v[16:17], v16 offset:0\n\t" \
  "v_smfmac_f32_16x16x64_bf16 v[48:51], v[84:87], v[0:7], v182 abid:1\n\t" \
  "s_lshr_b32 s52, s54, 4\n\t" \
  "ds_read_b64 v[18:19], v18 offset:0\n\t" \
  "v_add_u32 v24, s38, v187\n\t" \
  "v_smfmac_f32_16x16x64_bf16 v[64:67], a[136:139], v[134:141], v157 abid:1\n\t" \
  "s_add_u32 s52, s52, s56\n\t" \
  "ds_read_b128 v[24:27], v24 offset:4096\n\t" \
  "v_add_u32_sdwa v0, s29, v118 dst_sel:DWORD dst_unused:UNUSED_PAD src0_sel:DWORD src1_sel:WORD_0\n\t" \
  "v_smfmac_f32_16x16x64_bf16 v[68:71], a[140:143], v[134:141], v179 abid:1\n\t" \
  "s_waitcnt lgkmcnt(3)\n\t" \
  "ds_read_b128 v[134:137], v183 offset:10240\n\t" \
  "v_add_u32_sdwa v2, s29, v118 dst_sel:DWORD dst_unused:UNUSED_PAD src0_sel:DWORD src1_sel:WORD_1\n\t" \
  "s_set_gpr_idx_on s6, gpr_idx(DST)\n\t" \
  "v_smfmac_f32_16x16x64_bf16 v[36:39], v[28:31], v[8:15], v182 abid:1\n\t" \
  "s_set_gpr_idx_off\n\t" \
  "ds_read_b128 v[138:141], v184 offset:10240\n\t" \
  "v_add_u32_sdwa v4, s29, v119 dst_sel:DWORD dst_unused:UNUSED_PAD src0_sel:DWORD src1_sel:WORD_0\n\t" \
  "v_smfmac_f32_16x16x64_bf16 v[36:39], a[144:147], v[126:133], v158\n\t" \
  "s_add_u32 s36, s35, s39\n\t" \
  "ds_read_b64 v[20:21], v20 offset:0\n\t" \
  "v_add_u32_sdwa v6, s29, v119 dst_sel:DWORD dst_unused:UNUSED_PAD src0_sel:DWORD src1_sel:WORD_1\n\t" \
  "v_smfmac_f32_16x16x64_bf16 v[40:43], a[148:151], v[126:133], v158 abid:1\n\t" \
  "s_waitcnt lgkmcnt(3)\n\t" \
  "buffer_store_dwordx4 v[24:27], v34, s[20:23], s36 offen sc1\n\t" \
  "v_add_u32_sdwa v8, s29, v33 dst_sel:DWORD dst_unused:UNUSED_PAD src0_sel:DWORD src1_sel:WORD_0\n\t" \
  "v_smfmac_f32_16x16x64_bf16 v[44:47], a[152:155], v[126:133], v159\n\t" \
  "s_lshr_b32 s28, s51, 4\n\t" \
  "ds_read_b64 v[22:23], v22 offset:0\n\t" \
  "v_xad_u32 v24, v187, 64, s38\n\t" \
  "v_smfmac_f32_16x16x64_bf16 v[48:51], a[156:159], v[126:133], v159 abid:1\n\t" \
  "s_add_u32 s28, s28, s72\n\t" \
  "ds_read_b128 v[24:27], v24 offset:5120\n\t" \
  "v_add_u32_sdwa v10, s29, v33 dst_sel:DWORD dst_unused:UNUSED_PAD src0_sel:DWORD src1_sel:WORD_1\n\t" \
  "v_smfmac_f32_16x16x64_bf16 v[52:55], a[160:163], v[126:133], v160\n\t" \
  "s_sub_u32 s28, s28, s101\n\t" \
  "ds_read_b64 v[0:1], v0 offset:0\n\t" \
  "v_add_u32 v28, s60, v187\n\t" \
  "v_smfmac_f32_16x16x64_bf16 v[56:59], a[164:167], v[126:133], v160 abid:1\n\t" \
  "s_max_u32 s52, s52, s28\n\t" \
  "ds_read_b64 v[2:3], v2 offset:0\n\t" \
  "v_smfmac_f32_16x16x64_bf16 v[60:63], a[168:171], v[126:133], v161\n\t" \
  "s_add_u32 s36, s36, 4608\n\t" \
  "s_waitcnt lgkmcnt(3)\n\t" \
  "ds_read_b64 v[4:5], v4 offset:0\n\t" \
  "v_smfmac_f32_16x16x64_bf16 v[52:55], v[88:91], v[16:23], v182 abid:1\n\t" \
  "s_waitcnt lgkmcnt(3)\n\t" \
  "buffer_store_dwordx4 v[24:27], v34, s[20:23], s36 offen sc1\n\t" \
  "v_add_u32_sdwa v16, s29, v120 dst_sel:DWORD dst_unused:UNUSED_PAD src0_sel:DWORD src1_sel:WORD_0\n\t" \
  "v_smfmac_f32_16x16x64_bf16 v[64:67], a[172:175], v[126:133], v161 abid:1\n\t" \
  "s_sub_u32 s51, s51, 96\n\t" \
  "ds_read_b64 v[6:7], v6 offset:0\n\t" \
  "v_add_u32 v24, s38, v187\n\t" \
  "v_smfmac_f32_16x16x64_bf16 v[68:71], a[176:179], v[126:133], v180\n\t" \
  "s_max_u32 s51, s51, s53\n\t" \
  "ds_read_b128 v[24:27], v24 offset:6144\n\t" \
  "v_add_u32_sdwa v18, s29, v120 dst_sel:DWORD dst_unused:UNUSED_PAD src0_sel:DWORD src1_sel:WORD_1\n\t" \
  "v_smfmac_f32_16x16x64_bf16 v[36:39], a[180:183], v[134:141], v162\n\t" \
  "ds_read_b128 v[126:129], v183 offset:12288\n\t" \
  "v_add_u32_sdwa v20, s29, v121 dst_sel:DWORD dst_unused:UNUSED_PAD src0_sel:DWORD src1_sel:WORD_0\n\t" \
  "v_smfmac_f32_16x16x64_bf16 v[40:43], a[184:187], v[134:141], v162 abid:1\n\t" \
  "ds_read_b128 v[130:133], v184 offset:12288\n\t" \
  "v_add_u32_sdwa v22, s29, v121 dst_sel:DWORD dst_unused:UNUSED_PAD src0_sel:DWORD src1_sel:WORD_1\n\t" \
  "v_smfmac_f32_16x16x64_bf16 v[44:47], a[188:191], v[134:141], v163\n\t" \
  "s_add_u32 s36, s36, 4608\n\t" \
  "s_waitcnt lgkmcnt(3)\n\t" \
  "ds_read_b64 v[8:9], v8 offset:0\n\t" \
  "v_smfmac_f32_16x16x64_bf16 v[56:59], v[92:95], v[0:7], v182 abid:1\n\t" \
  "s_waitcnt lgkmcnt(3)\n\t" \
  "buffer_store_dwordx4 v[24:27], v34, s[20:23], s36 offen sc1\n\t" \
  "v_add_u32_sdwa v0, s29, v122 dst_sel:DWORD dst_unused:UNUSED_PAD src0_sel:DWORD src1_sel:WORD_0\n\t" \
  "v_smfmac_f32_16x16x64_bf16 v[48:51], a[192:195], v[134:141], v163 abid:1\n\t" \
  "ds_read_b64 v[10:11], v10 offset:0\n\t" \
  "v_xad_u32 v24, v187, 64, s38\n\t" \
  "v_smfmac_f32_16x16x64_bf16 v[52:55], a[196:199], v[134:141], v164\n\t" \
  "s_add_u32 s38, s38, 18432\n\t" \
  "ds_read_b128 v[24:27], v24 offset:7168\n\t" \
  "v_add_u32_sdwa v2, s29, v122 dst_sel:DWORD dst_unused:UNUSED_PAD src0_sel:DWORD src1_sel:WORD_1\n\t" \
  "v_smfmac_f32_16x16x64_bf16 v[56:59], a[200:203], v[134:141], v164 abid:1\n\t" \
  "s_sub_u32 s44, s38, 129024\n\t" \
  "ds_read_b128 v[28:31], v28 offset:1280\n\t" \
  "v_add_u32_sdwa v4, s29, v123 dst_sel:DWORD dst_unused:UNUSED_PAD src0_sel:DWORD src1_sel:WORD_0\n\t" \
  "v_smfmac_f32_16x16x64_bf16 v[60:63], a[204:207], v[134:141], v165\n\t" \
  "s_min_u32 s38, s38, s44\n\t" \
  "ds_read_b64 v[16:17], v16 offset:0\n\t" \
  "v_add_u32_sdwa v6, s29, v123 dst_sel:DWORD dst_unused:UNUSED_PAD src0_sel:DWORD src1_sel:WORD_1\n\t" \
  "v_smfmac_f32_16x16x64_bf16 v[64:67], a[208:211], v[134:141], v165 abid:1\n\t" \
  "s_add_u32 s36, s36, 4608\n\t" \
  "ds_read_b64 v[18:19], v18 offset:0\n\t" \
  "v_smfmac_f32_16x16x64_bf16 v[68:71], a[212:215], v[134:141], v180 abid:1\n\t" \
  "s_waitcnt lgkmcnt(3)\n\t" \
  "buffer_store_dwordx4 v[24:27], v34, s[20:23], s36 offen sc1\n\t" \
  "v_smfmac_f32_16x16x64_bf16 v[36:39], a[216:219], v[126:133], v166\n\t" \
  "s_waitcnt lgkmcnt(2)\n\t" \
  "ds_read_b128 v[134:137], v183 offset:14336\n\t" \
  "v_add_u32 v24, s43, v187\n\t" \
  "s_set_gpr_idx_on s7, gpr_idx(DST)\n\t" \
  "v_smfmac_f32_16x16x64_bf16 v[36:39], v[28:31], v[8:15], v182 abid:1\n\t" \
  "s_set_gpr_idx_off\n\t" \
  "ds_read_b128 v[24:27], v24 offset:16384\n\t" \
  "v_add_u32_sdwa v8, s29, v124 dst_sel:DWORD dst_unused:UNUSED_PAD src0_sel:DWORD src1_sel:WORD_0\n\t" \
  "v_smfmac_f32_16x16x64_bf16 v[40:43], a[220:223], v[126:133], v166 abid:1\n\t" \
  "s_add_u32 s35, s35, 4718592\n\t" \
  "ds_read_b128 v[138:141], v184 offset:14336\n\t" \
  "v_add_u32_sdwa v10, s29, v124 dst_sel:DWORD dst_unused:UNUSED_PAD src0_sel:DWORD src1_sel:WORD_1\n\t" \
  "v_smfmac_f32_16x16x64_bf16 v[44:47], a[224:227], v[126:133], v167\n\t" \
  "ds_read_b64 v[20:21], v20 offset:0\n\t" \
  "v_add_u32_sdwa v12, s29, v125 dst_sel:DWORD dst_unused:UNUSED_PAD src0_sel:DWORD src1_sel:WORD_0\n\t" \
  "v_smfmac_f32_16x16x64_bf16 v[48:51], a[228:231], v[126:133], v167 abid:1\n\t" \
  "ds_read_b64 v[22:23], v22 offset:0\n\t" \
  "v_add_u32_sdwa v14, s29, v125 dst_sel:DWORD dst_unused:UNUSED_PAD src0_sel:DWORD src1_sel:WORD_1\n\t" \
  "v_smfmac_f32_16x16x64_bf16 v[52:55], a[232:235], v[126:133], v168\n\t" \
  "s_waitcnt lgkmcnt(3)\n\t" \
  "buffer_store_dwordx4 v[24:27], v186, s[20:23], s40 offen offset:384 sc1\n\t" \
  "v_smfmac_f32_16x16x64_bf16 v[56:59], a[236:239], v[126:133], v168 abid:1\n\t" \
  "s_add_u32 s29, s29, s30\n\t" \
  "ds_read_b64 v[0:1], v0 offset:0\n\t" \
  "v_smfmac_f32_16x16x64_bf16 v[60:63], a[240:243], v[126:133], v169\n\t" \
  "s_add_u32 s43, s43, 18432\n\t" \
  "ds_read_b64 v[2:3], v2 offset:0\n\t" \
  "v_smfmac_f32_16x16x64_bf16 v[64:67], a[244:247], v[126:133], v169 abid:1\n\t" \
  "s_waitcnt lgkmcnt(2)\n\t" \
  "ds_read_b64 v[4:5], v4 offset:0\n\t" \
  "v_smfmac_f32_16x16x64_bf16 v[60:63], v[96:99], v[16:23], v182 abid:1\n\t" \
  "s_sub_u32 s42, s43, 129024\n\t" \
  "ds_read_b64 v[6:7], v6 offset:0\n\t" \
  "v_smfmac_f32_16x16x64_bf16 v[68:71], a[248:251], v[126:133], v181\n\t" \
  "s_min_u32 s43, s43, s42\n\t" \
  "ds_read_b64 v[8:9], v8 offset:0\n\t" \
  "v_smfmac_f32_16x16x64_bf16 v[36:39], a[252:255], v[134:141], v170\n\t" \
  "s_add_u32 s40, s40, 4718592\n\t" \
  "ds_read_b64 v[10:11], v10 offset:0\n\t" \
  "v_smfmac_f32_16x16x64_bf16 v[40:43], v[188:191], v[134:141], v170 abid:1\n\t" \
  "ds_read_b64 v[12:13], v12 offset:0\n\t" \
  "v_smfmac_f32_16x16x64_bf16 v[44:47], v[192:195], v[134:141], v171\n\t" \
  "s_waitcnt lgkmcnt(3)\n\t" \
  "ds_read_b64 v[14:15], v14 offset:0\n\t" \
  "v_smfmac_f32_16x16x64_bf16 v[64:67], v[100:103], v[0:7], v182 abid:1\n\t" \
  "ds_read_b128 v[0:3], v183 offset:16384\n\t" \
  "v_smfmac_f32_16x16x64_bf16 v[48:51], v[196:199], v[134:141], v171 abid:1\n\t" \
  "ds_read_b128 v[4:7], v184 offset:16384\n\t" \
  "v_add_u32 v183, s30, v183\n\t" \
  "v_smfmac_f32_16x16x64_bf16 v[52:55], v[200:203], v[134:141], v172\n\t" \
  "s_memrealtime s[72:73]\n\t" \
  "v_add_u32 v184, s30, v184\n\t" \
  "v_smfmac_f32_16x16x64_bf16 v[56:59], v[204:207], v[134:141], v172 abid:1\n\t" \
  "s_waitcnt lgkmcnt(2)\n\t" \
  "v_smfmac_f32_16x16x64_bf16 v[68:71], v[104:107], v[8:15], v182 abid:1\n\t" \
  "v_smfmac_f32_16x16x64_bf16 v[60:63], v[208:211], v[134:141], v173\n\t" \
  "v_smfmac_f32_16x16x64_bf16 v[64:67], v[212:215], v[134:141], v173 abid:1\n\t" \
  "v_smfmac_f32_16x16x64_bf16 v[68:71], v[216:219], v[134:141], v181 abid:1\n\t" \
  "s_setpc_b64 s[66:67]\n\t" \
  ".Lls12%=:\n\t" \
  "s_memrealtime s[72:73]\n\t" \
  "s_waitcnt lgkmcnt(0)\n\t" \
  "s_sub_i32 s28, s72, s52\n\t" \
  "s_cmp_lt_i32 s28, 0\n\t" \
  "s_cbranch_scc1 .Lls12%=\n\t" \
  "s_branch .Llg12%=\n\t" \
  ".fill (.Lvar%=+20480-.)/4, 4, 0xbf800000\n\t" \
  "s_branch .Lfin%=\n\t" \
  "s_branch .Lhe%=\n\t" \
  "s_waitcnt vmcnt(6)\n\t" \
  "s_waitcnt vmcnt(10)\n\t" \
  "s_waitcnt vmcnt(12)\n\t" \
  "s_waitcnt vmcnt(17)\n\t" \
  "s_waitcnt vmcnt(21) lgkmcnt(0)\n\t" \
  "s_cmp_lt_u32 s72, s52\n\t" \
  "s_cbranch_scc1 .Lns13%=\n\t" \
  ".Lng13%=:\n\t" \
  "s_barrier\n\t" \
  "v_smfmac_f32_16x16x64_bf16 v[36:39], v[220:223], v[0:7], v174\n\t" \
  "ds_read_b128 v[126:129], v183 offset:0\n\t" \
  "v_add_u32 v24, s37, v185\n\t" \
  "v_lshrrev_b32 v28, 4, v187\n\t" \
  "v_smfmac_f32_16x16x64_bf16 v[40:43], v[224:227], v[0:7], v174 abid:1\n\t" \
  "s_movk_i32 s53, 1952\n\t" \
  "ds_read_b128 v[130:133], v184 offset:0\n\t" \
  "v_xor_b32 v25, s41, v183\n\t" \
  "v_and_or_b32 v28, v28, 48, s45\n\t" \
  "v_smfmac_f32_16x16x64_bf16 v[44:47], v[228:231], v[0:7], v175\n\t" \
  "s_mov_b32 s51, s53\n\t" \
  "ds_read_b128 v[134:137], v183 offset:2048\n\t" \
  "v_subrev_u32 v25, s30, v25\n\t" \
  "v_smfmac_f32_16x16x64_bf16 v[48:51], v[232:235], v[0:7], v175 abid:1\n\t" \
  "s_add_u32 s30, s29, 18432\n\t" \
  "ds_read_b128 v[138:141], v184 offset:2048\n\t" \
  "v_cvt_pk_bf16_f32 v8, v36, v37\n\t" \
  "v_cvt_pk_bf16_f32 v9, v38, v39\n\t" \
  "v_smfmac_f32_16x16x64_bf16 v[52:55], v[236:239], v[0:7], v176\n\t" \
  "s_sub_u32 s25, s30, 129024\n\t" \
  "ds_read_b128 v[36:39], v28 offset:0\n\t" \
  "v_cvt_pk_bf16_f32 v10, v40, v41\n\t" \
  "v_cvt_pk_bf16_f32 v11, v42, v43\n\t" \
  "v_smfmac_f32_16x16x64_bf16 v[56:59], v[240:243], v[0:7], v176 abid:1\n\t" \
  "s_min_u32 s30, s30, s25\n\t" \
  "ds_read_b128 v[40:43], v28 offset:64\n\t" \
  "v_cvt_pk_bf16_f32 v16, v44, v45\n\t" \
  "v_cvt_pk_bf16_f32 v17, v46, v47\n\t" \
  "v_smfmac_f32_16x16x64_bf16 v[60:63], v[244:247], v[0:7], v177\n\t" \
  "s_sub_u32 s30, s30, s29\n\t" \
  "ds_read_b128 v[44:47], v28 offset:128\n\t" \
  "v_cvt_pk_bf16_f32 v18, v48, v49\n\t" \
  "v_cvt_pk_bf16_f32 v19, v50, v51\n\t" \
  "v_smfmac_f32_16x16x64_bf16 v[64:67], v[248:251], v[0:7], v177 abid:1\n\t" \
  "s_mov_b32 s37, s29\n\t" \
  "ds_read_b128 v[48:51], v28 offset:192\n\t" \
  "v_cvt_pk_bf16_f32 v12, v52, v53\n\t" \
  "v_cvt_pk_bf16_f32 v13, v54, v55\n\t" \
  "v_smfmac_f32_16x16x64_bf16 v[68:71], v[252:255], v[0:7], v182\n\t" \
  "s_waitcnt lgkmcnt(3)\n\t" \
  "ds_read_b128 v[52:55], v28 offset:256\n\t" \
  "v_cvt_pk_bf16_f32 v14, v56, v57\n\t" \
  "v_cvt_pk_bf16_f32 v15, v58, v59\n\t" \
  "v_smfmac_f32_16x16x64_bf16 v[36:39], a[0:3], v[126:133], v142\n\t" \
  "s_waitcnt lgkmcnt(3)\n\t" \
  "ds_read_b128 v[56:59], v28 offset:320\n\t" \
  "v_cvt_pk_bf16_f32 v20, v60, v61\n\t" \
  "v_cvt_pk_bf16_f32 v21, v62, v63\n\t" \
  "v_smfmac_f32_16x16x64_bf16 v[40:43], a[4:7], v[126:133], v142 abid:1\n\t" \
  "s_waitcnt lgkmcnt(3)\n\t" \
  "ds_read_b128 v[60:63], v28 offset:384\n\t" \
  "v_cvt_pk_bf16_f32 v22, v64, v65\n\t" \
  "v_cvt_pk_bf16_f32 v23, v66, v67\n\t" \
  "v_smfmac_f32_16x16x64_bf16 v[44:47], a[8:11], v[126:133], v143\n\t" \
  "s_waitcnt lgkmcnt(3)\n\t" \
  "ds_read_b128 v[64:67], v28 offset:448\n\t" \
  "v_cvt_pk_bf16_f32 v26, v68, v69\n\t" \
  "v_cvt_pk_bf16_f32 v27, v70, v71\n\t" \
  "v_smfmac_f32_16x16x64_bf16 v[48:51], a[12:15], v[126:133], v143 abid:1\n\t" \
  "s_waitcnt lgkmcnt(3)\n\t" \
  "ds_read_b128 v[68:71], v28 offset:512\n\t" \
  "v_add_u32_sdwa v0, s29, v108 dst_sel:DWORD dst_unused:UNUSED_PAD src0_sel:DWORD src1_sel:WORD_0\n\t" \
  "v_smfmac_f32_16x16x64_bf16 v[52:55], a[16:19], v[126:133], v144\n\t" \
  "s_waitcnt lgkmcnt(3)\n\t" \
  "ds_read_b64 v[0:1], v0 offset:0\n\t" \
  "v_add_u32_sdwa v2, s29, v108 dst_sel:DWORD dst_unused:UNUSED_PAD src0_sel:DWORD src1_sel:WORD_1\n\t" \
  "v_smfmac_f32_16x16x64_bf16 v[56:59], a[20:23], v[126:133], v144 abid:1\n\t" \
  "s_waitcnt lgkmcnt(3)\n\t" \
  "s_bfe_i32 s47, s48, 0x10000\n\t" \
  "ds_write_b128 v24, v[8:11] offset:0\n\t" \
  "v_add_u32_sdwa v4, s29, v109 dst_sel:DWORD dst_unused:UNUSED_PAD src0_sel:DWORD src1_sel:WORD_0\n\t" \
  "v_add_u32 v28, s38, v187\n\t" \
  "v_smfmac_f32_16x16x64_bf16 v[60:63], a[24:27], v[126:133], v145\n\t" \
  "s_waitcnt lgkmcnt(3)\n\t" \
  "s_lshr_b32 s48, s48, 1\n\t" \
  "ds_read_b128 v[28:31], v28 offset:4096\n\t" \
  "v_add_u32_sdwa v6, s29, v109 dst_sel:DWORD dst_unused:UNUSED_PAD src0_sel:DWORD src1_sel:WORD_1\n\t" \
  "v_smfmac_f32_16x16x64_bf16 v[64:67], a[28:31], v[126:133], v145 abid:1\n\t" \
  "s_waitcnt lgkmcnt(2)\n\t" \
  "s_add_u32 s36, s35, s39\n\t" \
  "ds_read_b64 v[2:3], v2 offset:0\n\t" \
  "v_smfmac_f32_16x16x64_bf16 v[68:71], a[32:35], v[126:133], v178\n\t" \
  "s_andn2_b32 s44, 0x40000000, s47\n\t" \
  "v_smfmac_f32_16x16x64_bf16 v[36:39], a[36:39], v[134:141], v146\n\t" \
  "s_or_b32 s36, s36, s44\n\t" \
  "ds_write_b128 v24, v[12:15] offset:128\n\t" \
  "v_xor_b32 v24, 64, v24\n\t" \
  "v_smfmac_f32_16x16x64_bf16 v[40:43], a[40:43], v[134:141], v146 abid:1\n\t" \
  "s_waitcnt lgkmcnt(2)\n\t" \
  "buffer_store_dwordx4 v[28:31], v34, s[20:23], s36 offen sc1\n\t" \
  "v_add_u32_sdwa v8, s29, v110 dst_sel:DWORD dst_unused:UNUSED_PAD src0_sel:DWORD src1_sel:WORD_0\n\t" \
  "v_smfmac_f32_16x16x64_bf16 v[44:47], a[44:47], v[134:141], v147\n\t" \
  "s_and_b32 s44, s47, 4718592\n\t" \
  "ds_read_b128 v[126:129], v183 offset:4096\n\t" \
  "v_add_u32_sdwa v10, s29, v110 dst_sel:DWORD dst_unused:UNUSED_PAD src0_sel:DWORD src1_sel:WORD_1\n\t" \
  "v_xad_u32 v28, v187, 64, s38\n\t" \
  "v_smfmac_f32_16x16x64_bf16 v[48:51], a[48:51], v[134:141], v147 abid:1\n\t" \
  "s_add_u32 s35, s35, s44\n\t" \
  "ds_read_b128 v[28:31], v28 offset:5120\n\t" \
  "v_add_u32_sdwa v12, s29, v111 dst_sel:DWORD dst_unused:UNUSED_PAD src0_sel:DWORD src1_sel:WORD_0\n\t" \
  "v_smfmac_f32_16x16x64_bf16 v[52:55], a[52:55], v[134:141], v148\n\t" \
  "s_add_u32 s36, s36, 4608\n\t" \
  "ds_write_b128 v24, v[16:19] offset:0\n\t" \
  "v_add_u32_sdwa v14, s29, v111 dst_sel:DWORD dst_unused:UNUSED_PAD src0_sel:DWORD src1_sel:WORD_1\n\t" \
  "v_smfmac_f32_16x16x64_bf16 v[56:59], a[56:59], v[134:141], v148 abid:1\n\t" \
  "ds_read_b128 v[130:133], v184 offset:4096\n\t" \
  "v_smfmac_f32_16x16x64_bf16 v[60:63], a[60:63], v[134:141], v149\n\t" \
  "ds_read_b64 v[4:5], v4 offset:0\n\t" \
  "v_smfmac_f32_16x16x64_bf16 v[64:67], a[64:67], v[134:141], v149 abid:1\n\t" \
  "ds_read_b64 v[6:7], v6 offset:0\n\t" \
  "s_waitcnt lgkmcnt(4)\n\t" \
  "buffer_store_dwordx4 v[28:31], v34, s[20:23], s36 offen sc1\n\t" \
  "v_smfmac_f32_16x16x64_bf16 v[68:71], a[68:71], v[134:141], v178 abid:1\n\t" \
  "s_waitcnt lgkmcnt(2)\n\t" \
  "s_and_b32 s44, s47, 18432\n\t" \
  "ds_write_b128 v24, v[20:23] offset:128\n\t" \
  "v_add_u32_sdwa v16, s29, v112 dst_sel:DWORD dst_unused:UNUSED_PAD src0_sel:DWORD src1_sel:WORD_0\n\t" \
  "v_add_u32 v28, s38, v187\n\t" \
  "v_smfmac_f32_16x16x64_bf16 v[36:39], a[72:75], v[126:133], v150\n\t" \
  "ds_read_b128 v[134:137], v183 offset:6144\n\t" \
  "ds_read_b128 v[28:31], v28 offset:6144\n\t" \
  "v_add_u32_sdwa v18, s29, v112 dst_sel:DWORD dst_unused:UNUSED_PAD src0_sel:DWORD src1_sel:WORD_1\n\t" \
  "v_smfmac_f32_16x16x64_bf16 v[40:43], a[76:79], v[126:133], v150 abid:1\n\t" \
  "s_and_b32 s27, s64, 7\n\t" \
  "s_add_u32 s36, s36, 4608\n\t" \
  "ds_read_b128 v[138:141], v184 offset:6144\n\t" \
  "v_add_u32_sdwa v20, s29, v113 dst_sel:DWORD dst_unused:UNUSED_PAD src0_sel:DWORD src1_sel:WORD_0\n\t" \
  "v_smfmac_f32_16x16x64_bf16 v[44:47], a[80:83], v[126:133], v151\n\t" \
  "s_waitcnt lgkmcnt(3)\n\t" \
  "ds_read_b64 v[8:9], v8 offset:0\n\t" \
  "v_add_u32_sdwa v22, s29, v113 dst_sel:DWORD dst_unused:UNUSED_PAD src0_sel:DWORD src1_sel:WORD_1\n\t" \
  "v_smfmac_f32_16x16x64_bf16 v[36:39], v[72:75], v[0:7], v182 abid:1\n\t" \
  "s_lshl2_add_u32 s66, s27, s68\n\t" \
  "ds_write_b64 v25, v[26:27] offset:16384\n\t" \
  "v_add_u32_sdwa v0, s29, v114 dst_sel:DWORD dst_unused:UNUSED_PAD src0_sel:DWORD src1_sel:WORD_0\n\t" \
  "v_smfmac_f32_16x16x64_bf16 v[48:51], a[84:87], v[126:133], v151 abid:1\n\t" \
  "s_and_b32 s27, s64, 8\n\t" \
  "ds_read_b64 v[10:11], v10 offset:0\n\t" \
  "ds_read_b64 v[12:13], v12 offset:0\n\t" \
  "s_waitcnt lgkmcnt(5)\n\t" \
  "buffer_store_dwordx4 v[28:31], v34, s[20:23], s36 offen sc1\n\t" \
  "v_add_u32_sdwa v2, s29, v114 dst_sel:DWORD dst_unused:UNUSED_PAD src0_sel:DWORD src1_sel:WORD_1\n\t" \
  "v_smfmac_f32_16x16x64_bf16 v[52:55], a[88:91], v[126:133], v152\n\t" \
  "s_mul_i32 s27, s27, 176\n\t" \
  "ds_read_b64 v[14:15], v14 offset:0\n\t" \
  "ds_read_b64 v[16:17], v16 offset:0\n\t" \
  "v_add_u32_sdwa v4, s29, v115 dst_sel:DWORD dst_unused:UNUSED_PAD src0_sel:DWORD src1_sel:WORD_0\n\t" \
  "v_xad_u32 v28, v187, 64, s38\n\t" \
  "v_smfmac_f32_16x16x64_bf16 v[56:59], a[92:95], v[126:133], v152 abid:1\n\t" \
  "s_add_u32 s66, s66, s27\n\t" \
  "s_add_u32 s38, s38, s44\n\t" \
  "ds_read_b64 v[18:19], v18 offset:0\n\t" \
  "ds_read_b64 v[20:21], v20 offset:0\n\t" \
  "ds_read_b128 v[28:31], v28 offset:7168\n\t" \
  "v_add_u32_sdwa v6, s29, v115 dst_sel:DWORD dst_unused:UNUSED_PAD src0_sel:DWORD src1_sel:WORD_1\n\t" \
  "v_smfmac_f32_16x16x64_bf16 v[60:63], a[96:99], v[126:133], v153\n\t" \
  "s_lshr_b64 s[64:65], s[64:65], 4\n\t" \
  "s_add_u32 s36, s36, 4608\n\t" \
  "ds_read_b64 v[22:23], v22 offset:0\n\t" \
  "ds_read_b64 v[0:1], v0 offset:0\n\t" \
  "v_smfmac_f32_16x16x64_bf16 v[64:67], a[100:103], v[126:133], v153 abid:1\n\t" \
  "s_sub_u32 s28, s72, s46\n\t" \
  "s_sub_u32 s44, s38, 129024\n\t" \
  "ds_read_b64 v[2:3], v2 offset:0\n\t" \
  "v_smfmac_f32_16x16x64_bf16 v[68:71], a[104:107], v[126:133], v179\n\t" \
  "s_waitcnt lgkmcnt(6)\n\t" \
  "s_min_u32 s38, s38, s44\n\t" \
  "ds_read_b128 v[126:129], v183 offset:8192\n\t" \
  "v_smfmac_f32_16x16x64_bf16 v[40:43], v[76:79], v[8:15], v182 abid:1\n\t" \
  "s_add_u32 s46, s72, 185\n\t" \
  "ds_read_b128 v[130:133], v184 offset:8192\n\t" \
  "s_waitcnt lgkmcnt(5)\n\t" \
  "buffer_store_dwordx4 v[28:31], v34, s[20:23], s36 offen sc1\n\t" \
  "v_smfmac_f32_16x16x64_bf16 v[36:39], a[108:111], v[134:141], v154\n\t" \
  "s_waitcnt lgkmcnt(4)\n\t" \
  "ds_read_b64 v[4:5], v4 offset:0\n\t" \
  "v_add_u32_sdwa v8, s29, v32 dst_sel:DWORD dst_unused:UNUSED_PAD src0_sel:DWORD src1_sel:WORD_0\n\t" \
  "v_smfmac_f32_16x16x64_bf16 v[44:47], v[80:83], v[16:23], v182 abid:1\n\t" \
  "s_branch .Ljn13%=\n\t" \
  ".Lns13%=:\n\t" \
  "s_memrealtime s[72:73]\n\t" \
  "s_waitcnt lgkmcnt(0)\n\t" \
  "s_sub_i32 s28, s72, s52\n\t" \
  "s_cmp_lt_i32 s28, 0\n\t" \
  "s_cbranch_scc1 .Lns13%=\n\t" \
  "s_branch .Lng13%=\n\t" \
  ".fill (.Lvar%=+21884-.)/4, 4, 0xbf800000\n\t" \
  "s_nop 0\n\t" \
  "s_branch .Lfin%=\n\t" \
  "s_branch .Lhe%=\n\t" \
  "s_waitcnt vmcnt(6)\n\t" \
  "s_waitcnt vmcnt(10)\n\t" \
  "s_waitcnt vmcnt(12)\n\t" \
  "s_waitcnt vmcnt(17)\n\t" \
  "s_waitcnt vmcnt(21) lgkmcnt(0)\n\t" \
  "s_cmp_lt_u32 s72, s52\n\t" \
  "s_cbranch_scc1 .Lls13%=\n\t" \
  ".Llg13%=:\n\t" \
  "s_barrier\n\t" \
  "v_smfmac_f32_16x16x64_bf16 v[36:39], v[220:223], v[0:7], v174\n\t" \
  "ds_read_b128 v[126:129], v183 offset:0\n\t" \
  "v_add_u32 v24, s37, v185\n\t" \
  "v_lshrrev_b32 v28, 4, v187\n\t" \
  "v_smfmac_f32_16x16x64_bf16 v[40:43], v[224:227], v[0:7], v174 abid:1\n\t" \
  "ds_read_b128 v[130:133], v184 offset:0\n\t" \
  "v_xor_b32 v25, s41, v183\n\t" \
  "v_and_or_b32 v28, v28, 48, s45\n\t" \
  "v_smfmac_f32_16x16x64_bf16 v[44:47], v[228:231], v[0:7], v175\n\t" \
  "ds_read_b128 v[134:137], v183 offset:2048\n\t" \
  "v_subrev_u32 v25, s30, v25\n\t" \
  "v_smfmac_f32_16x16x64_bf16 v[48:51], v[232:235], v[0:7], v175 abid:1\n\t" \
  "s_add_u32 s30, s29, 18432\n\t" \
  "ds_read_b128 v[138:141], v184 offset:2048\n\t" \
  "v_cvt_pk_bf16_f32 v8, v36, v37\n\t" \
  "v_cvt_pk_bf16_f32 v9, v38, v39\n\t" \
  "v_smfmac_f32_16x16x64_bf16 v[52:55], v[236:239], v[0:7], v176\n\t" \
  "s_sub_u32 s25, s30, 129024\n\t" \
  "ds_read_b128 v[36:39], v28 offset:0\n\t" \
  "v_cvt_pk_bf16_f32 v10, v40, v41\n\t" \
  "v_cvt_pk_bf16_f32 v11, v42, v43\n\t" \
  "v_smfmac_f32_16x16x64_bf16 v[56:59], v[240:243], v[0:7], v176 abid:1\n\t" \
  "s_min_u32 s30, s30, s25\n\t" \
  "ds_read_b128 v[40:43], v28 offset:64\n\t" \
  "v_cvt_pk_bf16_f32 v16, v44, v45\n\t" \
  "v_cvt_pk_bf16_f32 v17, v46, v47\n\t" \
  "v_smfmac_f32_16x16x64_bf16 v[60:63], v[244:247], v[0:7], v177\n\t" \
  "s_sub_u32 s30, s30, s29\n\t" \
  "ds_read_b128 v[44:47], v28 offset:128\n\t" \
  "v_cvt_pk_bf16_f32 v18, v48, v49\n\t" \
  "v_cvt_pk_bf16_f32 v19, v50, v51\n\t" \
  "v_smfmac_f32_16x16x64_bf16 v[64:67], v[248:251], v[0:7], v177 abid:1\n\t" \
  "s_mov_b32 s37, s29\n\t" \
  "ds_read_b128 v[48:51], v28 offset:192\n\t" \
  "v_cvt_pk_bf16_f32 v12, v52, v53\n\t" \
  "v_cvt_pk_bf16_f32 v13, v54, v55\n\t" \
  "v_smfmac_f32_16x16x64_bf16 v[68:71], v[252:255], v[0:7], v182\n\t" \
  "s_waitcnt lgkmcnt(3)\n\t" \
  "ds_read_b128 v[52:55], v28 offset:256\n\t" \
  "v_cvt_pk_bf16_f32 v14, v56, v57\n\t" \
  "v_cvt_pk_bf16_f32 v15, v58, v59\n\t" \
  "v_smfmac_f32_16x16x64_bf16 v[36:39], a[0:3], v[126:133], v142\n\t" \
  "s_waitcnt lgkmcnt(3)\n\t" \
  "ds_read_b128 v[56:59], v28 offset:320\n\t" \
  "v_cvt_pk_bf16_f32 v20, v60, v61\n\t" \
  "v_cvt_pk_bf16_f32 v21, v62, v63\n\t" \
  "v_smfmac_f32_16x16x64_bf16 v[40:43], a[4:7], v[126:133], v142 abid:1\n\t" \
  "s_waitcnt lgkmcnt(3)\n\t" \
  "ds_read_b128 v[60:63], v28 offset:384\n\t" \
  "v_cvt_pk_bf16_f32 v22, v64, v65\n\t" \
  "v_cvt_pk_bf16_f32 v23, v66, v67\n\t" \
  "v_smfmac_f32_16x16x64_bf16 v[44:47], a[8:11], v[126:133], v143\n\t" \
  "s_waitcnt lgkmcnt(3)\n\t" \
  "ds_read_b128 v[64:67], v28 offset:448\n\t" \
  "v_cvt_pk_bf16_f32 v26, v68, v69\n\t" \
  "v_cvt_pk_bf16_f32 v27, v70, v71\n\t" \
  "v_smfmac_f32_16x16x64_bf16 v[48:51], a[12:15], v[126:133], v143 abid:1\n\t" \
  "s_waitcnt lgkmcnt(3)\n\t" \
  "ds_read_b128 v[68:71], v28 offset:512\n\t" \
  "v_add_u32_sdwa v0, s29, v108 dst_sel:DWORD dst_unused:UNUSED_PAD src0_sel:DWORD src1_sel:WORD_0\n\t" \
  "v_smfmac_f32_16x16x64_bf16 v[52:55], a[16:19], v[126:133], v144\n\t" \
  "s_waitcnt lgkmcnt(3)\n\t" \
  "ds_read_b64 v[0:1], v0 offset:0\n\t" \
  "v_add_u32_sdwa v2, s29, v108 dst_sel:DWORD dst_unused:UNUSED_PAD src0_sel:DWORD src1_sel:WORD_1\n\t" \
  "v_smfmac_f32_16x16x64_bf16 v[56:59], a[20:23], v[126:133], v144 abid:1\n\t" \
  "s_add_u32 m0, s33, 0\n\t" \
  "s_waitcnt lgkmcnt(3)\n\t" \
  "ds_write_b128 v24, v[8:11] offset:0\n\t" \
  "v_add_u32_sdwa v4, s29, v109 dst_sel:DWORD dst_unused:UNUSED_PAD src0_sel:DWORD src1_sel:WORD_0\n\t" \
  "v_smfmac_f32_16x16x64_bf16 v[60:63], a[24:27], v[126:133], v145\n\t" \
  "s_waitcnt lgkmcnt(3)\n\t" \
  "buffer_load_dwordx4 v186, s[16:19], s34 offen offset:0 nt lds\n\t" \
  "v_add_u32_sdwa v6, s29, v109 dst_sel:DWORD dst_unused:UNUSED_PAD src0_sel:DWORD src1_sel:WORD_1\n\t" \
  "v_smfmac_f32_16x16x64_bf16 v[64:67], a[28:31], v[126:133], v145 abid:1\n\t" \
  "s_add_u32 m0, s33, 1920\n\t" \
  "s_waitcnt lgkmcnt(2)\n\t" \
  "ds_read_b64 v[2:3], v2 offset:0\n\t" \
  "v_smfmac_f32_16x16x64_bf16 v[68:71], a[32:35], v[126:133], v178\n\t" \
  "buffer_load_dwordx4 v186, s[16:19], s34 offen offset:128 nt lds\n\t" \
  "v_smfmac_f32_16x16x64_bf16 v[36:39], a[36:39], v[134:141], v146\n\t" \
  "s_add_u32 m0, s33, 3840\n\t" \
  "ds_write_b128 v24, v[12:15] offset:128\n\t" \
  "v_xor_b32 v24, 64, v24\n\t" \
  "v_smfmac_f32_16x16x64_bf16 v[40:43], a[40:43], v[134:141], v146 abid:1\n\t" \
  "buffer_load_dwordx4 v186, s[16:19], s34 offen offset:256 nt lds\n\t" \
  "v_add_u32_sdwa v8, s29, v110 dst_sel:DWORD dst_unused:UNUSED_PAD src0_sel:DWORD src1_sel:WORD_0\n\t" \
  "v_smfmac_f32_16x16x64_bf16 v[44:47], a[44:47], v[134:141], v147\n\t" \
  "s_add_u32 m0, s33, 5760\n\t" \
  "ds_read_b128 v[126:129], v183 offset:4096\n\t" \
  "v_add_u32_sdwa v10, s29, v110 dst_sel:DWORD dst_unused:UNUSED_PAD src0_sel:DWORD src1_sel:WORD_1\n\t" \
  "v_smfmac_f32_16x16x64_bf16 v[48:51], a[48:51], v[134:141], v147 abid:1\n\t" \
  "buffer_load_dwordx4 v186, s[16:19], s34 offen offset:384 nt lds\n\t" \
  "v_add_u32_sdwa v12, s29, v111 dst_sel:DWORD dst_unused:UNUSED_PAD src0_sel:DWORD src1_sel:WORD_0\n\t" \
  "v_smfmac_f32_16x16x64_bf16 v[52:55], a[52:55], v[134:141], v148\n\t" \
  "s_add_u32 s31, s31, 18432\n\t" \
  "ds_write_b128 v24, v[16:19] offset:0\n\t" \
  "v_add_u32_sdwa v14, s29, v111 dst_sel:DWORD dst_unused:UNUSED_PAD src0_sel:DWORD src1_sel:WORD_1\n\t" \
  "v_smfmac_f32_16x16x64_bf16 v[56:59], a[56:59], v[134:141], v148 abid:1\n\t" \
  "s_sub_u32 s26, s31, 129024\n\t" \
  "ds_read_b128 v[130:133], v184 offset:4096\n\t" \
  "v_smfmac_f32_16x16x64_bf16 v[60:63], a[60:63], v[134:141], v149\n\t" \
  "s_min_u32 s31, s31, s26\n\t" \
  "ds_read_b64 v[4:5], v4 offset:0\n\t" \
  "v_smfmac_f32_16x16x64_bf16 v[64:67], a[64:67], v[134:141], v149 abid:1\n\t" \
  "s_add_u32 s33, s31, s32\n\t" \
  "ds_read_b64 v[6:7], v6 offset:0\n\t" \
  "v_smfmac_f32_16x16x64_bf16 v[68:71], a[68:71], v[134:141], v178 abid:1\n\t" \
  "s_waitcnt lgkmcnt(2)\n\t" \
  "ds_write_b128 v24, v[20:23] offset:128\n\t" \
  "v_add_u32_sdwa v16, s29, v112 dst_sel:DWORD dst_unused:UNUSED_PAD src0_sel:DWORD src1_sel:WORD_0\n\t" \
  "v_smfmac_f32_16x16x64_bf16 v[36:39], a[72:75], v[126:133], v150\n\t" \
  "s_add_u32 s34, s34, 4718592\n\t" \
  "ds_read_b128 v[134:137], v183 offset:6144\n\t" \
  "v_add_u32_sdwa v18, s29, v112 dst_sel:DWORD dst_unused:UNUSED_PAD src0_sel:DWORD src1_sel:WORD_1\n\t" \
  "v_smfmac_f32_16x16x64_bf16 v[40:43], a[76:79], v[126:133], v150 abid:1\n\t" \
  "s_and_b32 s27, s64, 7\n\t" \
  "ds_read_b128 v[138:141], v184 offset:6144\n\t" \
  "v_add_u32_sdwa v20, s29, v113 dst_sel:DWORD dst_unused:UNUSED_PAD src0_sel:DWORD src1_sel:WORD_0\n\t" \
  "v_smfmac_f32_16x16x64_bf16 v[44:47], a[80:83], v[126:133], v151\n\t" \
  "s_waitcnt lgkmcnt(3)\n\t" \
  "ds_read_b64 v[8:9], v8 offset:0\n\t" \
  "v_add_u32_sdwa v22, s29, v113 dst_sel:DWORD dst_unused:UNUSED_PAD src0_sel:DWORD src1_sel:WORD_1\n\t" \
  "v_smfmac_f32_16x16x64_bf16 v[36:39], v[72:75], v[0:7], v182 abid:1\n\t" \
  "s_lshl2_add_u32 s66, s27, s68\n\t" \
  "ds_write_b64 v25, v[26:27] offset:16384\n\t" \
  "v_add_u32_sdwa v0, s29, v114 dst_sel:DWORD dst_unused:UNUSED_PAD src0_sel:DWORD src1_sel:WORD_0\n\t" \
  "v_smfmac_f32_16x16x64_bf16 v[48:51], a[84:87], v[126:133], v151 abid:1\n\t" \
  "s_and_b32 s27, s64, 8\n\t" \
  "ds_read_b64 v[10:11], v10 offset:0\n\t" \
  "ds_read_b64 v[12:13], v12 offset:0\n\t" \
  "v_add_u32_sdwa v2, s29, v114 dst_sel:DWORD dst_unused:UNUSED_PAD src0_sel:DWORD src1_sel:WORD_1\n\t" \
  "v_smfmac_f32_16x16x64_bf16 v[52:55], a[88:91], v[126:133], v152\n\t" \
  "s_mul_i32 s27, s27, 176\n\t" \
  "ds_read_b64 v[14:15], v14 offset:0\n\t" \
  "ds_read_b64 v[16:17], v16 offset:0\n\t" \
  "v_add_u32_sdwa v4, s29, v115 dst_sel:DWORD dst_unused:UNUSED_PAD src0_sel:DWORD src1_sel:WORD_0\n\t" \
  "v_smfmac_f32_16x16x64_bf16 v[56:59], a[92:95], v[126:133], v152 abid:1\n\t" \
  "s_add_u32 s66, s66, s27\n\t" \
  "ds_read_b64 v[18:19], v18 offset:0\n\t" \
  "ds_read_b64 v[20:21], v20 offset:0\n\t" \
  "v_add_u32_sdwa v6, s29, v115 dst_sel:DWORD dst_unused:UNUSED_PAD src0_sel:DWORD src1_sel:WORD_1\n\t" \
  "v_smfmac_f32_16x16x64_bf16 v[60:63], a[96:99], v[126:133], v153\n\t" \
  "s_lshr_b64 s[64:65], s[64:65], 4\n\t" \
  "ds_read_b64 v[22:23], v22 offset:0\n\t" \
  "ds_read_b64 v[0:1], v0 offset:0\n\t" \
  "v_smfmac_f32_16x16x64_bf16 v[64:67], a[100:103], v[126:133], v153 abid:1\n\t" \
  "s_sub_u32 s28, s72, s46\n\t" \
  "ds_read_b64 v[2:3], v2 offset:0\n\t" \
  "v_smfmac_f32_16x16x64_bf16 v[68:71], a[104:107], v[126:133], v179\n\t" \
  "s_waitcnt lgkmcnt(6)\n\t" \
  "ds_read_b128 v[126:129], v183 offset:8192\n\t" \
  "v_smfmac_f32_16x16x64_bf16 v[40:43], v[76:79], v[8:15], v182 abid:1\n\t" \
  "s_add_u32 s46, s72, 185\n\t" \
  "ds_read_b128 v[130:133], v184 offset:8192\n\t" \
  "v_smfmac_f32_16x16x64_bf16 v[36:39], a[108:111], v[134:141], v154\n\t" \
  "s_waitcnt lgkmcnt(4)\n\t" \
  "ds_read_b64 v[4:5], v4 offset:0\n\t" \
  "v_add_u32_sdwa v8, s29, v32 dst_sel:DWORD dst_unused:UNUSED_PAD src0_sel:DWORD src1_sel:WORD_0\n\t" \
  "v_smfmac_f32_16x16x64_bf16 v[44:47], v[80:83], v[16:23], v182 abid:1\n\t" \
  ".Ljn13%=:\n\t" \
  "s_max_i32 s28, s28, 0\n\t" \
  "ds_read_b64 v[6:7], v6 offset:0\n\t" \
  "v_add_u32_sdwa v10, s29, v32 dst_sel:DWORD dst_unused:UNUSED_PAD src0_sel:DWORD src1_sel:WORD_1\n\t" \
  "v_smfmac_f32_16x16x64_bf16 v[40:43], a[112:115], v[134:141], v154 abid:1\n\t" \
  "s_min_u32 s28, s28, 6\n\t" \
  "ds_read_b64 v[8:9], v8 offset:0\n\t" \
  "v_add_u32 v28, s60, v187\n\t" \
  "v_smfmac_f32_16x16x64_bf16 v[44:47], a[116:119], v[134:141], v155\n\t" \
  "s_lshl_b32 s28, s28, 6\n\t" \
  "ds_read_b64 v[10:11], v10 offset:0\n\t" \
  "v_add_u32_sdwa v16, s29, v116 dst_sel:DWORD dst_unused:UNUSED_PAD src0_sel:DWORD src1_sel:WORD_0\n\t" \
  "v_smfmac_f32_16x16x64_bf16 v[48:51], a[120:123], v[134:141], v155 abid:1\n\t" \
  "s_add_u32 s28, s28, s53\n\t" \
  "ds_read_b128 v[28:31], v28 offset:0\n\t" \
  "v_add_u32_sdwa v18, s29, v116 dst_sel:DWORD dst_unused:UNUSED_PAD src0_sel:DWORD src1_sel:WORD_1\n\t" \
  "v_smfmac_f32_16x16x64_bf16 v[52:55], a[124:127], v[134:141], v156\n\t" \
  "s_waitcnt lgkmcnt(3)\n\t" \
  "ds_read_b64 v[16:17], v16 offset:0\n\t" \
  "v_add_u32_sdwa v20, s29, v117 dst_sel:DWORD dst_unused:UNUSED_PAD src0_sel:DWORD src1_sel:WORD_0\n\t" \
  "v_smfmac_f32_16x16x64_bf16 v[48:51], v[84:87], v[0:7], v182 abid:1\n\t" \
  "s_max_u32 s51, s51, s28\n\t" \
  "ds_read_b64 v[18:19], v18 offset:0\n\t" \
  "v_add_u32_sdwa v22, s29, v117 dst_sel:DWORD dst_unused:UNUSED_PAD src0_sel:DWORD src1_sel:WORD_1\n\t" \
  "v_smfmac_f32_16x16x64_bf16 v[56:59], a[128:131], v[134:141], v156 abid:1\n\t" \
  "s_add_u32 s54, s54, s51\n\t" \
  "ds_read_b64 v[20:21], v20 offset:0\n\t" \
  "v_add_u32_sdwa v0, s29, v118 dst_sel:DWORD dst_unused:UNUSED_PAD src0_sel:DWORD src1_sel:WORD_0\n\t" \
  "v_smfmac_f32_16x16x64_bf16 v[60:63], a[132:135], v[134:141], v157\n\t" \
  "s_waitcnt lgkmcnt(3)\n\t" \
  "ds_read_b64 v[22:23], v22 offset:0\n\t" \
  "v_add_u32 v24, s38, v187\n\t" \
  "s_set_gpr_idx_on s6, gpr_idx(DST)\n\t" \
  "v_smfmac_f32_16x16x64_bf16 v[36:39], v[28:31], v[8:15], v182 abid:1\n\t" \
  "s_set_gpr_idx_off\n\t" \
  "ds_read_b128 v[24:27], v24 offset:4096\n\t" \
  "v_add_u32_sdwa v2, s29, v118 dst_sel:DWORD dst_unused:UNUSED_PAD src0_sel:DWORD src1_sel:WORD_1\n\t" \
  "v_smfmac_f32_16x16x64_bf16 v[64:67], a[136:139], v[134:141], v157 abid:1\n\t" \
  "s_lshr_b32 s52, s54, 4\n\t" \
  "ds_read_b64 v[0:1], v0 offset:0\n\t" \
  "v_add_u32_sdwa v4, s29, v119 dst_sel:DWORD dst_unused:UNUSED_PAD src0_sel:DWORD src1_sel:WORD_0\n\t" \
  "v_smfmac_f32_16x16x64_bf16 v[68:71], a[140:143], v[134:141], v179 abid:1\n\t" \
  "s_add_u32 s52, s52, s56\n\t" \
  "ds_read_b128 v[134:137], v183 offset:10240\n\t" \
  "v_add_u32_sdwa v6, s29, v119 dst_sel:DWORD dst_unused:UNUSED_PAD src0_sel:DWORD src1_sel:WORD_1\n\t" \
  "v_smfmac_f32_16x16x64_bf16 v[36:39], a[144:147], v[126:133], v158\n\t" \
  "s_add_u32 s36, s35, s39\n\t" \
  "s_waitcnt lgkmcnt(3)\n\t" \
  "ds_read_b128 v[138:141], v184 offset:10240\n\t" \
  "v_add_u32_sdwa v8, s29, v33 dst_sel:DWORD dst_unused:UNUSED_PAD src0_sel:DWORD src1_sel:WORD_0\n\t" \
  "v_smfmac_f32_16x16x64_bf16 v[52:55], v[88:91], v[16:23], v182 abid:1\n\t" \
  "s_waitcnt lgkmcnt(3)\n\t" \
  "buffer_store_dwordx4 v[24:27], v34, s[20:23], s36 offen sc1\n\t" \
  "v_add_u32_sdwa v10, s29, v33 dst_sel:DWORD dst_unused:UNUSED_PAD src0_sel:DWORD src1_sel:WORD_1\n\t" \
  "v_smfmac_f32_16x16x64_bf16 v[40:43], a[148:151], v[126:133], v158 abid:1\n\t" \
  "s_lshr_b32 s28, s51, 4\n\t" \
  "ds_read_b64 v[2:3], v2 offset:0\n\t" \
  "v_xad_u32 v24, v187, 64, s38\n\t" \
  "v_smfmac_f32_16x16x64_bf16 v[44:47], a[152:155], v[126:133], v159\n\t" \
  "s_add_u32 s28, s28, s72\n\t" \
  "ds_read_b128 v[24:27], v24 offset:5120\n\t" \
  "v_add_u32 v28, s60, v187\n\t" \
  "v_smfmac_f32_16x16x64_bf16 v[48:51], a[156:159], v[126:133], v159 abid:1\n\t" \
  "s_sub_u32 s28, s28, s101\n\t" \
  "ds_read_b64 v[4:5], v4 offset:0\n\t" \
  "v_add_u32_sdwa v16, s29, v120 dst_sel:DWORD dst_unused:UNUSED_PAD src0_sel:DWORD src1_sel:WORD_0\n\t" \
  "v_smfmac_f32_16x16x64_bf16 v[52:55], a[160:163], v[126:133], v160\n\t" \
  "s_max_u32 s52, s52, s28\n\t" \
  "ds_read_b64 v[6:7], v6 offset:0\n\t" \
  "v_add_u32_sdwa v18, s29, v120 dst_sel:DWORD dst_unused:UNUSED_PAD src0_sel:DWORD src1_sel:WORD_1\n\t" \
  "v_smfmac_f32_16x16x64_bf16 v[56:59], a[164:167], v[126:133], v160 abid:1\n\t" \
  "s_add_u32 s36, s36, 4608\n\t" \
  "ds_read_b64 v[8:9], v8 offset:0\n\t" \
  "v_add_u32_sdwa v20, s29, v121 dst_sel:DWORD dst_unused:UNUSED_PAD src0_sel:DWORD src1_sel:WORD_0\n\t" \
  "v_smfmac_f32_16x16x64_bf16 v[60:63], a[168:171], v[126:133], v161\n\t" \
  "s_waitcnt lgkmcnt(3)\n\t" \
  "buffer_store_dwordx4 v[24:27], v34, s[20:23], s36 offen sc1\n\t" \
  "v_add_u32_sdwa v22, s29, v121 dst_sel:DWORD dst_unused:UNUSED_PAD src0_sel:DWORD src1_sel:WORD_1\n\t" \
  "v_smfmac_f32_16x16x64_bf16 v[64:67], a[172:175], v[126:133], v161 abid:1\n\t" \
  "s_sub_u32 s51, s51, 96\n\t" \
  "ds_read_b64 v[10:11], v10 offset:0\n\t" \
  "v_add_u32 v24, s38, v187\n\t" \
  "v_smfmac_f32_16x16x64_bf16 v[68:71], a[176:179], v[126:133], v180\n\t" \
  "s_waitcnt lgkmcnt(2)\n\t" \
  "ds_read_b128 v[24:27], v24 offset:6144\n\t" \
  "v_smfmac_f32_16x16x64_bf16 v[56:59], v[92:95], v[0:7], v182 abid:1\n\t" \
  "s_max_u32 s51, s51, s53\n\t" \
  "ds_read_b128 v[126:129], v183 offset:12288\n\t" \
  "v_add_u32_sdwa v0, s29, v122 dst_sel:DWORD dst_unused:UNUSED_PAD src0_sel:DWORD src1_sel:WORD_0\n\t" \
  "v_smfmac_f32_16x16x64_bf16 v[36:39], a[180:183], v[134:141], v162\n\t" \
  "ds_read_b128 v[130:133], v184 offset:12288\n\t" \
  "v_add_u32_sdwa v2, s29, v122 dst_sel:DWORD dst_unused:UNUSED_PAD src0_sel:DWORD src1_sel:WORD_1\n\t" \
  "v_smfmac_f32_16x16x64_bf16 v[40:43], a[184:187], v[134:141], v162 abid:1\n\t" \
  "s_add_u32 s36, s36, 4608\n\t" \
  "ds_read_b128 v[28:31], v28 offset:1280\n\t" \
  "v_add_u32_sdwa v4, s29, v123 dst_sel:DWORD dst_unused:UNUSED_PAD src0_sel:DWORD src1_sel:WORD_0\n\t" \
  "v_smfmac_f32_16x16x64_bf16 v[44:47], a[188:191], v[134:141], v163\n\t" \
  "s_waitcnt lgkmcnt(3)\n\t" \
  "buffer_store_dwordx4 v[24:27], v34, s[20:23], s36 offen sc1\n\t" \
  "v_add_u32_sdwa v6, s29, v123 dst_sel:DWORD dst_unused:UNUSED_PAD src0_sel:DWORD src1_sel:WORD_1\n\t" \
  "v_smfmac_f32_16x16x64_bf16 v[48:51], a[192:195], v[134:141], v163 abid:1\n\t" \
  "ds_read_b64 v[16:17], v16 offset:0\n\t" \
  "v_xad_u32 v24, v187, 64, s38\n\t" \
  "v_smfmac_f32_16x16x64_bf16 v[52:55], a[196:199], v[134:141], v164\n\t" \
  "s_add_u32 s38, s38, 18432\n\t" \
  "ds_read_b128 v[24:27], v24 offset:7168\n\t" \
  "v_smfmac_f32_16x16x64_bf16 v[56:59], a[200:203], v[134:141], v164 abid:1\n\t" \
  "s_waitcnt lgkmcnt(2)\n\t" \
  "ds_read_b64 v[18:19], v18 offset:0\n\t" \
  "s_set_gpr_idx_on s7, gpr_idx(DST)\n\t" \
  "v_smfmac_f32_16x16x64_bf16 v[36:39], v[28:31], v[8:15], v182 abid:1\n\t" \
  "s_set_gpr_idx_off\n\t" \
  "ds_read_b64 v[20:21], v20 offset:0\n\t" \
  "v_add_u32_sdwa v8, s29, v35 dst_sel:DWORD dst_unused:UNUSED_PAD src0_sel:DWORD src1_sel:WORD_0\n\t" \
  "v_smfmac_f32_16x16x64_bf16 v[60:63], a[204:207], v[134:141], v165\n\t" \
  "s_add_u32 s36, s36, 4608\n\t" \
  "ds_read_b64 v[22:23], v22 offset:0\n\t" \
  "v_add_u32_sdwa v10, s29, v35 dst_sel:DWORD dst_unused:UNUSED_PAD src0_sel:DWORD src1_sel:WORD_1\n\t" \
  "v_smfmac_f32_16x16x64_bf16 v[64:67], a[208:211], v[134:141], v165 abid:1\n\t" \
  "s_waitcnt lgkmcnt(3)\n\t" \
  "buffer_store_dwordx4 v[24:27], v34, s[20:23], s36 offen sc1\n\t" \
  "v_add_u32 v28, s60, v187\n\t" \
  "v_smfmac_f32_16x16x64_bf16 v[68:71], a[212:215], v[134:141], v180 abid:1\n\t" \
  "s_sub_u32 s44, s38, 129024\n\t" \
  "ds_read_b128 v[134:137], v183 offset:14336\n\t" \
  "v_add_u32 v24, s43, v187\n\t" \
  "v_smfmac_f32_16x16x64_bf16 v[36:39], a[216:219], v[126:133], v166\n\t" \
  "s_min_u32 s38, s38, s44\n\t" \
  "ds_read_b128 v[24:27], v24 offset:16384\n\t" \
  "v_smfmac_f32_16x16x64_bf16 v[40:43], a[220:223], v[126:133], v166 abid:1\n\t" \
  "s_waitcnt lgkmcnt(2)\n\t" \
  "ds_read_b128 v[138:141], v184 offset:14336\n\t" \
  "v_smfmac_f32_16x16x64_bf16 v[60:63], v[96:99], v[16:23], v182 abid:1\n\t" \
  "s_add_u32 s35, s35, 4718592\n\t" \
  "ds_read_b64 v[0:1], v0 offset:0\n\t" \
  "v_add_u32_sdwa v16, s29, v124 dst_sel:DWORD dst_unused:UNUSED_PAD src0_sel:DWORD src1_sel:WORD_0\n\t" \
  "v_smfmac_f32_16x16x64_bf16 v[44:47], a[224:227], v[126:133], v167\n\t" \
  "ds_read_b64 v[2:3], v2 offset:0\n\t" \
  "v_add_u32_sdwa v18, s29, v124 dst_sel:DWORD dst_unused:UNUSED_PAD src0_sel:DWORD src1_sel:WORD_1\n\t" \
  "v_smfmac_f32_16x16x64_bf16 v[48:51], a[228:231], v[126:133], v167 abid:1\n\t" \
  "s_waitcnt lgkmcnt(3)\n\t" \
  "buffer_store_dwordx4 v[24:27], v186, s[20:23], s40 offen offset:384 sc1\n\t" \
  "v_add_u32_sdwa v20, s29, v125 dst_sel:DWORD dst_unused:UNUSED_PAD src0_sel:DWORD src1_sel:WORD_0\n\t" \
  "v_smfmac_f32_16x16x64_bf16 v[52:55], a[232:235], v[126:133], v168\n\t" \
  "s_add_u32 s43, s43, 18432\n\t" \
  "ds_read_b64 v[4:5], v4 offset:0\n\t" \
  "v_add_u32_sdwa v22, s29, v125 dst_sel:DWORD dst_unused:UNUSED_PAD src0_sel:DWORD src1_sel:WORD_1\n\t" \
  "v_smfmac_f32_16x16x64_bf16 v[56:59], a[236:239], v[126:133], v168 abid:1\n\t" \
  "s_add_u32 s29, s29, s30\n\t" \
  "ds_read_b64 v[6:7], v6 offset:0\n\t" \
  "v_smfmac_f32_16x16x64_bf16 v[60:63], a[240:243], v[126:133], v169\n\t" \
  "s_sub_u32 s42, s43, 129024\n\t" \
  "ds_read_b64 v[8:9], v8 offset:0\n\t" \
  "v_smfmac_f32_16x16x64_bf16 v[64:67], a[244:247], v[126:133], v169 abid:1\n\t" \
  "s_min_u32 s43, s43, s42\n\t" \
  "ds_read_b64 v[10:11], v10 offset:0\n\t" \
  "v_smfmac_f32_16x16x64_bf16 v[68:71], a[248:251], v[126:133], v181\n\t" \
  "s_waitcnt lgkmcnt(6)\n\t" \
  "ds_read_b128 v[28:31], v28 offset:2560\n\t" \
  "v_smfmac_f32_16x16x64_bf16 v[36:39], a[252:255], v[134:141], v170\n\t" \
  "s_waitcnt lgkmcnt(3)\n\t" \
  "ds_read_b64 v[16:17], v16 offset:0\n\t" \
  "v_smfmac_f32_16x16x64_bf16 v[64:67], v[100:103], v[0:7], v182 abid:1\n\t" \
  "s_add_u32 s40, s40, 4718592\n\t" \
  "ds_read_b128 v[0:3], v183 offset:16384\n\t" \
  "v_smfmac_f32_16x16x64_bf16 v[40:43], v[188:191], v[134:141], v170 abid:1\n\t" \
  "ds_read_b128 v[4:7], v184 offset:16384\n\t" \
  "v_add_u32 v183, s30, v183\n\t" \
  "v_smfmac_f32_16x16x64_bf16 v[44:47], v[192:195], v[134:141], v171\n\t" \
  "s_waitcnt lgkmcnt(3)\n\t" \
  "ds_read_b64 v[18:19], v18 offset:0\n\t" \
  "v_add_u32 v184, s30, v184\n\t" \
  "s_set_gpr_idx_on s8, gpr_idx(DST)\n\t" \
  "v_smfmac_f32_16x16x64_bf16 v[36:39], v[28:31], v[8:15], v182 abid:1\n\t" \
  "s_set_gpr_idx_off\n\t" \
  "ds_read_b64 v[20:21], v20 offset:0\n\t" \
  "v_smfmac_f32_16x16x64_bf16 v[48:51], v[196:199], v[134:141], v171 abid:1\n\t" \
  "ds_read_b64 v[22:23], v22 offset:0\n\t" \
  "v_smfmac_f32_16x16x64_bf16 v[52:55], v[200:203], v[134:141], v172\n\t" \
  "s_memrealtime s[72:73]\n\t" \
  "v_smfmac_f32_16x16x64_bf16 v[56:59], v[204:207], v[134:141], v172 abid:1\n\t" \
  "v_smfmac_f32_16x16x64_bf16 v[60:63], v[208:211], v[134:141], v173\n\t" \
  "v_smfmac_f32_16x16x64_bf16 v[64:67], v[212:215], v[134:141], v173 abid:1\n\t" \
  "s_waitcnt lgkmcnt(0)\n\t" \
  "v_smfmac_f32_16x16x64_bf16 v[68:71], v[104:107], v[16:23], v182 abid:1\n\t" \
  "v_smfmac_f32_16x16x64_bf16 v[68:71], v[216:219], v[134:141], v181 abid:1\n\t" \
  "s_setpc_b64 s[66:67]\n\t" \
  ".Lls13%=:\n\t" \
  "s_memrealtime s[72:73]\n\t" \
  "s_waitcnt lgkmcnt(0)\n\t" \
  "s_sub_i32 s28, s72, s52\n\t" \
  "s_cmp_lt_i32 s28, 0\n\t" \
  "s_cbranch_scc1 .Lls13%=\n\t" \
  "s_branch .Llg13%=\n\t" \
  ".fill (.Lvar%=+24576-.)/4, 4, 0xbf800000\n\t" \
  "s_branch .Lfin%=\n\t" \
  "s_branch .Lhe%=\n\t" \
  "s_waitcnt vmcnt(6)\n\t" \
  "s_waitcnt vmcnt(10)\n\t" \
  "s_waitcnt vmcnt(12)\n\t" \
  "s_waitcnt vmcnt(17)\n\t" \
  "s_waitcnt vmcnt(21) lgkmcnt(0)\n\t" \
  "s_barrier\n\t" \
  "v_smfmac_f32_16x16x64_bf16 v[36:39], v[220:223], v[0:7], v174\n\t" \
  "ds_read_b128 v[126:129], v183 offset:0\n\t" \
  "v_add_u32 v24, s37, v185\n\t" \
  "v_smfmac_f32_16x16x64_bf16 v[40:43], v[224:227], v[0:7], v174 abid:1\n\t" \
  "ds_read_b128 v[130:133], v184 offset:0\n\t" \
  "v_xor_b32 v25, s41, v183\n\t" \
  "v_smfmac_f32_16x16x64_bf16 v[44:47], v[228:231], v[0:7], v175\n\t" \
  "ds_read_b128 v[134:137], v183 offset:2048\n\t" \
  "v_subrev_u32 v25, s30, v25\n\t" \
  "v_smfmac_f32_16x16x64_bf16 v[48:51], v[232:235], v[0:7], v175 abid:1\n\t" \
  "s_add_u32 s30, s29, 18432\n\t" \
  "ds_read_b128 v[138:141], v184 offset:2048\n\t" \
  "v_cvt_pk_bf16_f32 v8, v36, v37\n\t" \
  "v_cvt_pk_bf16_f32 v9, v38, v39\n\t" \
  "v_smfmac_f32_16x16x64_bf16 v[52:55], v[236:239], v[0:7], v176\n\t" \
  "s_sub_u32 s25, s30, 129024\n\t" \
  "ds_read_b128 v[36:39], v35 offset:0\n\t" \
  "v_cvt_pk_bf16_f32 v10, v40, v41\n\t" \
  "v_cvt_pk_bf16_f32 v11, v42, v43\n\t" \
  "v_smfmac_f32_16x16x64_bf16 v[56:59], v[240:243], v[0:7], v176 abid:1\n\t" \
  "s_min_u32 s30, s30, s25\n\t" \
  "ds_read_b128 v[40:43], v35 offset:64\n\t" \
  "v_cvt_pk_bf16_f32 v16, v44, v45\n\t" \
  "v_cvt_pk_bf16_f32 v17, v46, v47\n\t" \
  "v_smfmac_f32_16x16x64_bf16 v[60:63], v[244:247], v[0:7], v177\n\t" \
  "s_sub_u32 s30, s30, s29\n\t" \
  "ds_read_b128 v[44:47], v35 offset:128\n\t" \
  "v_cvt_pk_bf16_f32 v18, v48, v49\n\t" \
  "v_cvt_pk_bf16_f32 v19, v50, v51\n\t" \
  "v_smfmac_f32_16x16x64_bf16 v[64:67], v[248:251], v[0:7], v177 abid:1\n\t" \
  "s_mov_b32 s37, s29\n\t" \
  "ds_read_b128 v[48:51], v35 offset:192\n\t" \
  "v_cvt_pk_bf16_f32 v12, v52, v53\n\t" \
  "v_cvt_pk_bf16_f32 v13, v54, v55\n\t" \
  "v_smfmac_f32_16x16x64_bf16 v[68:71], v[252:255], v[0:7], v182\n\t" \
  "s_waitcnt lgkmcnt(3)\n\t" \
  "ds_read_b128 v[52:55], v35 offset:256\n\t" \
  "v_cvt_pk_bf16_f32 v14, v56, v57\n\t" \
  "v_cvt_pk_bf16_f32 v15, v58, v59\n\t" \
  "v_smfmac_f32_16x16x64_bf16 v[36:39], a[0:3], v[126:133], v142\n\t" \
  "s_waitcnt lgkmcnt(3)\n\t" \
  "ds_read_b128 v[56:59], v35 offset:320\n\t" \
  "v_cvt_pk_bf16_f32 v20, v60, v61\n\t" \
  "v_cvt_pk_bf16_f32 v21, v62, v63\n\t" \
  "v_smfmac_f32_16x16x64_bf16 v[40:43], a[4:7], v[126:133], v142 abid:1\n\t" \
  "s_waitcnt lgkmcnt(3)\n\t" \
  "ds_read_b128 v[60:63], v35 offset:384\n\t" \
  "v_cvt_pk_bf16_f32 v22, v64, v65\n\t" \
  "v_cvt_pk_bf16_f32 v23, v66, v67\n\t" \
  "v_smfmac_f32_16x16x64_bf16 v[44:47], a[8:11], v[126:133], v143\n\t" \
  "s_waitcnt lgkmcnt(3)\n\t" \
  "ds_read_b128 v[64:67], v35 offset:448\n\t" \
  "v_cvt_pk_bf16_f32 v26, v68, v69\n\t" \
  "v_cvt_pk_bf16_f32 v27, v70, v71\n\t" \
  "v_smfmac_f32_16x16x64_bf16 v[48:51], a[12:15], v[126:133], v143 abid:1\n\t" \
  "s_waitcnt lgkmcnt(3)\n\t" \
  "ds_read_b128 v[68:71], v35 offset:512\n\t" \
  "v_add_u32_sdwa v0, s29, v108 dst_sel:DWORD dst_unused:UNUSED_PAD src0_sel:DWORD src1_sel:WORD_0\n\t" \
  "v_smfmac_f32_16x16x64_bf16 v[52:55], a[16:19], v[126:133], v144\n\t" \
  "s_waitcnt lgkmcnt(3)\n\t" \
  "ds_read_b64 v[0:1], v0 offset:0\n\t" \
  "v_add_u32_sdwa v2, s29, v108 dst_sel:DWORD dst_unused:UNUSED_PAD src0_sel:DWORD src1_sel:WORD_1\n\t" \
  "v_smfmac_f32_16x16x64_bf16 v[56:59], a[20:23], v[126:133], v144 abid:1\n\t" \
  "s_waitcnt lgkmcnt(3)\n\t" \
  "v_add_u32_sdwa v4, s29, v109 dst_sel:DWORD dst_unused:UNUSED_PAD src0_sel:DWORD src1_sel:WORD_0\n\t" \
  "v_smfmac_f32_16x16x64_bf16 v[60:63], a[24:27], v[126:133], v145\n\t" \
  "s_waitcnt lgkmcnt(2)\n\t" \
  "ds_write_b128 v24, v[8:11] offset:0\n\t" \
  "v_add_u32_sdwa v6, s29, v109 dst_sel:DWORD dst_unused:UNUSED_PAD src0_sel:DWORD src1_sel:WORD_1\n\t" \
  "v_smfmac_f32_16x16x64_bf16 v[64:67], a[28:31], v[126:133], v145 abid:1\n\t" \
  "s_waitcnt lgkmcnt(2)\n\t" \
  "s_bfe_i32 s47, s48, 0x10000\n\t" \
  "v_add_u32 v28, s38, v187\n\t" \
  "v_smfmac_f32_16x16x64_bf16 v[68:71], a[32:35], v[126:133], v178\n\t" \
  "s_lshr_b32 s48, s48, 1\n\t" \
  "ds_read_b128 v[126:129], v183 offset:4096\n\t" \
  "ds_read_b128 v[28:31], v28 offset:8192\n\t" \
  "v_smfmac_f32_16x16x64_bf16 v[36:39], a[36:39], v[134:141], v146\n\t" \
  "s_add_u32 s36, s35, s39\n\t" \
  "v_smfmac_f32_16x16x64_bf16 v[40:43], a[40:43], v[134:141], v146 abid:1\n\t" \
  "s_andn2_b32 s44, 0x40000000, s47\n\t" \
  "ds_write_b128 v24, v[12:15] offset:128\n\t" \
  "v_xor_b32 v24, 64, v24\n\t" \
  "v_smfmac_f32_16x16x64_bf16 v[44:47], a[44:47], v[134:141], v147\n\t" \
  "s_or_b32 s36, s36, s44\n\t" \
  "v_add_u32_sdwa v8, s29, v110 dst_sel:DWORD dst_unused:UNUSED_PAD src0_sel:DWORD src1_sel:WORD_0\n\t" \
  "v_smfmac_f32_16x16x64_bf16 v[48:51], a[48:51], v[134:141], v147 abid:1\n\t" \
  "ds_read_b128 v[130:133], v184 offset:4096\n\t" \
  "s_waitcnt lgkmcnt(2)\n\t" \
  "buffer_store_dwordx4 v[28:31], v34, s[20:23], s36 offen sc1\n\t" \
  "v_add_u32_sdwa v10, s29, v110 dst_sel:DWORD dst_unused:UNUSED_PAD src0_sel:DWORD src1_sel:WORD_1\n\t" \
  "v_smfmac_f32_16x16x64_bf16 v[52:55], a[52:55], v[134:141], v148\n\t" \
  "s_and_b32 s44, s47, 4718592\n\t" \
  "v_add_u32_sdwa v12, s29, v111 dst_sel:DWORD dst_unused:UNUSED_PAD src0_sel:DWORD src1_sel:WORD_0\n\t" \
  "v_xad_u32 v28, v187, 64, s38\n\t" \
  "v_smfmac_f32_16x16x64_bf16 v[56:59], a[56:59], v[134:141], v148 abid:1\n\t" \
  "s_add_u32 s35, s35, s44\n\t" \
  "ds_write_b128 v24, v[16:19] offset:0\n\t" \
  "ds_read_b128 v[28:31], v28 offset:9216\n\t" \
  "v_add_u32_sdwa v14, s29, v111 dst_sel:DWORD dst_unused:UNUSED_PAD src0_sel:DWORD src1_sel:WORD_1\n\t" \
  "v_smfmac_f32_16x16x64_bf16 v[60:63], a[60:63], v[134:141], v149\n\t" \
  "s_add_u32 s36, s36, 4608\n\t" \
  "ds_read_b64 v[2:3], v2 offset:0\n\t" \
  "v_smfmac_f32_16x16x64_bf16 v[64:67], a[64:67], v[134:141], v149 abid:1\n\t" \
  "ds_read_b64 v[4:5], v4 offset:0\n\t" \
  "v_smfmac_f32_16x16x64_bf16 v[68:71], a[68:71], v[134:141], v178 abid:1\n\t" \
  "s_waitcnt lgkmcnt(3)\n\t" \
  "ds_read_b128 v[134:137], v183 offset:6144\n\t" \
  "v_smfmac_f32_16x16x64_bf16 v[36:39], a[72:75], v[126:133], v150\n\t" \
  "ds_write_b128 v24, v[20:23] offset:128\n\t" \
  "s_waitcnt lgkmcnt(4)\n\t" \
  "buffer_store_dwordx4 v[28:31], v34, s[20:23], s36 offen sc1\n\t" \
  "v_add_u32_sdwa v16, s29, v112 dst_sel:DWORD dst_unused:UNUSED_PAD src0_sel:DWORD src1_sel:WORD_0\n\t" \
  "v_smfmac_f32_16x16x64_bf16 v[40:43], a[76:79], v[126:133], v150 abid:1\n\t" \
  "s_and_b32 s44, s47, 18432\n\t" \
  "ds_read_b128 v[138:141], v184 offset:6144\n\t" \
  "v_add_u32_sdwa v18, s29, v112 dst_sel:DWORD dst_unused:UNUSED_PAD src0_sel:DWORD src1_sel:WORD_1\n\t" \
  "v_add_u32 v28, s38, v187\n\t" \
  "v_smfmac_f32_16x16x64_bf16 v[44:47], a[80:83], v[126:133], v151\n\t" \
  "s_and_b32 s27, s64, 7\n\t" \
  "ds_read_b64 v[6:7], v6 offset:0\n\t" \
  "ds_read_b128 v[28:31], v28 offset:10240\n\t" \
  "v_add_u32_sdwa v20, s29, v113 dst_sel:DWORD dst_unused:UNUSED_PAD src0_sel:DWORD src1_sel:WORD_0\n\t" \
  "v_smfmac_f32_16x16x64_bf16 v[48:51], a[84:87], v[126:133], v151 abid:1\n\t" \
  "s_lshl2_add_u32 s66, s27, s68\n\t" \
  "s_add_u32 s36, s36, 4608\n\t" \
  "ds_read_b64 v[8:9], v8 offset:0\n\t" \
  "v_add_u32_sdwa v22, s29, v113 dst_sel:DWORD dst_unused:UNUSED_PAD src0_sel:DWORD src1_sel:WORD_1\n\t" \
  "v_smfmac_f32_16x16x64_bf16 v[52:55], a[88:91], v[126:133], v152\n\t" \
  "s_and_b32 s27, s64, 8\n\t" \
  "ds_write_b64 v25, v[26:27] offset:16384\n\t" \
  "v_smfmac_f32_16x16x64_bf16 v[56:59], a[92:95], v[126:133], v152 abid:1\n\t" \
  "s_mul_i32 s27, s27, 176\n\t" \
  "ds_read_b64 v[10:11], v10 offset:0\n\t" \
  "v_smfmac_f32_16x16x64_bf16 v[60:63], a[96:99], v[126:133], v153\n\t" \
  "s_waitcnt lgkmcnt(3)\n\t" \
  "ds_read_b64 v[12:13], v12 offset:0\n\t" \
  "s_waitcnt lgkmcnt(4)\n\t" \
  "buffer_store_dwordx4 v[28:31], v34, s[20:23], s36 offen sc1\n\t" \
  "v_smfmac_f32_16x16x64_bf16 v[36:39], v[72:75], v[0:7], v182 abid:1\n\t" \
  "s_add_u32 s66, s66, s27\n\t" \
  "ds_read_b64 v[14:15], v14 offset:0\n\t" \
  "v_add_u32_sdwa v0, s29, v114 dst_sel:DWORD dst_unused:UNUSED_PAD src0_sel:DWORD src1_sel:WORD_0\n\t" \
  "v_xad_u32 v28, v187, 64, s38\n\t" \
  "v_smfmac_f32_16x16x64_bf16 v[64:67], a[100:103], v[126:133], v153 abid:1\n\t" \
  "s_lshr_b64 s[64:65], s[64:65], 4\n\t" \
  "s_add_u32 s38, s38, s44\n\t" \
  "ds_read_b64 v[16:17], v16 offset:0\n\t" \
  "ds_read_b128 v[28:31], v28 offset:11264\n\t" \
  "v_add_u32_sdwa v2, s29, v114 dst_sel:DWORD dst_unused:UNUSED_PAD src0_sel:DWORD src1_sel:WORD_1\n\t" \
  "v_smfmac_f32_16x16x64_bf16 v[68:71], a[104:107], v[126:133], v179\n\t" \
  "s_add_u32 s36, s36, 4608\n\t" \
  "ds_read_b128 v[126:129], v183 offset:8192\n\t" \
  "v_add_u32_sdwa v4, s29, v115 dst_sel:DWORD dst_unused:UNUSED_PAD src0_sel:DWORD src1_sel:WORD_0\n\t" \
  "v_smfmac_f32_16x16x64_bf16 v[36:39], a[108:111], v[134:141], v154\n\t" \
  "s_sub_u32 s44, s38, 129024\n\t" \
  "ds_read_b128 v[130:133], v184 offset:8192\n\t" \
  "v_add_u32_sdwa v6, s29, v115 dst_sel:DWORD dst_unused:UNUSED_PAD src0_sel:DWORD src1_sel:WORD_1\n\t" \
  "v_smfmac_f32_16x16x64_bf16 v[40:43], a[112:115], v[134:141], v154 abid:1\n\t" \
  "s_waitcnt lgkmcnt(3)\n\t" \
  "s_min_u32 s38, s38, s44\n\t" \
  "ds_read_b64 v[18:19], v18 offset:0\n\t" \
  "v_smfmac_f32_16x16x64_bf16 v[40:43], v[76:79], v[8:15], v182 abid:1\n\t" \
  "ds_read_b64 v[20:21], v20 offset:0\n\t" \
  "s_waitcnt lgkmcnt(4)\n\t" \
  "buffer_store_dwordx4 v[28:31], v34, s[20:23], s36 offen sc1\n\t" \
  "v_smfmac_f32_16x16x64_bf16 v[44:47], a[116:119], v[134:141], v155\n\t" \
  "ds_read_b64 v[22:23], v22 offset:0\n\t" \
  "v_add_u32_sdwa v8, s29, v32 dst_sel:DWORD dst_unused:UNUSED_PAD src0_sel:DWORD src1_sel:WORD_0\n\t" \
  "v_smfmac_f32_16x16x64_bf16 v[48:51], a[120:123], v[134:141], v155 abid:1\n\t" \
  "s_branch .Ljn21%=\n\t" \
  ".fill (.Lvar%=+25980-.)/4, 4, 0xbf800000\n\t" \
  "s_nop 0\n\t" \
  "s_branch .Lfin%=\n\t" \
  "s_branch .Lhe%=\n\t" \
  "s_waitcnt vmcnt(6)\n\t" \
  "s_waitcnt vmcnt(10)\n\t" \
  "s_waitcnt vmcnt(12)\n\t" \
  "s_waitcnt vmcnt(17)\n\t" \
  "s_waitcnt vmcnt(21) lgkmcnt(0)\n\t" \
  "s_barrier\n\t" \
  "v_smfmac_f32_16x16x64_bf16 v[36:39], v[220:223], v[0:7], v174\n\t" \
  "ds_read_b128 v[126:129], v183 offset:0\n\t" \
  "v_add_u32 v24, s37, v185\n\t" \
  "v_smfmac_f32_16x16x64_bf16 v[40:43], v[224:227], v[0:7], v174 abid:1\n\t" \
  "ds_read_b128 v[130:133], v184 offset:0\n\t" \
  "v_xor_b32 v25, s41, v183\n\t" \
  "v_smfmac_f32_16x16x64_bf16 v[44:47], v[228:231], v[0:7], v175\n\t" \
  "ds_read_b128 v[134:137], v183 offset:2048\n\t" \
  "v_subrev_u32 v25, s30, v25\n\t" \
  "v_smfmac_f32_16x16x64_bf16 v[48:51], v[232:235], v[0:7], v175 abid:1\n\t" \
  "s_add_u32 s30, s29, 18432\n\t" \
  "ds_read_b128 v[138:141], v184 offset:2048\n\t" \
  "v_cvt_pk_bf16_f32 v8, v36, v37\n\t" \
  "v_cvt_pk_bf16_f32 v9, v38, v39\n\t" \
  "v_smfmac_f32_16x16x64_bf16 v[52:55], v[236:239], v[0:7], v176\n\t" \
  "s_sub_u32 s25, s30, 129024\n\t" \
  "ds_read_b128 v[36:39], v35 offset:0\n\t" \
  "v_cvt_pk_bf16_f32 v10, v40, v41\n\t" \
  "v_cvt_pk_bf16_f32 v11, v42, v43\n\t" \
  "v_smfmac_f32_16x16x64_bf16 v[56:59], v[240:243], v[0:7], v176 abid:1\n\t" \
  "s_min_u32 s30, s30, s25\n\t" \
  "ds_read_b128 v[40:43], v35 offset:64\n\t" \
  "v_cvt_pk_bf16_f32 v16, v44, v45\n\t" \
  "v_cvt_pk_bf16_f32 v17, v46, v47\n\t" \
  "v_smfmac_f32_16x16x64_bf16 v[60:63], v[244:247], v[0:7], v177\n\t" \
  "s_sub_u32 s30, s30, s29\n\t" \
  "ds_read_b128 v[44:47], v35 offset:128\n\t" \
  "v_cvt_pk_bf16_f32 v18, v48, v49\n\t" \
  "v_cvt_pk_bf16_f32 v19, v50, v51\n\t" \
  "v_smfmac_f32_16x16x64_bf16 v[64:67], v[248:251], v[0:7], v177 abid:1\n\t" \
  "s_mov_b32 s37, s29\n\t" \
  "ds_read_b128 v[48:51], v35 offset:192\n\t" \
  "v_cvt_pk_bf16_f32 v12, v52, v53\n\t" \
  "v_cvt_pk_bf16_f32 v13, v54, v55\n\t" \
  "v_smfmac_f32_16x16x64_bf16 v[68:71], v[252:255], v[0:7], v182\n\t" \
  "s_waitcnt lgkmcnt(3)\n\t" \
  "ds_read_b128 v[52:55], v35 offset:256\n\t" \
  "v_cvt_pk_bf16_f32 v14, v56, v57\n\t" \
  "v_cvt_pk_bf16_f32 v15, v58, v59\n\t" \
  "v_smfmac_f32_16x16x64_bf16 v[36:39], a[0:3], v[126:133], v142\n\t" \
  "s_waitcnt lgkmcnt(3)\n\t" \
  "ds_read_b128 v[56:59], v35 offset:320\n\t" \
  "v_cvt_pk_bf16_f32 v20, v60, v61\n\t" \
  "v_cvt_pk_bf16_f32 v21, v62, v63\n\t" \
  "v_smfmac_f32_16x16x64_bf16 v[40:43], a[4:7], v[126:133], v142 abid:1\n\t" \
  "s_waitcnt lgkmcnt(3)\n\t" \
  "ds_read_b128 v[60:63], v35 offset:384\n\t" \
  "v_cvt_pk_bf16_f32 v22, v64, v65\n\t" \
  "v_cvt_pk_bf16_f32 v23, v66, v67\n\t" \
  "v_smfmac_f32_16x16x64_bf16 v[44:47], a[8:11], v[126:133], v143\n\t" \
  "s_waitcnt lgkmcnt(3)\n\t" \
  "ds_read_b128 v[64:67], v35 offset:448\n\t" \
  "v_cvt_pk_bf16_f32 v26, v68, v69\n\t" \
  "v_cvt_pk_bf16_f32 v27, v70, v71\n\t" \
  "v_smfmac_f32_16x16x64_bf16 v[48:51], a[12:15], v[126:133], v143 abid:1\n\t" \
  "s_waitcnt lgkmcnt(3)\n\t" \
  "ds_read_b128 v[68:71], v35 offset:512\n\t" \
  "v_add_u32_sdwa v0, s29, v108 dst_sel:DWORD dst_unused:UNUSED_PAD src0_sel:DWORD src1_sel:WORD_0\n\t" \
  "v_smfmac_f32_16x16x64_bf16 v[52:55], a[16:19], v[126:133], v144\n\t" \
  "s_add_u32 m0, s33, 0\n\t" \
  "s_waitcnt lgkmcnt(3)\n\t" \
  "ds_read_b64 v[0:1], v0 offset:0\n\t" \
  "v_add_u32_sdwa v2, s29, v108 dst_sel:DWORD dst_unused:UNUSED_PAD src0_sel:DWORD src1_sel:WORD_1\n\t" \
  "v_smfmac_f32_16x16x64_bf16 v[56:59], a[20:23], v[126:133], v144 abid:1\n\t" \
  "s_waitcnt lgkmcnt(3)\n\t" \
  "buffer_load_dwordx4 v186, s[16:19], s34 offen offset:0 nt lds\n\t" \
  "v_add_u32_sdwa v4, s29, v109 dst_sel:DWORD dst_unused:UNUSED_PAD src0_sel:DWORD src1_sel:WORD_0\n\t" \
  "v_smfmac_f32_16x16x64_bf16 v[60:63], a[24:27], v[126:133], v145\n\t" \
  "s_add_u32 m0, s33, 1920\n\t" \
  "s_waitcnt lgkmcnt(2)\n\t" \
  "ds_write_b128 v24, v[8:11] offset:0\n\t" \
  "v_add_u32_sdwa v6, s29, v109 dst_sel:DWORD dst_unused:UNUSED_PAD src0_sel:DWORD src1_sel:WORD_1\n\t" \
  "v_smfmac_f32_16x16x64_bf16 v[64:67], a[28:31], v[126:133], v145 abid:1\n\t" \
  "s_waitcnt lgkmcnt(2)\n\t" \
  "buffer_load_dwordx4 v186, s[16:19], s34 offen offset:128 nt lds\n\t" \
  "v_smfmac_f32_16x16x64_bf16 v[68:71], a[32:35], v[126:133], v178\n\t" \
  "s_add_u32 m0, s33, 3840\n\t" \
  "ds_read_b128 v[126:129], v183 offset:4096\n\t" \
  "v_smfmac_f32_16x16x64_bf16 v[36:39], a[36:39], v[134:141], v146\n\t" \
  "buffer_load_dwordx4 v186, s[16:19], s34 offen offset:256 nt lds\n\t" \
  "v_smfmac_f32_16x16x64_bf16 v[40:43], a[40:43], v[134:141], v146 abid:1\n\t" \
  "s_add_u32 m0, s33, 5760\n\t" \
  "ds_write_b128 v24, v[12:15] offset:128\n\t" \
  "v_xor_b32 v24, 64, v24\n\t" \
  "v_smfmac_f32_16x16x64_bf16 v[44:47], a[44:47], v[134:141], v147\n\t" \
  "buffer_load_dwordx4 v186, s[16:19], s34 offen offset:384 nt lds\n\t" \
  "v_add_u32_sdwa v8, s29, v110 dst_sel:DWORD dst_unused:UNUSED_PAD src0_sel:DWORD src1_sel:WORD_0\n\t" \
  "v_smfmac_f32_16x16x64_bf16 v[48:51], a[48:51], v[134:141], v147 abid:1\n\t" \
  "s_add_u32 m0, s33, 7680\n\t" \
  "ds_read_b128 v[130:133], v184 offset:4096\n\t" \
  "v_add_u32_sdwa v10, s29, v110 dst_sel:DWORD dst_unused:UNUSED_PAD src0_sel:DWORD src1_sel:WORD_1\n\t" \
  "v_smfmac_f32_16x16x64_bf16 v[52:55], a[52:55], v[134:141], v148\n\t" \
  "buffer_load_dwordx4 v186, s[16:19], s34 offen offset:512 nt lds\n\t" \
  "v_add_u32_sdwa v12, s29, v111 dst_sel:DWORD dst_unused:UNUSED_PAD src0_sel:DWORD src1_sel:WORD_0\n\t" \
  "v_smfmac_f32_16x16x64_bf16 v[56:59], a[56:59], v[134:141], v148 abid:1\n\t" \
  "s_add_u32 s31, s31, 18432\n\t" \
  "ds_write_b128 v24, v[16:19] offset:0\n\t" \
  "v_add_u32_sdwa v14, s29, v111 dst_sel:DWORD dst_unused:UNUSED_PAD src0_sel:DWORD src1_sel:WORD_1\n\t" \
  "v_smfmac_f32_16x16x64_bf16 v[60:63], a[60:63], v[134:141], v149\n\t" \
  "s_sub_u32 s26, s31, 129024\n\t" \
  "ds_read_b64 v[2:3], v2 offset:0\n\t" \
  "v_smfmac_f32_16x16x64_bf16 v[64:67], a[64:67], v[134:141], v149 abid:1\n\t" \
  "s_min_u32 s31, s31, s26\n\t" \
  "ds_read_b64 v[4:5], v4 offset:0\n\t" \
  "v_smfmac_f32_16x16x64_bf16 v[68:71], a[68:71], v[134:141], v178 abid:1\n\t" \
  "s_waitcnt lgkmcnt(3)\n\t" \
  "ds_read_b128 v[134:137], v183 offset:6144\n\t" \
  "v_smfmac_f32_16x16x64_bf16 v[36:39], a[72:75], v[126:133], v150\n\t" \
  "s_add_u32 s33, s31, s32\n\t" \
  "ds_write_b128 v24, v[20:23] offset:128\n\t" \
  "v_add_u32_sdwa v16, s29, v112 dst_sel:DWORD dst_unused:UNUSED_PAD src0_sel:DWORD src1_sel:WORD_0\n\t" \
  "v_smfmac_f32_16x16x64_bf16 v[40:43], a[76:79], v[126:133], v150 abid:1\n\t" \
  "s_add_u32 s34, s34, 4718592\n\t" \
  "ds_read_b128 v[138:141], v184 offset:6144\n\t" \
  "v_add_u32_sdwa v18, s29, v112 dst_sel:DWORD dst_unused:UNUSED_PAD src0_sel:DWORD src1_sel:WORD_1\n\t" \
  "v_smfmac_f32_16x16x64_bf16 v[44:47], a[80:83], v[126:133], v151\n\t" \
  "s_and_b32 s27, s64, 7\n\t" \
  "ds_read_b64 v[6:7], v6 offset:0\n\t" \
  "v_add_u32_sdwa v20, s29, v113 dst_sel:DWORD dst_unused:UNUSED_PAD src0_sel:DWORD src1_sel:WORD_0\n\t" \
  "v_smfmac_f32_16x16x64_bf16 v[48:51], a[84:87], v[126:133], v151 abid:1\n\t" \
  "s_lshl2_add_u32 s66, s27, s68\n\t" \
  "ds_read_b64 v[8:9], v8 offset:0\n\t" \
  "v_add_u32_sdwa v22, s29, v113 dst_sel:DWORD dst_unused:UNUSED_PAD src0_sel:DWORD src1_sel:WORD_1\n\t" \
  "v_smfmac_f32_16x16x64_bf16 v[52:55], a[88:91], v[126:133], v152\n\t" \
  "s_and_b32 s27, s64, 8\n\t" \
  "ds_write_b64 v25, v[26:27] offset:16384\n\t" \
  "v_smfmac_f32_16x16x64_bf16 v[56:59], a[92:95], v[126:133], v152 abid:1\n\t" \
  "s_mul_i32 s27, s27, 176\n\t" \
  "ds_read_b64 v[10:11], v10 offset:0\n\t" \
  "v_smfmac_f32_16x16x64_bf16 v[60:63], a[96:99], v[126:133], v153\n\t" \
  "s_waitcnt lgkmcnt(3)\n\t" \
  "ds_read_b64 v[12:13], v12 offset:0\n\t" \
  "v_smfmac_f32_16x16x64_bf16 v[36:39], v[72:75], v[0:7], v182 abid:1\n\t" \
  "s_add_u32 s66, s66, s27\n\t" \
  "ds_read_b64 v[14:15], v14 offset:0\n\t" \
  "v_add_u32_sdwa v0, s29, v114 dst_sel:DWORD dst_unused:UNUSED_PAD src0_sel:DWORD src1_sel:WORD_0\n\t" \
  "v_smfmac_f32_16x16x64_bf16 v[64:67], a[100:103], v[126:133], v153 abid:1\n\t" \
  "s_lshr_b64 s[64:65], s[64:65], 4\n\t" \
  "ds_read_b64 v[16:17], v16 offset:0\n\t" \
  "v_add_u32_sdwa v2, s29, v114 dst_sel:DWORD dst_unused:UNUSED_PAD src0_sel:DWORD src1_sel:WORD_1\n\t" \
  "v_smfmac_f32_16x16x64_bf16 v[68:71], a[104:107], v[126:133], v179\n\t" \
  "ds_read_b128 v[126:129], v183 offset:8192\n\t" \
  "v_add_u32_sdwa v4, s29, v115 dst_sel:DWORD dst_unused:UNUSED_PAD src0_sel:DWORD src1_sel:WORD_0\n\t" \
  "v_smfmac_f32_16x16x64_bf16 v[36:39], a[108:111], v[134:141], v154\n\t" \
  "ds_read_b128 v[130:133], v184 offset:8192\n\t" \
  "v_add_u32_sdwa v6, s29, v115 dst_sel:DWORD dst_unused:UNUSED_PAD src0_sel:DWORD src1_sel:WORD_1\n\t" \
  "v_smfmac_f32_16x16x64_bf16 v[40:43], a[112:115], v[134:141], v154 abid:1\n\t" \
  "s_waitcnt lgkmcnt(3)\n\t" \
  "ds_read_b64 v[18:19], v18 offset:0\n\t" \
  "v_smfmac_f32_16x16x64_bf16 v[40:43], v[76:79], v[8:15], v182 abid:1\n\t" \
  "ds_read_b64 v[20:21], v20 offset:0\n\t" \
  "v_smfmac_f32_16x16x64_bf16 v[44:47], a[116:119], v[134:141], v155\n\t" \
  "ds_read_b64 v[22:23], v22 offset:0\n\t" \
  "v_add_u32_sdwa v8, s29, v32 dst_sel:DWORD dst_unused:UNUSED_PAD src0_sel:DWORD src1_sel:WORD_0\n\t" \
  "v_smfmac_f32_16x16x64_bf16 v[48:51], a[120:123], v[134:141], v155 abid:1\n\t" \
  ".Ljn21%=:\n\t" \
  "ds_read_b64 v[0:1], v0 offset:0\n\t" \
  "v_add_u32_sdwa v10, s29, v32 dst_sel:DWORD dst_unused:UNUSED_PAD src0_sel:DWORD src1_sel:WORD_1\n\t" \
  "v_smfmac_f32_16x16x64_bf16 v[52:55], a[124:127], v[134:141], v156\n\t" \
  "ds_read_b64 v[2:3], v2 offset:0\n\t" \
  "v_add_u32 v28, s60, v187\n\t" \
  "v_smfmac_f32_16x16x64_bf16 v[56:59], a[128:131], v[134:141], v156 abid:1\n\t" \
  "ds_read_b64 v[4:5], v4 offset:0\n\t" \
  "v_smfmac_f32_16x16x64_bf16 v[60:63], a[132:135], v[134:141], v157\n\t" \
  "s_waitcnt lgkmcnt(3)\n\t" \
  "ds_read_b64 v[6:7], v6 offset:0\n\t" \
  "v_smfmac_f32_16x16x64_bf16 v[44:47], v[80:83], v[16:23], v182 abid:1\n\t" \
  "ds_read_b64 v[8:9], v8 offset:0\n\t" \
  "v_add_u32_sdwa v16, s29, v116 dst_sel:DWORD dst_unused:UNUSED_PAD src0_sel:DWORD src1_sel:WORD_0\n\t" \
  "v_smfmac_f32_16x16x64_bf16 v[64:67], a[136:139], v[134:141], v157 abid:1\n\t" \
  "ds_read_b64 v[10:11], v10 offset:0\n\t" \
  "v_add_u32_sdwa v18, s29, v116 dst_sel:DWORD dst_unused:UNUSED_PAD src0_sel:DWORD src1_sel:WORD_1\n\t" \
  "v_smfmac_f32_16x16x64_bf16 v[68:71], a[140:143], v[134:141], v179 abid:1\n\t" \
  "ds_read_b128 v[134:137], v183 offset:10240\n\t" \
  "v_add_u32_sdwa v20, s29, v117 dst_sel:DWORD dst_unused:UNUSED_PAD src0_sel:DWORD src1_sel:WORD_0\n\t" \
  "v_smfmac_f32_16x16x64_bf16 v[36:39], a[144:147], v[126:133], v158\n\t" \
  "s_waitcnt lgkmcnt(3)\n\t" \
  "ds_read_b128 v[138:141], v184 offset:10240\n\t" \
  "v_add_u32 v24, s38, v187\n\t" \
  "v_smfmac_f32_16x16x64_bf16 v[48:51], v[84:87], v[0:7], v182 abid:1\n\t" \
  "ds_read_b128 v[24:27], v24 offset:8192\n\t" \
  "v_add_u32_sdwa v22, s29, v117 dst_sel:DWORD dst_unused:UNUSED_PAD src0_sel:DWORD src1_sel:WORD_1\n\t" \
  "v_smfmac_f32_16x16x64_bf16 v[40:43], a[148:151], v[126:133], v158 abid:1\n\t" \
  "ds_read_b128 v[28:31], v28 offset:0\n\t" \
  "v_add_u32_sdwa v0, s29, v118 dst_sel:DWORD dst_unused:UNUSED_PAD src0_sel:DWORD src1_sel:WORD_0\n\t" \
  "v_smfmac_f32_16x16x64_bf16 v[44:47], a[152:155], v[126:133], v159\n\t" \
  "ds_read_b64 v[16:17], v16 offset:0\n\t" \
  "v_add_u32_sdwa v2, s29, v118 dst_sel:DWORD dst_unused:UNUSED_PAD src0_sel:DWORD src1_sel:WORD_1\n\t" \
  "v_smfmac_f32_16x16x64_bf16 v[48:51], a[156:159], v[126:133], v159 abid:1\n\t" \
  "s_add_u32 s36, s35, s39\n\t" \
  "ds_read_b64 v[18:19], v18 offset:0\n\t" \
  "v_add_u32_sdwa v4, s29, v119 dst_sel:DWORD dst_unused:UNUSED_PAD src0_sel:DWORD src1_sel:WORD_0\n\t" \
  "v_smfmac_f32_16x16x64_bf16 v[52:55], a[160:163], v[126:133], v160\n\t" \
  "s_waitcnt lgkmcnt(3)\n\t" \
  "buffer_store_dwordx4 v[24:27], v34, s[20:23], s36 offen sc1\n\t" \
  "v_add_u32_sdwa v6, s29, v119 dst_sel:DWORD dst_unused:UNUSED_PAD src0_sel:DWORD src1_sel:WORD_1\n\t" \
  "v_smfmac_f32_16x16x64_bf16 v[56:59], a[164:167], v[126:133], v160 abid:1\n\t" \
  "s_waitcnt lgkmcnt(2)\n\t" \
  "ds_read_b64 v[20:21], v20 offset:0\n\t" \
  "v_xad_u32 v24, v187, 64, s38\n\t" \
  "s_set_gpr_idx_on s6, gpr_idx(DST)\n\t" \
  "v_smfmac_f32_16x16x64_bf16 v[36:39], v[28:31], v[8:15], v182 abid:1\n\t" \
  "s_set_gpr_idx_off\n\t" \
  "ds_read_b128 v[24:27], v24 offset:9216\n\t" \
  "v_add_u32_sdwa v8, s29, v120 dst_sel:DWORD dst_unused:UNUSED_PAD src0_sel:DWORD src1_sel:WORD_0\n\t" \
  "v_smfmac_f32_16x16x64_bf16 v[60:63], a[168:171], v[126:133], v161\n\t" \
  "ds_read_b64 v[22:23], v22 offset:0\n\t" \
  "v_add_u32_sdwa v10, s29, v120 dst_sel:DWORD dst_unused:UNUSED_PAD src0_sel:DWORD src1_sel:WORD_1\n\t" \
  "v_smfmac_f32_16x16x64_bf16 v[64:67], a[172:175], v[126:133], v161 abid:1\n\t" \
  "ds_read_b64 v[0:1], v0 offset:0\n\t" \
  "v_add_u32_sdwa v12, s29, v121 dst_sel:DWORD dst_unused:UNUSED_PAD src0_sel:DWORD src1_sel:WORD_0\n\t" \
  "v_smfmac_f32_16x16x64_bf16 v[68:71], a[176:179], v[126:133], v180\n\t" \
  "s_add_u32 s36, s36, 4608\n\t" \
  "ds_read_b128 v[126:129], v183 offset:12288\n\t" \
  "v_add_u32_sdwa v14, s29, v121 dst_sel:DWORD dst_unused:UNUSED_PAD src0_sel:DWORD src1_sel:WORD_1\n\t" \
  "v_smfmac_f32_16x16x64_bf16 v[36:39], a[180:183], v[134:141], v162\n\t" \
  "s_waitcnt lgkmcnt(3)\n\t" \
  "buffer_store_dwordx4 v[24:27], v34, s[20:23], s36 offen sc1\n\t" \
  "v_smfmac_f32_16x16x64_bf16 v[40:43], a[184:187], v[134:141], v162 abid:1\n\t" \
  "s_waitcnt lgkmcnt(2)\n\t" \
  "ds_read_b128 v[130:133], v184 offset:12288\n\t" \
  "v_add_u32 v24, s38, v187\n\t" \
  "v_smfmac_f32_16x16x64_bf16 v[52:55], v[88:91], v[16:23], v182 abid:1\n\t" \
  "ds_read_b128 v[24:27], v24 offset:10240\n\t" \
  "v_add_u32_sdwa v16, s29, v122 dst_sel:DWORD dst_unused:UNUSED_PAD src0_sel:DWORD src1_sel:WORD_0\n\t" \
  "v_smfmac_f32_16x16x64_bf16 v[44:47], a[188:191], v[134:141], v163\n\t" \
  "ds_read_b64 v[2:3], v2 offset:0\n\t" \
  "v_add_u32_sdwa v18, s29, v122 dst_sel:DWORD dst_unused:UNUSED_PAD src0_sel:DWORD src1_sel:WORD_1\n\t" \
  "v_smfmac_f32_16x16x64_bf16 v[48:51], a[192:195], v[134:141], v163 abid:1\n\t" \
  "ds_read_b64 v[4:5], v4 offset:0\n\t" \
  "v_add_u32_sdwa v20, s29, v123 dst_sel:DWORD dst_unused:UNUSED_PAD src0_sel:DWORD src1_sel:WORD_0\n\t" \
  "v_smfmac_f32_16x16x64_bf16 v[52:55], a[196:199], v[134:141], v164\n\t" \
  "s_add_u32 s36, s36, 4608\n\t" \
  "ds_read_b64 v[6:7], v6 offset:0\n\t" \
  "v_add_u32_sdwa v22, s29, v123 dst_sel:DWORD dst_unused:UNUSED_PAD src0_sel:DWORD src1_sel:WORD_1\n\t" \
  "v_smfmac_f32_16x16x64_bf16 v[56:59], a[200:203], v[134:141], v164 abid:1\n\t" \
  "s_waitcnt lgkmcnt(3)\n\t" \
  "buffer_store_dwordx4 v[24:27], v34, s[20:23], s36 offen sc1\n\t" \
  "v_smfmac_f32_16x16x64_bf16 v[60:63], a[204:207], v[134:141], v165\n\t" \
  "ds_read_b64 v[8:9], v8 offset:0\n\t" \
  "v_xad_u32 v24, v187, 64, s38\n\t" \
  "v_smfmac_f32_16x16x64_bf16 v[64:67], a[208:211], v[134:141], v165 abid:1\n\t" \
  "s_add_u32 s38, s38, 18432\n\t" \
  "ds_read_b128 v[24:27], v24 offset:11264\n\t" \
  "v_smfmac_f32_16x16x64_bf16 v[68:71], a[212:215], v[134:141], v180 abid:1\n\t" \
  "s_waitcnt lgkmcnt(2)\n\t" \
  "ds_read_b128 v[134:137], v183 offset:14336\n\t" \
  "v_smfmac_f32_16x16x64_bf16 v[56:59], v[92:95], v[0:7], v182 abid:1\n\t" \
  "s_sub_u32 s44, s38, 129024\n\t" \
  "ds_read_b128 v[138:141], v184 offset:14336\n\t" \
  "v_smfmac_f32_16x16x64_bf16 v[36:39], a[216:219], v[126:133], v166\n\t" \
  "s_add_u32 s36, s36, 4608\n\t" \
  "ds_read_b64 v[10:11], v10 offset:0\n\t" \
  "v_smfmac_f32_16x16x64_bf16 v[40:43], a[220:223], v[126:133], v166 abid:1\n\t" \
  "s_waitcnt lgkmcnt(3)\n\t" \
  "buffer_store_dwordx4 v[24:27], v34, s[20:23], s36 offen sc1\n\t" \
  "v_smfmac_f32_16x16x64_bf16 v[44:47], a[224:227], v[126:133], v167\n\t" \
  "s_min_u32 s38, s38, s44\n\t" \
  "ds_read_b64 v[12:13], v12 offset:0\n\t" \
  "v_smfmac_f32_16x16x64_bf16 v[48:51], a[228:231], v[126:133], v167 abid:1\n\t" \
  "s_add_u32 s35, s35, 4718592\n\t" \
  "ds_read_b64 v[14:15], v14 offset:0\n\t" \
  "v_smfmac_f32_16x16x64_bf16 v[52:55], a[232:235], v[126:133], v168\n\t" \
  "ds_read_b64 v[16:17], v16 offset:0\n\t" \
  "v_smfmac_f32_16x16x64_bf16 v[56:59], a[236:239], v[126:133], v168 abid:1\n\t" \
  "ds_read_b64 v[18:19], v18 offset:0\n\t" \
  "v_smfmac_f32_16x16x64_bf16 v[60:63], a[240:243], v[126:133], v169\n\t" \
  "ds_read_b64 v[20:21], v20 offset:0\n\t" \
  "v_smfmac_f32_16x16x64_bf16 v[64:67], a[244:247], v[126:133], v169 abid:1\n\t" \
  "s_waitcnt lgkmcnt(3)\n\t" \
  "ds_read_b64 v[22:23], v22 offset:0\n\t" \
  "v_smfmac_f32_16x16x64_bf16 v[60:63], v[96:99], v[8:15], v182 abid:1\n\t" \
  "v_add_u32_sdwa v8, s29, v124 dst_sel:DWORD dst_unused:UNUSED_PAD src0_sel:DWORD src1_sel:WORD_0\n\t" \
  "v_smfmac_f32_16x16x64_bf16 v[68:71], a[248:251], v[126:133], v181\n\t" \
  "ds_read_b128 v[0:3], v183 offset:16384\n\t" \
  "v_add_u32_sdwa v10, s29, v124 dst_sel:DWORD dst_unused:UNUSED_PAD src0_sel:DWORD src1_sel:WORD_1\n\t" \
  "v_smfmac_f32_16x16x64_bf16 v[36:39], a[252:255], v[134:141], v170\n\t" \
  "ds_read_b128 v[4:7], v184 offset:16384\n\t" \
  "v_add_u32_sdwa v12, s29, v125 dst_sel:DWORD dst_unused:UNUSED_PAD src0_sel:DWORD src1_sel:WORD_0\n\t" \
  "v_smfmac_f32_16x16x64_bf16 v[40:43], v[188:191], v[134:141], v170 abid:1\n\t" \
  "s_waitcnt lgkmcnt(2)\n\t" \
  "ds_read_b64 v[8:9], v8 offset:0\n\t" \
  "v_add_u32_sdwa v14, s29, v125 dst_sel:DWORD dst_unused:UNUSED_PAD src0_sel:DWORD src1_sel:WORD_1\n\t" \
  "v_smfmac_f32_16x16x64_bf16 v[64:67], v[100:103], v[16:23], v182 abid:1\n\t" \
  "s_add_u32 s29, s29, s30\n\t" \
  "ds_read_b64 v[10:11], v10 offset:0\n\t" \
  "v_add_u32 v183, s30, v183\n\t" \
  "v_smfmac_f32_16x16x64_bf16 v[44:47], v[192:195], v[134:141], v171\n\t" \
  "ds_read_b64 v[12:13], v12 offset:0\n\t" \
  "v_add_u32 v184, s30, v184\n\t" \
  "v_smfmac_f32_16x16x64_bf16 v[48:51], v[196:199], v[134:141], v171 abid:1\n\t" \
  "ds_read_b64 v[14:15], v14 offset:0\n\t" \
  "v_smfmac_f32_16x16x64_bf16 v[52:55], v[200:203], v[134:141], v172\n\t" \
  "v_smfmac_f32_16x16x64_bf16 v[56:59], v[204:207], v[134:141], v172 abid:1\n\t" \
  "v_smfmac_f32_16x16x64_bf16 v[60:63], v[208:211], v[134:141], v173\n\t" \
  "v_smfmac_f32_16x16x64_bf16 v[64:67], v[212:215], v[134:141], v173 abid:1\n\t" \
  "s_waitcnt lgkmcnt(0)\n\t" \
  "v_smfmac_f32_16x16x64_bf16 v[68:71], v[104:107], v[8:15], v182 abid:1\n\t" \
  "v_smfmac_f32_16x16x64_bf16 v[68:71], v[216:219], v[134:141], v181 abid:1\n\t" \
  "s_setpc_b64 s[66:67]\n\t" \
  ".fill (.Lvar%=+28672-.)/4, 4, 0xbf800000\n\t" \
  "s_branch .Lfin%=\n\t" \
  "s_branch .Lhe%=\n\t" \
  "s_waitcnt vmcnt(6)\n\t" \
  "s_waitcnt vmcnt(10)\n\t" \
  "s_waitcnt vmcnt(12)\n\t" \
  "s_waitcnt vmcnt(17)\n\t" \
  "s_waitcnt vmcnt(21) lgkmcnt(0)\n\t" \
  "s_barrier\n\t" \
  "v_smfmac_f32_16x16x64_bf16 v[36:39], v[220:223], v[0:7], v174\n\t" \
  "ds_read_b128 v[126:129], v183 offset:0\n\t" \
  "v_add_u32 v24, s37, v185\n\t" \
  "v_smfmac_f32_16x16x64_bf16 v[40:43], v[224:227], v[0:7], v174 abid:1\n\t" \
  "ds_read_b128 v[130:133], v184 offset:0\n\t" \
  "v_xor_b32 v25, s41, v183\n\t" \
  "v_smfmac_f32_16x16x64_bf16 v[44:47], v[228:231], v[0:7], v175\n\t" \
  "ds_read_b128 v[134:137], v183 offset:2048\n\t" \
  "v_subrev_u32 v25, s30, v25\n\t" \
  "v_smfmac_f32_16x16x64_bf16 v[48:51], v[232:235], v[0:7], v175 abid:1\n\t" \
  "s_add_u32 s30, s29, 18432\n\t" \
  "ds_read_b128 v[138:141], v184 offset:2048\n\t" \
  "v_cvt_pk_bf16_f32 v8, v36, v37\n\t" \
  "v_cvt_pk_bf16_f32 v9, v38, v39\n\t" \
  "v_smfmac_f32_16x16x64_bf16 v[52:55], v[236:239], v[0:7], v176\n\t" \
  "s_sub_u32 s25, s30, 129024\n\t" \
  "ds_read_b128 v[36:39], v35 offset:0\n\t" \
  "v_cvt_pk_bf16_f32 v10, v40, v41\n\t" \
  "v_cvt_pk_bf16_f32 v11, v42, v43\n\t" \
  "v_smfmac_f32_16x16x64_bf16 v[56:59], v[240:243], v[0:7], v176 abid:1\n\t" \
  "s_min_u32 s30, s30, s25\n\t" \
  "ds_read_b128 v[40:43], v35 offset:64\n\t" \
  "v_cvt_pk_bf16_f32 v16, v44, v45\n\t" \
  "v_cvt_pk_bf16_f32 v17, v46, v47\n\t" \
  "v_smfmac_f32_16x16x64_bf16 v[60:63], v[244:247], v[0:7], v177\n\t" \
  "s_sub_u32 s30, s30, s29\n\t" \
  "ds_read_b128 v[44:47], v35 offset:128\n\t" \
  "v_cvt_pk_bf16_f32 v18, v48, v49\n\t" \
  "v_cvt_pk_bf16_f32 v19, v50, v51\n\t" \
  "v_smfmac_f32_16x16x64_bf16 v[64:67], v[248:251], v[0:7], v177 abid:1\n\t" \
  "s_mov_b32 s37, s29\n\t" \
  "ds_read_b128 v[48:51], v35 offset:192\n\t" \
  "v_cvt_pk_bf16_f32 v12, v52, v53\n\t" \
  "v_cvt_pk_bf16_f32 v13, v54, v55\n\t" \
  "v_smfmac_f32_16x16x64_bf16 v[68:71], v[252:255], v[0:7], v182\n\t" \
  "s_waitcnt lgkmcnt(3)\n\t" \
  "ds_read_b128 v[52:55], v35 offset:256\n\t" \
  "v_cvt_pk_bf16_f32 v14, v56, v57\n\t" \
  "v_cvt_pk_bf16_f32 v15, v58, v59\n\t" \
  "v_smfmac_f32_16x16x64_bf16 v[36:39], a[0:3], v[126:133], v142\n\t" \
  "s_waitcnt lgkmcnt(3)\n\t" \
  "ds_read_b128 v[56:59], v35 offset:320\n\t" \
  "v_cvt_pk_bf16_f32 v20, v60, v61\n\t" \
  "v_cvt_pk_bf16_f32 v21, v62, v63\n\t" \
  "v_smfmac_f32_16x16x64_bf16 v[40:43], a[4:7], v[126:133], v142 abid:1\n\t" \
  "s_waitcnt lgkmcnt(3)\n\t" \
  "ds_read_b128 v[60:63], v35 offset:384\n\t" \
  "v_cvt_pk_bf16_f32 v22, v64, v65\n\t" \
  "v_cvt_pk_bf16_f32 v23, v66, v67\n\t" \
  "v_smfmac_f32_16x16x64_bf16 v[44:47], a[8:11], v[126:133], v143\n\t" \
  "s_waitcnt lgkmcnt(3)\n\t" \
  "ds_read_b128 v[64:67], v35 offset:448\n\t" \
  "v_cvt_pk_bf16_f32 v26, v68, v69\n\t" \
  "v_cvt_pk_bf16_f32 v27, v70, v71\n\t" \
  "v_smfmac_f32_16x16x64_bf16 v[48:51], a[12:15], v[126:133], v143 abid:1\n\t" \
  "s_waitcnt lgkmcnt(3)\n\t" \
  "ds_read_b128 v[68:71], v35 offset:512\n\t" \
  "v_add_u32_sdwa v0, s29, v108 dst_sel:DWORD dst_unused:UNUSED_PAD src0_sel:DWORD src1_sel:WORD_0\n\t" \
  "v_smfmac_f32_16x16x64_bf16 v[52:55], a[16:19], v[126:133], v144\n\t" \
  "s_waitcnt lgkmcnt(3)\n\t" \
  "ds_read_b64 v[0:1], v0 offset:0\n\t" \
  "v_add_u32_sdwa v2, s29, v108 dst_sel:DWORD dst_unused:UNUSED_PAD src0_sel:DWORD src1_sel:WORD_1\n\t" \
  "v_smfmac_f32_16x16x64_bf16 v[56:59], a[20:23], v[126:133], v144 abid:1\n\t" \
  "s_waitcnt lgkmcnt(3)\n\t" \
  "v_add_u32_sdwa v4, s29, v109 dst_sel:DWORD dst_unused:UNUSED_PAD src0_sel:DWORD src1_sel:WORD_0\n\t" \
  "v_smfmac_f32_16x16x64_bf16 v[60:63], a[24:27], v[126:133], v145\n\t" \
  "s_waitcnt lgkmcnt(2)\n\t" \
  "ds_write_b128 v24, v[8:11] offset:0\n\t" \
  "v_add_u32_sdwa v6, s29, v109 dst_sel:DWORD dst_unused:UNUSED_PAD src0_sel:DWORD src1_sel:WORD_1\n\t" \
  "v_smfmac_f32_16x16x64_bf16 v[64:67], a[28:31], v[126:133], v145 abid:1\n\t" \
  "s_waitcnt lgkmcnt(2)\n\t" \
  "s_bfe_i32 s47, s48, 0x10000\n\t" \
  "v_add_u32 v28, s38, v187\n\t" \
  "v_smfmac_f32_16x16x64_bf16 v[68:71], a[32:35], v[126:133], v178\n\t" \
  "s_lshr_b32 s48, s48, 1\n\t" \
  "ds_read_b128 v[126:129], v183 offset:4096\n\t" \
  "ds_read_b128 v[28:31], v28 offset:8192\n\t" \
  "v_smfmac_f32_16x16x64_bf16 v[36:39], a[36:39], v[134:141], v146\n\t" \
  "s_add_u32 s36, s35, s39\n\t" \
  "v_smfmac_f32_16x16x64_bf16 v[40:43], a[40:43], v[134:141], v146 abid:1\n\t" \
  "s_andn2_b32 s44, 0x40000000, s47\n\t" \
  "ds_write_b128 v24, v[12:15] offset:128\n\t" \
  "v_xor_b32 v24, 64, v24\n\t" \
  "v_smfmac_f32_16x16x64_bf16 v[44:47], a[44:47], v[134:141], v147\n\t" \
  "s_or_b32 s36, s36, s44\n\t" \
  "v_add_u32_sdwa v8, s29, v110 dst_sel:DWORD dst_unused:UNUSED_PAD src0_sel:DWORD src1_sel:WORD_0\n\t" \
  "v_smfmac_f32_16x16x64_bf16 v[48:51], a[48:51], v[134:141], v147 abid:1\n\t" \
  "ds_read_b128 v[130:133], v184 offset:4096\n\t" \
  "s_waitcnt lgkmcnt(2)\n\t" \
  "buffer_store_dwordx4 v[28:31], v34, s[20:23], s36 offen sc1\n\t" \
  "v_add_u32_sdwa v10, s29, v110 dst_sel:DWORD dst_unused:UNUSED_PAD src0_sel:DWORD src1_sel:WORD_1\n\t" \
  "v_smfmac_f32_16x16x64_bf16 v[52:55], a[52:55], v[134:141], v148\n\t" \
  "s_and_b32 s44, s47, 4718592\n\t" \
  "v_add_u32_sdwa v12, s29, v111 dst_sel:DWORD dst_unused:UNUSED_PAD src0_sel:DWORD src1_sel:WORD_0\n\t" \
  "v_xad_u32 v28, v187, 64, s38\n\t" \
  "v_smfmac_f32_16x16x64_bf16 v[56:59], a[56:59], v[134:141], v148 abid:1\n\t" \
  "s_add_u32 s35, s35, s44\n\t" \
  "ds_write_b128 v24, v[16:19] offset:0\n\t" \
  "ds_read_b128 v[28:31], v28 offset:9216\n\t" \
  "v_add_u32_sdwa v14, s29, v111 dst_sel:DWORD dst_unused:UNUSED_PAD src0_sel:DWORD src1_sel:WORD_1\n\t" \
  "v_smfmac_f32_16x16x64_bf16 v[60:63], a[60:63], v[134:141], v149\n\t" \
  "s_add_u32 s36, s36, 4608\n\t" \
  "ds_read_b64 v[2:3], v2 offset:0\n\t" \
  "v_smfmac_f32_16x16x64_bf16 v[64:67], a[64:67], v[134:141], v149 abid:1\n\t" \
  "ds_read_b64 v[4:5], v4 offset:0\n\t" \
  "v_smfmac_f32_16x16x64_bf16 v[68:71], a[68:71], v[134:141], v178 abid:1\n\t" \
  "s_waitcnt lgkmcnt(3)\n\t" \
  "ds_read_b128 v[134:137], v183 offset:6144\n\t" \
  "v_smfmac_f32_16x16x64_bf16 v[36:39], a[72:75], v[126:133], v150\n\t" \
  "ds_write_b128 v24, v[20:23] offset:128\n\t" \
  "s_waitcnt lgkmcnt(4)\n\t" \
  "buffer_store_dwordx4 v[28:31], v34, s[20:23], s36 offen sc1\n\t" \
  "v_add_u32_sdwa v16, s29, v112 dst_sel:DWORD dst_unused:UNUSED_PAD src0_sel:DWORD src1_sel:WORD_0\n\t" \
  "v_smfmac_f32_16x16x64_bf16 v[40:43], a[76:79], v[126:133], v150 abid:1\n\t" \
  "s_and_b32 s44, s47, 18432\n\t" \
  "ds_read_b128 v[138:141], v184 offset:6144\n\t" \
  "v_add_u32_sdwa v18, s29, v112 dst_sel:DWORD dst_unused:UNUSED_PAD src0_sel:DWORD src1_sel:WORD_1\n\t" \
  "v_add_u32 v28, s38, v187\n\t" \
  "v_smfmac_f32_16x16x64_bf16 v[44:47], a[80:83], v[126:133], v151\n\t" \
  "s_and_b32 s27, s64, 7\n\t" \
  "ds_read_b64 v[6:7], v6 offset:0\n\t" \
  "ds_read_b128 v[28:31], v28 offset:10240\n\t" \
  "v_add_u32_sdwa v20, s29, v113 dst_sel:DWORD dst_unused:UNUSED_PAD src0_sel:DWORD src1_sel:WORD_0\n\t" \
  "v_smfmac_f32_16x16x64_bf16 v[48:51], a[84:87], v[126:133], v151 abid:1\n\t" \
  "s_lshl2_add_u32 s66, s27, s68\n\t" \
  "s_add_u32 s36, s36, 4608\n\t" \
  "ds_read_b64 v[8:9], v8 offset:0\n\t" \
  "v_add_u32_sdwa v22, s29, v113 dst_sel:DWORD dst_unused:UNUSED_PAD src0_sel:DWORD src1_sel:WORD_1\n\t" \
  "v_smfmac_f32_16x16x64_bf16 v[52:55], a[88:91], v[126:133], v152\n\t" \
  "s_and_b32 s27, s64, 8\n\t" \
  "ds_write_b64 v25, v[26:27] offset:16384\n\t" \
  "v_smfmac_f32_16x16x64_bf16 v[56:59], a[92:95], v[126:133], v152 abid:1\n\t" \
  "s_mul_i32 s27, s27, 176\n\t" \
  "ds_read_b64 v[10:11], v10 offset:0\n\t" \
  "ds_read_b64 v[12:13], v12 offset:0\n\t" \
  "v_smfmac_f32_16x16x64_bf16 v[60:63], a[96:99], v[126:133], v153\n\t" \
  "s_waitcnt lgkmcnt(4)\n\t" \
  "ds_read_b64 v[14:15], v14 offset:0\n\t" \
  "s_waitcnt lgkmcnt(5)\n\t" \
  "buffer_store_dwordx4 v[28:31], v34, s[20:23], s36 offen sc1\n\t" \
  "v_smfmac_f32_16x16x64_bf16 v[36:39], v[72:75], v[0:7], v182 abid:1\n\t" \
  "s_add_u32 s66, s66, s27\n\t" \
  "ds_read_b64 v[16:17], v16 offset:0\n\t" \
  "v_add_u32_sdwa v0, s29, v114 dst_sel:DWORD dst_unused:UNUSED_PAD src0_sel:DWORD src1_sel:WORD_0\n\t" \
  "v_xad_u32 v28, v187, 64, s38\n\t" \
  "v_smfmac_f32_16x16x64_bf16 v[64:67], a[100:103], v[126:133], v153 abid:1\n\t" \
  "s_lshr_b64 s[64:65], s[64:65], 4\n\t" \
  "s_add_u32 s38, s38, s44\n\t" \
  "ds_read_b64 v[18:19], v18 offset:0\n\t" \
  "ds_read_b128 v[28:31], v28 offset:11264\n\t" \
  "v_add_u32_sdwa v2, s29, v114 dst_sel:DWORD dst_unused:UNUSED_PAD src0_sel:DWORD src1_sel:WORD_1\n\t" \
  "v_smfmac_f32_16x16x64_bf16 v[68:71], a[104:107], v[126:133], v179\n\t" \
  "s_add_u32 s36, s36, 4608\n\t" \
  "ds_read_b128 v[126:129], v183 offset:8192\n\t" \
  "v_add_u32_sdwa v4, s29, v115 dst_sel:DWORD dst_unused:UNUSED_PAD src0_sel:DWORD src1_sel:WORD_0\n\t" \
  "v_smfmac_f32_16x16x64_bf16 v[36:39], a[108:111], v[134:141], v154\n\t" \
  "s_waitcnt lgkmcnt(3)\n\t" \
  "s_sub_u32 s44, s38, 129024\n\t" \
  "ds_read_b128 v[130:133], v184 offset:8192\n\t" \
  "v_add_u32_sdwa v6, s29, v115 dst_sel:DWORD dst_unused:UNUSED_PAD src0_sel:DWORD src1_sel:WORD_1\n\t" \
  "v_smfmac_f32_16x16x64_bf16 v[40:43], v[76:79], v[8:15], v182 abid:1\n\t" \
  "s_min_u32 s38, s38, s44\n\t" \
  "ds_read_b64 v[20:21], v20 offset:0\n\t" \
  "v_smfmac_f32_16x16x64_bf16 v[40:43], a[112:115], v[134:141], v154 abid:1\n\t" \
  "ds_read_b64 v[22:23], v22 offset:0\n\t" \
  "s_waitcnt lgkmcnt(4)\n\t" \
  "buffer_store_dwordx4 v[28:31], v34, s[20:23], s36 offen sc1\n\t" \
  "v_smfmac_f32_16x16x64_bf16 v[44:47], a[116:119], v[134:141], v155\n\t" \
  "ds_read_b64 v[0:1], v0 offset:0\n\t" \
  "v_add_u32_sdwa v8, s29, v32 dst_sel:DWORD dst_unused:UNUSED_PAD src0_sel:DWORD src1_sel:WORD_0\n\t" \
  "v_smfmac_f32_16x16x64_bf16 v[48:51], a[120:123], v[134:141], v155 abid:1\n\t" \
  "s_branch .Ljn22%=\n\t" \
  ".fill (.Lvar%=+30076-.)/4, 4, 0xbf800000\n\t" \
  "s_nop 0\n\t" \
  "s_branch .Lfin%=\n\t" \
  "s_branch .Lhe%=\n\t" \
  "s_waitcnt vmcnt(6)\n\t" \
  "s_waitcnt vmcnt(10)\n\t" \
  "s_waitcnt vmcnt(12)\n\t" \
  "s_waitcnt vmcnt(17)\n\t" \
  "s_waitcnt vmcnt(21) lgkmcnt(0)\n\t" \
  "s_barrier\n\t" \
  "v_smfmac_f32_16x16x64_bf16 v[36:39], v[220:223], v[0:7], v174\n\t" \
  "ds_read_b128 v[126:129], v183 offset:0\n\t" \
  "v_add_u32 v24, s37, v185\n\t" \
  "v_smfmac_f32_16x16x64_bf16 v[40:43], v[224:227], v[0:7], v174 abid:1\n\t" \
  "ds_read_b128 v[130:133], v184 offset:0\n\t" \
  "v_xor_b32 v25, s41, v183\n\t" \
  "v_smfmac_f32_16x16x64_bf16 v[44:47], v[228:231], v[0:7], v175\n\t" \
  "ds_read_b128 v[134:137], v183 offset:2048\n\t" \
  "v_subrev_u32 v25, s30, v25\n\t" \
  "v_smfmac_f32_16x16x64_bf16 v[48:51], v[232:235], v[0:7], v175 abid:1\n\t" \
  "s_add_u32 s30, s29, 18432\n\t" \
  "ds_read_b128 v[138:141], v184 offset:2048\n\t" \
  "v_cvt_pk_bf16_f32 v8, v36, v37\n\t" \
  "v_cvt_pk_bf16_f32 v9, v38, v39\n\t" \
  "v_smfmac_f32_16x16x64_bf16 v[52:55], v[236:239], v[0:7], v176\n\t" \
  "s_sub_u32 s25, s30, 129024\n\t" \
  "ds_read_b128 v[36:39], v35 offset:0\n\t" \
  "v_cvt_pk_bf16_f32 v10, v40, v41\n\t" \
  "v_cvt_pk_bf16_f32 v11, v42, v43\n\t" \
  "v_smfmac_f32_16x16x64_bf16 v[56:59], v[240:243], v[0:7], v176 abid:1\n\t" \
  "s_min_u32 s30, s30, s25\n\t" \
  "ds_read_b128 v[40:43], v35 offset:64\n\t" \
  "v_cvt_pk_bf16_f32 v16, v44, v45\n\t" \
  "v_cvt_pk_bf16_f32 v17, v46, v47\n\t" \
  "v_smfmac_f32_16x16x64_bf16 v[60:63], v[244:247], v[0:7], v177\n\t" \
  "s_sub_u32 s30, s30, s29\n\t" \
  "ds_read_b128 v[44:47], v35 offset:128\n\t" \
  "v_cvt_pk_bf16_f32 v18, v48, v49\n\t" \
  "v_cvt_pk_bf16_f32 v19, v50, v51\n\t" \
  "v_smfmac_f32_16x16x64_bf16 v[64:67], v[248:251], v[0:7], v177 abid:1\n\t" \
  "s_mov_b32 s37, s29\n\t" \
  "ds_read_b128 v[48:51], v35 offset:192\n\t" \
  "v_cvt_pk_bf16_f32 v12, v52, v53\n\t" \
  "v_cvt_pk_bf16_f32 v13, v54, v55\n\t" \
  "v_smfmac_f32_16x16x64_bf16 v[68:71], v[252:255], v[0:7], v182\n\t" \
  "s_waitcnt lgkmcnt(3)\n\t" \
  "ds_read_b128 v[52:55], v35 offset:256\n\t" \
  "v_cvt_pk_bf16_f32 v14, v56, v57\n\t" \
  "v_cvt_pk_bf16_f32 v15, v58, v59\n\t" \
  "v_smfmac_f32_16x16x64_bf16 v[36:39], a[0:3], v[126:133], v142\n\t" \
  "s_waitcnt lgkmcnt(3)\n\t" \
  "ds_read_b128 v[56:59], v35 offset:320\n\t" \
  "v_cvt_pk_bf16_f32 v20, v60, v61\n\t" \
  "v_cvt_pk_bf16_f32 v21, v62, v63\n\t" \
  "v_smfmac_f32_16x16x64_bf16 v[40:43], a[4:7], v[126:133], v142 abid:1\n\t" \
  "s_waitcnt lgkmcnt(3)\n\t" \
  "ds_read_b128 v[60:63], v35 offset:384\n\t" \
  "v_cvt_pk_bf16_f32 v22, v64, v65\n\t" \
  "v_cvt_pk_bf16_f32 v23, v66, v67\n\t" \
  "v_smfmac_f32_16x16x64_bf16 v[44:47], a[8:11], v[126:133], v143\n\t" \
  "s_waitcnt lgkmcnt(3)\n\t" \
  "ds_read_b128 v[64:67], v35 offset:448\n\t" \
  "v_cvt_pk_bf16_f32 v26, v68, v69\n\t" \
  "v_cvt_pk_bf16_f32 v27, v70, v71\n\t" \
  "v_smfmac_f32_16x16x64_bf16 v[48:51], a[12:15], v[126:133], v143 abid:1\n\t" \
  "s_waitcnt lgkmcnt(3)\n\t" \
  "ds_read_b128 v[68:71], v35 offset:512\n\t" \
  "v_add_u32_sdwa v0, s29, v108 dst_sel:DWORD dst_unused:UNUSED_PAD src0_sel:DWORD src1_sel:WORD_0\n\t" \
  "v_smfmac_f32_16x16x64_bf16 v[52:55], a[16:19], v[126:133], v144\n\t" \
  "s_add_u32 m0, s33, 0\n\t" \
  "s_waitcnt lgkmcnt(3)\n\t" \
  "ds_read_b64 v[0:1], v0 offset:0\n\t" \
  "v_add_u32_sdwa v2, s29, v108 dst_sel:DWORD dst_unused:UNUSED_PAD src0_sel:DWORD src1_sel:WORD_1\n\t" \
  "v_smfmac_f32_16x16x64_bf16 v[56:59], a[20:23], v[126:133], v144 abid:1\n\t" \
  "s_waitcnt lgkmcnt(3)\n\t" \
  "buffer_load_dwordx4 v186, s[16:19], s34 offen offset:0 nt lds\n\t" \
  "v_add_u32_sdwa v4, s29, v109 dst_sel:DWORD dst_unused:UNUSED_PAD src0_sel:DWORD src1_sel:WORD_0\n\t" \
  "v_smfmac_f32_16x16x64_bf16 v[60:63], a[24:27], v[126:133], v145\n\t" \
  "s_add_u32 m0, s33, 1920\n\t" \
  "s_waitcnt lgkmcnt(2)\n\t" \
  "ds_write_b128 v24, v[8:11] offset:0\n\t" \
  "v_add_u32_sdwa v6, s29, v109 dst_sel:DWORD dst_unused:UNUSED_PAD src0_sel:DWORD src1_sel:WORD_1\n\t" \
  "v_smfmac_f32_16x16x64_bf16 v[64:67], a[28:31], v[126:133], v145 abid:1\n\t" \
  "s_waitcnt lgkmcnt(2)\n\t" \
  "buffer_load_dwordx4 v186, s[16:19], s34 offen offset:128 nt lds\n\t" \
  "v_smfmac_f32_16x16x64_bf16 v[68:71], a[32:35], v[126:133], v178\n\t" \
  "s_add_u32 m0, s33, 3840\n\t" \
  "ds_read_b128 v[126:129], v183 offset:4096\n\t" \
  "v_smfmac_f32_16x16x64_bf16 v[36:39], a[36:39], v[134:141], v146\n\t" \
  "buffer_load_dwordx4 v186, s[16:19], s34 offen offset:256 nt lds\n\t" \
  "v_smfmac_f32_16x16x64_bf16 v[40:43], a[40:43], v[134:141], v146 abid:1\n\t" \
  "s_add_u32 m0, s33, 5760\n\t" \
  "ds_write_b128 v24, v[12:15] offset:128\n\t" \
  "v_xor_b32 v24, 64, v24\n\t" \
  "v_smfmac_f32_16x16x64_bf16 v[44:47], a[44:47], v[134:141], v147\n\t" \
  "buffer_load_dwordx4 v186, s[16:19], s34 offen offset:384 nt lds\n\t" \
  "v_add_u32_sdwa v8, s29, v110 dst_sel:DWORD dst_unused:UNUSED_PAD src0_sel:DWORD src1_sel:WORD_0\n\t" \
  "v_smfmac_f32_16x16x64_bf16 v[48:51], a[48:51], v[134:141], v147 abid:1\n\t" \
  "s_add_u32 m0, s33, 7680\n\t" \
  "ds_read_b128 v[130:133], v184 offset:4096\n\t" \
  "v_add_u32_sdwa v10, s29, v110 dst_sel:DWORD dst_unused:UNUSED_PAD src0_sel:DWORD src1_sel:WORD_1\n\t" \
  "v_smfmac_f32_16x16x64_bf16 v[52:55], a[52:55], v[134:141], v148\n\t" \
  "buffer_load_dwordx4 v186, s[16:19], s34 offen offset:512 nt lds\n\t" \
  "v_add_u32_sdwa v12, s29, v111 dst_sel:DWORD dst_unused:UNUSED_PAD src0_sel:DWORD src1_sel:WORD_0\n\t" \
  "v_smfmac_f32_16x16x64_bf16 v[56:59], a[56:59], v[134:141], v148 abid:1\n\t" \
  "s_add_u32 s31, s31, 18432\n\t" \
  "ds_write_b128 v24, v[16:19] offset:0\n\t" \
  "v_add_u32_sdwa v14, s29, v111 dst_sel:DWORD dst_unused:UNUSED_PAD src0_sel:DWORD src1_sel:WORD_1\n\t" \
  "v_smfmac_f32_16x16x64_bf16 v[60:63], a[60:63], v[134:141], v149\n\t" \
  "s_sub_u32 s26, s31, 129024\n\t" \
  "ds_read_b64 v[2:3], v2 offset:0\n\t" \
  "v_smfmac_f32_16x16x64_bf16 v[64:67], a[64:67], v[134:141], v149 abid:1\n\t" \
  "s_min_u32 s31, s31, s26\n\t" \
  "ds_read_b64 v[4:5], v4 offset:0\n\t" \
  "v_smfmac_f32_16x16x64_bf16 v[68:71], a[68:71], v[134:141], v178 abid:1\n\t" \
  "s_waitcnt lgkmcnt(3)\n\t" \
  "ds_read_b128 v[134:137], v183 offset:6144\n\t" \
  "v_smfmac_f32_16x16x64_bf16 v[36:39], a[72:75], v[126:133], v150\n\t" \
  "s_add_u32 s33, s31, s32\n\t" \
  "ds_write_b128 v24, v[20:23] offset:128\n\t" \
  "v_add_u32_sdwa v16, s29, v112 dst_sel:DWORD dst_unused:UNUSED_PAD src0_sel:DWORD src1_sel:WORD_0\n\t" \
  "v_smfmac_f32_16x16x64_bf16 v[40:43], a[76:79], v[126:133], v150 abid:1\n\t" \
  "s_add_u32 s34, s34, 4718592\n\t" \
  "ds_read_b128 v[138:141], v184 offset:6144\n\t" \
  "v_add_u32_sdwa v18, s29, v112 dst_sel:DWORD dst_unused:UNUSED_PAD src0_sel:DWORD src1_sel:WORD_1\n\t" \
  "v_smfmac_f32_16x16x64_bf16 v[44:47], a[80:83], v[126:133], v151\n\t" \
  "s_and_b32 s27, s64, 7\n\t" \
  "ds_read_b64 v[6:7], v6 offset:0\n\t" \
  "v_add_u32_sdwa v20, s29, v113 dst_sel:DWORD dst_unused:UNUSED_PAD src0_sel:DWORD src1_sel:WORD_0\n\t" \
  "v_smfmac_f32_16x16x64_bf16 v[48:51], a[84:87], v[126:133], v151 abid:1\n\t" \
  "s_lshl2_add_u32 s66, s27, s68\n\t" \
  "ds_read_b64 v[8:9], v8 offset:0\n\t" \
  "v_add_u32_sdwa v22, s29, v113 dst_sel:DWORD dst_unused:UNUSED_PAD src0_sel:DWORD src1_sel:WORD_1\n\t" \
  "v_smfmac_f32_16x16x64_bf16 v[52:55], a[88:91], v[126:133], v152\n\t" \
  "s_and_b32 s27, s64, 8\n\t" \
  "ds_write_b64 v25, v[26:27] offset:16384\n\t" \
  "v_smfmac_f32_16x16x64_bf16 v[56:59], a[92:95], v[126:133], v152 abid:1\n\t" \
  "s_mul_i32 s27, s27, 176\n\t" \
  "ds_read_b64 v[10:11], v10 offset:0\n\t" \
  "ds_read_b64 v[12:13], v12 offset:0\n\t" \
  "v_smfmac_f32_16x16x64_bf16 v[60:63], a[96:99], v[126:133], v153\n\t" \
  "s_waitcnt lgkmcnt(4)\n\t" \
  "ds_read_b64 v[14:15], v14 offset:0\n\t" \
  "v_smfmac_f32_16x16x64_bf16 v[36:39], v[72:75], v[0:7], v182 abid:1\n\t" \
  "s_add_u32 s66, s66, s27\n\t" \
  "ds_read_b64 v[16:17], v16 offset:0\n\t" \
  "v_add_u32_sdwa v0, s29, v114 dst_sel:DWORD dst_unused:UNUSED_PAD src0_sel:DWORD src1_sel:WORD_0\n\t" \
  "v_smfmac_f32_16x16x64_bf16 v[64:67], a[100:103], v[126:133], v153 abid:1\n\t" \
  "s_lshr_b64 s[64:65], s[64:65], 4\n\t" \
  "ds_read_b64 v[18:19], v18 offset:0\n\t" \
  "v_add_u32_sdwa v2, s29, v114 dst_sel:DWORD dst_unused:UNUSED_PAD src0_sel:DWORD src1_sel:WORD_1\n\t" \
  "v_smfmac_f32_16x16x64_bf16 v[68:71], a[104:107], v[126:133], v179\n\t" \
  "ds_read_b128 v[126:129], v183 offset:8192\n\t" \
  "v_add_u32_sdwa v4, s29, v115 dst_sel:DWORD dst_unused:UNUSED_PAD src0_sel:DWORD src1_sel:WORD_0\n\t" \
  "v_smfmac_f32_16x16x64_bf16 v[36:39], a[108:111], v[134:141], v154\n\t" \
  "s_waitcnt lgkmcnt(3)\n\t" \
  "ds_read_b128 v[130:133], v184 offset:8192\n\t" \
  "v_add_u32_sdwa v6, s29, v115 dst_sel:DWORD dst_unused:UNUSED_PAD src0_sel:DWORD src1_sel:WORD_1\n\t" \
  "v_smfmac_f32_16x16x64_bf16 v[40:43], v[76:79], v[8:15], v182 abid:1\n\t" \
  "ds_read_b64 v[20:21], v20 offset:0\n\t" \
  "v_smfmac_f32_16x16x64_bf16 v[40:43], a[112:115], v[134:141], v154 abid:1\n\t" \
  "ds_read_b64 v[22:23], v22 offset:0\n\t" \
  "v_smfmac_f32_16x16x64_bf16 v[44:47], a[116:119], v[134:141], v155\n\t" \
  "ds_read_b64 v[0:1], v0 offset:0\n\t" \
  "v_add_u32_sdwa v8, s29, v32 dst_sel:DWORD dst_unused:UNUSED_PAD src0_sel:DWORD src1_sel:WORD_0\n\t" \
  "v_smfmac_f32_16x16x64_bf16 v[48:51], a[120:123], v[134:141], v155 abid:1\n\t" \
  ".Ljn22%=:\n\t" \
  "ds_read_b64 v[2:3], v2 offset:0\n\t" \
  "v_add_u32_sdwa v10, s29, v32 dst_sel:DWORD dst_unused:UNUSED_PAD src0_sel:DWORD src1_sel:WORD_1\n\t" \
  "v_smfmac_f32_16x16x64_bf16 v[52:55], a[124:127], v[134:141], v156\n\t" \
  "ds_read_b64 v[4:5], v4 offset:0\n\t" \
  "v_add_u32 v28, s60, v187\n\t" \
  "v_smfmac_f32_16x16x64_bf16 v[56:59], a[128:131], v[134:141], v156 abid:1\n\t" \
  "s_waitcnt lgkmcnt(3)\n\t" \
  "ds_read_b64 v[6:7], v6 offset:0\n\t" \
  "v_smfmac_f32_16x16x64_bf16 v[44:47], v[80:83], v[16:23], v182 abid:1\n\t" \
  "ds_read_b64 v[8:9], v8 offset:0\n\t" \
  "v_add_u32_sdwa v16, s29, v116 dst_sel:DWORD dst_unused:UNUSED_PAD src0_sel:DWORD src1_sel:WORD_0\n\t" \
  "v_smfmac_f32_16x16x64_bf16 v[60:63], a[132:135], v[134:141], v157\n\t" \
  "ds_read_b64 v[10:11], v10 offset:0\n\t" \
  "v_add_u32_sdwa v18, s29, v116 dst_sel:DWORD dst_unused:UNUSED_PAD src0_sel:DWORD src1_sel:WORD_1\n\t" \
  "v_smfmac_f32_16x16x64_bf16 v[64:67], a[136:139], v[134:141], v157 abid:1\n\t" \
  "ds_read_b128 v[28:31], v28 offset:0\n\t" \
  "v_add_u32_sdwa v20, s29, v117 dst_sel:DWORD dst_unused:UNUSED_PAD src0_sel:DWORD src1_sel:WORD_0\n\t" \
  "v_smfmac_f32_16x16x64_bf16 v[68:71], a[140:143], v[134:141], v179 abid:1\n\t" \
  "s_waitcnt lgkmcnt(3)\n\t" \
  "ds_read_b128 v[134:137], v183 offset:10240\n\t" \
  "v_add_u32_sdwa v22, s29, v117 dst_sel:DWORD dst_unused:UNUSED_PAD src0_sel:DWORD src1_sel:WORD_1\n\t" \
  "v_smfmac_f32_16x16x64_bf16 v[48:51], v[84:87], v[0:7], v182 abid:1\n\t" \
  "ds_read_b128 v[138:141], v184 offset:10240\n\t" \
  "v_add_u32 v24, s38, v187\n\t" \
  "v_smfmac_f32_16x16x64_bf16 v[36:39], a[144:147], v[126:133], v158\n\t" \
  "ds_read_b128 v[24:27], v24 offset:8192\n\t" \
  "v_add_u32_sdwa v0, s29, v118 dst_sel:DWORD dst_unused:UNUSED_PAD src0_sel:DWORD src1_sel:WORD_0\n\t" \
  "v_smfmac_f32_16x16x64_bf16 v[40:43], a[148:151], v[126:133], v158 abid:1\n\t" \
  "s_waitcnt lgkmcnt(3)\n\t" \
  "ds_read_b64 v[16:17], v16 offset:0\n\t" \
  "v_add_u32_sdwa v2, s29, v118 dst_sel:DWORD dst_unused:UNUSED_PAD src0_sel:DWORD src1_sel:WORD_1\n\t" \
  "s_set_gpr_idx_on s6, gpr_idx(DST)\n\t" \
  "v_smfmac_f32_16x16x64_bf16 v[36:39], v[28:31], v[8:15], v182 abid:1\n\t" \
  "s_set_gpr_idx_off\n\t" \
  "ds_read_b64 v[18:19], v18 offset:0\n\t" \
  "v_add_u32_sdwa v4, s29, v119 dst_sel:DWORD dst_unused:UNUSED_PAD src0_sel:DWORD src1_sel:WORD_0\n\t" \
  "v_smfmac_f32_16x16x64_bf16 v[44:47], a[152:155], v[126:133], v159\n\t" \
  "s_add_u32 s36, s35, s39\n\t" \
  "ds_read_b64 v[20:21], v20 offset:0\n\t" \
  "v_add_u32_sdwa v6, s29, v119 dst_sel:DWORD dst_unused:UNUSED_PAD src0_sel:DWORD src1_sel:WORD_1\n\t" \
  "v_smfmac_f32_16x16x64_bf16 v[48:51], a[156:159], v[126:133], v159 abid:1\n\t" \
  "s_waitcnt lgkmcnt(3)\n\t" \
  "buffer_store_dwordx4 v[24:27], v34, s[20:23], s36 offen sc1\n\t" \
  "v_add_u32_sdwa v8, s29, v33 dst_sel:DWORD dst_unused:UNUSED_PAD src0_sel:DWORD src1_sel:WORD_0\n\t" \
  "v_smfmac_f32_16x16x64_bf16 v[52:55], a[160:163], v[126:133], v160\n\t" \
  "ds_read_b64 v[22:23], v22 offset:0\n\t" \
  "v_xad_u32 v24, v187, 64, s38\n\t" \
  "v_smfmac_f32_16x16x64_bf16 v[56:59], a[164:167], v[126:133], v160 abid:1\n\t" \
  "ds_read_b128 v[24:27], v24 offset:9216\n\t" \
  "v_add_u32_sdwa v10, s29, v33 dst_sel:DWORD dst_unused:UNUSED_PAD src0_sel:DWORD src1_sel:WORD_1\n\t" \
  "v_smfmac_f32_16x16x64_bf16 v[60:63], a[168:171], v[126:133], v161\n\t" \
  "ds_read_b64 v[0:1], v0 offset:0\n\t" \
  "v_add_u32 v28, s60, v187\n\t" \
  "v_smfmac_f32_16x16x64_bf16 v[64:67], a[172:175], v[126:133], v161 abid:1\n\t" \
  "ds_read_b64 v[2:3], v2 offset:0\n\t" \
  "v_smfmac_f32_16x16x64_bf16 v[68:71], a[176:179], v[126:133], v180\n\t" \
  "s_add_u32 s36, s36, 4608\n\t" \
  "s_waitcnt lgkmcnt(3)\n\t" \
  "ds_read_b128 v[126:129], v183 offset:12288\n\t" \
  "v_smfmac_f32_16x16x64_bf16 v[52:55], v[88:91], v[16:23], v182 abid:1\n\t" \
  "s_waitcnt lgkmcnt(3)\n\t" \
  "buffer_store_dwordx4 v[24:27], v34, s[20:23], s36 offen sc1\n\t" \
  "v_add_u32_sdwa v16, s29, v120 dst_sel:DWORD dst_unused:UNUSED_PAD src0_sel:DWORD src1_sel:WORD_0\n\t" \
  "v_smfmac_f32_16x16x64_bf16 v[36:39], a[180:183], v[134:141], v162\n\t" \
  "ds_read_b128 v[130:133], v184 offset:12288\n\t" \
  "v_add_u32 v24, s38, v187\n\t" \
  "v_smfmac_f32_16x16x64_bf16 v[40:43], a[184:187], v[134:141], v162 abid:1\n\t" \
  "ds_read_b128 v[24:27], v24 offset:10240\n\t" \
  "v_add_u32_sdwa v18, s29, v120 dst_sel:DWORD dst_unused:UNUSED_PAD src0_sel:DWORD src1_sel:WORD_1\n\t" \
  "v_smfmac_f32_16x16x64_bf16 v[44:47], a[188:191], v[134:141], v163\n\t" \
  "ds_read_b64 v[4:5], v4 offset:0\n\t" \
  "v_add_u32_sdwa v20, s29, v121 dst_sel:DWORD dst_unused:UNUSED_PAD src0_sel:DWORD src1_sel:WORD_0\n\t" \
  "v_smfmac_f32_16x16x64_bf16 v[48:51], a[192:195], v[134:141], v163 abid:1\n\t" \
  "ds_read_b64 v[6:7], v6 offset:0\n\t" \
  "v_add_u32_sdwa v22, s29, v121 dst_sel:DWORD dst_unused:UNUSED_PAD src0_sel:DWORD src1_sel:WORD_1\n\t" \
  "v_smfmac_f32_16x16x64_bf16 v[52:55], a[196:199], v[134:141], v164\n\t" \
  "s_add_u32 s36, s36, 4608\n\t" \
  "ds_read_b64 v[8:9], v8 offset:0\n\t" \
  "v_smfmac_f32_16x16x64_bf16 v[56:59], a[200:203], v[134:141], v164 abid:1\n\t" \
  "s_waitcnt lgkmcnt(3)\n\t" \
  "buffer_store_dwordx4 v[24:27], v34, s[20:23], s36 offen sc1\n\t" \
  "v_smfmac_f32_16x16x64_bf16 v[60:63], a[204:207], v[134:141], v165\n\t" \
  "ds_read_b64 v[10:11], v10 offset:0\n\t" \
  "v_xad_u32 v24, v187, 64, s38\n\t" \
  "v_smfmac_f32_16x16x64_bf16 v[64:67], a[208:211], v[134:141], v165 abid:1\n\t" \
  "s_waitcnt lgkmcnt(2)\n\t" \
  "ds_read_b128 v[24:27], v24 offset:11264\n\t" \
  "v_smfmac_f32_16x16x64_bf16 v[56:59], v[92:95], v[0:7], v182 abid:1\n\t" \
  "s_add_u32 s38, s38, 18432\n\t" \
  "ds_read_b128 v[28:31], v28 offset:1280\n\t" \
  "v_smfmac_f32_16x16x64_bf16 v[68:71], a[212:215], v[134:141], v180 abid:1\n\t" \
  "s_sub_u32 s44, s38, 129024\n\t" \
  "ds_read_b128 v[134:137], v183 offset:14336\n\t" \
  "v_smfmac_f32_16x16x64_bf16 v[36:39], a[216:219], v[126:133], v166\n\t" \
  "s_add_u32 s36, s36, 4608\n\t" \
  "ds_read_b128 v[138:141], v184 offset:14336\n\t" \
  "v_smfmac_f32_16x16x64_bf16 v[40:43], a[220:223], v[126:133], v166 abid:1\n\t" \
  "s_waitcnt lgkmcnt(3)\n\t" \
  "buffer_store_dwordx4 v[24:27], v34, s[20:23], s36 offen sc1\n\t" \
  "v_smfmac_f32_16x16x64_bf16 v[44:47], a[224:227], v[126:133], v167\n\t" \
  "s_waitcnt lgkmcnt(2)\n\t" \
  "ds_read_b64 v[16:17], v16 offset:0\n\t" \
  "s_set_gpr_idx_on s7, gpr_idx(DST)\n\t" \
  "v_smfmac_f32_16x16x64_bf16 v[36:39], v[28:31], v[8:15], v182 abid:1\n\t" \
  "s_set_gpr_idx_off\n\t" \
  "ds_read_b64 v[18:19], v18 offset:0\n\t" \
  "v_add_u32_sdwa v8, s29, v122 dst_sel:DWORD dst_unused:UNUSED_PAD src0_sel:DWORD src1_sel:WORD_0\n\t" \
  "v_smfmac_f32_16x16x64_bf16 v[48:51], a[228:231], v[126:133], v167 abid:1\n\t" \
  "s_min_u32 s38, s38, s44\n\t" \
  "ds_read_b64 v[20:21], v20 offset:0\n\t" \
  "v_add_u32_sdwa v10, s29, v122 dst_sel:DWORD dst_unused:UNUSED_PAD src0_sel:DWORD src1_sel:WORD_1\n\t" \
  "v_smfmac_f32_16x16x64_bf16 v[52:55], a[232:235], v[126:133], v168\n\t" \
  "s_add_u32 s35, s35, 4718592\n\t" \
  "ds_read_b64 v[22:23], v22 offset:0\n\t" \
  "v_add_u32_sdwa v12, s29, v123 dst_sel:DWORD dst_unused:UNUSED_PAD src0_sel:DWORD src1_sel:WORD_0\n\t" \
  "v_smfmac_f32_16x16x64_bf16 v[56:59], a[236:239], v[126:133], v168 abid:1\n\t" \
  "ds_read_b64 v[8:9], v8 offset:0\n\t" \
  "v_add_u32_sdwa v14, s29, v123 dst_sel:DWORD dst_unused:UNUSED_PAD src0_sel:DWORD src1_sel:WORD_1\n\t" \
  "v_smfmac_f32_16x16x64_bf16 v[60:63], a[240:243], v[126:133], v169\n\t" \
  "ds_read_b64 v[10:11], v10 offset:0\n\t" \
  "v_smfmac_f32_16x16x64_bf16 v[64:67], a[244:247], v[126:133], v169 abid:1\n\t" \
  "ds_read_b64 v[12:13], v12 offset:0\n\t" \
  "v_smfmac_f32_16x16x64_bf16 v[68:71], a[248:251], v[126:133], v181\n\t" \
  "s_waitcnt lgkmcnt(3)\n\t" \
  "ds_read_b128 v[0:3], v183 offset:16384\n\t" \
  "v_smfmac_f32_16x16x64_bf16 v[60:63], v[96:99], v[16:23], v182 abid:1\n\t" \
  "ds_read_b128 v[4:7], v184 offset:16384\n\t" \
  "v_add_u32_sdwa v16, s29, v124 dst_sel:DWORD dst_unused:UNUSED_PAD src0_sel:DWORD src1_sel:WORD_0\n\t" \
  "v_smfmac_f32_16x16x64_bf16 v[36:39], a[252:255], v[134:141], v170\n\t" \
  "ds_read_b64 v[14:15], v14 offset:0\n\t" \
  "v_add_u32_sdwa v18, s29, v124 dst_sel:DWORD dst_unused:UNUSED_PAD src0_sel:DWORD src1_sel:WORD_1\n\t" \
  "v_smfmac_f32_16x16x64_bf16 v[40:43], v[188:191], v[134:141], v170 abid:1\n\t" \
  "ds_read_b64 v[16:17], v16 offset:0\n\t" \
  "v_add_u32_sdwa v20, s29, v125 dst_sel:DWORD dst_unused:UNUSED_PAD src0_sel:DWORD src1_sel:WORD_0\n\t" \
  "v_smfmac_f32_16x16x64_bf16 v[44:47], v[192:195], v[134:141], v171\n\t" \
  "ds_read_b64 v[18:19], v18 offset:0\n\t" \
  "v_add_u32_sdwa v22, s29, v125 dst_sel:DWORD dst_unused:UNUSED_PAD src0_sel:DWORD src1_sel:WORD_1\n\t" \
  "v_smfmac_f32_16x16x64_bf16 v[48:51], v[196:199], v[134:141], v171 abid:1\n\t" \
  "s_add_u32 s29, s29, s30\n\t" \
  "ds_read_b64 v[20:21], v20 offset:0\n\t" \
  "v_add_u32 v183, s30, v183\n\t" \
  "v_smfmac_f32_16x16x64_bf16 v[52:55], v[200:203], v[134:141], v172\n\t" \
  "s_waitcnt lgkmcnt(3)\n\t" \
  "ds_read_b64 v[22:23], v22 offset:0\n\t" \
  "v_add_u32 v184, s30, v184\n\t" \
  "v_smfmac_f32_16x16x64_bf16 v[64:67], v[100:103], v[8:15], v182 abid:1\n\t" \
  "v_smfmac_f32_16x16x64_bf16 v[56:59], v[204:207], v[134:141], v172 abid:1\n\t" \
  "v_smfmac_f32_16x16x64_bf16 v[60:63], v[208:211], v[134:141], v173\n\t" \
  "v_smfmac_f32_16x16x64_bf16 v[64:67], v[212:215], v[134:141], v173 abid:1\n\t" \
  "s_waitcnt lgkmcnt(0)\n\t" \
  "v_smfmac_f32_16x16x64_bf16 v[68:71], v[104:107], v[16:23], v182 abid:1\n\t" \
  "v_smfmac_f32_16x16x64_bf16 v[68:71], v[216:219], v[134:141], v181 abid:1\n\t" \
  "s_setpc_b64 s[66:67]\n\t" \
  ".fill (.Lvar%=+32768-.)/4, 4, 0xbf800000\n\t" \
  "s_branch .Lfin%=\n\t" \
  "s_branch .Lhe%=\n\t" \
  "s_waitcnt vmcnt(6)\n\t" \
  "s_waitcnt vmcnt(10)\n\t" \
  "s_waitcnt vmcnt(12)\n\t" \
  "s_waitcnt vmcnt(17)\n\t" \
  "s_waitcnt vmcnt(21) lgkmcnt(0)\n\t" \
  "s_barrier\n\t" \
  "v_smfmac_f32_16x16x64_bf16 v[36:39], v[220:223], v[0:7], v174\n\t" \
  "ds_read_b128 v[126:129], v183 offset:0\n\t" \
  "v_add_u32 v24, s37, v185\n\t" \
  "v_lshrrev_b32 v28, 4, v187\n\t" \
  "v_smfmac_f32_16x16x64_bf16 v[40:43], v[224:227], v[0:7], v174 abid:1\n\t" \
  "ds_read_b128 v[130:133], v184 offset:0\n\t" \
  "v_xor_b32 v25, s41, v183\n\t" \
  "v_and_or_b32 v28, v28, 48, s45\n\t" \
  "v_smfmac_f32_16x16x64_bf16 v[44:47], v[228:231], v[0:7], v175\n\t" \
  "ds_read_b128 v[134:137], v183 offset:2048\n\t" \
  "v_subrev_u32 v25, s30, v25\n\t" \
  "v_smfmac_f32_16x16x64_bf16 v[48:51], v[232:235], v[0:7], v175 abid:1\n\t" \
  "s_add_u32 s30, s29, 18432\n\t" \
  "ds_read_b128 v[138:141], v184 offset:2048\n\t" \
  "v_cvt_pk_bf16_f32 v8, v36, v37\n\t" \
  "v_cvt_pk_bf16_f32 v9, v38, v39\n\t" \
  "v_smfmac_f32_16x16x64_bf16 v[52:55], v[236:239], v[0:7], v176\n\t" \
  "s_sub_u32 s25, s30, 129024\n\t" \
  "ds_read_b128 v[36:39], v28 offset:0\n\t" \
  "v_cvt_pk_bf16_f32 v10, v40, v41\n\t" \
  "v_cvt_pk_bf16_f32 v11, v42, v43\n\t" \
  "v_smfmac_f32_16x16x64_bf16 v[56:59], v[240:243], v[0:7], v176 abid:1\n\t" \
  "s_min_u32 s30, s30, s25\n\t" \
  "ds_read_b128 v[40:43], v28 offset:64\n\t" \
  "v_cvt_pk_bf16_f32 v16, v44, v45\n\t" \
  "v_cvt_pk_bf16_f32 v17, v46, v47\n\t" \
  "v_smfmac_f32_16x16x64_bf16 v[60:63], v[244:247], v[0:7], v177\n\t" \
  "s_sub_u32 s30, s30, s29\n\t" \
  "ds_read_b128 v[44:47], v28 offset:128\n\t" \
  "v_cvt_pk_bf16_f32 v18, v48, v49\n\t" \
  "v_cvt_pk_bf16_f32 v19, v50, v51\n\t" \
  "v_smfmac_f32_16x16x64_bf16 v[64:67], v[248:251], v[0:7], v177 abid:1\n\t" \
  "s_mov_b32 s37, s29\n\t" \
  "ds_read_b128 v[48:51], v28 offset:192\n\t" \
  "v_cvt_pk_bf16_f32 v12, v52, v53\n\t" \
  "v_cvt_pk_bf16_f32 v13, v54, v55\n\t" \
  "v_smfmac_f32_16x16x64_bf16 v[68:71], v[252:255], v[0:7], v182\n\t" \
  "s_waitcnt lgkmcnt(3)\n\t" \
  "ds_read_b128 v[52:55], v28 offset:256\n\t" \
  "v_cvt_pk_bf16_f32 v14, v56, v57\n\t" \
  "v_cvt_pk_bf16_f32 v15, v58, v59\n\t" \
  "v_smfmac_f32_16x16x64_bf16 v[36:39], a[0:3], v[126:133], v142\n\t" \
  "s_waitcnt lgkmcnt(3)\n\t" \
  "ds_read_b128 v[56:59], v28 offset:320\n\t" \
  "v_cvt_pk_bf16_f32 v20, v60, v61\n\t" \
  "v_cvt_pk_bf16_f32 v21, v62, v63\n\t" \
  "v_smfmac_f32_16x16x64_bf16 v[40:43], a[4:7], v[126:133], v142 abid:1\n\t" \
  "s_waitcnt lgkmcnt(3)\n\t" \
  "ds_read_b128 v[60:63], v28 offset:384\n\t" \
  "v_cvt_pk_bf16_f32 v22, v64, v65\n\t" \
  "v_cvt_pk_bf16_f32 v23, v66, v67\n\t" \
  "v_smfmac_f32_16x16x64_bf16 v[44:47], a[8:11], v[126:133], v143\n\t" \
  "s_waitcnt lgkmcnt(3)\n\t" \
  "ds_read_b128 v[64:67], v28 offset:448\n\t" \
  "v_cvt_pk_bf16_f32 v26, v68, v69\n\t" \
  "v_cvt_pk_bf16_f32 v27, v70, v71\n\t" \
  "v_smfmac_f32_16x16x64_bf16 v[48:51], a[12:15], v[126:133], v143 abid:1\n\t" \
  "s_waitcnt lgkmcnt(3)\n\t" \
  "ds_read_b128 v[68:71], v28 offset:512\n\t" \
  "v_add_u32_sdwa v0, s29, v108 dst_sel:DWORD dst_unused:UNUSED_PAD src0_sel:DWORD src1_sel:WORD_0\n\t" \
  "v_smfmac_f32_16x16x64_bf16 v[52:55], a[16:19], v[126:133], v144\n\t" \
  "s_waitcnt lgkmcnt(3)\n\t" \
  "ds_read_b64 v[0:1], v0 offset:0\n\t" \
  "v_add_u32_sdwa v2, s29, v108 dst_sel:DWORD dst_unused:UNUSED_PAD src0_sel:DWORD src1_sel:WORD_1\n\t" \
  "v_smfmac_f32_16x16x64_bf16 v[56:59], a[20:23], v[126:133], v144 abid:1\n\t" \
  "s_waitcnt lgkmcnt(3)\n\t" \
  "v_add_u32_sdwa v4, s29, v109 dst_sel:DWORD dst_unused:UNUSED_PAD src0_sel:DWORD src1_sel:WORD_0\n\t" \
  "v_smfmac_f32_16x16x64_bf16 v[60:63], a[24:27], v[126:133], v145\n\t" \
  "s_waitcnt lgkmcnt(2)\n\t" \
  "ds_write_b128 v24, v[8:11] offset:0\n\t" \
  "v_add_u32_sdwa v6, s29, v109 dst_sel:DWORD dst_unused:UNUSED_PAD src0_sel:DWORD src1_sel:WORD_1\n\t" \
  "v_smfmac_f32_16x16x64_bf16 v[64:67], a[28:31], v[126:133], v145 abid:1\n\t" \
  "s_waitcnt lgkmcnt(2)\n\t" \
  "s_bfe_i32 s47, s48, 0x10000\n\t" \
  "v_add_u32 v28, s38, v187\n\t" \
  "v_smfmac_f32_16x16x64_bf16 v[68:71], a[32:35], v[126:133], v178\n\t" \
  "s_lshr_b32 s48, s48, 1\n\t" \
  "ds_read_b128 v[126:129], v183 offset:4096\n\t" \
  "ds_read_b128 v[28:31], v28 offset:8192\n\t" \
  "v_smfmac_f32_16x16x64_bf16 v[36:39], a[36:39], v[134:141], v146\n\t" \
  "s_add_u32 s36, s35, s39\n\t" \
  "v_smfmac_f32_16x16x64_bf16 v[40:43], a[40:43], v[134:141], v146 abid:1\n\t" \
  "s_andn2_b32 s44, 0x40000000, s47\n\t" \
  "ds_write_b128 v24, v[12:15] offset:128\n\t" \
  "v_xor_b32 v24, 64, v24\n\t" \
  "v_smfmac_f32_16x16x64_bf16 v[44:47], a[44:47], v[134:141], v147\n\t" \
  "s_or_b32 s36, s36, s44\n\t" \
  "v_add_u32_sdwa v8, s29, v110 dst_sel:DWORD dst_unused:UNUSED_PAD src0_sel:DWORD src1_sel:WORD_0\n\t" \
  "v_smfmac_f32_16x16x64_bf16 v[48:51], a[48:51], v[134:141], v147 abid:1\n\t" \
  "ds_read_b128 v[130:133], v184 offset:4096\n\t" \
  "s_waitcnt lgkmcnt(2)\n\t" \
  "buffer_store_dwordx4 v[28:31], v34, s[20:23], s36 offen sc1\n\t" \
  "v_add_u32_sdwa v10, s29, v110 dst_sel:DWORD dst_unused:UNUSED_PAD src0_sel:DWORD src1_sel:WORD_1\n\t" \
  "v_smfmac_f32_16x16x64_bf16 v[52:55], a[52:55], v[134:141], v148\n\t" \
  "s_and_b32 s44, s47, 4718592\n\t" \
  "v_add_u32_sdwa v12, s29, v111 dst_sel:DWORD dst_unused:UNUSED_PAD src0_sel:DWORD src1_sel:WORD_0\n\t" \
  "v_xad_u32 v28, v187, 64, s38\n\t" \
  "v_smfmac_f32_16x16x64_bf16 v[56:59], a[56:59], v[134:141], v148 abid:1\n\t" \
  "s_add_u32 s35, s35, s44\n\t" \
  "ds_write_b128 v24, v[16:19] offset:0\n\t" \
  "ds_read_b128 v[28:31], v28 offset:9216\n\t" \
  "v_add_u32_sdwa v14, s29, v111 dst_sel:DWORD dst_unused:UNUSED_PAD src0_sel:DWORD src1_sel:WORD_1\n\t" \
  "v_smfmac_f32_16x16x64_bf16 v[60:63], a[60:63], v[134:141], v149\n\t" \
  "s_add_u32 s36, s36, 4608\n\t" \
  "ds_read_b64 v[2:3], v2 offset:0\n\t" \
  "v_smfmac_f32_16x16x64_bf16 v[64:67], a[64:67], v[134:141], v149 abid:1\n\t" \
  "ds_read_b64 v[4:5], v4 offset:0\n\t" \
  "v_smfmac_f32_16x16x64_bf16 v[68:71], a[68:71], v[134:141], v178 abid:1\n\t" \
  "s_waitcnt lgkmcnt(3)\n\t" \
  "ds_read_b128 v[134:137], v183 offset:6144\n\t" \
  "v_smfmac_f32_16x16x64_bf16 v[36:39], a[72:75], v[126:133], v150\n\t" \
  "ds_write_b128 v24, v[20:23] offset:128\n\t" \
  "s_waitcnt lgkmcnt(4)\n\t" \
  "buffer_store_dwordx4 v[28:31], v34, s[20:23], s36 offen sc1\n\t" \
  "v_add_u32_sdwa v16, s29, v112 dst_sel:DWORD dst_unused:UNUSED_PAD src0_sel:DWORD src1_sel:WORD_0\n\t" \
  "v_smfmac_f32_16x16x64_bf16 v[40:43], a[76:79], v[126:133], v150 abid:1\n\t" \
  "s_and_b32 s44, s47, 18432\n\t" \
  "ds_read_b128 v[138:141], v184 offset:6144\n\t" \
  "v_add_u32_sdwa v18, s29, v112 dst_sel:DWORD dst_unused:UNUSED_PAD src0_sel:DWORD src1_sel:WORD_1\n\t" \
  "v_add_u32 v28, s38, v187\n\t" \
  "v_smfmac_f32_16x16x64_bf16 v[44:47], a[80:83], v[126:133], v151\n\t" \
  "s_and_b32 s27, s64, 7\n\t" \
  "ds_read_b64 v[6:7], v6 offset:0\n\t" \
  "ds_read_b128 v[28:31], v28 offset:10240\n\t" \
  "v_add_u32_sdwa v20, s29, v113 dst_sel:DWORD dst_unused:UNUSED_PAD src0_sel:DWORD src1_sel:WORD_0\n\t" \
  "v_smfmac_f32_16x16x64_bf16 v[48:51], a[84:87], v[126:133], v151 abid:1\n\t" \
  "s_lshl2_add_u32 s66, s27, s68\n\t" \
  "s_add_u32 s36, s36, 4608\n\t" \
  "ds_read_b64 v[8:9], v8 offset:0\n\t" \
  "v_add_u32_sdwa v22, s29, v113 dst_sel:DWORD dst_unused:UNUSED_PAD src0_sel:DWORD src1_sel:WORD_1\n\t" \
  "v_smfmac_f32_16x16x64_bf16 v[52:55], a[88:91], v[126:133], v152\n\t" \
  "s_and_b32 s27, s64, 8\n\t" \
  "ds_write_b64 v25, v[26:27] offset:16384\n\t" \
  "v_smfmac_f32_16x16x64_bf16 v[56:59], a[92:95], v[126:133], v152 abid:1\n\t" \
  "s_mul_i32 s27, s27, 176\n\t" \
  "ds_read_b64 v[10:11], v10 offset:0\n\t" \
  "ds_read_b64 v[12:13], v12 offset:0\n\t" \
  "v_smfmac_f32_16x16x64_bf16 v[60:63], a[96:99], v[126:133], v153\n\t" \
  "s_waitcnt lgkmcnt(4)\n\t" \
  "ds_read_b64 v[14:15], v14 offset:0\n\t" \
  "ds_read_b64 v[16:17], v16 offset:0\n\t" \
  "s_waitcnt lgkmcnt(6)\n\t" \
  "buffer_store_dwordx4 v[28:31], v34, s[20:23], s36 offen sc1\n\t" \
  "v_smfmac_f32_16x16x64_bf16 v[36:39], v[72:75], v[0:7], v182 abid:1\n\t" \
  "s_add_u32 s66, s66, s27\n\t" \
  "ds_read_b64 v[18:19], v18 offset:0\n\t" \
  "ds_read_b64 v[20:21], v20 offset:0\n\t" \
  "v_add_u32_sdwa v0, s29, v114 dst_sel:DWORD dst_unused:UNUSED_PAD src0_sel:DWORD src1_sel:WORD_0\n\t" \
  "v_xad_u32 v28, v187, 64, s38\n\t" \
  "v_smfmac_f32_16x16x64_bf16 v[64:67], a[100:103], v[126:133], v153 abid:1\n\t" \
  "s_lshr_b64 s[64:65], s[64:65], 4\n\t" \
  "s_add_u32 s38, s38, s44\n\t" \
  "ds_read_b64 v[22:23], v22 offset:0\n\t" \
  "ds_read_b128 v[28:31], v28 offset:11264\n\t" \
  "v_add_u32_sdwa v2, s29, v114 dst_sel:DWORD dst_unused:UNUSED_PAD src0_sel:DWORD src1_sel:WORD_1\n\t" \
  "v_smfmac_f32_16x16x64_bf16 v[68:71], a[104:107], v[126:133], v179\n\t" \
  "s_add_u32 s36, s36, 4608\n\t" \
  "ds_read_b128 v[126:129], v183 offset:8192\n\t" \
  "v_add_u32_sdwa v4, s29, v115 dst_sel:DWORD dst_unused:UNUSED_PAD src0_sel:DWORD src1_sel:WORD_0\n\t" \
  "v_smfmac_f32_16x16x64_bf16 v[36:39], a[108:111], v[134:141], v154\n\t" \
  "s_waitcnt lgkmcnt(5)\n\t" \
  "s_sub_u32 s44, s38, 129024\n\t" \
  "ds_read_b128 v[130:133], v184 offset:8192\n\t" \
  "v_add_u32_sdwa v6, s29, v115 dst_sel:DWORD dst_unused:UNUSED_PAD src0_sel:DWORD src1_sel:WORD_1\n\t" \
  "v_smfmac_f32_16x16x64_bf16 v[40:43], v[76:79], v[8:15], v182 abid:1\n\t" \
  "s_min_u32 s38, s38, s44\n\t" \
  "ds_read_b64 v[0:1], v0 offset:0\n\t" \
  "v_smfmac_f32_16x16x64_bf16 v[40:43], a[112:115], v[134:141], v154 abid:1\n\t" \
  "s_waitcnt lgkmcnt(3)\n\t" \
  "ds_read_b64 v[2:3], v2 offset:0\n\t" \
  "s_waitcnt lgkmcnt(4)\n\t" \
  "buffer_store_dwordx4 v[28:31], v34, s[20:23], s36 offen sc1\n\t" \
  "v_smfmac_f32_16x16x64_bf16 v[44:47], v[80:83], v[16:23], v182 abid:1\n\t" \
  "ds_read_b64 v[4:5], v4 offset:0\n\t" \
  "v_add_u32_sdwa v8, s29, v32 dst_sel:DWORD dst_unused:UNUSED_PAD src0_sel:DWORD src1_sel:WORD_0\n\t" \
  "v_smfmac_f32_16x16x64_bf16 v[44:47], a[116:119], v[134:141], v155\n\t" \
  "s_branch .Ljn23%=\n\t" \
  ".fill (.Lvar%=+34172-.)/4, 4, 0xbf800000\n\t" \
  "s_nop 0\n\t" \
  "s_branch .Lfin%=\n\t" \
  "s_branch .Lhe%=\n\t" \
  "s_waitcnt vmcnt(6)\n\t" \
  "s_waitcnt vmcnt(10)\n\t" \
  "s_waitcnt vmcnt(12)\n\t" \
  "s_waitcnt vmcnt(17)\n\t" \
  "s_waitcnt vmcnt(21) lgkmcnt(0)\n\t" \
  "s_barrier\n\t" \
  "v_smfmac_f32_16x16x64_bf16 v[36:39], v[220:223], v[0:7], v174\n\t" \
  "ds_read_b128 v[126:129], v183 offset:0\n\t" \
  "v_add_u32 v24, s37, v185\n\t" \
  "v_lshrrev_b32 v28, 4, v187\n\t" \
  "v_smfmac_f32_16x16x64_bf16 v[40:43], v[224:227], v[0:7], v174 abid:1\n\t" \
  "ds_read_b128 v[130:133], v184 offset:0\n\t" \
  "v_xor_b32 v25, s41, v183\n\t" \
  "v_and_or_b32 v28, v28, 48, s45\n\t" \
  "v_smfmac_f32_16x16x64_bf16 v[44:47], v[228:231], v[0:7], v175\n\t" \
  "ds_read_b128 v[134:137], v183 offset:2048\n\t" \
  "v_subrev_u32 v25, s30, v25\n\t" \
  "v_smfmac_f32_16x16x64_bf16 v[48:51], v[232:235], v[0:7], v175 abid:1\n\t" \
  "s_add_u32 s30, s29, 18432\n\t" \
  "ds_read_b128 v[138:141], v184 offset:2048\n\t" \
  "v_cvt_pk_bf16_f32 v8, v36, v37\n\t" \
  "v_cvt_pk_bf16_f32 v9, v38, v39\n\t" \
  "v_smfmac_f32_16x16x64_bf16 v[52:55], v[236:239], v[0:7], v176\n\t" \
  "s_sub_u32 s25, s30, 129024\n\t" \
  "ds_read_b128 v[36:39], v28 offset:0\n\t" \
  "v_cvt_pk_bf16_f32 v10, v40, v41\n\t" \
  "v_cvt_pk_bf16_f32 v11, v42, v43\n\t" \
  "v_smfmac_f32_16x16x64_bf16 v[56:59], v[240:243], v[0:7], v176 abid:1\n\t" \
  "s_min_u32 s30, s30, s25\n\t" \
  "ds_read_b128 v[40:43], v28 offset:64\n\t" \
  "v_cvt_pk_bf16_f32 v16, v44, v45\n\t" \
  "v_cvt_pk_bf16_f32 v17, v46, v47\n\t" \
  "v_smfmac_f32_16x16x64_bf16 v[60:63], v[244:247], v[0:7], v177\n\t" \
  "s_sub_u32 s30, s30, s29\n\t" \
  "ds_read_b128 v[44:47], v28 offset:128\n\t" \
  "v_cvt_pk_bf16_f32 v18, v48, v49\n\t" \
  "v_cvt_pk_bf16_f32 v19, v50, v51\n\t" \
  "v_smfmac_f32_16x16x64_bf16 v[64:67], v[248:251], v[0:7], v177 abid:1\n\t" \
  "s_mov_b32 s37, s29\n\t" \
  "ds_read_b128 v[48:51], v28 offset:192\n\t" \
  "v_cvt_pk_bf16_f32 v12, v52, v53\n\t" \
  "v_cvt_pk_bf16_f32 v13, v54, v55\n\t" \
  "v_smfmac_f32_16x16x64_bf16 v[68:71], v[252:255], v[0:7], v182\n\t" \
  "s_waitcnt lgkmcnt(3)\n\t" \
  "ds_read_b128 v[52:55], v28 offset:256\n\t" \
  "v_cvt_pk_bf16_f32 v14, v56, v57\n\t" \
  "v_cvt_pk_bf16_f32 v15, v58, v59\n\t" \
  "v_smfmac_f32_16x16x64_bf16 v[36:39], a[0:3], v[126:133], v142\n\t" \
  "s_waitcnt lgkmcnt(3)\n\t" \
  "ds_read_b128 v[56:59], v28 offset:320\n\t" \
  "v_cvt_pk_bf16_f32 v20, v60, v61\n\t" \
  "v_cvt_pk_bf16_f32 v21, v62, v63\n\t" \
  "v_smfmac_f32_16x16x64_bf16 v[40:43], a[4:7], v[126:133], v142 abid:1\n\t" \
  "s_waitcnt lgkmcnt(3)\n\t" \
  "ds_read_b128 v[60:63], v28 offset:384\n\t" \
  "v_cvt_pk_bf16_f32 v22, v64, v65\n\t" \
  "v_cvt_pk_bf16_f32 v23, v66, v67\n\t" \
  "v_smfmac_f32_16x16x64_bf16 v[44:47], a[8:11], v[126:133], v143\n\t" \
  "s_waitcnt lgkmcnt(3)\n\t" \
  "ds_read_b128 v[64:67], v28 offset:448\n\t" \
  "v_cvt_pk_bf16_f32 v26, v68, v69\n\t" \
  "v_cvt_pk_bf16_f32 v27, v70, v71\n\t" \
  "v_smfmac_f32_16x16x64_bf16 v[48:51], a[12:15], v[126:133], v143 abid:1\n\t" \
  "s_waitcnt lgkmcnt(3)\n\t" \
  "ds_read_b128 v[68:71], v28 offset:512\n\t" \
  "v_add_u32_sdwa v0, s29, v108 dst_sel:DWORD dst_unused:UNUSED_PAD src0_sel:DWORD src1_sel:WORD_0\n\t" \
  "v_smfmac_f32_16x16x64_bf16 v[52:55], a[16:19], v[126:133], v144\n\t" \
  "s_add_u32 m0, s33, 0\n\t" \
  "s_waitcnt lgkmcnt(3)\n\t" \
  "ds_read_b64 v[0:1], v0 offset:0\n\t" \
  "v_add_u32_sdwa v2, s29, v108 dst_sel:DWORD dst_unused:UNUSED_PAD src0_sel:DWORD src1_sel:WORD_1\n\t" \
  "v_smfmac_f32_16x16x64_bf16 v[56:59], a[20:23], v[126:133], v144 abid:1\n\t" \
  "s_waitcnt lgkmcnt(3)\n\t" \
  "buffer_load_dwordx4 v186, s[16:19], s34 offen offset:0 nt lds\n\t" \
  "v_add_u32_sdwa v4, s29, v109 dst_sel:DWORD dst_unused:UNUSED_PAD src0_sel:DWORD src1_sel:WORD_0\n\t" \
  "v_smfmac_f32_16x16x64_bf16 v[60:63], a[24:27], v[126:133], v145\n\t" \
  "s_add_u32 m0, s33, 1920\n\t" \
  "s_waitcnt lgkmcnt(2)\n\t" \
  "ds_write_b128 v24, v[8:11] offset:0\n\t" \
  "v_add_u32_sdwa v6, s29, v109 dst_sel:DWORD dst_unused:UNUSED_PAD src0_sel:DWORD src1_sel:WORD_1\n\t" \
  "v_smfmac_f32_16x16x64_bf16 v[64:67], a[28:31], v[126:133], v145 abid:1\n\t" \
  "s_waitcnt lgkmcnt(2)\n\t" \
  "buffer_load_dwordx4 v186, s[16:19], s34 offen offset:128 nt lds\n\t" \
  "v_smfmac_f32_16x16x64_bf16 v[68:71], a[32:35], v[126:133], v178\n\t" \
  "s_add_u32 m0, s33, 3840\n\t" \
  "ds_read_b128 v[126:129], v183 offset:4096\n\t" \
  "v_smfmac_f32_16x16x64_bf16 v[36:39], a[36:39], v[134:141], v146\n\t" \
  "buffer_load_dwordx4 v186, s[16:19], s34 offen offset:256 nt lds\n\t" \
  "v_smfmac_f32_16x16x64_bf16 v[40:43], a[40:43], v[134:141], v146 abid:1\n\t" \
  "s_add_u32 m0, s33, 5760\n\t" \
  "ds_write_b128 v24, v[12:15] offset:128\n\t" \
  "v_xor_b32 v24, 64, v24\n\t" \
  "v_smfmac_f32_16x16x64_bf16 v[44:47], a[44:47], v[134:141], v147\n\t" \
  "buffer_load_dwordx4 v186, s[16:19], s34 offen offset:384 nt lds\n\t" \
  "v_add_u32_sdwa v8, s29, v110 dst_sel:DWORD dst_unused:UNUSED_PAD src0_sel:DWORD src1_sel:WORD_0\n\t" \
  "v_smfmac_f32_16x16x64_bf16 v[48:51], a[48:51], v[134:141], v147 abid:1\n\t" \
  "s_add_u32 m0, s33, 7680\n\t" \
  "ds_read_b128 v[130:133], v184 offset:4096\n\t" \
  "v_add_u32_sdwa v10, s29, v110 dst_sel:DWORD dst_unused:UNUSED_PAD src0_sel:DWORD src1_sel:WORD_1\n\t" \
  "v_smfmac_f32_16x16x64_bf16 v[52:55], a[52:55], v[134:141], v148\n\t" \
  "buffer_load_dwordx4 v186, s[16:19], s34 offen offset:512 nt lds\n\t" \
  "v_add_u32_sdwa v12, s29, v111 dst_sel:DWORD dst_unused:UNUSED_PAD src0_sel:DWORD src1_sel:WORD_0\n\t" \
  "v_smfmac_f32_16x16x64_bf16 v[56:59], a[56:59], v[134:141], v148 abid:1\n\t" \
  "s_add_u32 s31, s31, 18432\n\t" \
  "ds_write_b128 v24, v[16:19] offset:0\n\t" \
  "v_add_u32_sdwa v14, s29, v111 dst_sel:DWORD dst_unused:UNUSED_PAD src0_sel:DWORD src1_sel:WORD_1\n\t" \
  "v_smfmac_f32_16x16x64_bf16 v[60:63], a[60:63], v[134:141], v149\n\t" \
  "s_sub_u32 s26, s31, 129024\n\t" \
  "ds_read_b64 v[2:3], v2 offset:0\n\t" \
  "v_smfmac_f32_16x16x64_bf16 v[64:67], a[64:67], v[134:141], v149 abid:1\n\t" \
  "s_min_u32 s31, s31, s26\n\t" \
  "ds_read_b64 v[4:5], v4 offset:0\n\t" \
  "v_smfmac_f32_16x16x64_bf16 v[68:71], a[68:71], v[134:141], v178 abid:1\n\t" \
  "s_waitcnt lgkmcnt(3)\n\t" \
  "ds_read_b128 v[134:137], v183 offset:6144\n\t" \
  "v_smfmac_f32_16x16x64_bf16 v[36:39], a[72:75], v[126:133], v150\n\t" \
  "s_add_u32 s33, s31, s32\n\t" \
  "ds_write_b128 v24, v[20:23] offset:128\n\t" \
  "v_add_u32_sdwa v16, s29, v112 dst_sel:DWORD dst_unused:UNUSED_PAD src0_sel:DWORD src1_sel:WORD_0\n\t" \
  "v_smfmac_f32_16x16x64_bf16 v[40:43], a[76:79], v[126:133], v150 abid:1\n\t" \
  "s_add_u32 s34, s34, 4718592\n\t" \
  "ds_read_b128 v[138:141], v184 offset:6144\n\t" \
  "v_add_u32_sdwa v18, s29, v112 dst_sel:DWORD dst_unused:UNUSED_PAD src0_sel:DWORD src1_sel:WORD_1\n\t" \
  "v_smfmac_f32_16x16x64_bf16 v[44:47], a[80:83], v[126:133], v151\n\t" \
  "s_and_b32 s27, s64, 7\n\t" \
  "ds_read_b64 v[6:7], v6 offset:0\n\t" \
  "v_add_u32_sdwa v20, s29, v113 dst_sel:DWORD dst_unused:UNUSED_PAD src0_sel:DWORD src1_sel:WORD_0\n\t" \
  "v_smfmac_f32_16x16x64_bf16 v[48:51], a[84:87], v[126:133], v151 abid:1\n\t" \
  "s_lshl2_add_u32 s66, s27, s68\n\t" \
  "ds_read_b64 v[8:9], v8 offset:0\n\t" \
  "v_add_u32_sdwa v22, s29, v113 dst_sel:DWORD dst_unused:UNUSED_PAD src0_sel:DWORD src1_sel:WORD_1\n\t" \
  "v_smfmac_f32_16x16x64_bf16 v[52:55], a[88:91], v[126:133], v152\n\t" \
  "s_and_b32 s27, s64, 8\n\t" \
  "ds_write_b64 v25, v[26:27] offset:16384\n\t" \
  "v_smfmac_f32_16x16x64_bf16 v[56:59], a[92:95], v[126:133], v152 abid:1\n\t" \
  "s_mul_i32 s27, s27, 176\n\t" \
  "ds_read_b64 v[10:11], v10 offset:0\n\t" \
  "ds_read_b64 v[12:13], v12 offset:0\n\t" \
  "v_smfmac_f32_16x16x64_bf16 v[60:63], a[96:99], v[126:133], v153\n\t" \
  "s_waitcnt lgkmcnt(4)\n\t" \
  "ds_read_b64 v[14:15], v14 offset:0\n\t" \
  "ds_read_b64 v[16:17], v16 offset:0\n\t" \
  "v_smfmac_f32_16x16x64_bf16 v[36:39], v[72:75], v[0:7], v182 abid:1\n\t" \
  "s_add_u32 s66, s66, s27\n\t" \
  "ds_read_b64 v[18:19], v18 offset:0\n\t" \
  "ds_read_b64 v[20:21], v20 offset:0\n\t" \
  "v_add_u32_sdwa v0, s29, v114 dst_sel:DWORD dst_unused:UNUSED_PAD src0_sel:DWORD src1_sel:WORD_0\n\t" \
  "v_smfmac_f32_16x16x64_bf16 v[64:67], a[100:103], v[126:133], v153 abid:1\n\t" \
  "s_lshr_b64 s[64:65], s[64:65], 4\n\t" \
  "ds_read_b64 v[22:23], v22 offset:0\n\t" \
  "v_add_u32_sdwa v2, s29, v114 dst_sel:DWORD dst_unused:UNUSED_PAD src0_sel:DWORD src1_sel:WORD_1\n\t" \
  "v_smfmac_f32_16x16x64_bf16 v[68:71], a[104:107], v[126:133], v179\n\t" \
  "ds_read_b128 v[126:129], v183 offset:8192\n\t" \
  "v_add_u32_sdwa v4, s29, v115 dst_sel:DWORD dst_unused:UNUSED_PAD src0_sel:DWORD src1_sel:WORD_0\n\t" \
  "v_smfmac_f32_16x16x64_bf16 v[36:39], a[108:111], v[134:141], v154\n\t" \
  "s_waitcnt lgkmcnt(5)\n\t" \
  "ds_read_b128 v[130:133], v184 offset:8192\n\t" \
  "v_add_u32_sdwa v6, s29, v115 dst_sel:DWORD dst_unused:UNUSED_PAD src0_sel:DWORD src1_sel:WORD_1\n\t" \
  "v_smfmac_f32_16x16x64_bf16 v[40:43], v[76:79], v[8:15], v182 abid:1\n\t" \
  "ds_read_b64 v[0:1], v0 offset:0\n\t" \
  "v_smfmac_f32_16x16x64_bf16 v[40:43], a[112:115], v[134:141], v154 abid:1\n\t" \
  "s_waitcnt lgkmcnt(3)\n\t" \
  "ds_read_b64 v[2:3], v2 offset:0\n\t" \
  "v_smfmac_f32_16x16x64_bf16 v[44:47], v[80:83], v[16:23], v182 abid:1\n\t" \
  "ds_read_b64 v[4:5], v4 offset:0\n\t" \
  "v_add_u32_sdwa v8, s29, v32 dst_sel:DWORD dst_unused:UNUSED_PAD src0_sel:DWORD src1_sel:WORD_0\n\t" \
  "v_smfmac_f32_16x16x64_bf16 v[44:47], a[116:119], v[134:141], v155\n\t" \
  ".Ljn23%=:\n\t" \
  "ds_read_b64 v[6:7], v6 offset:0\n\t" \
  "v_add_u32_sdwa v10, s29, v32 dst_sel:DWORD dst_unused:UNUSED_PAD src0_sel:DWORD src1_sel:WORD_1\n\t" \
  "v_smfmac_f32_16x16x64_bf16 v[48:51], a[120:123], v[134:141], v155 abid:1\n\t" \
  "ds_read_b64 v[8:9], v8 offset:0\n\t" \
  "v_add_u32 v28, s60, v187\n\t" \
  "v_smfmac_f32_16x16x64_bf16 v[52:55], a[124:127], v[134:141], v156\n\t" \
  "ds_read_b64 v[10:11], v10 offset:0\n\t" \
  "v_add_u32_sdwa v16, s29, v116 dst_sel:DWORD dst_unused:UNUSED_PAD src0_sel:DWORD src1_sel:WORD_0\n\t" \
  "v_smfmac_f32_16x16x64_bf16 v[56:59], a[128:131], v[134:141], v156 abid:1\n\t" \
  "ds_read_b128 v[28:31], v28 offset:0\n\t" \
  "v_add_u32_sdwa v18, s29, v116 dst_sel:DWORD dst_unused:UNUSED_PAD src0_sel:DWORD src1_sel:WORD_1\n\t" \
  "v_smfmac_f32_16x16x64_bf16 v[60:63], a[132:135], v[134:141], v157\n\t" \
  "s_waitcnt lgkmcnt(3)\n\t" \
  "ds_read_b64 v[16:17], v16 offset:0\n\t" \
  "v_add_u32_sdwa v20, s29, v117 dst_sel:DWORD dst_unused:UNUSED_PAD src0_sel:DWORD src1_sel:WORD_0\n\t" \
  "v_smfmac_f32_16x16x64_bf16 v[48:51], v[84:87], v[0:7], v182 abid:1\n\t" \
  "ds_read_b64 v[18:19], v18 offset:0\n\t" \
  "v_add_u32_sdwa v22, s29, v117 dst_sel:DWORD dst_unused:UNUSED_PAD src0_sel:DWORD src1_sel:WORD_1\n\t" \
  "v_smfmac_f32_16x16x64_bf16 v[64:67], a[136:139], v[134:141], v157 abid:1\n\t" \
  "ds_read_b64 v[20:21], v20 offset:0\n\t" \
  "v_add_u32_sdwa v0, s29, v118 dst_sel:DWORD dst_unused:UNUSED_PAD src0_sel:DWORD src1_sel:WORD_0\n\t" \
  "v_smfmac_f32_16x16x64_bf16 v[68:71], a[140:143], v[134:141], v179 abid:1\n\t" \
  "s_waitcnt lgkmcnt(3)\n\t" \
  "ds_read_b128 v[134:137], v183 offset:10240\n\t" \
  "v_add_u32 v24, s38, v187\n\t" \
  "s_set_gpr_idx_on s6, gpr_idx(DST)\n\t" \
  "v_smfmac_f32_16x16x64_bf16 v[36:39], v[28:31], v[8:15], v182 abid:1\n\t" \
  "s_set_gpr_idx_off\n\t" \
  "ds_read_b128 v[24:27], v24 offset:8192\n\t" \
  "v_add_u32_sdwa v2, s29, v118 dst_sel:DWORD dst_unused:UNUSED_PAD src0_sel:DWORD src1_sel:WORD_1\n\t" \
  "v_smfmac_f32_16x16x64_bf16 v[36:39], a[144:147], v[126:133], v158\n\t" \
  "ds_read_b128 v[138:141], v184 offset:10240\n\t" \
  "v_add_u32_sdwa v4, s29, v119 dst_sel:DWORD dst_unused:UNUSED_PAD src0_sel:DWORD src1_sel:WORD_0\n\t" \
  "v_smfmac_f32_16x16x64_bf16 v[40:43], a[148:151], v[126:133], v158 abid:1\n\t" \
  "ds_read_b64 v[22:23], v22 offset:0\n\t" \
  "v_add_u32_sdwa v6, s29, v119 dst_sel:DWORD dst_unused:UNUSED_PAD src0_sel:DWORD src1_sel:WORD_1\n\t" \
  "v_smfmac_f32_16x16x64_bf16 v[44:47], a[152:155], v[126:133], v159\n\t" \
  "s_add_u32 s36, s35, s39\n\t" \
  "ds_read_b64 v[0:1], v0 offset:0\n\t" \
  "v_add_u32_sdwa v8, s29, v33 dst_sel:DWORD dst_unused:UNUSED_PAD src0_sel:DWORD src1_sel:WORD_0\n\t" \
  "v_smfmac_f32_16x16x64_bf16 v[48:51], a[156:159], v[126:133], v159 abid:1\n\t" \
  "s_waitcnt lgkmcnt(3)\n\t" \
  "buffer_store_dwordx4 v[24:27], v34, s[20:23], s36 offen sc1\n\t" \
  "v_add_u32_sdwa v10, s29, v33 dst_sel:DWORD dst_unused:UNUSED_PAD src0_sel:DWORD src1_sel:WORD_1\n\t" \
  "v_smfmac_f32_16x16x64_bf16 v[52:55], a[160:163], v[126:133], v160\n\t" \
  "ds_read_b64 v[2:3], v2 offset:0\n\t" \
  "v_xad_u32 v24, v187, 64, s38\n\t" \
  "v_smfmac_f32_16x16x64_bf16 v[56:59], a[164:167], v[126:133], v160 abid:1\n\t" \
  "s_waitcnt lgkmcnt(2)\n\t" \
  "ds_read_b128 v[24:27], v24 offset:9216\n\t" \
  "v_add_u32 v28, s60, v187\n\t" \
  "v_smfmac_f32_16x16x64_bf16 v[52:55], v[88:91], v[16:23], v182 abid:1\n\t" \
  "ds_read_b64 v[4:5], v4 offset:0\n\t" \
  "v_add_u32_sdwa v16, s29, v120 dst_sel:DWORD dst_unused:UNUSED_PAD src0_sel:DWORD src1_sel:WORD_0\n\t" \
  "v_smfmac_f32_16x16x64_bf16 v[60:63], a[168:171], v[126:133], v161\n\t" \
  "ds_read_b64 v[6:7], v6 offset:0\n\t" \
  "v_add_u32_sdwa v18, s29, v120 dst_sel:DWORD dst_unused:UNUSED_PAD src0_sel:DWORD src1_sel:WORD_1\n\t" \
  "v_smfmac_f32_16x16x64_bf16 v[64:67], a[172:175], v[126:133], v161 abid:1\n\t" \
  "s_add_u32 s36, s36, 4608\n\t" \
  "ds_read_b64 v[8:9], v8 offset:0\n\t" \
  "v_add_u32_sdwa v20, s29, v121 dst_sel:DWORD dst_unused:UNUSED_PAD src0_sel:DWORD src1_sel:WORD_0\n\t" \
  "v_smfmac_f32_16x16x64_bf16 v[68:71], a[176:179], v[126:133], v180\n\t" \
  "s_waitcnt lgkmcnt(3)\n\t" \
  "buffer_store_dwordx4 v[24:27], v34, s[20:23], s36 offen sc1\n\t" \
  "v_add_u32_sdwa v22, s29, v121 dst_sel:DWORD dst_unused:UNUSED_PAD src0_sel:DWORD src1_sel:WORD_1\n\t" \
  "v_smfmac_f32_16x16x64_bf16 v[36:39], a[180:183], v[134:141], v162\n\t" \
  "ds_read_b128 v[126:129], v183 offset:12288\n\t" \
  "v_add_u32 v24, s38, v187\n\t" \
  "v_smfmac_f32_16x16x64_bf16 v[40:43], a[184:187], v[134:141], v162 abid:1\n\t" \
  "s_waitcnt lgkmcnt(2)\n\t" \
  "ds_read_b128 v[24:27], v24 offset:10240\n\t" \
  "v_smfmac_f32_16x16x64_bf16 v[56:59], v[92:95], v[0:7], v182 abid:1\n\t" \
  "ds_read_b128 v[130:133], v184 offset:12288\n\t" \
  "v_add_u32_sdwa v0, s29, v122 dst_sel:DWORD dst_unused:UNUSED_PAD src0_sel:DWORD src1_sel:WORD_0\n\t" \
  "v_smfmac_f32_16x16x64_bf16 v[44:47], a[188:191], v[134:141], v163\n\t" \
  "ds_read_b64 v[10:11], v10 offset:0\n\t" \
  "v_add_u32_sdwa v2, s29, v122 dst_sel:DWORD dst_unused:UNUSED_PAD src0_sel:DWORD src1_sel:WORD_1\n\t" \
  "v_smfmac_f32_16x16x64_bf16 v[48:51], a[192:195], v[134:141], v163 abid:1\n\t" \
  "s_add_u32 s36, s36, 4608\n\t" \
  "ds_read_b128 v[28:31], v28 offset:1280\n\t" \
  "v_add_u32_sdwa v4, s29, v123 dst_sel:DWORD dst_unused:UNUSED_PAD src0_sel:DWORD src1_sel:WORD_0\n\t" \
  "v_smfmac_f32_16x16x64_bf16 v[52:55], a[196:199], v[134:141], v164\n\t" \
  "s_waitcnt lgkmcnt(3)\n\t" \
  "buffer_store_dwordx4 v[24:27], v34, s[20:23], s36 offen sc1\n\t" \
  "v_add_u32_sdwa v6, s29, v123 dst_sel:DWORD dst_unused:UNUSED_PAD src0_sel:DWORD src1_sel:WORD_1\n\t" \
  "v_smfmac_f32_16x16x64_bf16 v[56:59], a[200:203], v[134:141], v164 abid:1\n\t" \
  "ds_read_b64 v[16:17], v16 offset:0\n\t" \
  "v_xad_u32 v24, v187, 64, s38\n\t" \
  "v_smfmac_f32_16x16x64_bf16 v[60:63], a[204:207], v[134:141], v165\n\t" \
  "s_add_u32 s38, s38, 18432\n\t" \
  "ds_read_b128 v[24:27], v24 offset:11264\n\t" \
  "v_smfmac_f32_16x16x64_bf16 v[64:67], a[208:211], v[134:141], v165 abid:1\n\t" \
  "s_waitcnt lgkmcnt(2)\n\t" \
  "ds_read_b64 v[18:19], v18 offset:0\n\t" \
  "s_set_gpr_idx_on s7, gpr_idx(DST)\n\t" \
  "v_smfmac_f32_16x16x64_bf16 v[36:39], v[28:31], v[8:15], v182 abid:1\n\t" \
  "s_set_gpr_idx_off\n\t" \
  "ds_read_b64 v[20:21], v20 offset:0\n\t" \
  "v_add_u32_sdwa v8, s29, v35 dst_sel:DWORD dst_unused:UNUSED_PAD src0_sel:DWORD src1_sel:WORD_0\n\t" \
  "v_smfmac_f32_16x16x64_bf16 v[68:71], a[212:215], v[134:141], v180 abid:1\n\t" \
  "s_add_u32 s36, s36, 4608\n\t" \
  "ds_read_b128 v[134:137], v183 offset:14336\n\t" \
  "v_add_u32_sdwa v10, s29, v35 dst_sel:DWORD dst_unused:UNUSED_PAD src0_sel:DWORD src1_sel:WORD_1\n\t" \
  "v_smfmac_f32_16x16x64_bf16 v[36:39], a[216:219], v[126:133], v166\n\t" \
  "s_waitcnt lgkmcnt(3)\n\t" \
  "buffer_store_dwordx4 v[24:27], v34, s[20:23], s36 offen sc1\n\t" \
  "v_add_u32 v28, s60, v187\n\t" \
  "v_smfmac_f32_16x16x64_bf16 v[40:43], a[220:223], v[126:133], v166 abid:1\n\t" \
  "s_sub_u32 s44, s38, 129024\n\t" \
  "ds_read_b128 v[138:141], v184 offset:14336\n\t" \
  "v_smfmac_f32_16x16x64_bf16 v[44:47], a[224:227], v[126:133], v167\n\t" \
  "s_min_u32 s38, s38, s44\n\t" \
  "ds_read_b64 v[22:23], v22 offset:0\n\t" \
  "v_smfmac_f32_16x16x64_bf16 v[48:51], a[228:231], v[126:133], v167 abid:1\n\t" \
  "s_add_u32 s35, s35, 4718592\n\t" \
  "ds_read_b64 v[0:1], v0 offset:0\n\t" \
  "v_smfmac_f32_16x16x64_bf16 v[52:55], a[232:235], v[126:133], v168\n\t" \
  "ds_read_b64 v[2:3], v2 offset:0\n\t" \
  "v_smfmac_f32_16x16x64_bf16 v[56:59], a[236:239], v[126:133], v168 abid:1\n\t" \
  "ds_read_b64 v[4:5], v4 offset:0\n\t" \
  "v_smfmac_f32_16x16x64_bf16 v[60:63], a[240:243], v[126:133], v169\n\t" \
  "s_waitcnt lgkmcnt(3)\n\t" \
  "ds_read_b64 v[6:7], v6 offset:0\n\t" \
  "v_smfmac_f32_16x16x64_bf16 v[60:63], v[96:99], v[16:23], v182 abid:1\n\t" \
  "ds_read_b64 v[8:9], v8 offset:0\n\t" \
  "v_add_u32_sdwa v16, s29, v124 dst_sel:DWORD dst_unused:UNUSED_PAD src0_sel:DWORD src1_sel:WORD_0\n\t" \
  "v_smfmac_f32_16x16x64_bf16 v[64:67], a[244:247], v[126:133], v169 abid:1\n\t" \
  "ds_read_b64 v[10:11], v10 offset:0\n\t" \
  "v_add_u32_sdwa v18, s29, v124 dst_sel:DWORD dst_unused:UNUSED_PAD src0_sel:DWORD src1_sel:WORD_1\n\t" \
  "v_smfmac_f32_16x16x64_bf16 v[68:71], a[248:251], v[126:133], v181\n\t" \
  "ds_read_b128 v[28:31], v28 offset:2560\n\t" \
  "v_add_u32_sdwa v20, s29, v125 dst_sel:DWORD dst_unused:UNUSED_PAD src0_sel:DWORD src1_sel:WORD_0\n\t" \
  "v_smfmac_f32_16x16x64_bf16 v[36:39], a[252:255], v[134:141], v170\n\t" \
  "s_waitcnt lgkmcnt(3)\n\t" \
  "ds_read_b64 v[16:17], v16 offset:0\n\t" \
  "v_add_u32_sdwa v22, s29, v125 dst_sel:DWORD dst_unused:UNUSED_PAD src0_sel:DWORD src1_sel:WORD_1\n\t" \
  "v_smfmac_f32_16x16x64_bf16 v[64:67], v[100:103], v[0:7], v182 abid:1\n\t" \
  "s_add_u32 s29, s29, s30\n\t" \
  "ds_read_b128 v[0:3], v183 offset:16384\n\t" \
  "v_smfmac_f32_16x16x64_bf16 v[40:43], v[188:191], v[134:141], v170 abid:1\n\t" \
  "ds_read_b128 v[4:7], v184 offset:16384\n\t" \
  "v_add_u32 v183, s30, v183\n\t" \
  "v_smfmac_f32_16x16x64_bf16 v[44:47], v[192:195], v[134:141], v171\n\t" \
  "s_waitcnt lgkmcnt(3)\n\t" \
  "ds_read_b64 v[18:19], v18 offset:0\n\t" \
  "v_add_u32 v184, s30, v184\n\t" \
  "s_set_gpr_idx_on s8, gpr_idx(DST)\n\t" \
  "v_smfmac_f32_16x16x64_bf16 v[36:39], v[28:31], v[8:15], v182 abid:1\n\t" \
  "s_set_gpr_idx_off\n\t" \
  "ds_read_b64 v[20:21], v20 offset:0\n\t" \
  "v_smfmac_f32_16x16x64_bf16 v[48:51], v[196:199], v[134:141], v171 abid:1\n\t" \
  "ds_read_b64 v[22:23], v22 offset:0\n\t" \
  "v_smfmac_f32_16x16x64_bf16 v[52:55], v[200:203], v[134:141], v172\n\t" \
  "v_smfmac_f32_16x16x64_bf16 v[56:59], v[204:207], v[134:141], v172 abid:1\n\t" \
  "v_smfmac_f32_16x16x64_bf16 v[60:63], v[208:211], v[134:141], v173\n\t" \
  "v_smfmac_f32_16x16x64_bf16 v[64:67], v[212:215], v[134:141], v173 abid:1\n\t" \
  "s_waitcnt lgkmcnt(0)\n\t" \
  "v_smfmac_f32_16x16x64_bf16 v[68:71], v[104:107], v[16:23], v182 abid:1\n\t" \
  "v_smfmac_f32_16x16x64_bf16 v[68:71], v[216:219], v[134:141], v181 abid:1\n\t" \
  "s_setpc_b64 s[66:67]\n\t" \
  ".fill (.Lvar%=+36864-.)/4, 4, 0xbf800000\n\t" \
  "s_branch .Lfin%=\n\t" \
  "s_branch .Lhe%=\n\t" \
  "s_waitcnt vmcnt(6)\n\t" \
  "s_waitcnt vmcnt(10)\n\t" \
  "s_waitcnt vmcnt(12)\n\t" \
  "s_waitcnt vmcnt(17)\n\t" \
  "s_waitcnt vmcnt(21) lgkmcnt(0)\n\t" \
  "s_barrier\n\t" \
  "v_smfmac_f32_16x16x64_bf16 v[36:39], v[220:223], v[0:7], v174\n\t" \
  "ds_read_b128 v[126:129], v183 offset:0\n\t" \
  "v_add_u32 v24, s37, v185\n\t" \
  "v_smfmac_f32_16x16x64_bf16 v[40:43], v[224:227], v[0:7], v174 abid:1\n\t" \
  "ds_read_b128 v[130:133], v184 offset:0\n\t" \
  "v_xor_b32 v25, s41, v183\n\t" \
  "v_smfmac_f32_16x16x64_bf16 v[44:47], v[228:231], v[0:7], v175\n\t" \
  "ds_read_b128 v[134:137], v183 offset:2048\n\t" \
  "v_subrev_u32 v25, s30, v25\n\t" \
  "v_smfmac_f32_16x16x64_bf16 v[48:51], v[232:235], v[0:7], v175 abid:1\n\t" \
  "s_add_u32 s30, s29, 18432\n\t" \
  "ds_read_b128 v[138:141], v184 offset:2048\n\t" \
  "v_cvt_pk_bf16_f32 v8, v36, v37\n\t" \
  "v_cvt_pk_bf16_f32 v9, v38, v39\n\t" \
  "v_smfmac_f32_16x16x64_bf16 v[52:55], v[236:239], v[0:7], v176\n\t" \
  "s_sub_u32 s25, s30, 129024\n\t" \
  "ds_read_b128 v[36:39], v35 offset:0\n\t" \
  "v_cvt_pk_bf16_f32 v10, v40, v41\n\t" \
  "v_cvt_pk_bf16_f32 v11, v42, v43\n\t" \
  "v_smfmac_f32_16x16x64_bf16 v[56:59], v[240:243], v[0:7], v176 abid:1\n\t" \
  "s_min_u32 s30, s30, s25\n\t" \
  "ds_read_b128 v[40:43], v35 offset:64\n\t" \
  "v_cvt_pk_bf16_f32 v16, v44, v45\n\t" \
  "v_cvt_pk_bf16_f32 v17, v46, v47\n\t" \
  "v_smfmac_f32_16x16x64_bf16 v[60:63], v[244:247], v[0:7], v177\n\t" \
  "s_sub_u32 s30, s30, s29\n\t" \
  "ds_read_b128 v[44:47], v35 offset:128\n\t" \
  "v_cvt_pk_bf16_f32 v18, v48, v49\n\t" \
  "v_cvt_pk_bf16_f32 v19, v50, v51\n\t" \
  "v_smfmac_f32_16x16x64_bf16 v[64:67], v[248:251], v[0:7], v177 abid:1\n\t" \
  "s_mov_b32 s37, s29\n\t" \
  "ds_read_b128 v[48:51], v35 offset:192\n\t" \
  "v_cvt_pk_bf16_f32 v12, v52, v53\n\t" \
  "v_cvt_pk_bf16_f32 v13, v54, v55\n\t" \
  "v_smfmac_f32_16x16x64_bf16 v[68:71], v[252:255], v[0:7], v182\n\t" \
  "s_waitcnt lgkmcnt(3)\n\t" \
  "ds_read_b128 v[52:55], v35 offset:256\n\t" \
  "v_cvt_pk_bf16_f32 v14, v56, v57\n\t" \
  "v_cvt_pk_bf16_f32 v15, v58, v59\n\t" \
  "v_smfmac_f32_16x16x64_bf16 v[36:39], a[0:3], v[126:133], v142\n\t" \
  "s_waitcnt lgkmcnt(3)\n\t" \
  "ds_read_b128 v[56:59], v35 offset:320\n\t" \
  "v_cvt_pk_bf16_f32 v20, v60, v61\n\t" \
  "v_cvt_pk_bf16_f32 v21, v62, v63\n\t" \
  "v_smfmac_f32_16x16x64_bf16 v[40:43], a[4:7], v[126:133], v142 abid:1\n\t" \
  "s_waitcnt lgkmcnt(3)\n\t" \
  "ds_read_b128 v[60:63], v35 offset:384\n\t" \
  "v_cvt_pk_bf16_f32 v22, v64, v65\n\t" \
  "v_cvt_pk_bf16_f32 v23, v66, v67\n\t" \
  "v_smfmac_f32_16x16x64_bf16 v[44:47], a[8:11], v[126:133], v143\n\t" \
  "s_waitcnt lgkmcnt(3)\n\t" \
  "ds_read_b128 v[64:67], v35 offset:448\n\t" \
  "v_cvt_pk_bf16_f32 v26, v68, v69\n\t" \
  "v_cvt_pk_bf16_f32 v27, v70, v71\n\t" \
  "v_smfmac_f32_16x16x64_bf16 v[48:51], a[12:15], v[126:133], v143 abid:1\n\t" \
  "s_waitcnt lgkmcnt(3)\n\t" \
  "ds_read_b128 v[68:71], v35 offset:512\n\t" \
  "v_add_u32_sdwa v0, s29, v108 dst_sel:DWORD dst_unused:UNUSED_PAD src0_sel:DWORD src1_sel:WORD_0\n\t" \
  "v_smfmac_f32_16x16x64_bf16 v[52:55], a[16:19], v[126:133], v144\n\t" \
  "s_waitcnt lgkmcnt(3)\n\t" \
  "ds_read_b64 v[0:1], v0 offset:0\n\t" \
  "v_add_u32_sdwa v2, s29, v108 dst_sel:DWORD dst_unused:UNUSED_PAD src0_sel:DWORD src1_sel:WORD_1\n\t" \
  "v_smfmac_f32_16x16x64_bf16 v[56:59], a[20:23], v[126:133], v144 abid:1\n\t" \
  "s_waitcnt lgkmcnt(3)\n\t" \
  "v_add_u32_sdwa v4, s29, v109 dst_sel:DWORD dst_unused:UNUSED_PAD src0_sel:DWORD src1_sel:WORD_0\n\t" \
  "v_smfmac_f32_16x16x64_bf16 v[60:63], a[24:27], v[126:133], v145\n\t" \
  "s_waitcnt lgkmcnt(2)\n\t" \
  "ds_read_b64 v[2:3], v2 offset:0\n\t" \
  "v_add_u32_sdwa v6, s29, v109 dst_sel:DWORD dst_unused:UNUSED_PAD src0_sel:DWORD src1_sel:WORD_1\n\t" \
  "v_smfmac_f32_16x16x64_bf16 v[64:67], a[28:31], v[126:133], v145 abid:1\n\t" \
  "s_waitcnt lgkmcnt(2)\n\t" \
  "ds_write_b128 v24, v[8:11] offset:0\n\t" \
  "v_smfmac_f32_16x16x64_bf16 v[68:71], a[32:35], v[126:133], v178\n\t" \
  "s_bfe_i32 s47, s48, 0x10000\n\t" \
  "v_add_u32 v28, s38, v187\n\t" \
  "v_smfmac_f32_16x16x64_bf16 v[36:39], a[36:39], v[134:141], v146\n\t" \
  "s_lshr_b32 s48, s48, 1\n\t" \
  "ds_read_b128 v[126:129], v183 offset:4096\n\t" \
  "ds_read_b128 v[28:31], v28 offset:12288\n\t" \
  "v_smfmac_f32_16x16x64_bf16 v[40:43], a[40:43], v[134:141], v146 abid:1\n\t" \
  "s_add_u32 s36, s35, s39\n\t" \
  "v_smfmac_f32_16x16x64_bf16 v[44:47], a[44:47], v[134:141], v147\n\t" \
  "s_andn2_b32 s44, 0x40000000, s47\n\t" \
  "ds_write_b128 v24, v[12:15] offset:128\n\t" \
  "v_xor_b32 v24, 64, v24\n\t" \
  "v_smfmac_f32_16x16x64_bf16 v[48:51], a[48:51], v[134:141], v147 abid:1\n\t" \
  "s_or_b32 s36, s36, s44\n\t" \
  "v_add_u32_sdwa v8, s29, v110 dst_sel:DWORD dst_unused:UNUSED_PAD src0_sel:DWORD src1_sel:WORD_0\n\t" \
  "v_smfmac_f32_16x16x64_bf16 v[52:55], a[52:55], v[134:141], v148\n\t" \
  "ds_read_b128 v[130:133], v184 offset:4096\n\t" \
  "s_waitcnt lgkmcnt(2)\n\t" \
  "buffer_store_dwordx4 v[28:31], v34, s[20:23], s36 offen sc1\n\t" \
  "v_add_u32_sdwa v10, s29, v110 dst_sel:DWORD dst_unused:UNUSED_PAD src0_sel:DWORD src1_sel:WORD_1\n\t" \
  "v_smfmac_f32_16x16x64_bf16 v[56:59], a[56:59], v[134:141], v148 abid:1\n\t" \
  "s_and_b32 s44, s47, 4718592\n\t" \
  "ds_read_b64 v[4:5], v4 offset:0\n\t" \
  "v_add_u32_sdwa v12, s29, v111 dst_sel:DWORD dst_unused:UNUSED_PAD src0_sel:DWORD src1_sel:WORD_0\n\t" \
  "v_xad_u32 v28, v187, 64, s38\n\t" \
  "v_smfmac_f32_16x16x64_bf16 v[60:63], a[60:63], v[134:141], v149\n\t" \
  "s_add_u32 s35, s35, s44\n\t" \
  "ds_write_b128 v24, v[16:19] offset:0\n\t" \
  "ds_read_b128 v[28:31], v28 offset:13312\n\t" \
  "v_add_u32_sdwa v14, s29, v111 dst_sel:DWORD dst_unused:UNUSED_PAD src0_sel:DWORD src1_sel:WORD_1\n\t" \
  "v_smfmac_f32_16x16x64_bf16 v[64:67], a[64:67], v[134:141], v149 abid:1\n\t" \
  "s_add_u32 s36, s36, 4608\n\t" \
  "ds_read_b64 v[6:7], v6 offset:0\n\t" \
  "v_smfmac_f32_16x16x64_bf16 v[68:71], a[68:71], v[134:141], v178 abid:1\n\t" \
  "s_waitcnt lgkmcnt(3)\n\t" \
  "ds_read_b128 v[134:137], v183 offset:6144\n\t" \
  "v_smfmac_f32_16x16x64_bf16 v[36:39], a[72:75], v[126:133], v150\n\t" \
  "ds_read_b128 v[138:141], v184 offset:6144\n\t" \
  "v_smfmac_f32_16x16x64_bf16 v[40:43], a[76:79], v[126:133], v150 abid:1\n\t" \
  "s_and_b32 s27, s64, 7\n\t" \
  "ds_write_b128 v24, v[20:23] offset:128\n\t" \
  "s_waitcnt lgkmcnt(4)\n\t" \
  "buffer_store_dwordx4 v[28:31], v34, s[20:23], s36 offen sc1\n\t" \
  "v_add_u32_sdwa v16, s29, v112 dst_sel:DWORD dst_unused:UNUSED_PAD src0_sel:DWORD src1_sel:WORD_0\n\t" \
  "v_smfmac_f32_16x16x64_bf16 v[44:47], a[80:83], v[126:133], v151\n\t" \
  "s_waitcnt lgkmcnt(3)\n\t" \
  "s_and_b32 s44, s47, 18432\n\t" \
  "ds_read_b64 v[8:9], v8 offset:0\n\t" \
  "v_add_u32_sdwa v18, s29, v112 dst_sel:DWORD dst_unused:UNUSED_PAD src0_sel:DWORD src1_sel:WORD_1\n\t" \
  "v_add_u32 v28, s38, v187\n\t" \
  "v_smfmac_f32_16x16x64_bf16 v[36:39], v[72:75], v[0:7], v182 abid:1\n\t" \
  "s_lshl2_add_u32 s66, s27, s68\n\t" \
  "ds_read_b64 v[10:11], v10 offset:0\n\t" \
  "ds_read_b128 v[28:31], v28 offset:14336\n\t" \
  "v_add_u32_sdwa v20, s29, v113 dst_sel:DWORD dst_unused:UNUSED_PAD src0_sel:DWORD src1_sel:WORD_0\n\t" \
  "v_smfmac_f32_16x16x64_bf16 v[48:51], a[84:87], v[126:133], v151 abid:1\n\t" \
  "s_and_b32 s27, s64, 8\n\t" \
  "s_add_u32 s36, s36, 4608\n\t" \
  "ds_read_b64 v[12:13], v12 offset:0\n\t" \
  "v_add_u32_sdwa v22, s29, v113 dst_sel:DWORD dst_unused:UNUSED_PAD src0_sel:DWORD src1_sel:WORD_1\n\t" \
  "v_smfmac_f32_16x16x64_bf16 v[52:55], a[88:91], v[126:133], v152\n\t" \
  "s_mul_i32 s27, s27, 176\n\t" \
  "ds_write_b64 v25, v[26:27] offset:16384\n\t" \
  "v_add_u32_sdwa v0, s29, v114 dst_sel:DWORD dst_unused:UNUSED_PAD src0_sel:DWORD src1_sel:WORD_0\n\t" \
  "v_smfmac_f32_16x16x64_bf16 v[56:59], a[92:95], v[126:133], v152 abid:1\n\t" \
  "s_add_u32 s66, s66, s27\n\t" \
  "ds_read_b64 v[14:15], v14 offset:0\n\t" \
  "v_add_u32_sdwa v2, s29, v114 dst_sel:DWORD dst_unused:UNUSED_PAD src0_sel:DWORD src1_sel:WORD_1\n\t" \
  "v_smfmac_f32_16x16x64_bf16 v[60:63], a[96:99], v[126:133], v153\n\t" \
  "s_lshr_b64 s[64:65], s[64:65], 4\n\t" \
  "ds_read_b64 v[16:17], v16 offset:0\n\t" \
  "s_waitcnt lgkmcnt(4)\n\t" \
  "buffer_store_dwordx4 v[28:31], v34, s[20:23], s36 offen sc1\n\t" \
  "v_add_u32_sdwa v4, s29, v115 dst_sel:DWORD dst_unused:UNUSED_PAD src0_sel:DWORD src1_sel:WORD_0\n\t" \
  "v_smfmac_f32_16x16x64_bf16 v[64:67], a[100:103], v[126:133], v153 abid:1\n\t" \
  "ds_read_b64 v[18:19], v18 offset:0\n\t" \
  "v_add_u32_sdwa v6, s29, v115 dst_sel:DWORD dst_unused:UNUSED_PAD src0_sel:DWORD src1_sel:WORD_1\n\t" \
  "v_xad_u32 v28, v187, 64, s38\n\t" \
  "v_smfmac_f32_16x16x64_bf16 v[68:71], a[104:107], v[126:133], v179\n\t" \
  "s_waitcnt lgkmcnt(8)\n\t" \
  "s_add_u32 s38, s38, s44\n\t" \
  "ds_read_b128 v[126:129], v183 offset:8192\n\t" \
  "ds_read_b128 v[28:31], v28 offset:15360\n\t" \
  "v_smfmac_f32_16x16x64_bf16 v[36:39], a[108:111], v[134:141], v154\n\t" \
  "s_waitcnt lgkmcnt(3)\n\t" \
  "s_add_u32 s36, s36, 4608\n\t" \
  "ds_read_b128 v[130:133], v184 offset:8192\n\t" \
  "v_smfmac_f32_16x16x64_bf16 v[40:43], v[76:79], v[8:15], v182 abid:1\n\t" \
  "s_sub_u32 s44, s38, 129024\n\t" \
  "ds_read_b64 v[20:21], v20 offset:0\n\t" \
  "v_smfmac_f32_16x16x64_bf16 v[40:43], a[112:115], v[134:141], v154 abid:1\n\t" \
  "s_min_u32 s38, s38, s44\n\t" \
  "ds_read_b64 v[22:23], v22 offset:0\n\t" \
  "v_smfmac_f32_16x16x64_bf16 v[44:47], a[116:119], v[134:141], v155\n\t" \
  "ds_read_b64 v[0:1], v0 offset:0\n\t" \
  "s_waitcnt lgkmcnt(4)\n\t" \
  "buffer_store_dwordx4 v[28:31], v34, s[20:23], s36 offen sc1\n\t" \
  "v_smfmac_f32_16x16x64_bf16 v[48:51], a[120:123], v[134:141], v155 abid:1\n\t" \
  "ds_read_b64 v[2:3], v2 offset:0\n\t" \
  "v_add_u32_sdwa v8, s29, v32 dst_sel:DWORD dst_unused:UNUSED_PAD src0_sel:DWORD src1_sel:WORD_0\n\t" \
  "v_smfmac_f32_16x16x64_bf16 v[52:55], a[124:127], v[134:141], v156\n\t" \
  "s_branch .Ljn31%=\n\t" \
  ".fill (.Lvar%=+38268-.)/4, 4, 0xbf800000\n\t" \
  "s_nop 0\n\t" \
  "s_branch .Lfin%=\n\t" \
  "s_branch .Lhe%=\n\t" \
  "s_waitcnt vmcnt(6)\n\t" \
  "s_waitcnt vmcnt(10)\n\t" \
  "s_waitcnt vmcnt(12)\n\t" \
  "s_waitcnt vmcnt(17)\n\t" \
  "s_waitcnt vmcnt(21) lgkmcnt(0)\n\t" \
  "s_barrier\n\t" \
  "v_smfmac_f32_16x16x64_bf16 v[36:39], v[220:223], v[0:7], v174\n\t" \
  "ds_read_b128 v[126:129], v183 offset:0\n\t" \
  "v_add_u32 v24, s37, v185\n\t" \
  "v_smfmac_f32_16x16x64_bf16 v[40:43], v[224:227], v[0:7], v174 abid:1\n\t" \
  "ds_read_b128 v[130:133], v184 offset:0\n\t" \
  "v_xor_b32 v25, s41, v183\n\t" \
  "v_smfmac_f32_16x16x64_bf16 v[44:47], v[228:231], v[0:7], v175\n\t" \
  "ds_read_b128 v[134:137], v183 offset:2048\n\t" \
  "v_subrev_u32 v25, s30, v25\n\t" \
  "v_smfmac_f32_16x16x64_bf16 v[48:51], v[232:235], v[0:7], v175 abid:1\n\t" \
  "s_add_u32 s30, s29, 18432\n\t" \
  "ds_read_b128 v[138:141], v184 offset:2048\n\t" \
  "v_cvt_pk_bf16_f32 v8, v36, v37\n\t" \
  "v_cvt_pk_bf16_f32 v9, v38, v39\n\t" \
  "v_smfmac_f32_16x16x64_bf16 v[52:55], v[236:239], v[0:7], v176\n\t" \
  "s_sub_u32 s25, s30, 129024\n\t" \
  "ds_read_b128 v[36:39], v35 offset:0\n\t" \
  "v_cvt_pk_bf16_f32 v10, v40, v41\n\t" \
  "v_cvt_pk_bf16_f32 v11, v42, v43\n\t" \
  "v_smfmac_f32_16x16x64_bf16 v[56:59], v[240:243], v[0:7], v176 abid:1\n\t" \
  "s_min_u32 s30, s30, s25\n\t" \
  "ds_read_b128 v[40:43], v35 offset:64\n\t" \
  "v_cvt_pk_bf16_f32 v16, v44, v45\n\t" \
  "v_cvt_pk_bf16_f32 v17, v46, v47\n\t" \
  "v_smfmac_f32_16x16x64_bf16 v[60:63], v[244:247], v[0:7], v177\n\t" \
  "s_sub_u32 s30, s30, s29\n\t" \
  "ds_read_b128 v[44:47], v35 offset:128\n\t" \
  "v_cvt_pk_bf16_f32 v18, v48, v49\n\t" \
  "v_cvt_pk_bf16_f32 v19, v50, v51\n\t" \
  "v_smfmac_f32_16x16x64_bf16 v[64:67], v[248:251], v[0:7], v177 abid:1\n\t" \
  "s_mov_b32 s37, s29\n\t" \
  "ds_read_b128 v[48:51], v35 offset:192\n\t" \
  "v_cvt_pk_bf16_f32 v12, v52, v53\n\t" \
  "v_cvt_pk_bf16_f32 v13, v54, v55\n\t" \
  "v_smfmac_f32_16x16x64_bf16 v[68:71], v[252:255], v[0:7], v182\n\t" \
  "s_waitcnt lgkmcnt(3)\n\t" \
  "ds_read_b128 v[52:55], v35 offset:256\n\t" \
  "v_cvt_pk_bf16_f32 v14, v56, v57\n\t" \
  "v_cvt_pk_bf16_f32 v15, v58, v59\n\t" \
  "v_smfmac_f32_16x16x64_bf16 v[36:39], a[0:3], v[126:133], v142\n\t" \
  "s_waitcnt lgkmcnt(3)\n\t" \
  "ds_read_b128 v[56:59], v35 offset:320\n\t" \
  "v_cvt_pk_bf16_f32 v20, v60, v61\n\t" \
  "v_cvt_pk_bf16_f32 v21, v62, v63\n\t" \
  "v_smfmac_f32_16x16x64_bf16 v[40:43], a[4:7], v[126:133], v142 abid:1\n\t" \
  "s_waitcnt lgkmcnt(3)\n\t" \
  "ds_read_b128 v[60:63], v35 offset:384\n\t" \
  "v_cvt_pk_bf16_f32 v22, v64, v65\n\t" \
  "v_cvt_pk_bf16_f32 v23, v66, v67\n\t" \
  "v_smfmac_f32_16x16x64_bf16 v[44:47], a[8:11], v[126:133], v143\n\t" \
  "s_waitcnt lgkmcnt(3)\n\t" \
  "ds_read_b128 v[64:67], v35 offset:448\n\t" \
  "v_cvt_pk_bf16_f32 v26, v68, v69\n\t" \
  "v_cvt_pk_bf16_f32 v27, v70, v71\n\t" \
  "v_smfmac_f32_16x16x64_bf16 v[48:51], a[12:15], v[126:133], v143 abid:1\n\t" \
  "s_waitcnt lgkmcnt(3)\n\t" \
  "ds_read_b128 v[68:71], v35 offset:512\n\t" \
  "v_add_u32_sdwa v0, s29, v108 dst_sel:DWORD dst_unused:UNUSED_PAD src0_sel:DWORD src1_sel:WORD_0\n\t" \
  "v_smfmac_f32_16x16x64_bf16 v[52:55], a[16:19], v[126:133], v144\n\t" \
  "s_add_u32 m0, s33, 0\n\t" \
  "s_waitcnt lgkmcnt(3)\n\t" \
  "ds_read_b64 v[0:1], v0 offset:0\n\t" \
  "v_add_u32_sdwa v2, s29, v108 dst_sel:DWORD dst_unused:UNUSED_PAD src0_sel:DWORD src1_sel:WORD_1\n\t" \
  "v_smfmac_f32_16x16x64_bf16 v[56:59], a[20:23], v[126:133], v144 abid:1\n\t" \
  "s_waitcnt lgkmcnt(3)\n\t" \
  "buffer_load_dwordx4 v186, s[16:19], s34 offen offset:0 nt lds\n\t" \
  "v_add_u32_sdwa v4, s29, v109 dst_sel:DWORD dst_unused:UNUSED_PAD src0_sel:DWORD src1_sel:WORD_0\n\t" \
  "v_smfmac_f32_16x16x64_bf16 v[60:63], a[24:27], v[126:133], v145\n\t" \
  "s_waitcnt lgkmcnt(2)\n\t" \
  "ds_read_b64 v[2:3], v2 offset:0\n\t" \
  "v_add_u32_sdwa v6, s29, v109 dst_sel:DWORD dst_unused:UNUSED_PAD src0_sel:DWORD src1_sel:WORD_1\n\t" \
  "v_smfmac_f32_16x16x64_bf16 v[64:67], a[28:31], v[126:133], v145 abid:1\n\t" \
  "s_add_u32 m0, s33, 1920\n\t" \
  "s_waitcnt lgkmcnt(2)\n\t" \
  "ds_write_b128 v24, v[8:11] offset:0\n\t" \
  "v_smfmac_f32_16x16x64_bf16 v[68:71], a[32:35], v[126:133], v178\n\t" \
  "buffer_load_dwordx4 v186, s[16:19], s34 offen offset:128 nt lds\n\t" \
  "v_smfmac_f32_16x16x64_bf16 v[36:39], a[36:39], v[134:141], v146\n\t" \
  "s_add_u32 m0, s33, 3840\n\t" \
  "ds_read_b128 v[126:129], v183 offset:4096\n\t" \
  "v_smfmac_f32_16x16x64_bf16 v[40:43], a[40:43], v[134:141], v146 abid:1\n\t" \
  "buffer_load_dwordx4 v186, s[16:19], s34 offen offset:256 nt lds\n\t" \
  "v_smfmac_f32_16x16x64_bf16 v[44:47], a[44:47], v[134:141], v147\n\t" \
  "s_add_u32 m0, s33, 5760\n\t" \
  "ds_write_b128 v24, v[12:15] offset:128\n\t" \
  "v_xor_b32 v24, 64, v24\n\t" \
  "v_smfmac_f32_16x16x64_bf16 v[48:51], a[48:51], v[134:141], v147 abid:1\n\t" \
  "buffer_load_dwordx4 v186, s[16:19], s34 offen offset:384 nt lds\n\t" \
  "v_add_u32_sdwa v8, s29, v110 dst_sel:DWORD dst_unused:UNUSED_PAD src0_sel:DWORD src1_sel:WORD_0\n\t" \
  "v_smfmac_f32_16x16x64_bf16 v[52:55], a[52:55], v[134:141], v148\n\t" \
  "s_add_u32 s31, s31, 18432\n\t" \
  "ds_read_b128 v[130:133], v184 offset:4096\n\t" \
  "v_add_u32_sdwa v10, s29, v110 dst_sel:DWORD dst_unused:UNUSED_PAD src0_sel:DWORD src1_sel:WORD_1\n\t" \
  "v_smfmac_f32_16x16x64_bf16 v[56:59], a[56:59], v[134:141], v148 abid:1\n\t" \
  "s_sub_u32 s26, s31, 129024\n\t" \
  "ds_read_b64 v[4:5], v4 offset:0\n\t" \
  "v_add_u32_sdwa v12, s29, v111 dst_sel:DWORD dst_unused:UNUSED_PAD src0_sel:DWORD src1_sel:WORD_0\n\t" \
  "v_smfmac_f32_16x16x64_bf16 v[60:63], a[60:63], v[134:141], v149\n\t" \
  "s_min_u32 s31, s31, s26\n\t" \
  "ds_write_b128 v24, v[16:19] offset:0\n\t" \
  "v_add_u32_sdwa v14, s29, v111 dst_sel:DWORD dst_unused:UNUSED_PAD src0_sel:DWORD src1_sel:WORD_1\n\t" \
  "v_smfmac_f32_16x16x64_bf16 v[64:67], a[64:67], v[134:141], v149 abid:1\n\t" \
  "s_add_u32 s33, s31, s32\n\t" \
  "ds_read_b64 v[6:7], v6 offset:0\n\t" \
  "v_smfmac_f32_16x16x64_bf16 v[68:71], a[68:71], v[134:141], v178 abid:1\n\t" \
  "s_waitcnt lgkmcnt(3)\n\t" \
  "ds_read_b128 v[134:137], v183 offset:6144\n\t" \
  "v_smfmac_f32_16x16x64_bf16 v[36:39], a[72:75], v[126:133], v150\n\t" \
  "s_add_u32 s34, s34, 4718592\n\t" \
  "ds_read_b128 v[138:141], v184 offset:6144\n\t" \
  "v_smfmac_f32_16x16x64_bf16 v[40:43], a[76:79], v[126:133], v150 abid:1\n\t" \
  "s_and_b32 s27, s64, 7\n\t" \
  "ds_write_b128 v24, v[20:23] offset:128\n\t" \
  "v_add_u32_sdwa v16, s29, v112 dst_sel:DWORD dst_unused:UNUSED_PAD src0_sel:DWORD src1_sel:WORD_0\n\t" \
  "v_smfmac_f32_16x16x64_bf16 v[44:47], a[80:83], v[126:133], v151\n\t" \
  "s_waitcnt lgkmcnt(3)\n\t" \
  "ds_read_b64 v[8:9], v8 offset:0\n\t" \
  "v_add_u32_sdwa v18, s29, v112 dst_sel:DWORD dst_unused:UNUSED_PAD src0_sel:DWORD src1_sel:WORD_1\n\t" \
  "v_smfmac_f32_16x16x64_bf16 v[36:39], v[72:75], v[0:7], v182 abid:1\n\t" \
  "s_lshl2_add_u32 s66, s27, s68\n\t" \
  "ds_read_b64 v[10:11], v10 offset:0\n\t" \
  "v_add_u32_sdwa v20, s29, v113 dst_sel:DWORD dst_unused:UNUSED_PAD src0_sel:DWORD src1_sel:WORD_0\n\t" \
  "v_smfmac_f32_16x16x64_bf16 v[48:51], a[84:87], v[126:133], v151 abid:1\n\t" \
  "s_and_b32 s27, s64, 8\n\t" \
  "ds_read_b64 v[12:13], v12 offset:0\n\t" \
  "v_add_u32_sdwa v22, s29, v113 dst_sel:DWORD dst_unused:UNUSED_PAD src0_sel:DWORD src1_sel:WORD_1\n\t" \
  "v_smfmac_f32_16x16x64_bf16 v[52:55], a[88:91], v[126:133], v152\n\t" \
  "s_mul_i32 s27, s27, 176\n\t" \
  "ds_write_b64 v25, v[26:27] offset:16384\n\t" \
  "v_add_u32_sdwa v0, s29, v114 dst_sel:DWORD dst_unused:UNUSED_PAD src0_sel:DWORD src1_sel:WORD_0\n\t" \
  "v_smfmac_f32_16x16x64_bf16 v[56:59], a[92:95], v[126:133], v152 abid:1\n\t" \
  "s_add_u32 s66, s66, s27\n\t" \
  "ds_read_b64 v[14:15], v14 offset:0\n\t" \
  "v_add_u32_sdwa v2, s29, v114 dst_sel:DWORD dst_unused:UNUSED_PAD src0_sel:DWORD src1_sel:WORD_1\n\t" \
  "v_smfmac_f32_16x16x64_bf16 v[60:63], a[96:99], v[126:133], v153\n\t" \
  "s_lshr_b64 s[64:65], s[64:65], 4\n\t" \
  "ds_read_b64 v[16:17], v16 offset:0\n\t" \
  "v_add_u32_sdwa v4, s29, v115 dst_sel:DWORD dst_unused:UNUSED_PAD src0_sel:DWORD src1_sel:WORD_0\n\t" \
  "v_smfmac_f32_16x16x64_bf16 v[64:67], a[100:103], v[126:133], v153 abid:1\n\t" \
  "ds_read_b64 v[18:19], v18 offset:0\n\t" \
  "v_add_u32_sdwa v6, s29, v115 dst_sel:DWORD dst_unused:UNUSED_PAD src0_sel:DWORD src1_sel:WORD_1\n\t" \
  "v_smfmac_f32_16x16x64_bf16 v[68:71], a[104:107], v[126:133], v179\n\t" \
  "s_waitcnt lgkmcnt(8)\n\t" \
  "ds_read_b128 v[126:129], v183 offset:8192\n\t" \
  "v_smfmac_f32_16x16x64_bf16 v[36:39], a[108:111], v[134:141], v154\n\t" \
  "s_waitcnt lgkmcnt(3)\n\t" \
  "ds_read_b128 v[130:133], v184 offset:8192\n\t" \
  "v_smfmac_f32_16x16x64_bf16 v[40:43], v[76:79], v[8:15], v182 abid:1\n\t" \
  "ds_read_b64 v[20:21], v20 offset:0\n\t" \
  "v_smfmac_f32_16x16x64_bf16 v[40:43], a[112:115], v[134:141], v154 abid:1\n\t" \
  "ds_read_b64 v[22:23], v22 offset:0\n\t" \
  "v_smfmac_f32_16x16x64_bf16 v[44:47], a[116:119], v[134:141], v155\n\t" \
  "ds_read_b64 v[0:1], v0 offset:0\n\t" \
  "v_smfmac_f32_16x16x64_bf16 v[48:51], a[120:123], v[134:141], v155 abid:1\n\t" \
  "ds_read_b64 v[2:3], v2 offset:0\n\t" \
  "v_add_u32_sdwa v8, s29, v32 dst_sel:DWORD dst_unused:UNUSED_PAD src0_sel:DWORD src1_sel:WORD_0\n\t" \
  "v_smfmac_f32_16x16x64_bf16 v[52:55], a[124:127], v[134:141], v156\n\t" \
  ".Ljn31%=:\n\t" \
  "ds_read_b64 v[4:5], v4 offset:0\n\t" \
  "v_add_u32_sdwa v10, s29, v32 dst_sel:DWORD dst_unused:UNUSED_PAD src0_sel:DWORD src1_sel:WORD_1\n\t" \
  "v_smfmac_f32_16x16x64_bf16 v[56:59], a[128:131], v[134:141], v156 abid:1\n\t" \
  "s_waitcnt lgkmcnt(3)\n\t" \
  "ds_read_b64 v[6:7], v6 offset:0\n\t" \
  "v_add_u32 v28, s60, v187\n\t" \
  "v_smfmac_f32_16x16x64_bf16 v[44:47], v[80:83], v[16:23], v182 abid:1\n\t" \
  "ds_read_b64 v[8:9], v8 offset:0\n\t" \
  "v_add_u32_sdwa v16, s29, v116 dst_sel:DWORD dst_unused:UNUSED_PAD src0_sel:DWORD src1_sel:WORD_0\n\t" \
  "v_smfmac_f32_16x16x64_bf16 v[60:63], a[132:135], v[134:141], v157\n\t" \
  "ds_read_b64 v[10:11], v10 offset:0\n\t" \
  "v_add_u32_sdwa v18, s29, v116 dst_sel:DWORD dst_unused:UNUSED_PAD src0_sel:DWORD src1_sel:WORD_1\n\t" \
  "v_smfmac_f32_16x16x64_bf16 v[64:67], a[136:139], v[134:141], v157 abid:1\n\t" \
  "ds_read_b128 v[28:31], v28 offset:0\n\t" \
  "v_add_u32_sdwa v20, s29, v117 dst_sel:DWORD dst_unused:UNUSED_PAD src0_sel:DWORD src1_sel:WORD_0\n\t" \
  "v_smfmac_f32_16x16x64_bf16 v[68:71], a[140:143], v[134:141], v179 abid:1\n\t" \
  "s_waitcnt lgkmcnt(3)\n\t" \
  "ds_read_b128 v[134:137], v183 offset:10240\n\t" \
  "v_add_u32_sdwa v22, s29, v117 dst_sel:DWORD dst_unused:UNUSED_PAD src0_sel:DWORD src1_sel:WORD_1\n\t" \
  "v_smfmac_f32_16x16x64_bf16 v[48:51], v[84:87], v[0:7], v182 abid:1\n\t" \
  "ds_read_b128 v[138:141], v184 offset:10240\n\t" \
  "v_add_u32_sdwa v0, s29, v118 dst_sel:DWORD dst_unused:UNUSED_PAD src0_sel:DWORD src1_sel:WORD_0\n\t" \
  "v_smfmac_f32_16x16x64_bf16 v[36:39], a[144:147], v[126:133], v158\n\t" \
  "ds_read_b64 v[16:17], v16 offset:0\n\t" \
  "v_add_u32 v24, s38, v187\n\t" \
  "v_smfmac_f32_16x16x64_bf16 v[40:43], a[148:151], v[126:133], v158 abid:1\n\t" \
  "s_waitcnt lgkmcnt(3)\n\t" \
  "ds_read_b128 v[24:27], v24 offset:12288\n\t" \
  "v_add_u32_sdwa v2, s29, v118 dst_sel:DWORD dst_unused:UNUSED_PAD src0_sel:DWORD src1_sel:WORD_1\n\t" \
  "s_set_gpr_idx_on s6, gpr_idx(DST)\n\t" \
  "v_smfmac_f32_16x16x64_bf16 v[36:39], v[28:31], v[8:15], v182 abid:1\n\t" \
  "s_set_gpr_idx_off\n\t" \
  "ds_read_b64 v[18:19], v18 offset:0\n\t" \
  "v_add_u32_sdwa v4, s29, v119 dst_sel:DWORD dst_unused:UNUSED_PAD src0_sel:DWORD src1_sel:WORD_0\n\t" \
  "v_smfmac_f32_16x16x64_bf16 v[44:47], a[152:155], v[126:133], v159\n\t" \
  "ds_read_b64 v[20:21], v20 offset:0\n\t" \
  "v_add_u32_sdwa v6, s29, v119 dst_sel:DWORD dst_unused:UNUSED_PAD src0_sel:DWORD src1_sel:WORD_1\n\t" \
  "v_smfmac_f32_16x16x64_bf16 v[48:51], a[156:159], v[126:133], v159 abid:1\n\t" \
  "s_add_u32 s36, s35, s39\n\t" \
  "ds_read_b64 v[22:23], v22 offset:0\n\t" \
  "v_add_u32_sdwa v8, s29, v120 dst_sel:DWORD dst_unused:UNUSED_PAD src0_sel:DWORD src1_sel:WORD_0\n\t" \
  "v_smfmac_f32_16x16x64_bf16 v[52:55], a[160:163], v[126:133], v160\n\t" \
  "s_waitcnt lgkmcnt(3)\n\t" \
  "buffer_store_dwordx4 v[24:27], v34, s[20:23], s36 offen sc1\n\t" \
  "v_add_u32_sdwa v10, s29, v120 dst_sel:DWORD dst_unused:UNUSED_PAD src0_sel:DWORD src1_sel:WORD_1\n\t" \
  "v_smfmac_f32_16x16x64_bf16 v[56:59], a[164:167], v[126:133], v160 abid:1\n\t" \
  "ds_read_b64 v[0:1], v0 offset:0\n\t" \
  "v_xad_u32 v24, v187, 64, s38\n\t" \
  "v_smfmac_f32_16x16x64_bf16 v[60:63], a[168:171], v[126:133], v161\n\t" \
  "ds_read_b128 v[24:27], v24 offset:13312\n\t" \
  "v_add_u32_sdwa v12, s29, v121 dst_sel:DWORD dst_unused:UNUSED_PAD src0_sel:DWORD src1_sel:WORD_0\n\t" \
  "v_smfmac_f32_16x16x64_bf16 v[64:67], a[172:175], v[126:133], v161 abid:1\n\t" \
  "s_waitcnt lgkmcnt(2)\n\t" \
  "ds_read_b64 v[2:3], v2 offset:0\n\t" \
  "v_add_u32_sdwa v14, s29, v121 dst_sel:DWORD dst_unused:UNUSED_PAD src0_sel:DWORD src1_sel:WORD_1\n\t" \
  "v_smfmac_f32_16x16x64_bf16 v[52:55], v[88:91], v[16:23], v182 abid:1\n\t" \
  "ds_read_b64 v[4:5], v4 offset:0\n\t" \
  "v_add_u32_sdwa v16, s29, v122 dst_sel:DWORD dst_unused:UNUSED_PAD src0_sel:DWORD src1_sel:WORD_0\n\t" \
  "v_smfmac_f32_16x16x64_bf16 v[68:71], a[176:179], v[126:133], v180\n\t" \
  "s_add_u32 s36, s36, 4608\n\t" \
  "ds_read_b128 v[126:129], v183 offset:12288\n\t" \
  "v_add_u32_sdwa v18, s29, v122 dst_sel:DWORD dst_unused:UNUSED_PAD src0_sel:DWORD src1_sel:WORD_1\n\t" \
  "v_smfmac_f32_16x16x64_bf16 v[36:39], a[180:183], v[134:141], v162\n\t" \
  "s_waitcnt lgkmcnt(3)\n\t" \
  "buffer_store_dwordx4 v[24:27], v34, s[20:23], s36 offen sc1\n\t" \
  "v_add_u32_sdwa v20, s29, v123 dst_sel:DWORD dst_unused:UNUSED_PAD src0_sel:DWORD src1_sel:WORD_0\n\t" \
  "v_smfmac_f32_16x16x64_bf16 v[40:43], a[184:187], v[134:141], v162 abid:1\n\t" \
  "ds_read_b128 v[130:133], v184 offset:12288\n\t" \
  "v_add_u32 v24, s38, v187\n\t" \
  "v_smfmac_f32_16x16x64_bf16 v[44:47], a[188:191], v[134:141], v163\n\t" \
  "ds_read_b128 v[24:27], v24 offset:14336\n\t" \
  "v_add_u32_sdwa v22, s29, v123 dst_sel:DWORD dst_unused:UNUSED_PAD src0_sel:DWORD src1_sel:WORD_1\n\t" \
  "v_smfmac_f32_16x16x64_bf16 v[48:51], a[192:195], v[134:141], v163 abid:1\n\t" \
  "ds_read_b64 v[6:7], v6 offset:0\n\t" \
  "v_smfmac_f32_16x16x64_bf16 v[52:55], a[196:199], v[134:141], v164\n\t" \
  "ds_read_b64 v[8:9], v8 offset:0\n\t" \
  "v_smfmac_f32_16x16x64_bf16 v[56:59], a[200:203], v[134:141], v164 abid:1\n\t" \
  "s_add_u32 s36, s36, 4608\n\t" \
  "ds_read_b64 v[10:11], v10 offset:0\n\t" \
  "v_smfmac_f32_16x16x64_bf16 v[60:63], a[204:207], v[134:141], v165\n\t" \
  "s_waitcnt lgkmcnt(3)\n\t" \
  "buffer_store_dwordx4 v[24:27], v34, s[20:23], s36 offen sc1\n\t" \
  "v_smfmac_f32_16x16x64_bf16 v[64:67], a[208:211], v[134:141], v165 abid:1\n\t" \
  "s_waitcnt lgkmcnt(2)\n\t" \
  "ds_read_b64 v[12:13], v12 offset:0\n\t" \
  "v_xad_u32 v24, v187, 64, s38\n\t" \
  "v_smfmac_f32_16x16x64_bf16 v[56:59], v[92:95], v[0:7], v182 abid:1\n\t" \
  "s_add_u32 s38, s38, 18432\n\t" \
  "ds_read_b128 v[24:27], v24 offset:15360\n\t" \
  "v_smfmac_f32_16x16x64_bf16 v[68:71], a[212:215], v[134:141], v180 abid:1\n\t" \
  "s_sub_u32 s44, s38, 129024\n\t" \
  "ds_read_b128 v[134:137], v183 offset:14336\n\t" \
  "v_smfmac_f32_16x16x64_bf16 v[36:39], a[216:219], v[126:133], v166\n\t" \
  "s_min_u32 s38, s38, s44\n\t" \
  "ds_read_b128 v[138:141], v184 offset:14336\n\t" \
  "v_smfmac_f32_16x16x64_bf16 v[40:43], a[220:223], v[126:133], v166 abid:1\n\t" \
  "s_add_u32 s36, s36, 4608\n\t" \
  "ds_read_b64 v[14:15], v14 offset:0\n\t" \
  "v_smfmac_f32_16x16x64_bf16 v[44:47], a[224:227], v[126:133], v167\n\t" \
  "s_waitcnt lgkmcnt(3)\n\t" \
  "buffer_store_dwordx4 v[24:27], v34, s[20:23], s36 offen sc1\n\t" \
  "v_smfmac_f32_16x16x64_bf16 v[48:51], a[228:231], v[126:133], v167 abid:1\n\t" \
  "s_add_u32 s35, s35, 4718592\n\t" \
  "ds_read_b64 v[16:17], v16 offset:0\n\t" \
  "v_add_u32 v24, s43, v187\n\t" \
  "v_smfmac_f32_16x16x64_bf16 v[52:55], a[232:235], v[126:133], v168\n\t" \
  "ds_read_b128 v[24:27], v24 offset:17408\n\t" \
  "v_smfmac_f32_16x16x64_bf16 v[56:59], a[236:239], v[126:133], v168 abid:1\n\t" \
  "s_waitcnt lgkmcnt(2)\n\t" \
  "ds_read_b64 v[18:19], v18 offset:0\n\t" \
  "v_smfmac_f32_16x16x64_bf16 v[60:63], v[96:99], v[8:15], v182 abid:1\n\t" \
  "ds_read_b64 v[20:21], v20 offset:0\n\t" \
  "v_add_u32_sdwa v8, s29, v124 dst_sel:DWORD dst_unused:UNUSED_PAD src0_sel:DWORD src1_sel:WORD_0\n\t" \
  "v_smfmac_f32_16x16x64_bf16 v[60:63], a[240:243], v[126:133], v169\n\t" \
  "ds_read_b64 v[22:23], v22 offset:0\n\t" \
  "v_add_u32_sdwa v10, s29, v124 dst_sel:DWORD dst_unused:UNUSED_PAD src0_sel:DWORD src1_sel:WORD_1\n\t" \
  "v_smfmac_f32_16x16x64_bf16 v[64:67], a[244:247], v[126:133], v169 abid:1\n\t" \
  "s_waitcnt lgkmcnt(3)\n\t" \
  "buffer_store_dwordx4 v[24:27], v186, s[20:23], s40 offen offset:384 sc1\n\t" \
  "v_add_u32_sdwa v12, s29, v125 dst_sel:DWORD dst_unused:UNUSED_PAD src0_sel:DWORD src1_sel:WORD_0\n\t" \
  "v_smfmac_f32_16x16x64_bf16 v[68:71], a[248:251], v[126:133], v181\n\t" \
  "s_add_u32 s43, s43, 18432\n\t" \
  "ds_read_b128 v[0:3], v183 offset:16384\n\t" \
  "v_add_u32_sdwa v14, s29, v125 dst_sel:DWORD dst_unused:UNUSED_PAD src0_sel:DWORD src1_sel:WORD_1\n\t" \
  "v_smfmac_f32_16x16x64_bf16 v[36:39], a[252:255], v[134:141], v170\n\t" \
  "s_add_u32 s29, s29, s30\n\t" \
  "ds_read_b128 v[4:7], v184 offset:16384\n\t" \
  "v_add_u32 v183, s30, v183\n\t" \
  "v_smfmac_f32_16x16x64_bf16 v[40:43], v[188:191], v[134:141], v170 abid:1\n\t" \
  "s_waitcnt lgkmcnt(2)\n\t" \
  "ds_read_b64 v[8:9], v8 offset:0\n\t" \
  "v_add_u32 v184, s30, v184\n\t" \
  "v_smfmac_f32_16x16x64_bf16 v[64:67], v[100:103], v[16:23], v182 abid:1\n\t" \
  "s_sub_u32 s42, s43, 129024\n\t" \
  "ds_read_b64 v[10:11], v10 offset:0\n\t" \
  "v_smfmac_f32_16x16x64_bf16 v[44:47], v[192:195], v[134:141], v171\n\t" \
  "s_min_u32 s43, s43, s42\n\t" \
  "ds_read_b64 v[12:13], v12 offset:0\n\t" \
  "v_smfmac_f32_16x16x64_bf16 v[48:51], v[196:199], v[134:141], v171 abid:1\n\t" \
  "s_add_u32 s40, s40, 4718592\n\t" \
  "ds_read_b64 v[14:15], v14 offset:0\n\t" \
  "v_smfmac_f32_16x16x64_bf16 v[52:55], v[200:203], v[134:141], v172\n\t" \
  "v_smfmac_f32_16x16x64_bf16 v[56:59], v[204:207], v[134:141], v172 abid:1\n\t" \
  "v_smfmac_f32_16x16x64_bf16 v[60:63], v[208:211], v[134:141], v173\n\t" \
  "v_smfmac_f32_16x16x64_bf16 v[64:67], v[212:215], v[134:141], v173 abid:1\n\t" \
  "s_waitcnt lgkmcnt(0)\n\t" \
  "v_smfmac_f32_16x16x64_bf16 v[68:71], v[104:107], v[8:15], v182 abid:1\n\t" \
  "v_smfmac_f32_16x16x64_bf16 v[68:71], v[216:219], v[134:141], v181 abid:1\n\t" \
  "s_setpc_b64 s[66:67]\n\t" \
  ".fill (.Lvar%=+40960-.)/4, 4, 0xbf800000\n\t" \
  "s_branch .Lfin%=\n\t" \
  "s_branch .Lhe%=\n\t" \
  "s_waitcnt vmcnt(6)\n\t" \
  "s_waitcnt vmcnt(10)\n\t" \
  "s_waitcnt vmcnt(12)\n\t" \
  "s_waitcnt vmcnt(17)\n\t" \
  "s_waitcnt vmcnt(21) lgkmcnt(0)\n\t" \
  "s_barrier\n\t" \
  "v_smfmac_f32_16x16x64_bf16 v[36:39], v[220:223], v[0:7], v174\n\t" \
  "ds_read_b128 v[126:129], v183 offset:0\n\t" \
  "v_add_u32 v24, s37, v185\n\t" \
  "v_smfmac_f32_16x16x64_bf16 v[40:43], v[224:227], v[0:7], v174 abid:1\n\t" \
  "ds_read_b128 v[130:133], v184 offset:0\n\t" \
  "v_xor_b32 v25, s41, v183\n\t" \
  "v_smfmac_f32_16x16x64_bf16 v[44:47], v[228:231], v[0:7], v175\n\t" \
  "ds_read_b128 v[134:137], v183 offset:2048\n\t" \
  "v_subrev_u32 v25, s30, v25\n\t" \
  "v_smfmac_f32_16x16x64_bf16 v[48:51], v[232:235], v[0:7], v175 abid:1\n\t" \
  "s_add_u32 s30, s29, 18432\n\t" \
  "ds_read_b128 v[138:141], v184 offset:2048\n\t" \
  "v_cvt_pk_bf16_f32 v8, v36, v37\n\t" \
  "v_cvt_pk_bf16_f32 v9, v38, v39\n\t" \
  "v_smfmac_f32_16x16x64_bf16 v[52:55], v[236:239], v[0:7], v176\n\t" \
  "s_sub_u32 s25, s30, 129024\n\t" \
  "ds_read_b128 v[36:39], v35 offset:0\n\t" \
  "v_cvt_pk_bf16_f32 v10, v40, v41\n\t" \
  "v_cvt_pk_bf16_f32 v11, v42, v43\n\t" \
  "v_smfmac_f32_16x16x64_bf16 v[56:59], v[240:243], v[0:7], v176 abid:1\n\t" \
  "s_min_u32 s30, s30, s25\n\t" \
  "ds_read_b128 v[40:43], v35 offset:64\n\t" \
  "v_cvt_pk_bf16_f32 v16, v44, v45\n\t" \
  "v_cvt_pk_bf16_f32 v17, v46, v47\n\t" \
  "v_smfmac_f32_16x16x64_bf16 v[60:63], v[244:247], v[0:7], v177\n\t" \
  "s_sub_u32 s30, s30, s29\n\t" \
  "ds_read_b128 v[44:47], v35 offset:128\n\t" \
  "v_cvt_pk_bf16_f32 v18, v48, v49\n\t" \
  "v_cvt_pk_bf16_f32 v19, v50, v51\n\t" \
  "v_smfmac_f32_16x16x64_bf16 v[64:67], v[248:251], v[0:7], v177 abid:1\n\t" \
  "s_mov_b32 s37, s29\n\t" \
  "ds_read_b128 v[48:51], v35 offset:192\n\t" \
  "v_cvt_pk_bf16_f32 v12, v52, v53\n\t" \
  "v_cvt_pk_bf16_f32 v13, v54, v55\n\t" \
  "v_smfmac_f32_16x16x64_bf16 v[68:71], v[252:255], v[0:7], v182\n\t" \
  "s_waitcnt lgkmcnt(3)\n\t" \
  "ds_read_b128 v[52:55], v35 offset:256\n\t" \
  "v_cvt_pk_bf16_f32 v14, v56, v57\n\t" \
  "v_cvt_pk_bf16_f32 v15, v58, v59\n\t" \
  "v_smfmac_f32_16x16x64_bf16 v[36:39], a[0:3], v[126:133], v142\n\t" \
  "s_waitcnt lgkmcnt(3)\n\t" \
  "ds_read_b128 v[56:59], v35 offset:320\n\t" \
  "v_cvt_pk_bf16_f32 v20, v60, v61\n\t" \
  "v_cvt_pk_bf16_f32 v21, v62, v63\n\t" \
  "v_smfmac_f32_16x16x64_bf16 v[40:43], a[4:7], v[126:133], v142 abid:1\n\t" \
  "s_waitcnt lgkmcnt(3)\n\t" \
  "ds_read_b128 v[60:63], v35 offset:384\n\t" \
  "v_cvt_pk_bf16_f32 v22, v64, v65\n\t" \
  "v_cvt_pk_bf16_f32 v23, v66, v67\n\t" \
  "v_smfmac_f32_16x16x64_bf16 v[44:47], a[8:11], v[126:133], v143\n\t" \
  "s_waitcnt lgkmcnt(3)\n\t" \
  "ds_read_b128 v[64:67], v35 offset:448\n\t" \
  "v_cvt_pk_bf16_f32 v26, v68, v69\n\t" \
  "v_cvt_pk_bf16_f32 v27, v70, v71\n\t" \
  "v_smfmac_f32_16x16x64_bf16 v[48:51], a[12:15], v[126:133], v143 abid:1\n\t" \
  "s_waitcnt lgkmcnt(3)\n\t" \
  "ds_read_b128 v[68:71], v35 offset:512\n\t" \
  "v_add_u32_sdwa v0, s29, v108 dst_sel:DWORD dst_unused:UNUSED_PAD src0_sel:DWORD src1_sel:WORD_0\n\t" \
  "v_smfmac_f32_16x16x64_bf16 v[52:55], a[16:19], v[126:133], v144\n\t" \
  "s_waitcnt lgkmcnt(3)\n\t" \
  "ds_read_b64 v[0:1], v0 offset:0\n\t" \
  "v_add_u32_sdwa v2, s29, v108 dst_sel:DWORD dst_unused:UNUSED_PAD src0_sel:DWORD src1_sel:WORD_1\n\t" \
  "v_smfmac_f32_16x16x64_bf16 v[56:59], a[20:23], v[126:133], v144 abid:1\n\t" \
  "s_waitcnt lgkmcnt(3)\n\t" \
  "v_add_u32_sdwa v4, s29, v109 dst_sel:DWORD dst_unused:UNUSED_PAD src0_sel:DWORD src1_sel:WORD_0\n\t" \
  "v_smfmac_f32_16x16x64_bf16 v[60:63], a[24:27], v[126:133], v145\n\t" \
  "s_waitcnt lgkmcnt(2)\n\t" \
  "ds_read_b64 v[2:3], v2 offset:0\n\t" \
  "v_add_u32_sdwa v6, s29, v109 dst_sel:DWORD dst_unused:UNUSED_PAD src0_sel:DWORD src1_sel:WORD_1\n\t" \
  "v_smfmac_f32_16x16x64_bf16 v[64:67], a[28:31], v[126:133], v145 abid:1\n\t" \
  "s_waitcnt lgkmcnt(2)\n\t" \
  "ds_write_b128 v24, v[8:11] offset:0\n\t" \
  "v_smfmac_f32_16x16x64_bf16 v[68:71], a[32:35], v[126:133], v178\n\t" \
  "s_bfe_i32 s47, s48, 0x10000\n\t" \
  "v_add_u32 v28, s38, v187\n\t" \
  "v_smfmac_f32_16x16x64_bf16 v[36:39], a[36:39], v[134:141], v146\n\t" \
  "s_lshr_b32 s48, s48, 1\n\t" \
  "ds_read_b128 v[126:129], v183 offset:4096\n\t" \
  "ds_read_b128 v[28:31], v28 offset:12288\n\t" \
  "v_smfmac_f32_16x16x64_bf16 v[40:43], a[40:43], v[134:141], v146 abid:1\n\t" \
  "s_add_u32 s36, s35, s39\n\t" \
  "v_smfmac_f32_16x16x64_bf16 v[44:47], a[44:47], v[134:141], v147\n\t" \
  "s_andn2_b32 s44, 0x40000000, s47\n\t" \
  "ds_write_b128 v24, v[12:15] offset:128\n\t" \
  "v_xor_b32 v24, 64, v24\n\t" \
  "v_smfmac_f32_16x16x64_bf16 v[48:51], a[48:51], v[134:141], v147 abid:1\n\t" \
  "s_or_b32 s36, s36, s44\n\t" \
  "v_add_u32_sdwa v8, s29, v110 dst_sel:DWORD dst_unused:UNUSED_PAD src0_sel:DWORD src1_sel:WORD_0\n\t" \
  "v_smfmac_f32_16x16x64_bf16 v[52:55], a[52:55], v[134:141], v148\n\t" \
  "ds_read_b128 v[130:133], v184 offset:4096\n\t" \
  "s_waitcnt lgkmcnt(2)\n\t" \
  "buffer_store_dwordx4 v[28:31], v34, s[20:23], s36 offen sc1\n\t" \
  "v_add_u32_sdwa v10, s29, v110 dst_sel:DWORD dst_unused:UNUSED_PAD src0_sel:DWORD src1_sel:WORD_1\n\t" \
  "v_smfmac_f32_16x16x64_bf16 v[56:59], a[56:59], v[134:141], v148 abid:1\n\t" \
  "s_and_b32 s44, s47, 4718592\n\t" \
  "ds_read_b64 v[4:5], v4 offset:0\n\t" \
  "v_add_u32_sdwa v12, s29, v111 dst_sel:DWORD dst_unused:UNUSED_PAD src0_sel:DWORD src1_sel:WORD_0\n\t" \
  "v_xad_u32 v28, v187, 64, s38\n\t" \
  "v_smfmac_f32_16x16x64_bf16 v[60:63], a[60:63], v[134:141], v149\n\t" \
  "s_add_u32 s35, s35, s44\n\t" \
  "ds_write_b128 v24, v[16:19] offset:0\n\t" \
  "ds_read_b128 v[28:31], v28 offset:13312\n\t" \
  "v_add_u32_sdwa v14, s29, v111 dst_sel:DWORD dst_unused:UNUSED_PAD src0_sel:DWORD src1_sel:WORD_1\n\t" \
  "v_smfmac_f32_16x16x64_bf16 v[64:67], a[64:67], v[134:141], v149 abid:1\n\t" \
  "s_add_u32 s36, s36, 4608\n\t" \
  "ds_read_b64 v[6:7], v6 offset:0\n\t" \
  "v_smfmac_f32_16x16x64_bf16 v[68:71], a[68:71], v[134:141], v178 abid:1\n\t" \
  "s_waitcnt lgkmcnt(3)\n\t" \
  "ds_read_b128 v[134:137], v183 offset:6144\n\t" \
  "v_smfmac_f32_16x16x64_bf16 v[36:39], a[72:75], v[126:133], v150\n\t" \
  "ds_read_b128 v[138:141], v184 offset:6144\n\t" \
  "v_smfmac_f32_16x16x64_bf16 v[40:43], a[76:79], v[126:133], v150 abid:1\n\t" \
  "s_and_b32 s27, s64, 7\n\t" \
  "ds_write_b128 v24, v[20:23] offset:128\n\t" \
  "s_waitcnt lgkmcnt(4)\n\t" \
  "buffer_store_dwordx4 v[28:31], v34, s[20:23], s36 offen sc1\n\t" \
  "v_add_u32_sdwa v16, s29, v112 dst_sel:DWORD dst_unused:UNUSED_PAD src0_sel:DWORD src1_sel:WORD_0\n\t" \
  "v_smfmac_f32_16x16x64_bf16 v[44:47], a[80:83], v[126:133], v151\n\t" \
  "s_waitcnt lgkmcnt(3)\n\t" \
  "s_and_b32 s44, s47, 18432\n\t" \
  "ds_read_b64 v[8:9], v8 offset:0\n\t" \
  "v_add_u32_sdwa v18, s29, v112 dst_sel:DWORD dst_unused:UNUSED_PAD src0_sel:DWORD src1_sel:WORD_1\n\t" \
  "v_add_u32 v28, s38, v187\n\t" \
  "v_smfmac_f32_16x16x64_bf16 v[36:39], v[72:75], v[0:7], v182 abid:1\n\t" \
  "s_lshl2_add_u32 s66, s27, s68\n\t" \
  "ds_read_b64 v[10:11], v10 offset:0\n\t" \
  "ds_read_b128 v[28:31], v28 offset:14336\n\t" \
  "v_add_u32_sdwa v20, s29, v113 dst_sel:DWORD dst_unused:UNUSED_PAD src0_sel:DWORD src1_sel:WORD_0\n\t" \
  "v_smfmac_f32_16x16x64_bf16 v[48:51], a[84:87], v[126:133], v151 abid:1\n\t" \
  "s_and_b32 s27, s64, 8\n\t" \
  "s_add_u32 s36, s36, 4608\n\t" \
  "ds_read_b64 v[12:13], v12 offset:0\n\t" \
  "v_add_u32_sdwa v22, s29, v113 dst_sel:DWORD dst_unused:UNUSED_PAD src0_sel:DWORD src1_sel:WORD_1\n\t" \
  "v_smfmac_f32_16x16x64_bf16 v[52:55], a[88:91], v[126:133], v152\n\t" \
  "s_mul_i32 s27, s27, 176\n\t" \
  "ds_write_b64 v25, v[26:27] offset:16384\n\t" \
  "v_add_u32_sdwa v0, s29, v114 dst_sel:DWORD dst_unused:UNUSED_PAD src0_sel:DWORD src1_sel:WORD_0\n\t" \
  "v_smfmac_f32_16x16x64_bf16 v[56:59], a[92:95], v[126:133], v152 abid:1\n\t" \
  "s_add_u32 s66, s66, s27\n\t" \
  "ds_read_b64 v[14:15], v14 offset:0\n\t" \
  "ds_read_b64 v[16:17], v16 offset:0\n\t" \
  "v_add_u32_sdwa v2, s29, v114 dst_sel:DWORD dst_unused:UNUSED_PAD src0_sel:DWORD src1_sel:WORD_1\n\t" \
  "v_smfmac_f32_16x16x64_bf16 v[60:63], a[96:99], v[126:133], v153\n\t" \
  "s_lshr_b64 s[64:65], s[64:65], 4\n\t" \
  "ds_read_b64 v[18:19], v18 offset:0\n\t" \
  "ds_read_b64 v[20:21], v20 offset:0\n\t" \
  "s_waitcnt lgkmcnt(6)\n\t" \
  "buffer_store_dwordx4 v[28:31], v34, s[20:23], s36 offen sc1\n\t" \
  "v_add_u32_sdwa v4, s29, v115 dst_sel:DWORD dst_unused:UNUSED_PAD src0_sel:DWORD src1_sel:WORD_0\n\t" \
  "v_smfmac_f32_16x16x64_bf16 v[64:67], a[100:103], v[126:133], v153 abid:1\n\t" \
  "ds_read_b64 v[22:23], v22 offset:0\n\t" \
  "v_add_u32_sdwa v6, s29, v115 dst_sel:DWORD dst_unused:UNUSED_PAD src0_sel:DWORD src1_sel:WORD_1\n\t" \
  "v_xad_u32 v28, v187, 64, s38\n\t" \
  "v_smfmac_f32_16x16x64_bf16 v[68:71], a[104:107], v[126:133], v179\n\t" \
  "s_waitcnt lgkmcnt(10)\n\t" \
  "s_add_u32 s38, s38, s44\n\t" \
  "ds_read_b128 v[126:129], v183 offset:8192\n\t" \
  "ds_read_b128 v[28:31], v28 offset:15360\n\t" \
  "v_smfmac_f32_16x16x64_bf16 v[36:39], a[108:111], v[134:141], v154\n\t" \
  "s_waitcnt lgkmcnt(5)\n\t" \
  "s_add_u32 s36, s36, 4608\n\t" \
  "ds_read_b128 v[130:133], v184 offset:8192\n\t" \
  "v_smfmac_f32_16x16x64_bf16 v[40:43], v[76:79], v[8:15], v182 abid:1\n\t" \
  "s_sub_u32 s44, s38, 129024\n\t" \
  "ds_read_b64 v[0:1], v0 offset:0\n\t" \
  "v_smfmac_f32_16x16x64_bf16 v[40:43], a[112:115], v[134:141], v154 abid:1\n\t" \
  "s_waitcnt lgkmcnt(3)\n\t" \
  "s_min_u32 s38, s38, s44\n\t" \
  "ds_read_b64 v[2:3], v2 offset:0\n\t" \
  "v_smfmac_f32_16x16x64_bf16 v[44:47], v[80:83], v[16:23], v182 abid:1\n\t" \
  "ds_read_b64 v[4:5], v4 offset:0\n\t" \
  "s_waitcnt lgkmcnt(4)\n\t" \
  "buffer_store_dwordx4 v[28:31], v34, s[20:23], s36 offen sc1\n\t" \
  "v_smfmac_f32_16x16x64_bf16 v[44:47], a[116:119], v[134:141], v155\n\t" \
  "ds_read_b64 v[6:7], v6 offset:0\n\t" \
  "v_add_u32_sdwa v8, s29, v32 dst_sel:DWORD dst_unused:UNUSED_PAD src0_sel:DWORD src1_sel:WORD_0\n\t" \
  "v_smfmac_f32_16x16x64_bf16 v[48:51], a[120:123], v[134:141], v155 abid:1\n\t" \
  "s_branch .Ljn32%=\n\t" \
  ".fill (.Lvar%=+42364-.)/4, 4, 0xbf800000\n\t" \
  "s_nop 0\n\t" \
  "s_branch .Lfin%=\n\t" \
  "s_branch .Lhe%=\n\t" \
  "s_waitcnt vmcnt(6)\n\t" \
  "s_waitcnt vmcnt(10)\n\t" \
  "s_waitcnt vmcnt(12)\n\t" \
  "s_waitcnt vmcnt(17)\n\t" \
  "s_waitcnt vmcnt(21) lgkmcnt(0)\n\t" \
  "s_barrier\n\t" \
  "v_smfmac_f32_16x16x64_bf16 v[36:39], v[220:223], v[0:7], v174\n\t" \
  "ds_read_b128 v[126:129], v183 offset:0\n\t" \
  "v_add_u32 v24, s37, v185\n\t" \
  "v_smfmac_f32_16x16x64_bf16 v[40:43], v[224:227], v[0:7], v174 abid:1\n\t" \
  "ds_read_b128 v[130:133], v184 offset:0\n\t" \
  "v_xor_b32 v25, s41, v183\n\t" \
  "v_smfmac_f32_16x16x64_bf16 v[44:47], v[228:231], v[0:7], v175\n\t" \
  "ds_read_b128 v[134:137], v183 offset:2048\n\t" \
  "v_subrev_u32 v25, s30, v25\n\t" \
  "v_smfmac_f32_16x16x64_bf16 v[48:51], v[232:235], v[0:7], v175 abid:1\n\t" \
  "s_add_u32 s30, s29, 18432\n\t" \
  "ds_read_b128 v[138:141], v184 offset:2048\n\t" \
  "v_cvt_pk_bf16_f32 v8, v36, v37\n\t" \
  "v_cvt_pk_bf16_f32 v9, v38, v39\n\t" \
  "v_smfmac_f32_16x16x64_bf16 v[52:55], v[236:239], v[0:7], v176\n\t" \
  "s_sub_u32 s25, s30, 129024\n\t" \
  "ds_read_b128 v[36:39], v35 offset:0\n\t" \
  "v_cvt_pk_bf16_f32 v10, v40, v41\n\t" \
  "v_cvt_pk_bf16_f32 v11, v42, v43\n\t" \
  "v_smfmac_f32_16x16x64_bf16 v[56:59], v[240:243], v[0:7], v176 abid:1\n\t" \
  "s_min_u32 s30, s30, s25\n\t" \
  "ds_read_b128 v[40:43], v35 offset:64\n\t" \
  "v_cvt_pk_bf16_f32 v16, v44, v45\n\t" \
  "v_cvt_pk_bf16_f32 v17, v46, v47\n\t" \
  "v_smfmac_f32_16x16x64_bf16 v[60:63], v[244:247], v[0:7], v177\n\t" \
  "s_sub_u32 s30, s30, s29\n\t" \
  "ds_read_b128 v[44:47], v35 offset:128\n\t" \
  "v_cvt_pk_bf16_f32 v18, v48, v49\n\t" \
  "v_cvt_pk_bf16_f32 v19, v50, v51\n\t" \
  "v_smfmac_f32_16x16x64_bf16 v[64:67], v[248:251], v[0:7], v177 abid:1\n\t" \
  "s_mov_b32 s37, s29\n\t" \
  "ds_read_b128 v[48:51], v35 offset:192\n\t" \
  "v_cvt_pk_bf16_f32 v12, v52, v53\n\t" \
  "v_cvt_pk_bf16_f32 v13, v54, v55\n\t" \
  "v_smfmac_f32_16x16x64_bf16 v[68:71], v[252:255], v[0:7], v182\n\t" \
  "s_waitcnt lgkmcnt(3)\n\t" \
  "ds_read_b128 v[52:55], v35 offset:256\n\t" \
  "v_cvt_pk_bf16_f32 v14, v56, v57\n\t" \
  "v_cvt_pk_bf16_f32 v15, v58, v59\n\t" \
  "v_smfmac_f32_16x16x64_bf16 v[36:39], a[0:3], v[126:133], v142\n\t" \
  "s_waitcnt lgkmcnt(3)\n\t" \
  "ds_read_b128 v[56:59], v35 offset:320\n\t" \
  "v_cvt_pk_bf16_f32 v20, v60, v61\n\t" \
  "v_cvt_pk_bf16_f32 v21, v62, v63\n\t" \
  "v_smfmac_f32_16x16x64_bf16 v[40:43], a[4:7], v[126:133], v142 abid:1\n\t" \
  "s_waitcnt lgkmcnt(3)\n\t" \
  "ds_read_b128 v[60:63], v35 offset:384\n\t" \
  "v_cvt_pk_bf16_f32 v22, v64, v65\n\t" \
  "v_cvt_pk_bf16_f32 v23, v66, v67\n\t" \
  "v_smfmac_f32_16x16x64_bf16 v[44:47], a[8:11], v[126:133], v143\n\t" \
  "s_waitcnt lgkmcnt(3)\n\t" \
  "ds_read_b128 v[64:67], v35 offset:448\n\t" \
  "v_cvt_pk_bf16_f32 v26, v68, v69\n\t" \
  "v_cvt_pk_bf16_f32 v27, v70, v71\n\t" \
  "v_smfmac_f32_16x16x64_bf16 v[48:51], a[12:15], v[126:133], v143 abid:1\n\t" \
  "s_waitcnt lgkmcnt(3)\n\t" \
  "ds_read_b128 v[68:71], v35 offset:512\n\t" \
  "v_add_u32_sdwa v0, s29, v108 dst_sel:DWORD dst_unused:UNUSED_PAD src0_sel:DWORD src1_sel:WORD_0\n\t" \
  "v_smfmac_f32_16x16x64_bf16 v[52:55], a[16:19], v[126:133], v144\n\t" \
  "s_add_u32 m0, s33, 0\n\t" \
  "s_waitcnt lgkmcnt(3)\n\t" \
  "ds_read_b64 v[0:1], v0 offset:0\n\t" \
  "v_add_u32_sdwa v2, s29, v108 dst_sel:DWORD dst_unused:UNUSED_PAD src0_sel:DWORD src1_sel:WORD_1\n\t" \
  "v_smfmac_f32_16x16x64_bf16 v[56:59], a[20:23], v[126:133], v144 abid:1\n\t" \
  "s_waitcnt lgkmcnt(3)\n\t" \
  "buffer_load_dwordx4 v186, s[16:19], s34 offen offset:0 nt lds\n\t" \
  "v_add_u32_sdwa v4, s29, v109 dst_sel:DWORD dst_unused:UNUSED_PAD src0_sel:DWORD src1_sel:WORD_0\n\t" \
  "v_smfmac_f32_16x16x64_bf16 v[60:63], a[24:27], v[126:133], v145\n\t" \
  "s_waitcnt lgkmcnt(2)\n\t" \
  "ds_read_b64 v[2:3], v2 offset:0\n\t" \
  "v_add_u32_sdwa v6, s29, v109 dst_sel:DWORD dst_unused:UNUSED_PAD src0_sel:DWORD src1_sel:WORD_1\n\t" \
  "v_smfmac_f32_16x16x64_bf16 v[64:67], a[28:31], v[126:133], v145 abid:1\n\t" \
  "s_add_u32 m0, s33, 1920\n\t" \
  "s_waitcnt lgkmcnt(2)\n\t" \
  "ds_write_b128 v24, v[8:11] offset:0\n\t" \
  "v_smfmac_f32_16x16x64_bf16 v[68:71], a[32:35], v[126:133], v178\n\t" \
  "buffer_load_dwordx4 v186, s[16:19], s34 offen offset:128 nt lds\n\t" \
  "v_smfmac_f32_16x16x64_bf16 v[36:39], a[36:39], v[134:141], v146\n\t" \
  "s_add_u32 m0, s33, 3840\n\t" \
  "ds_read_b128 v[126:129], v183 offset:4096\n\t" \
  "v_smfmac_f32_16x16x64_bf16 v[40:43], a[40:43], v[134:141], v146 abid:1\n\t" \
  "buffer_load_dwordx4 v186, s[16:19], s34 offen offset:256 nt lds\n\t" \
  "v_smfmac_f32_16x16x64_bf16 v[44:47], a[44:47], v[134:141], v147\n\t" \
  "s_add_u32 m0, s33, 5760\n\t" \
  "ds_write_b128 v24, v[12:15] offset:128\n\t" \
  "v_xor_b32 v24, 64, v24\n\t" \
  "v_smfmac_f32_16x16x64_bf16 v[48:51], a[48:51], v[134:141], v147 abid:1\n\t" \
  "buffer_load_dwordx4 v186, s[16:19], s34 offen offset:384 nt lds\n\t" \
  "v_add_u32_sdwa v8, s29, v110 dst_sel:DWORD dst_unused:UNUSED_PAD src0_sel:DWORD src1_sel:WORD_0\n\t" \
  "v_smfmac_f32_16x16x64_bf16 v[52:55], a[52:55], v[134:141], v148\n\t" \
  "s_add_u32 s31, s31, 18432\n\t" \
  "ds_read_b128 v[130:133], v184 offset:4096\n\t" \
  "v_add_u32_sdwa v10, s29, v110 dst_sel:DWORD dst_unused:UNUSED_PAD src0_sel:DWORD src1_sel:WORD_1\n\t" \
  "v_smfmac_f32_16x16x64_bf16 v[56:59], a[56:59], v[134:141], v148 abid:1\n\t" \
  "s_sub_u32 s26, s31, 129024\n\t" \
  "ds_read_b64 v[4:5], v4 offset:0\n\t" \
  "v_add_u32_sdwa v12, s29, v111 dst_sel:DWORD dst_unused:UNUSED_PAD src0_sel:DWORD src1_sel:WORD_0\n\t" \
  "v_smfmac_f32_16x16x64_bf16 v[60:63], a[60:63], v[134:141], v149\n\t" \
  "s_min_u32 s31, s31, s26\n\t" \
  "ds_write_b128 v24, v[16:19] offset:0\n\t" \
  "v_add_u32_sdwa v14, s29, v111 dst_sel:DWORD dst_unused:UNUSED_PAD src0_sel:DWORD src1_sel:WORD_1\n\t" \
  "v_smfmac_f32_16x16x64_bf16 v[64:67], a[64:67], v[134:141], v149 abid:1\n\t" \
  "s_add_u32 s33, s31, s32\n\t" \
  "ds_read_b64 v[6:7], v6 offset:0\n\t" \
  "v_smfmac_f32_16x16x64_bf16 v[68:71], a[68:71], v[134:141], v178 abid:1\n\t" \
  "s_waitcnt lgkmcnt(3)\n\t" \
  "ds_read_b128 v[134:137], v183 offset:6144\n\t" \
  "v_smfmac_f32_16x16x64_bf16 v[36:39], a[72:75], v[126:133], v150\n\t" \
  "s_add_u32 s34, s34, 4718592\n\t" \
  "ds_read_b128 v[138:141], v184 offset:6144\n\t" \
  "v_smfmac_f32_16x16x64_bf16 v[40:43], a[76:79], v[126:133], v150 abid:1\n\t" \
  "s_and_b32 s27, s64, 7\n\t" \
  "ds_write_b128 v24, v[20:23] offset:128\n\t" \
  "v_add_u32_sdwa v16, s29, v112 dst_sel:DWORD dst_unused:UNUSED_PAD src0_sel:DWORD src1_sel:WORD_0\n\t" \
  "v_smfmac_f32_16x16x64_bf16 v[44:47], a[80:83], v[126:133], v151\n\t" \
  "s_waitcnt lgkmcnt(3)\n\t" \
  "ds_read_b64 v[8:9], v8 offset:0\n\t" \
  "v_add_u32_sdwa v18, s29, v112 dst_sel:DWORD dst_unused:UNUSED_PAD src0_sel:DWORD src1_sel:WORD_1\n\t" \
  "v_smfmac_f32_16x16x64_bf16 v[36:39], v[72:75], v[0:7], v182 abid:1\n\t" \
  "s_lshl2_add_u32 s66, s27, s68\n\t" \
  "ds_read_b64 v[10:11], v10 offset:0\n\t" \
  "v_add_u32_sdwa v20, s29, v113 dst_sel:DWORD dst_unused:UNUSED_PAD src0_sel:DWORD src1_sel:WORD_0\n\t" \
  "v_smfmac_f32_16x16x64_bf16 v[48:51], a[84:87], v[126:133], v151 abid:1\n\t" \
  "s_and_b32 s27, s64, 8\n\t" \
  "ds_read_b64 v[12:13], v12 offset:0\n\t" \
  "v_add_u32_sdwa v22, s29, v113 dst_sel:DWORD dst_unused:UNUSED_PAD src0_sel:DWORD src1_sel:WORD_1\n\t" \
  "v_smfmac_f32_16x16x64_bf16 v[52:55], a[88:91], v[126:133], v152\n\t" \
  "s_mul_i32 s27, s27, 176\n\t" \
  "ds_write_b64 v25, v[26:27] offset:16384\n\t" \
  "v_add_u32_sdwa v0, s29, v114 dst_sel:DWORD dst_unused:UNUSED_PAD src0_sel:DWORD src1_sel:WORD_0\n\t" \
  "v_smfmac_f32_16x16x64_bf16 v[56:59], a[92:95], v[126:133], v152 abid:1\n\t" \
  "s_add_u32 s66, s66, s27\n\t" \
  "ds_read_b64 v[14:15], v14 offset:0\n\t" \
  "ds_read_b64 v[16:17], v16 offset:0\n\t" \
  "v_add_u32_sdwa v2, s29, v114 dst_sel:DWORD dst_unused:UNUSED_PAD src0_sel:DWORD src1_sel:WORD_1\n\t" \
  "v_smfmac_f32_16x16x64_bf16 v[60:63], a[96:99], v[126:133], v153\n\t" \
  "s_lshr_b64 s[64:65], s[64:65], 4\n\t" \
  "ds_read_b64 v[18:19], v18 offset:0\n\t" \
  "ds_read_b64 v[20:21], v20 offset:0\n\t" \
  "v_add_u32_sdwa v4, s29, v115 dst_sel:DWORD dst_unused:UNUSED_PAD src0_sel:DWORD src1_sel:WORD_0\n\t" \
  "v_smfmac_f32_16x16x64_bf16 v[64:67], a[100:103], v[126:133], v153 abid:1\n\t" \
  "ds_read_b64 v[22:23], v22 offset:0\n\t" \
  "v_add_u32_sdwa v6, s29, v115 dst_sel:DWORD dst_unused:UNUSED_PAD src0_sel:DWORD src1_sel:WORD_1\n\t" \
  "v_smfmac_f32_16x16x64_bf16 v[68:71], a[104:107], v[126:133], v179\n\t" \
  "s_waitcnt lgkmcnt(10)\n\t" \
  "ds_read_b128 v[126:129], v183 offset:8192\n\t" \
  "v_smfmac_f32_16x16x64_bf16 v[36:39], a[108:111], v[134:141], v154\n\t" \
  "s_waitcnt lgkmcnt(5)\n\t" \
  "ds_read_b128 v[130:133], v184 offset:8192\n\t" \
  "v_smfmac_f32_16x16x64_bf16 v[40:43], v[76:79], v[8:15], v182 abid:1\n\t" \
  "ds_read_b64 v[0:1], v0 offset:0\n\t" \
  "v_smfmac_f32_16x16x64_bf16 v[40:43], a[112:115], v[134:141], v154 abid:1\n\t" \
  "s_waitcnt lgkmcnt(3)\n\t" \
  "ds_read_b64 v[2:3], v2 offset:0\n\t" \
  "v_smfmac_f32_16x16x64_bf16 v[44:47], v[80:83], v[16:23], v182 abid:1\n\t" \
  "ds_read_b64 v[4:5], v4 offset:0\n\t" \
  "v_smfmac_f32_16x16x64_bf16 v[44:47], a[116:119], v[134:141], v155\n\t" \
  "ds_read_b64 v[6:7], v6 offset:0\n\t" \
  "v_add_u32_sdwa v8, s29, v32 dst_sel:DWORD dst_unused:UNUSED_PAD src0_sel:DWORD src1_sel:WORD_0\n\t" \
  "v_smfmac_f32_16x16x64_bf16 v[48:51], a[120:123], v[134:141], v155 abid:1\n\t" \
  ".Ljn32%=:\n\t" \
  "ds_read_b64 v[8:9], v8 offset:0\n\t" \
  "v_add_u32_sdwa v10, s29, v32 dst_sel:DWORD dst_unused:UNUSED_PAD src0_sel:DWORD src1_sel:WORD_1\n\t" \
  "v_smfmac_f32_16x16x64_bf16 v[52:55], a[124:127], v[134:141], v156\n\t" \
  "ds_read_b64 v[10:11], v10 offset:0\n\t" \
  "v_add_u32 v28, s60, v187\n\t" \
  "v_smfmac_f32_16x16x64_bf16 v[56:59], a[128:131], v[134:141], v156 abid:1\n\t" \
  "ds_read_b128 v[28:31], v28 offset:0\n\t" \
  "v_add_u32_sdwa v16, s29, v116 dst_sel:DWORD dst_unused:UNUSED_PAD src0_sel:DWORD src1_sel:WORD_0\n\t" \
  "v_smfmac_f32_16x16x64_bf16 v[60:63], a[132:135], v[134:141], v157\n\t" \
  "s_waitcnt lgkmcnt(3)\n\t" \
  "ds_read_b64 v[16:17], v16 offset:0\n\t" \
  "v_add_u32_sdwa v18, s29, v116 dst_sel:DWORD dst_unused:UNUSED_PAD src0_sel:DWORD src1_sel:WORD_1\n\t" \
  "v_smfmac_f32_16x16x64_bf16 v[48:51], v[84:87], v[0:7], v182 abid:1\n\t" \
  "ds_read_b64 v[18:19], v18 offset:0\n\t" \
  "v_add_u32_sdwa v20, s29, v117 dst_sel:DWORD dst_unused:UNUSED_PAD src0_sel:DWORD src1_sel:WORD_0\n\t" \
  "v_smfmac_f32_16x16x64_bf16 v[64:67], a[136:139], v[134:141], v157 abid:1\n\t" \
  "ds_read_b64 v[20:21], v20 offset:0\n\t" \
  "v_add_u32_sdwa v22, s29, v117 dst_sel:DWORD dst_unused:UNUSED_PAD src0_sel:DWORD src1_sel:WORD_1\n\t" \
  "v_smfmac_f32_16x16x64_bf16 v[68:71], a[140:143], v[134:141], v179 abid:1\n\t" \
  "s_waitcnt lgkmcnt(3)\n\t" \
  "ds_read_b128 v[134:137], v183 offset:10240\n\t" \
  "v_add_u32_sdwa v0, s29, v118 dst_sel:DWORD dst_unused:UNUSED_PAD src0_sel:DWORD src1_sel:WORD_0\n\t" \
  "s_set_gpr_idx_on s6, gpr_idx(DST)\n\t" \
  "v_smfmac_f32_16x16x64_bf16 v[36:39], v[28:31], v[8:15], v182 abid:1\n\t" \
  "s_set_gpr_idx_off\n\t" \
  "ds_read_b128 v[138:141], v184 offset:10240\n\t" \
  "v_add_u32 v24, s38, v187\n\t" \
  "v_smfmac_f32_16x16x64_bf16 v[36:39], a[144:147], v[126:133], v158\n\t" \
  "ds_read_b128 v[24:27], v24 offset:12288\n\t" \
  "v_add_u32_sdwa v2, s29, v118 dst_sel:DWORD dst_unused:UNUSED_PAD src0_sel:DWORD src1_sel:WORD_1\n\t" \
  "v_smfmac_f32_16x16x64_bf16 v[40:43], a[148:151], v[126:133], v158 abid:1\n\t" \
  "ds_read_b64 v[22:23], v22 offset:0\n\t" \
  "v_add_u32_sdwa v4, s29, v119 dst_sel:DWORD dst_unused:UNUSED_PAD src0_sel:DWORD src1_sel:WORD_0\n\t" \
  "v_smfmac_f32_16x16x64_bf16 v[44:47], a[152:155], v[126:133], v159\n\t" \
  "ds_read_b64 v[0:1], v0 offset:0\n\t" \
  "v_add_u32_sdwa v6, s29, v119 dst_sel:DWORD dst_unused:UNUSED_PAD src0_sel:DWORD src1_sel:WORD_1\n\t" \
  "v_smfmac_f32_16x16x64_bf16 v[48:51], a[156:159], v[126:133], v159 abid:1\n\t" \
  "s_add_u32 s36, s35, s39\n\t" \
  "ds_read_b64 v[2:3], v2 offset:0\n\t" \
  "v_add_u32_sdwa v8, s29, v33 dst_sel:DWORD dst_unused:UNUSED_PAD src0_sel:DWORD src1_sel:WORD_0\n\t" \
  "v_smfmac_f32_16x16x64_bf16 v[52:55], a[160:163], v[126:133], v160\n\t" \
  "s_waitcnt lgkmcnt(3)\n\t" \
  "buffer_store_dwordx4 v[24:27], v34, s[20:23], s36 offen sc1\n\t" \
  "v_add_u32_sdwa v10, s29, v33 dst_sel:DWORD dst_unused:UNUSED_PAD src0_sel:DWORD src1_sel:WORD_1\n\t" \
  "v_smfmac_f32_16x16x64_bf16 v[56:59], a[164:167], v[126:133], v160 abid:1\n\t" \
  "s_waitcnt lgkmcnt(2)\n\t" \
  "ds_read_b64 v[4:5], v4 offset:0\n\t" \
  "v_xad_u32 v24, v187, 64, s38\n\t" \
  "v_smfmac_f32_16x16x64_bf16 v[52:55], v[88:91], v[16:23], v182 abid:1\n\t" \
  "ds_read_b128 v[24:27], v24 offset:13312\n\t" \
  "v_add_u32 v28, s60, v187\n\t" \
  "v_smfmac_f32_16x16x64_bf16 v[60:63], a[168:171], v[126:133], v161\n\t" \
  "ds_read_b64 v[6:7], v6 offset:0\n\t" \
  "v_add_u32_sdwa v16, s29, v120 dst_sel:DWORD dst_unused:UNUSED_PAD src0_sel:DWORD src1_sel:WORD_0\n\t" \
  "v_smfmac_f32_16x16x64_bf16 v[64:67], a[172:175], v[126:133], v161 abid:1\n\t" \
  "ds_read_b64 v[8:9], v8 offset:0\n\t" \
  "v_add_u32_sdwa v18, s29, v120 dst_sel:DWORD dst_unused:UNUSED_PAD src0_sel:DWORD src1_sel:WORD_1\n\t" \
  "v_smfmac_f32_16x16x64_bf16 v[68:71], a[176:179], v[126:133], v180\n\t" \
  "s_add_u32 s36, s36, 4608\n\t" \
  "ds_read_b128 v[126:129], v183 offset:12288\n\t" \
  "v_add_u32_sdwa v20, s29, v121 dst_sel:DWORD dst_unused:UNUSED_PAD src0_sel:DWORD src1_sel:WORD_0\n\t" \
  "v_smfmac_f32_16x16x64_bf16 v[36:39], a[180:183], v[134:141], v162\n\t" \
  "s_waitcnt lgkmcnt(3)\n\t" \
  "buffer_store_dwordx4 v[24:27], v34, s[20:23], s36 offen sc1\n\t" \
  "v_add_u32_sdwa v22, s29, v121 dst_sel:DWORD dst_unused:UNUSED_PAD src0_sel:DWORD src1_sel:WORD_1\n\t" \
  "v_smfmac_f32_16x16x64_bf16 v[40:43], a[184:187], v[134:141], v162 abid:1\n\t" \
  "s_waitcnt lgkmcnt(2)\n\t" \
  "ds_read_b128 v[130:133], v184 offset:12288\n\t" \
  "v_add_u32 v24, s38, v187\n\t" \
  "v_smfmac_f32_16x16x64_bf16 v[56:59], v[92:95], v[0:7], v182 abid:1\n\t" \
  "ds_read_b128 v[24:27], v24 offset:14336\n\t" \
  "v_add_u32_sdwa v0, s29, v122 dst_sel:DWORD dst_unused:UNUSED_PAD src0_sel:DWORD src1_sel:WORD_0\n\t" \
  "v_smfmac_f32_16x16x64_bf16 v[44:47], a[188:191], v[134:141], v163\n\t" \
  "ds_read_b64 v[10:11], v10 offset:0\n\t" \
  "v_add_u32_sdwa v2, s29, v122 dst_sel:DWORD dst_unused:UNUSED_PAD src0_sel:DWORD src1_sel:WORD_1\n\t" \
  "v_smfmac_f32_16x16x64_bf16 v[48:51], a[192:195], v[134:141], v163 abid:1\n\t" \
  "ds_read_b128 v[28:31], v28 offset:1280\n\t" \
  "v_add_u32_sdwa v4, s29, v123 dst_sel:DWORD dst_unused:UNUSED_PAD src0_sel:DWORD src1_sel:WORD_0\n\t" \
  "v_smfmac_f32_16x16x64_bf16 v[52:55], a[196:199], v[134:141], v164\n\t" \
  "s_add_u32 s36, s36, 4608\n\t" \
  "ds_read_b64 v[16:17], v16 offset:0\n\t" \
  "v_add_u32_sdwa v6, s29, v123 dst_sel:DWORD dst_unused:UNUSED_PAD src0_sel:DWORD src1_sel:WORD_1\n\t" \
  "v_smfmac_f32_16x16x64_bf16 v[56:59], a[200:203], v[134:141], v164 abid:1\n\t" \
  "s_waitcnt lgkmcnt(3)\n\t" \
  "buffer_store_dwordx4 v[24:27], v34, s[20:23], s36 offen sc1\n\t" \
  "v_smfmac_f32_16x16x64_bf16 v[60:63], a[204:207], v[134:141], v165\n\t" \
  "ds_read_b64 v[18:19], v18 offset:0\n\t" \
  "v_xad_u32 v24, v187, 64, s38\n\t" \
  "v_smfmac_f32_16x16x64_bf16 v[64:67], a[208:211], v[134:141], v165 abid:1\n\t" \
  "s_waitcnt lgkmcnt(2)\n\t" \
  "ds_read_b128 v[24:27], v24 offset:15360\n\t" \
  "s_set_gpr_idx_on s7, gpr_idx(DST)\n\t" \
  "v_smfmac_f32_16x16x64_bf16 v[36:39], v[28:31], v[8:15], v182 abid:1\n\t" \
  "s_set_gpr_idx_off\n\t" \
  "ds_read_b64 v[20:21], v20 offset:0\n\t" \
  "v_add_u32_sdwa v8, s29, v124 dst_sel:DWORD dst_unused:UNUSED_PAD src0_sel:DWORD src1_sel:WORD_0\n\t" \
  "v_smfmac_f32_16x16x64_bf16 v[68:71], a[212:215], v[134:141], v180 abid:1\n\t" \
  "s_add_u32 s38, s38, 18432\n\t" \
  "ds_read_b128 v[134:137], v183 offset:14336\n\t" \
  "v_add_u32_sdwa v10, s29, v124 dst_sel:DWORD dst_unused:UNUSED_PAD src0_sel:DWORD src1_sel:WORD_1\n\t" \
  "v_smfmac_f32_16x16x64_bf16 v[36:39], a[216:219], v[126:133], v166\n\t" \
  "s_add_u32 s36, s36, 4608\n\t" \
  "ds_read_b128 v[138:141], v184 offset:14336\n\t" \
  "v_add_u32_sdwa v12, s29, v125 dst_sel:DWORD dst_unused:UNUSED_PAD src0_sel:DWORD src1_sel:WORD_0\n\t" \
  "v_smfmac_f32_16x16x64_bf16 v[40:43], a[220:223], v[126:133], v166 abid:1\n\t" \
  "s_waitcnt lgkmcnt(3)\n\t" \
  "buffer_store_dwordx4 v[24:27], v34, s[20:23], s36 offen sc1\n\t" \
  "v_add_u32_sdwa v14, s29, v125 dst_sel:DWORD dst_unused:UNUSED_PAD src0_sel:DWORD src1_sel:WORD_1\n\t" \
  "v_smfmac_f32_16x16x64_bf16 v[44:47], a[224:227], v[126:133], v167\n\t" \
  "s_add_u32 s29, s29, s30\n\t" \
  "ds_read_b64 v[22:23], v22 offset:0\n\t" \
  "v_add_u32 v24, s43, v187\n\t" \
  "v_smfmac_f32_16x16x64_bf16 v[48:51], a[228:231], v[126:133], v167 abid:1\n\t" \
  "s_sub_u32 s44, s38, 129024\n\t" \
  "ds_read_b128 v[24:27], v24 offset:17408\n\t" \
  "v_smfmac_f32_16x16x64_bf16 v[52:55], a[232:235], v[126:133], v168\n\t" \
  "s_min_u32 s38, s38, s44\n\t" \
  "ds_read_b64 v[0:1], v0 offset:0\n\t" \
  "v_smfmac_f32_16x16x64_bf16 v[56:59], a[236:239], v[126:133], v168 abid:1\n\t" \
  "s_add_u32 s35, s35, 4718592\n\t" \
  "ds_read_b64 v[2:3], v2 offset:0\n\t" \
  "v_smfmac_f32_16x16x64_bf16 v[60:63], a[240:243], v[126:133], v169\n\t" \
  "s_waitcnt lgkmcnt(3)\n\t" \
  "ds_read_b64 v[4:5], v4 offset:0\n\t" \
  "v_smfmac_f32_16x16x64_bf16 v[60:63], v[96:99], v[16:23], v182 abid:1\n\t" \
  "s_waitcnt lgkmcnt(3)\n\t" \
  "buffer_store_dwordx4 v[24:27], v186, s[20:23], s40 offen offset:384 sc1\n\t" \
  "v_smfmac_f32_16x16x64_bf16 v[64:67], a[244:247], v[126:133], v169 abid:1\n\t" \
  "s_add_u32 s43, s43, 18432\n\t" \
  "ds_read_b64 v[6:7], v6 offset:0\n\t" \
  "v_smfmac_f32_16x16x64_bf16 v[68:71], a[248:251], v[126:133], v181\n\t" \
  "s_sub_u32 s42, s43, 129024\n\t" \
  "ds_read_b64 v[8:9], v8 offset:0\n\t" \
  "v_smfmac_f32_16x16x64_bf16 v[36:39], a[252:255], v[134:141], v170\n\t" \
  "s_min_u32 s43, s43, s42\n\t" \
  "ds_read_b64 v[10:11], v10 offset:0\n\t" \
  "v_smfmac_f32_16x16x64_bf16 v[40:43], v[188:191], v[134:141], v170 abid:1\n\t" \
  "s_add_u32 s40, s40, 4718592\n\t" \
  "ds_read_b64 v[12:13], v12 offset:0\n\t" \
  "v_smfmac_f32_16x16x64_bf16 v[44:47], v[192:195], v[134:141], v171\n\t" \
  "s_waitcnt lgkmcnt(3)\n\t" \
  "ds_read_b64 v[14:15], v14 offset:0\n\t" \
  "v_smfmac_f32_16x16x64_bf16 v[64:67], v[100:103], v[0:7], v182 abid:1\n\t" \
  "ds_read_b128 v[0:3], v183 offset:16384\n\t" \
  "v_smfmac_f32_16x16x64_bf16 v[48:51], v[196:199], v[134:141], v171 abid:1\n\t" \
  "ds_read_b128 v[4:7], v184 offset:16384\n\t" \
  "v_add_u32 v183, s30, v183\n\t" \
  "v_smfmac_f32_16x16x64_bf16 v[52:55], v[200:203], v[134:141], v172\n\t" \
  "v_add_u32 v184, s30, v184\n\t" \
  "v_smfmac_f32_16x16x64_bf16 v[56:59], v[204:207], v[134:141], v172 abid:1\n\t" \
  "s_waitcnt lgkmcnt(2)\n\t" \
  "v_smfmac_f32_16x16x64_bf16 v[68:71], v[104:107], v[8:15], v182 abid:1\n\t" \
  "v_smfmac_f32_16x16x64_bf16 v[60:63], v[208:211], v[134:141], v173\n\t" \
  "v_smfmac_f32_16x16x64_bf16 v[64:67], v[212:215], v[134:141], v173 abid:1\n\t" \
  "v_smfmac_f32_16x16x64_bf16 v[68:71], v[216:219], v[134:141], v181 abid:1\n\t" \
  "s_setpc_b64 s[66:67]\n\t" \
  ".fill (.Lvar%=+45056-.)/4, 4, 0xbf800000\n\t" \
  "s_branch .Lfin%=\n\t" \
  "s_branch .Lhe%=\n\t" \
  "s_waitcnt vmcnt(6)\n\t" \
  "s_waitcnt vmcnt(10)\n\t" \
  "s_waitcnt vmcnt(12)\n\t" \
  "s_waitcnt vmcnt(17)\n\t" \
  "s_waitcnt vmcnt(21) lgkmcnt(0)\n\t" \
  "s_barrier\n\t" \
  "v_smfmac_f32_16x16x64_bf16 v[36:39], v[220:223], v[0:7], v174\n\t" \
  "ds_read_b128 v[126:129], v183 offset:0\n\t" \
  "v_add_u32 v24, s37, v185\n\t" \
  "v_lshrrev_b32 v28, 4, v187\n\t" \
  "v_smfmac_f32_16x16x64_bf16 v[40:43], v[224:227], v[0:7], v174 abid:1\n\t" \
  "ds_read_b128 v[130:133], v184 offset:0\n\t" \
  "v_xor_b32 v25, s41, v183\n\t" \
  "v_and_or_b32 v28, v28, 48, s45\n\t" \
  "v_smfmac_f32_16x16x64_bf16 v[44:47], v[228:231], v[0:7], v175\n\t" \
  "ds_read_b128 v[134:137], v183 offset:2048\n\t" \
  "v_subrev_u32 v25, s30, v25\n\t" \
  "v_smfmac_f32_16x16x64_bf16 v[48:51], v[232:235], v[0:7], v175 abid:1\n\t" \
  "s_add_u32 s30, s29, 18432\n\t" \
  "ds_read_b128 v[138:141], v184 offset:2048\n\t" \
  "v_cvt_pk_bf16_f32 v8, v36, v37\n\t" \
  "v_cvt_pk_bf16_f32 v9, v38, v39\n\t" \
  "v_smfmac_f32_16x16x64_bf16 v[52:55], v[236:239], v[0:7], v176\n\t" \
  "s_sub_u32 s25, s30, 129024\n\t" \
  "ds_read_b128 v[36:39], v28 offset:0\n\t" \
  "v_cvt_pk_bf16_f32 v10, v40, v41\n\t" \
  "v_cvt_pk_bf16_f32 v11, v42, v43\n\t" \
  "v_smfmac_f32_16x16x64_bf16 v[56:59], v[240:243], v[0:7], v176 abid:1\n\t" \
  "s_min_u32 s30, s30, s25\n\t" \
  "ds_read_b128 v[40:43], v28 offset:64\n\t" \
  "v_cvt_pk_bf16_f32 v16, v44, v45\n\t" \
  "v_cvt_pk_bf16_f32 v17, v46, v47\n\t" \
  "v_smfmac_f32_16x16x64_bf16 v[60:63], v[244:247], v[0:7], v177\n\t" \
  "s_sub_u32 s30, s30, s29\n\t" \
  "ds_read_b128 v[44:47], v28 offset:128\n\t" \
  "v_cvt_pk_bf16_f32 v18, v48, v49\n\t" \
  "v_cvt_pk_bf16_f32 v19, v50, v51\n\t" \
  "v_smfmac_f32_16x16x64_bf16 v[64:67], v[248:251], v[0:7], v177 abid:1\n\t" \
  "s_mov_b32 s37, s29\n\t" \
  "ds_read_b128 v[48:51], v28 offset:192\n\t" \
  "v_cvt_pk_bf16_f32 v12, v52, v53\n\t" \
  "v_cvt_pk_bf16_f32 v13, v54, v55\n\t" \
  "v_smfmac_f32_16x16x64_bf16 v[68:71], v[252:255], v[0:7], v182\n\t" \
  "s_waitcnt lgkmcnt(3)\n\t" \
  "ds_read_b128 v[52:55], v28 offset:256\n\t" \
  "v_cvt_pk_bf16_f32 v14, v56, v57\n\t" \
  "v_cvt_pk_bf16_f32 v15, v58, v59\n\t" \
  "v_smfmac_f32_16x16x64_bf16 v[36:39], a[0:3], v[126:133], v142\n\t" \
  "s_waitcnt lgkmcnt(3)\n\t" \
  "ds_read_b128 v[56:59], v28 offset:320\n\t" \
  "v_cvt_pk_bf16_f32 v20, v60, v61\n\t" \
  "v_cvt_pk_bf16_f32 v21, v62, v63\n\t" \
  "v_smfmac_f32_16x16x64_bf16 v[40:43], a[4:7], v[126:133], v142 abid:1\n\t" \
  "s_waitcnt lgkmcnt(3)\n\t" \
  "ds_read_b128 v[60:63], v28 offset:384\n\t" \
  "v_cvt_pk_bf16_f32 v22, v64, v65\n\t" \
  "v_cvt_pk_bf16_f32 v23, v66, v67\n\t" \
  "v_smfmac_f32_16x16x64_bf16 v[44:47], a[8:11], v[126:133], v143\n\t" \
  "s_waitcnt lgkmcnt(3)\n\t" \
  "ds_read_b128 v[64:67], v28 offset:448\n\t" \
  "v_cvt_pk_bf16_f32 v26, v68, v69\n\t" \
  "v_cvt_pk_bf16_f32 v27, v70, v71\n\t" \
  "v_smfmac_f32_16x16x64_bf16 v[48:51], a[12:15], v[126:133], v143 abid:1\n\t" \
  "s_waitcnt lgkmcnt(3)\n\t" \
  "ds_read_b128 v[68:71], v28 offset:512\n\t" \
  "v_add_u32_sdwa v0, s29, v108 dst_sel:DWORD dst_unused:UNUSED_PAD src0_sel:DWORD src1_sel:WORD_0\n\t" \
  "v_smfmac_f32_16x16x64_bf16 v[52:55], a[16:19], v[126:133], v144\n\t" \
  "s_waitcnt lgkmcnt(3)\n\t" \
  "ds_read_b64 v[0:1], v0 offset:0\n\t" \
  "v_add_u32_sdwa v2, s29, v108 dst_sel:DWORD dst_unused:UNUSED_PAD src0_sel:DWORD src1_sel:WORD_1\n\t" \
  "v_smfmac_f32_16x16x64_bf16 v[56:59], a[20:23], v[126:133], v144 abid:1\n\t" \
  "s_waitcnt lgkmcnt(3)\n\t" \
  "v_add_u32_sdwa v4, s29, v109 dst_sel:DWORD dst_unused:UNUSED_PAD src0_sel:DWORD src1_sel:WORD_0\n\t" \
  "v_smfmac_f32_16x16x64_bf16 v[60:63], a[24:27], v[126:133], v145\n\t" \
  "s_waitcnt lgkmcnt(2)\n\t" \
  "ds_read_b64 v[2:3], v2 offset:0\n\t" \
  "v_add_u32_sdwa v6, s29, v109 dst_sel:DWORD dst_unused:UNUSED_PAD src0_sel:DWORD src1_sel:WORD_1\n\t" \
  "v_smfmac_f32_16x16x64_bf16 v[64:67], a[28:31], v[126:133], v145 abid:1\n\t" \
  "s_waitcnt lgkmcnt(2)\n\t" \
  "ds_write_b128 v24, v[8:11] offset:0\n\t" \
  "v_smfmac_f32_16x16x64_bf16 v[68:71], a[32:35], v[126:133], v178\n\t" \
  "s_bfe_i32 s47, s48, 0x10000\n\t" \
  "v_add_u32 v28, s38, v187\n\t" \
  "v_smfmac_f32_16x16x64_bf16 v[36:39], a[36:39], v[134:141], v146\n\t" \
  "s_lshr_b32 s48, s48, 1\n\t" \
  "ds_read_b128 v[126:129], v183 offset:4096\n\t" \
  "ds_read_b128 v[28:31], v28 offset:12288\n\t" \
  "v_smfmac_f32_16x16x64_bf16 v[40:43], a[40:43], v[134:141], v146 abid:1\n\t" \
  "s_add_u32 s36, s35, s39\n\t" \
  "v_smfmac_f32_16x16x64_bf16 v[44:47], a[44:47], v[134:141], v147\n\t" \
  "s_andn2_b32 s44, 0x40000000, s47\n\t" \
  "ds_write_b128 v24, v[12:15] offset:128\n\t" \
  "v_xor_b32 v24, 64, v24\n\t" \
  "v_smfmac_f32_16x16x64_bf16 v[48:51], a[48:51], v[134:141], v147 abid:1\n\t" \
  "s_or_b32 s36, s36, s44\n\t" \
  "v_add_u32_sdwa v8, s29, v110 dst_sel:DWORD dst_unused:UNUSED_PAD src0_sel:DWORD src1_sel:WORD_0\n\t" \
  "v_smfmac_f32_16x16x64_bf16 v[52:55], a[52:55], v[134:141], v148\n\t" \
  "ds_read_b128 v[130:133], v184 offset:4096\n\t" \
  "s_waitcnt lgkmcnt(2)\n\t" \
  "buffer_store_dwordx4 v[28:31], v34, s[20:23], s36 offen sc1\n\t" \
  "v_add_u32_sdwa v10, s29, v110 dst_sel:DWORD dst_unused:UNUSED_PAD src0_sel:DWORD src1_sel:WORD_1\n\t" \
  "v_smfmac_f32_16x16x64_bf16 v[56:59], a[56:59], v[134:141], v148 abid:1\n\t" \
  "s_and_b32 s44, s47, 4718592\n\t" \
  "ds_read_b64 v[4:5], v4 offset:0\n\t" \
  "v_add_u32_sdwa v12, s29, v111 dst_sel:DWORD dst_unused:UNUSED_PAD src0_sel:DWORD src1_sel:WORD_0\n\t" \
  "v_xad_u32 v28, v187, 64, s38\n\t" \
  "v_smfmac_f32_16x16x64_bf16 v[60:63], a[60:63], v[134:141], v149\n\t" \
  "s_add_u32 s35, s35, s44\n\t" \
  "ds_write_b128 v24, v[16:19] offset:0\n\t" \
  "ds_read_b128 v[28:31], v28 offset:13312\n\t" \
  "v_add_u32_sdwa v14, s29, v111 dst_sel:DWORD dst_unused:UNUSED_PAD src0_sel:DWORD src1_sel:WORD_1\n\t" \
  "v_smfmac_f32_16x16x64_bf16 v[64:67], a[64:67], v[134:141], v149 abid:1\n\t" \
  "s_add_u32 s36, s36, 4608\n\t" \
  "ds_read_b64 v[6:7], v6 offset:0\n\t" \
  "v_smfmac_f32_16x16x64_bf16 v[68:71], a[68:71], v[134:141], v178 abid:1\n\t" \
  "s_waitcnt lgkmcnt(3)\n\t" \
  "ds_read_b128 v[134:137], v183 offset:6144\n\t" \
  "v_smfmac_f32_16x16x64_bf16 v[36:39], a[72:75], v[126:133], v150\n\t" \
  "ds_read_b128 v[138:141], v184 offset:6144\n\t" \
  "v_smfmac_f32_16x16x64_bf16 v[40:43], a[76:79], v[126:133], v150 abid:1\n\t" \
  "s_and_b32 s27, s64, 7\n\t" \
  "ds_write_b128 v24, v[20:23] offset:128\n\t" \
  "s_waitcnt lgkmcnt(4)\n\t" \
  "buffer_store_dwordx4 v[28:31], v34, s[20:23], s36 offen sc1\n\t" \
  "v_add_u32_sdwa v16, s29, v112 dst_sel:DWORD dst_unused:UNUSED_PAD src0_sel:DWORD src1_sel:WORD_0\n\t" \
  "v_smfmac_f32_16x16x64_bf16 v[44:47], a[80:83], v[126:133], v151\n\t" \
  "s_waitcnt lgkmcnt(3)\n\t" \
  "s_and_b32 s44, s47, 18432\n\t" \
  "ds_read_b64 v[8:9], v8 offset:0\n\t" \
  "v_add_u32_sdwa v18, s29, v112 dst_sel:DWORD dst_unused:UNUSED_PAD src0_sel:DWORD src1_sel:WORD_1\n\t" \
  "v_add_u32 v28, s38, v187\n\t" \
  "v_smfmac_f32_16x16x64_bf16 v[36:39], v[72:75], v[0:7], v182 abid:1\n\t" \
  "s_lshl2_add_u32 s66, s27, s68\n\t" \
  "ds_read_b64 v[10:11], v10 offset:0\n\t" \
  "ds_read_b128 v[28:31], v28 offset:14336\n\t" \
  "v_add_u32_sdwa v20, s29, v113 dst_sel:DWORD dst_unused:UNUSED_PAD src0_sel:DWORD src1_sel:WORD_0\n\t" \
  "v_smfmac_f32_16x16x64_bf16 v[48:51], a[84:87], v[126:133], v151 abid:1\n\t" \
  "s_and_b32 s27, s64, 8\n\t" \
  "s_add_u32 s36, s36, 4608\n\t" \
  "ds_read_b64 v[12:13], v12 offset:0\n\t" \
  "v_add_u32_sdwa v22, s29, v113 dst_sel:DWORD dst_unused:UNUSED_PAD src0_sel:DWORD src1_sel:WORD_1\n\t" \
  "v_smfmac_f32_16x16x64_bf16 v[52:55], a[88:91], v[126:133], v152\n\t" \
  "s_mul_i32 s27, s27, 176\n\t" \
  "ds_write_b64 v25, v[26:27] offset:16384\n\t" \
  "v_add_u32_sdwa v0, s29, v114 dst_sel:DWORD dst_unused:UNUSED_PAD src0_sel:DWORD src1_sel:WORD_0\n\t" \
  "v_smfmac_f32_16x16x64_bf16 v[56:59], a[92:95], v[126:133], v152 abid:1\n\t" \
  "s_add_u32 s66, s66, s27\n\t" \
  "ds_read_b64 v[14:15], v14 offset:0\n\t" \
  "ds_read_b64 v[16:17], v16 offset:0\n\t" \
  "v_add_u32_sdwa v2, s29, v114 dst_sel:DWORD dst_unused:UNUSED_PAD src0_sel:DWORD src1_sel:WORD_1\n\t" \
  "v_smfmac_f32_16x16x64_bf16 v[60:63], a[96:99], v[126:133], v153\n\t" \
  "s_lshr_b64 s[64:65], s[64:65], 4\n\t" \
  "ds_read_b64 v[18:19], v18 offset:0\n\t" \
  "ds_read_b64 v[20:21], v20 offset:0\n\t" \
  "s_waitcnt lgkmcnt(6)\n\t" \
  "buffer_store_dwordx4 v[28:31], v34, s[20:23], s36 offen sc1\n\t" \
  "v_add_u32_sdwa v4, s29, v115 dst_sel:DWORD dst_unused:UNUSED_PAD src0_sel:DWORD src1_sel:WORD_0\n\t" \
  "v_smfmac_f32_16x16x64_bf16 v[64:67], a[100:103], v[126:133], v153 abid:1\n\t" \
  "ds_read_b64 v[22:23], v22 offset:0\n\t" \
  "ds_read_b64 v[0:1], v0 offset:0\n\t" \
  "v_add_u32_sdwa v6, s29, v115 dst_sel:DWORD dst_unused:UNUSED_PAD src0_sel:DWORD src1_sel:WORD_1\n\t" \
  "v_xad_u32 v28, v187, 64, s38\n\t" \
  "v_smfmac_f32_16x16x64_bf16 v[68:71], a[104:107], v[126:133], v179\n\t" \
  "s_waitcnt lgkmcnt(11)\n\t" \
  "s_add_u32 s38, s38, s44\n\t" \
  "ds_read_b128 v[126:129], v183 offset:8192\n\t" \
  "ds_read_b64 v[2:3], v2 offset:0\n\t" \
  "ds_read_b128 v[28:31], v28 offset:15360\n\t" \
  "v_smfmac_f32_16x16x64_bf16 v[36:39], a[108:111], v[134:141], v154\n\t" \
  "s_waitcnt lgkmcnt(7)\n\t" \
  "s_add_u32 s36, s36, 4608\n\t" \
  "ds_read_b128 v[130:133], v184 offset:8192\n\t" \
  "v_smfmac_f32_16x16x64_bf16 v[40:43], v[76:79], v[8:15], v182 abid:1\n\t" \
  "s_sub_u32 s44, s38, 129024\n\t" \
  "ds_read_b64 v[4:5], v4 offset:0\n\t" \
  "v_smfmac_f32_16x16x64_bf16 v[40:43], a[112:115], v[134:141], v154 abid:1\n\t" \
  "s_waitcnt lgkmcnt(5)\n\t" \
  "s_min_u32 s38, s38, s44\n\t" \
  "ds_read_b64 v[6:7], v6 offset:0\n\t" \
  "v_smfmac_f32_16x16x64_bf16 v[44:47], v[80:83], v[16:23], v182 abid:1\n\t" \
  "s_waitcnt lgkmcnt(3)\n\t" \
  "buffer_store_dwordx4 v[28:31], v34, s[20:23], s36 offen sc1\n\t" \
  "v_smfmac_f32_16x16x64_bf16 v[44:47], a[116:119], v[134:141], v155\n\t" \
  "v_add_u32_sdwa v8, s29, v32 dst_sel:DWORD dst_unused:UNUSED_PAD src0_sel:DWORD src1_sel:WORD_0\n\t" \
  "v_smfmac_f32_16x16x64_bf16 v[48:51], a[120:123], v[134:141], v155 abid:1\n\t" \
  "s_branch .Ljn33%=\n\t" \
  ".fill (.Lvar%=+46460-.)/4, 4, 0xbf800000\n\t" \
  "s_nop 0\n\t" \
  "s_branch .Lfin%=\n\t" \
  "s_branch .Lhe%=\n\t" \
  "s_waitcnt vmcnt(6)\n\t" \
  "s_waitcnt vmcnt(10)\n\t" \
  "s_waitcnt vmcnt(12)\n\t" \
  "s_waitcnt vmcnt(17)\n\t" \
  "s_waitcnt vmcnt(21) lgkmcnt(0)\n\t" \
  "s_barrier\n\t" \
  "v_smfmac_f32_16x16x64_bf16 v[36:39], v[220:223], v[0:7], v174\n\t" \
  "ds_read_b128 v[126:129], v183 offset:0\n\t" \
  "v_add_u32 v24, s37, v185\n\t" \
  "v_lshrrev_b32 v28, 4, v187\n\t" \
  "v_smfmac_f32_16x16x64_bf16 v[40:43], v[224:227], v[0:7], v174 abid:1\n\t" \
  "ds_read_b128 v[130:133], v184 offset:0\n\t" \
  "v_xor_b32 v25, s41, v183\n\t" \
  "v_and_or_b32 v28, v28, 48, s45\n\t" \
  "v_smfmac_f32_16x16x64_bf16 v[44:47], v[228:231], v[0:7], v175\n\t" \
  "ds_read_b128 v[134:137], v183 offset:2048\n\t" \
  "v_subrev_u32 v25, s30, v25\n\t" \
  "v_smfmac_f32_16x16x64_bf16 v[48:51], v[232:235], v[0:7], v175 abid:1\n\t" \
  "s_add_u32 s30, s29, 18432\n\t" \
  "ds_read_b128 v[138:141], v184 offset:2048\n\t" \
  "v_cvt_pk_bf16_f32 v8, v36, v37\n\t" \
  "v_cvt_pk_bf16_f32 v9, v38, v39\n\t" \
  "v_smfmac_f32_16x16x64_bf16 v[52:55], v[236:239], v[0:7], v176\n\t" \
  "s_sub_u32 s25, s30, 129024\n\t" \
  "ds_read_b128 v[36:39], v28 offset:0\n\t" \
  "v_cvt_pk_bf16_f32 v10, v40, v41\n\t" \
  "v_cvt_pk_bf16_f32 v11, v42, v43\n\t" \
  "v_smfmac_f32_16x16x64_bf16 v[56:59], v[240:243], v[0:7], v176 abid:1\n\t" \
  "s_min_u32 s30, s30, s25\n\t" \
  "ds_read_b128 v[40:43], v28 offset:64\n\t" \
  "v_cvt_pk_bf16_f32 v16, v44, v45\n\t" \
  "v_cvt_pk_bf16_f32 v17, v46, v47\n\t" \
  "v_smfmac_f32_16x16x64_bf16 v[60:63], v[244:247], v[0:7], v177\n\t" \
  "s_sub_u32 s30, s30, s29\n\t" \
  "ds_read_b128 v[44:47], v28 offset:128\n\t" \
  "v_cvt_pk_bf16_f32 v18, v48, v49\n\t" \
  "v_cvt_pk_bf16_f32 v19, v50, v51\n\t" \
  "v_smfmac_f32_16x16x64_bf16 v[64:67], v[248:251], v[0:7], v177 abid:1\n\t" \
  "s_mov_b32 s37, s29\n\t" \
  "ds_read_b128 v[48:51], v28 offset:192\n\t" \
  "v_cvt_pk_bf16_f32 v12, v52, v53\n\t" \
  "v_cvt_pk_bf16_f32 v13, v54, v55\n\t" \
  "v_smfmac_f32_16x16x64_bf16 v[68:71], v[252:255], v[0:7], v182\n\t" \
  "s_waitcnt lgkmcnt(3)\n\t" \
  "ds_read_b128 v[52:55], v28 offset:256\n\t" \
  "v_cvt_pk_bf16_f32 v14, v56, v57\n\t" \
  "v_cvt_pk_bf16_f32 v15, v58, v59\n\t" \
  "v_smfmac_f32_16x16x64_bf16 v[36:39], a[0:3], v[126:133], v142\n\t" \
  "s_waitcnt lgkmcnt(3)\n\t" \
  "ds_read_b128 v[56:59], v28 offset:320\n\t" \
  "v_cvt_pk_bf16_f32 v20, v60, v61\n\t" \
  "v_cvt_pk_bf16_f32 v21, v62, v63\n\t" \
  "v_smfmac_f32_16x16x64_bf16 v[40:43], a[4:7], v[126:133], v142 abid:1\n\t" \
  "s_waitcnt lgkmcnt(3)\n\t" \
  "ds_read_b128 v[60:63], v28 offset:384\n\t" \
  "v_cvt_pk_bf16_f32 v22, v64, v65\n\t" \
  "v_cvt_pk_bf16_f32 v23, v66, v67\n\t" \
  "v_smfmac_f32_16x16x64_bf16 v[44:47], a[8:11], v[126:133], v143\n\t" \
  "s_waitcnt lgkmcnt(3)\n\t" \
  "ds_read_b128 v[64:67], v28 offset:448\n\t" \
  "v_cvt_pk_bf16_f32 v26, v68, v69\n\t" \
  "v_cvt_pk_bf16_f32 v27, v70, v71\n\t" \
  "v_smfmac_f32_16x16x64_bf16 v[48:51], a[12:15], v[126:133], v143 abid:1\n\t" \
  "s_waitcnt lgkmcnt(3)\n\t" \
  "ds_read_b128 v[68:71], v28 offset:512\n\t" \
  "v_add_u32_sdwa v0, s29, v108 dst_sel:DWORD dst_unused:UNUSED_PAD src0_sel:DWORD src1_sel:WORD_0\n\t" \
  "v_smfmac_f32_16x16x64_bf16 v[52:55], a[16:19], v[126:133], v144\n\t" \
  "s_add_u32 m0, s33, 0\n\t" \
  "s_waitcnt lgkmcnt(3)\n\t" \
  "ds_read_b64 v[0:1], v0 offset:0\n\t" \
  "v_add_u32_sdwa v2, s29, v108 dst_sel:DWORD dst_unused:UNUSED_PAD src0_sel:DWORD src1_sel:WORD_1\n\t" \
  "v_smfmac_f32_16x16x64_bf16 v[56:59], a[20:23], v[126:133], v144 abid:1\n\t" \
  "s_waitcnt lgkmcnt(3)\n\t" \
  "buffer_load_dwordx4 v186, s[16:19], s34 offen offset:0 nt lds\n\t" \
  "v_add_u32_sdwa v4, s29, v109 dst_sel:DWORD dst_unused:UNUSED_PAD src0_sel:DWORD src1_sel:WORD_0\n\t" \
  "v_smfmac_f32_16x16x64_bf16 v[60:63], a[24:27], v[126:133], v145\n\t" \
  "s_waitcnt lgkmcnt(2)\n\t" \
  "ds_read_b64 v[2:3], v2 offset:0\n\t" \
  "v_add_u32_sdwa v6, s29, v109 dst_sel:DWORD dst_unused:UNUSED_PAD src0_sel:DWORD src1_sel:WORD_1\n\t" \
  "v_smfmac_f32_16x16x64_bf16 v[64:67], a[28:31], v[126:133], v145 abid:1\n\t" \
  "s_add_u32 m0, s33, 1920\n\t" \
  "s_waitcnt lgkmcnt(2)\n\t" \
  "ds_write_b128 v24, v[8:11] offset:0\n\t" \
  "v_smfmac_f32_16x16x64_bf16 v[68:71], a[32:35], v[126:133], v178\n\t" \
  "buffer_load_dwordx4 v186, s[16:19], s34 offen offset:128 nt lds\n\t" \
  "v_smfmac_f32_16x16x64_bf16 v[36:39], a[36:39], v[134:141], v146\n\t" \
  "s_add_u32 m0, s33, 3840\n\t" \
  "ds_read_b128 v[126:129], v183 offset:4096\n\t" \
  "v_smfmac_f32_16x16x64_bf16 v[40:43], a[40:43], v[134:141], v146 abid:1\n\t" \
  "buffer_load_dwordx4 v186, s[16:19], s34 offen offset:256 nt lds\n\t" \
  "v_smfmac_f32_16x16x64_bf16 v[44:47], a[44:47], v[134:141], v147\n\t" \
  "s_add_u32 m0, s33, 5760\n\t" \
  "ds_write_b128 v24, v[12:15] offset:128\n\t" \
  "v_xor_b32 v24, 64, v24\n\t" \
  "v_smfmac_f32_16x16x64_bf16 v[48:51], a[48:51], v[134:141], v147 abid:1\n\t" \
  "buffer_load_dwordx4 v186, s[16:19], s34 offen offset:384 nt lds\n\t" \
  "v_add_u32_sdwa v8, s29, v110 dst_sel:DWORD dst_unused:UNUSED_PAD src0_sel:DWORD src1_sel:WORD_0\n\t" \
  "v_smfmac_f32_16x16x64_bf16 v[52:55], a[52:55], v[134:141], v148\n\t" \
  "s_add_u32 s31, s31, 18432\n\t" \
  "ds_read_b128 v[130:133], v184 offset:4096\n\t" \
  "v_add_u32_sdwa v10, s29, v110 dst_sel:DWORD dst_unused:UNUSED_PAD src0_sel:DWORD src1_sel:WORD_1\n\t" \
  "v_smfmac_f32_16x16x64_bf16 v[56:59], a[56:59], v[134:141], v148 abid:1\n\t" \
  "s_sub_u32 s26, s31, 129024\n\t" \
  "ds_read_b64 v[4:5], v4 offset:0\n\t" \
  "v_add_u32_sdwa v12, s29, v111 dst_sel:DWORD dst_unused:UNUSED_PAD src0_sel:DWORD src1_sel:WORD_0\n\t" \
  "v_smfmac_f32_16x16x64_bf16 v[60:63], a[60:63], v[134:141], v149\n\t" \
  "s_min_u32 s31, s31, s26\n\t" \
  "ds_write_b128 v24, v[16:19] offset:0\n\t" \
  "v_add_u32_sdwa v14, s29, v111 dst_sel:DWORD dst_unused:UNUSED_PAD src0_sel:DWORD src1_sel:WORD_1\n\t" \
  "v_smfmac_f32_16x16x64_bf16 v[64:67], a[64:67], v[134:141], v149 abid:1\n\t" \
  "s_add_u32 s33, s31, s32\n\t" \
  "ds_read_b64 v[6:7], v6 offset:0\n\t" \
  "v_smfmac_f32_16x16x64_bf16 v[68:71], a[68:71], v[134:141], v178 abid:1\n\t" \
  "s_waitcnt lgkmcnt(3)\n\t" \
  "ds_read_b128 v[134:137], v183 offset:6144\n\t" \
  "v_smfmac_f32_16x16x64_bf16 v[36:39], a[72:75], v[126:133], v150\n\t" \
  "s_add_u32 s34, s34, 4718592\n\t" \
  "ds_read_b128 v[138:141], v184 offset:6144\n\t" \
  "v_smfmac_f32_16x16x64_bf16 v[40:43], a[76:79], v[126:133], v150 abid:1\n\t" \
  "s_and_b32 s27, s64, 7\n\t" \
  "ds_write_b128 v24, v[20:23] offset:128\n\t" \
  "v_add_u32_sdwa v16, s29, v112 dst_sel:DWORD dst_unused:UNUSED_PAD src0_sel:DWORD src1_sel:WORD_0\n\t" \
  "v_smfmac_f32_16x16x64_bf16 v[44:47], a[80:83], v[126:133], v151\n\t" \
  "s_waitcnt lgkmcnt(3)\n\t" \
  "ds_read_b64 v[8:9], v8 offset:0\n\t" \
  "v_add_u32_sdwa v18, s29, v112 dst_sel:DWORD dst_unused:UNUSED_PAD src0_sel:DWORD src1_sel:WORD_1\n\t" \
  "v_smfmac_f32_16x16x64_bf16 v[36:39], v[72:75], v[0:7], v182 abid:1\n\t" \
  "s_lshl2_add_u32 s66, s27, s68\n\t" \
  "ds_read_b64 v[10:11], v10 offset:0\n\t" \
  "v_add_u32_sdwa v20, s29, v113 dst_sel:DWORD dst_unused:UNUSED_PAD src0_sel:DWORD src1_sel:WORD_0\n\t" \
  "v_smfmac_f32_16x16x64_bf16 v[48:51], a[84:87], v[126:133], v151 abid:1\n\t" \
  "s_and_b32 s27, s64, 8\n\t" \
  "ds_read_b64 v[12:13], v12 offset:0\n\t" \
  "v_add_u32_sdwa v22, s29, v113 dst_sel:DWORD dst_unused:UNUSED_PAD src0_sel:DWORD src1_sel:WORD_1\n\t" \
  "v_smfmac_f32_16x16x64_bf16 v[52:55], a[88:91], v[126:133], v152\n\t" \
  "s_mul_i32 s27, s27, 176\n\t" \
  "ds_write_b64 v25, v[26:27] offset:16384\n\t" \
  "v_add_u32_sdwa v0, s29, v114 dst_sel:DWORD dst_unused:UNUSED_PAD src0_sel:DWORD src1_sel:WORD_0\n\t" \
  "v_smfmac_f32_16x16x64_bf16 v[56:59], a[92:95], v[126:133], v152 abid:1\n\t" \
  "s_add_u32 s66, s66, s27\n\t" \
  "ds_read_b64 v[14:15], v14 offset:0\n\t" \
  "ds_read_b64 v[16:17], v16 offset:0\n\t" \
  "v_add_u32_sdwa v2, s29, v114 dst_sel:DWORD dst_unused:UNUSED_PAD src0_sel:DWORD src1_sel:WORD_1\n\t" \
  "v_smfmac_f32_16x16x64_bf16 v[60:63], a[96:99], v[126:133], v153\n\t" \
  "s_lshr_b64 s[64:65], s[64:65], 4\n\t" \
  "ds_read_b64 v[18:19], v18 offset:0\n\t" \
  "ds_read_b64 v[20:21], v20 offset:0\n\t" \
  "v_add_u32_sdwa v4, s29, v115 dst_sel:DWORD dst_unused:UNUSED_PAD src0_sel:DWORD src1_sel:WORD_0\n\t" \
  "v_smfmac_f32_16x16x64_bf16 v[64:67], a[100:103], v[126:133], v153 abid:1\n\t" \
  "ds_read_b64 v[22:23], v22 offset:0\n\t" \
  "ds_read_b64 v[0:1], v0 offset:0\n\t" \
  "v_add_u32_sdwa v6, s29, v115 dst_sel:DWORD dst_unused:UNUSED_PAD src0_sel:DWORD src1_sel:WORD_1\n\t" \
  "v_smfmac_f32_16x16x64_bf16 v[68:71], a[104:107], v[126:133], v179\n\t" \
  "s_waitcnt lgkmcnt(11)\n\t" \
  "ds_read_b128 v[126:129], v183 offset:8192\n\t" \
  "ds_read_b64 v[2:3], v2 offset:0\n\t" \
  "v_smfmac_f32_16x16x64_bf16 v[36:39], a[108:111], v[134:141], v154\n\t" \
  "s_waitcnt lgkmcnt(7)\n\t" \
  "ds_read_b128 v[130:133], v184 offset:8192\n\t" \
  "v_smfmac_f32_16x16x64_bf16 v[40:43], v[76:79], v[8:15], v182 abid:1\n\t" \
  "ds_read_b64 v[4:5], v4 offset:0\n\t" \
  "v_smfmac_f32_16x16x64_bf16 v[40:43], a[112:115], v[134:141], v154 abid:1\n\t" \
  "s_waitcnt lgkmcnt(5)\n\t" \
  "ds_read_b64 v[6:7], v6 offset:0\n\t" \
  "v_smfmac_f32_16x16x64_bf16 v[44:47], v[80:83], v[16:23], v182 abid:1\n\t" \
  "v_smfmac_f32_16x16x64_bf16 v[44:47], a[116:119], v[134:141], v155\n\t" \
  "v_add_u32_sdwa v8, s29, v32 dst_sel:DWORD dst_unused:UNUSED_PAD src0_sel:DWORD src1_sel:WORD_0\n\t" \
  "v_smfmac_f32_16x16x64_bf16 v[48:51], a[120:123], v[134:141], v155 abid:1\n\t" \
  ".Ljn33%=:\n\t" \
  "ds_read_b64 v[8:9], v8 offset:0\n\t" \
  "v_add_u32_sdwa v10, s29, v32 dst_sel:DWORD dst_unused:UNUSED_PAD src0_sel:DWORD src1_sel:WORD_1\n\t" \
  "v_smfmac_f32_16x16x64_bf16 v[52:55], a[124:127], v[134:141], v156\n\t" \
  "s_waitcnt lgkmcnt(1)\n\t" \
  "ds_read_b64 v[10:11], v10 offset:0\n\t" \
  "v_add_u32 v28, s60, v187\n\t" \
  "v_smfmac_f32_16x16x64_bf16 v[48:51], v[84:87], v[0:7], v182 abid:1\n\t" \
  "ds_read_b128 v[28:31], v28 offset:0\n\t" \
  "v_add_u32_sdwa v0, s29, v116 dst_sel:DWORD dst_unused:UNUSED_PAD src0_sel:DWORD src1_sel:WORD_0\n\t" \
  "v_smfmac_f32_16x16x64_bf16 v[56:59], a[128:131], v[134:141], v156 abid:1\n\t" \
  "ds_read_b64 v[0:1], v0 offset:0\n\t" \
  "v_add_u32_sdwa v2, s29, v116 dst_sel:DWORD dst_unused:UNUSED_PAD src0_sel:DWORD src1_sel:WORD_1\n\t" \
  "v_smfmac_f32_16x16x64_bf16 v[60:63], a[132:135], v[134:141], v157\n\t" \
  "ds_read_b64 v[2:3], v2 offset:0\n\t" \
  "v_add_u32_sdwa v4, s29, v117 dst_sel:DWORD dst_unused:UNUSED_PAD src0_sel:DWORD src1_sel:WORD_0\n\t" \
  "v_smfmac_f32_16x16x64_bf16 v[64:67], a[136:139], v[134:141], v157 abid:1\n\t" \
  "ds_read_b64 v[4:5], v4 offset:0\n\t" \
  "v_add_u32_sdwa v6, s29, v117 dst_sel:DWORD dst_unused:UNUSED_PAD src0_sel:DWORD src1_sel:WORD_1\n\t" \
  "v_smfmac_f32_16x16x64_bf16 v[68:71], a[140:143], v[134:141], v179 abid:1\n\t" \
  "s_waitcnt lgkmcnt(3)\n\t" \
  "ds_read_b128 v[134:137], v183 offset:10240\n\t" \
  "ds_read_b64 v[6:7], v6 offset:0\n\t" \
  "v_add_u32_sdwa v16, s29, v118 dst_sel:DWORD dst_unused:UNUSED_PAD src0_sel:DWORD src1_sel:WORD_0\n\t" \
  "s_set_gpr_idx_on s6, gpr_idx(DST)\n\t" \
  "v_smfmac_f32_16x16x64_bf16 v[36:39], v[28:31], v[8:15], v182 abid:1\n\t" \
  "s_set_gpr_idx_off\n\t" \
  "ds_read_b128 v[138:141], v184 offset:10240\n\t" \
  "ds_read_b64 v[16:17], v16 offset:0\n\t" \
  "v_add_u32 v24, s38, v187\n\t" \
  "v_smfmac_f32_16x16x64_bf16 v[36:39], a[144:147], v[126:133], v158\n\t" \
  "ds_read_b128 v[24:27], v24 offset:12288\n\t" \
  "v_add_u32_sdwa v18, s29, v118 dst_sel:DWORD dst_unused:UNUSED_PAD src0_sel:DWORD src1_sel:WORD_1\n\t" \
  "v_smfmac_f32_16x16x64_bf16 v[40:43], a[148:151], v[126:133], v158 abid:1\n\t" \
  "ds_read_b64 v[18:19], v18 offset:0\n\t" \
  "v_add_u32_sdwa v20, s29, v119 dst_sel:DWORD dst_unused:UNUSED_PAD src0_sel:DWORD src1_sel:WORD_0\n\t" \
  "v_smfmac_f32_16x16x64_bf16 v[44:47], a[152:155], v[126:133], v159\n\t" \
  "s_waitcnt lgkmcnt(4)\n\t" \
  "ds_read_b64 v[20:21], v20 offset:0\n\t" \
  "v_add_u32_sdwa v22, s29, v119 dst_sel:DWORD dst_unused:UNUSED_PAD src0_sel:DWORD src1_sel:WORD_1\n\t" \
  "v_smfmac_f32_16x16x64_bf16 v[52:55], v[88:91], v[0:7], v182 abid:1\n\t" \
  "s_add_u32 s36, s35, s39\n\t" \
  "ds_read_b64 v[22:23], v22 offset:0\n\t" \
  "v_add_u32_sdwa v0, s29, v33 dst_sel:DWORD dst_unused:UNUSED_PAD src0_sel:DWORD src1_sel:WORD_0\n\t" \
  "v_smfmac_f32_16x16x64_bf16 v[48:51], a[156:159], v[126:133], v159 abid:1\n\t" \
  "s_waitcnt lgkmcnt(3)\n\t" \
  "buffer_store_dwordx4 v[24:27], v34, s[20:23], s36 offen sc1\n\t" \
  "v_add_u32_sdwa v2, s29, v33 dst_sel:DWORD dst_unused:UNUSED_PAD src0_sel:DWORD src1_sel:WORD_1\n\t" \
  "v_smfmac_f32_16x16x64_bf16 v[52:55], a[160:163], v[126:133], v160\n\t" \
  "ds_read_b64 v[0:1], v0 offset:0\n\t" \
  "v_xad_u32 v24, v187, 64, s38\n\t" \
  "v_smfmac_f32_16x16x64_bf16 v[56:59], a[164:167], v[126:133], v160 abid:1\n\t" \
  "ds_read_b128 v[24:27], v24 offset:13312\n\t" \
  "v_add_u32 v28, s60, v187\n\t" \
  "v_smfmac_f32_16x16x64_bf16 v[60:63], a[168:171], v[126:133], v161\n\t" \
  "s_waitcnt lgkmcnt(2)\n\t" \
  "ds_read_b64 v[2:3], v2 offset:0\n\t" \
  "v_add_u32_sdwa v8, s29, v120 dst_sel:DWORD dst_unused:UNUSED_PAD src0_sel:DWORD src1_sel:WORD_0\n\t" \
  "v_smfmac_f32_16x16x64_bf16 v[56:59], v[92:95], v[16:23], v182 abid:1\n\t" \
  "ds_read_b128 v[28:31], v28 offset:1280\n\t" \
  "v_add_u32_sdwa v10, s29, v120 dst_sel:DWORD dst_unused:UNUSED_PAD src0_sel:DWORD src1_sel:WORD_1\n\t" \
  "v_smfmac_f32_16x16x64_bf16 v[64:67], a[172:175], v[126:133], v161 abid:1\n\t" \
  "s_add_u32 s36, s36, 4608\n\t" \
  "ds_read_b64 v[8:9], v8 offset:0\n\t" \
  "v_add_u32_sdwa v12, s29, v121 dst_sel:DWORD dst_unused:UNUSED_PAD src0_sel:DWORD src1_sel:WORD_0\n\t" \
  "v_smfmac_f32_16x16x64_bf16 v[68:71], a[176:179], v[126:133], v180\n\t" \
  "s_waitcnt lgkmcnt(3)\n\t" \
  "buffer_store_dwordx4 v[24:27], v34, s[20:23], s36 offen sc1\n\t" \
  "v_add_u32_sdwa v14, s29, v121 dst_sel:DWORD dst_unused:UNUSED_PAD src0_sel:DWORD src1_sel:WORD_1\n\t" \
  "v_smfmac_f32_16x16x64_bf16 v[36:39], a[180:183], v[134:141], v162\n\t" \
  "ds_read_b128 v[126:129], v183 offset:12288\n\t" \
  "v_add_u32 v24, s38, v187\n\t" \
  "v_smfmac_f32_16x16x64_bf16 v[40:43], a[184:187], v[134:141], v162 abid:1\n\t" \
  "s_waitcnt lgkmcnt(2)\n\t" \
  "ds_read_b128 v[24:27], v24 offset:14336\n\t" \
  "v_add_u32_sdwa v16, s29, v122 dst_sel:DWORD dst_unused:UNUSED_PAD src0_sel:DWORD src1_sel:WORD_0\n\t" \
  "s_set_gpr_idx_on s7, gpr_idx(DST)\n\t" \
  "v_smfmac_f32_16x16x64_bf16 v[36:39], v[28:31], v[0:7], v182 abid:1\n\t" \
  "s_set_gpr_idx_off\n\t" \
  "ds_read_b128 v[130:133], v184 offset:12288\n\t" \
  "v_add_u32_sdwa v18, s29, v122 dst_sel:DWORD dst_unused:UNUSED_PAD src0_sel:DWORD src1_sel:WORD_1\n\t" \
  "v_smfmac_f32_16x16x64_bf16 v[44:47], a[188:191], v[134:141], v163\n\t" \
  "ds_read_b64 v[10:11], v10 offset:0\n\t" \
  "v_add_u32_sdwa v20, s29, v123 dst_sel:DWORD dst_unused:UNUSED_PAD src0_sel:DWORD src1_sel:WORD_0\n\t" \
  "v_smfmac_f32_16x16x64_bf16 v[48:51], a[192:195], v[134:141], v163 abid:1\n\t" \
  "s_add_u32 s36, s36, 4608\n\t" \
  "ds_read_b64 v[12:13], v12 offset:0\n\t" \
  "v_add_u32_sdwa v22, s29, v123 dst_sel:DWORD dst_unused:UNUSED_PAD src0_sel:DWORD src1_sel:WORD_1\n\t" \
  "v_smfmac_f32_16x16x64_bf16 v[52:55], a[196:199], v[134:141], v164\n\t" \
  "s_waitcnt lgkmcnt(3)\n\t" \
  "buffer_store_dwordx4 v[24:27], v34, s[20:23], s36 offen sc1\n\t" \
  "v_smfmac_f32_16x16x64_bf16 v[56:59], a[200:203], v[134:141], v164 abid:1\n\t" \
  "ds_read_b64 v[14:15], v14 offset:0\n\t" \
  "v_xad_u32 v24, v187, 64, s38\n\t" \
  "v_smfmac_f32_16x16x64_bf16 v[60:63], a[204:207], v[134:141], v165\n\t" \
  "s_add_u32 s38, s38, 18432\n\t" \
  "ds_read_b128 v[24:27], v24 offset:15360\n\t" \
  "v_smfmac_f32_16x16x64_bf16 v[64:67], a[208:211], v[134:141], v165 abid:1\n\t" \
  "s_sub_u32 s44, s38, 129024\n\t" \
  "ds_read_b64 v[16:17], v16 offset:0\n\t" \
  "v_smfmac_f32_16x16x64_bf16 v[68:71], a[212:215], v[134:141], v180 abid:1\n\t" \
  "s_waitcnt lgkmcnt(5)\n\t" \
  "ds_read_b128 v[134:137], v183 offset:14336\n\t" \
  "v_smfmac_f32_16x16x64_bf16 v[36:39], a[216:219], v[126:133], v166\n\t" \
  "s_add_u32 s36, s36, 4608\n\t" \
  "s_waitcnt lgkmcnt(3)\n\t" \
  "ds_read_b128 v[138:141], v184 offset:14336\n\t" \
  "v_smfmac_f32_16x16x64_bf16 v[60:63], v[96:99], v[8:15], v182 abid:1\n\t" \
  "s_waitcnt lgkmcnt(3)\n\t" \
  "buffer_store_dwordx4 v[24:27], v34, s[20:23], s36 offen sc1\n\t" \
  "v_add_u32_sdwa v8, s29, v35 dst_sel:DWORD dst_unused:UNUSED_PAD src0_sel:DWORD src1_sel:WORD_0\n\t" \
  "v_smfmac_f32_16x16x64_bf16 v[40:43], a[220:223], v[126:133], v166 abid:1\n\t" \
  "s_min_u32 s38, s38, s44\n\t" \
  "ds_read_b64 v[18:19], v18 offset:0\n\t" \
  "v_add_u32 v24, s43, v187\n\t" \
  "v_smfmac_f32_16x16x64_bf16 v[44:47], a[224:227], v[126:133], v167\n\t" \
  "s_add_u32 s35, s35, 4718592\n\t" \
  "ds_read_b128 v[24:27], v24 offset:17408\n\t" \
  "v_add_u32_sdwa v10, s29, v35 dst_sel:DWORD dst_unused:UNUSED_PAD src0_sel:DWORD src1_sel:WORD_1\n\t" \
  "v_smfmac_f32_16x16x64_bf16 v[48:51], a[228:231], v[126:133], v167 abid:1\n\t" \
  "ds_read_b64 v[20:21], v20 offset:0\n\t" \
  "v_add_u32 v28, s60, v187\n\t" \
  "v_smfmac_f32_16x16x64_bf16 v[52:55], a[232:235], v[126:133], v168\n\t" \
  "ds_read_b64 v[22:23], v22 offset:0\n\t" \
  "v_smfmac_f32_16x16x64_bf16 v[56:59], a[236:239], v[126:133], v168 abid:1\n\t" \
  "ds_read_b64 v[8:9], v8 offset:0\n\t" \
  "v_smfmac_f32_16x16x64_bf16 v[60:63], a[240:243], v[126:133], v169\n\t" \
  "s_waitcnt lgkmcnt(3)\n\t" \
  "buffer_store_dwordx4 v[24:27], v186, s[20:23], s40 offen offset:384 sc1\n\t" \
  "v_smfmac_f32_16x16x64_bf16 v[64:67], a[244:247], v[126:133], v169 abid:1\n\t" \
  "s_add_u32 s43, s43, 18432\n\t" \
  "ds_read_b64 v[10:11], v10 offset:0\n\t" \
  "v_smfmac_f32_16x16x64_bf16 v[68:71], a[248:251], v[126:133], v181\n\t" \
  "s_waitcnt lgkmcnt(2)\n\t" \
  "ds_read_b128 v[0:3], v183 offset:16384\n\t" \
  "v_smfmac_f32_16x16x64_bf16 v[64:67], v[100:103], v[16:23], v182 abid:1\n\t" \
  "s_sub_u32 s42, s43, 129024\n\t" \
  "ds_read_b128 v[4:7], v184 offset:16384\n\t" \
  "v_add_u32_sdwa v16, s29, v124 dst_sel:DWORD dst_unused:UNUSED_PAD src0_sel:DWORD src1_sel:WORD_0\n\t" \
  "v_smfmac_f32_16x16x64_bf16 v[36:39], a[252:255], v[134:141], v170\n\t" \
  "s_min_u32 s43, s43, s42\n\t" \
  "ds_read_b128 v[28:31], v28 offset:2560\n\t" \
  "v_add_u32_sdwa v18, s29, v124 dst_sel:DWORD dst_unused:UNUSED_PAD src0_sel:DWORD src1_sel:WORD_1\n\t" \
  "v_smfmac_f32_16x16x64_bf16 v[40:43], v[188:191], v[134:141], v170 abid:1\n\t" \
  "s_add_u32 s40, s40, 4718592\n\t" \
  "ds_read_b64 v[16:17], v16 offset:0\n\t" \
  "v_add_u32_sdwa v20, s29, v125 dst_sel:DWORD dst_unused:UNUSED_PAD src0_sel:DWORD src1_sel:WORD_0\n\t" \
  "v_smfmac_f32_16x16x64_bf16 v[44:47], v[192:195], v[134:141], v171\n\t" \
  "ds_read_b64 v[18:19], v18 offset:0\n\t" \
  "v_add_u32_sdwa v22, s29, v125 dst_sel:DWORD dst_unused:UNUSED_PAD src0_sel:DWORD src1_sel:WORD_1\n\t" \
  "v_smfmac_f32_16x16x64_bf16 v[48:51], v[196:199], v[134:141], v171 abid:1\n\t" \
  "s_add_u32 s29, s29, s30\n\t" \
  "ds_read_b64 v[20:21], v20 offset:0\n\t" \
  "v_add_u32 v183, s30, v183\n\t" \
  "v_smfmac_f32_16x16x64_bf16 v[52:55], v[200:203], v[134:141], v172\n\t" \
  "s_waitcnt lgkmcnt(3)\n\t" \
  "ds_read_b64 v[22:23], v22 offset:0\n\t" \
  "v_add_u32 v184, s30, v184\n\t" \
  "s_set_gpr_idx_on s8, gpr_idx(DST)\n\t" \
  "v_smfmac_f32_16x16x64_bf16 v[36:39], v[28:31], v[8:15], v182 abid:1\n\t" \
  "s_set_gpr_idx_off\n\t" \
  "v_smfmac_f32_16x16x64_bf16 v[56:59], v[204:207], v[134:141], v172 abid:1\n\t" \
  "v_smfmac_f32_16x16x64_bf16 v[60:63], v[208:211], v[134:141], v173\n\t" \
  "v_smfmac_f32_16x16x64_bf16 v[64:67], v[212:215], v[134:141], v173 abid:1\n\t" \
  "s_waitcnt lgkmcnt(0)\n\t" \
  "v_smfmac_f32_16x16x64_bf16 v[68:71], v[104:107], v[16:23], v182 abid:1\n\t" \
  "v_smfmac_f32_16x16x64_bf16 v[68:71], v[216:219], v[134:141], v181 abid:1\n\t" \
  "s_setpc_b64 s[66:67]\n\t" \
  ".Lstop%=:\n\t" \
  ""
#define SPMM_CLOBBERS "memory", "scc", "vcc", \
  "v1", "v2", "v3", "v4", "v5", "v6", "v7", "v8", "v9", "v10", "v11", "v12", "v13", "v14", "v15", "v16", \
  "v17", "v18", "v19", "v20", "v21", "v22", "v23", "v24", "v25", "v26", "v27", "v28", "v29", "v30", "v31", "v32", \
  "v33", "v34", "v35", "v36", "v37", "v38", "v39", "v40", "v41", "v42", "v43", "v44", "v45", "v46", "v47", "v48", \
  "v49", "v50", "v51", "v52", "v53", "v54", "v55", "v56", "v57", "v58", "v59", "v60", "v61", "v62", "v63", "v64", \
  "v65", "v66", "v67", "v68", "v69", "v70", "v71", "v72", "v73", "v74", "v75", "v76", "v77", "v78", "v79", "v80", \
  "v81", "v82", "v83", "v84", "v85", "v86", "v87", "v88", "v89", "v90", "v91", "v92", "v93", "v94", "v95", "v96", \
  "v97", "v98", "v99", "v100", "v101", "v102", "v103", "v104", "v105", "v106", "v107", "v108", "v109", "v110", "v111", "v112", \
  "v113", "v114", "v115", "v116", "v117", "v118", "v119", "v120", "v121", "v122", "v123", "v124", "v125", "v126", "v127", "v128", \
  "v129", "v130", "v131", "v132", "v133", "v134", "v135", "v136", "v137", "v138", "v139", "v140", "v141", "v142", "v143", "v144", \
  "v145", "v146", "v147", "v148", "v149", "v150", "v151", "v152", "v153", "v154", "v155", "v156", "v157", "v158", "v159", "v160", \
  "v161", "v162", "v163", "v164", "v165", "v166", "v167", "v168", "v169", "v170", "v171", "v172", "v173", "v174", "v175", "v176", \
  "v177", "v178", "v179", "v180", "v181", "v182", "v183", "v184", "v185", "v186", "v187", "v188", "v189", "v190", "v191", "v192", \
  "v193", "v194", "v195", "v196", "v197", "v198", "v199", "v200", "v201", "v202", "v203", "v204", "v205", "v206", "v207", "v208", \
  "v209", "v210", "v211", "v212", "v213", "v214", "v215", "v216", "v217", "v218", "v219", "v220", "v221", "v222", "v223", "v224", \
  "v225", "v226", "v227", "v228", "v229", "v230", "v231", "v232", "v233", "v234", "v235", "v236", "v237", "v238", "v239", "v240", \
  "v241", "v242", "v243", "v244", "v245", "v246", "v247", "v248", "v249", "v250", "v251", "v252", "v253", "v254", "v255", "a0", \
  "a1", "a2", "a3", "a4", "a5", "a6", "a7", "a8", "a9", "a10", "a11", "a12", "a13", "a14", "a15", "a16", \
  "a17", "a18", "a19", "a20", "a21", "a22", "a23", "a24", "a25", "a26", "a27", "a28", "a29", "a30", "a31", "a32", \
  "a33", "a34", "a35", "a36", "a37", "a38", "a39", "a40", "a41", "a42", "a43", "a44", "a45", "a46", "a47", "a48", \
  "a49", "a50", "a51", "a52", "a53", "a54", "a55", "a56", "a57", "a58", "a59", "a60", "a61", "a62", "a63", "a64", \
  "a65", "a66", "a67", "a68", "a69", "a70", "a71", "a72", "a73", "a74", "a75", "a76", "a77", "a78", "a79", "a80", \
  "a81", "a82", "a83", "a84", "a85", "a86", "a87", "a88", "a89", "a90", "a91", "a92", "a93", "a94", "a95", "a96", \
  "a97", "a98", "a99", "a100", "a101", "a102", "a103", "a104", "a105", "a106", "a107", "a108", "a109", "a110", "a111", "a112", \
  "a113", "a114", "a115", "a116", "a117", "a118", "a119", "a120", "a121", "a122", "a123", "a124", "a125", "a126", "a127", "a128", \
  "a129", "a130", "a131", "a132", "a133", "a134", "a135", "a136", "a137", "a138", "a139", "a140", "a141", "a142", "a143", "a144", \
  "a145", "a146", "a147", "a148", "a149", "a150", "a151", "a152", "a153", "a154", "a155", "a156", "a157", "a158", "a159", "a160", \
  "a161", "a162", "a163", "a164", "a165", "a166", "a167", "a168", "a169", "a170", "a171", "a172", "a173", "a174", "a175", "a176", \
  "a177", "a178", "a179", "a180", "a181", "a182", "a183", "a184", "a185", "a186", "a187", "a188", "a189", "a190", "a191", "a192", \
  "a193", "a194", "a195", "a196", "a197", "a198", "a199", "a200", "a201", "a202", "a203", "a204", "a205", "a206", "a207", "a208", \
  "a209", "a210", "a211", "a212", "a213", "a214", "a215", "a216", "a217", "a218", "a219", "a220", "a221", "a222", "a223", "a224", \
  "a225", "a226", "a227", "a228", "a229", "a230", "a231", "a232", "a233", "a234", "a235", "a236", "a237", "a238", "a239", "a240", \
  "a241", "a242", "a243", "a244", "a245", "a246", "a247", "a248", "a249", "a250", "a251", "a252", "a253", "a254", "a255", "s4", \
  "s5", "s6", "s7", "s8", "s9", "s10", "s11", "s12", "s13", "s14", "s15", "s16", "s17", "s18", "s19", "s20", \
  "s21", "s22", "s23", "s24", "s25", "s26", "s27", "s28", "s29", "s30", "s31", "s32", "s33", "s34", "s35", "s36", \
  "s37", "s38", "s39", "s40", "s41", "s42", "s43", "s44", "s45", "s46", "s47", "s48", "s49", "s50", "s51", "s52", \
  "s53", "s54", "s55", "s56", "s57", "s58", "s59", "s60", "s61", "s62", "s63", "s64", "s65", "s66", "s67", "s68", \
  "s69", "s70", "s71", "s72", "s73", "s74", "s75", "s76", "s77", "s78", "s79", "s80", "s81", "s82", "s83", "s84", \
  "s85", "s86", "s87", "s88", "s89", "s90", "s91", "s92", "s93", "s94", "s95", "s96", "s97", "s98", "s99", "s100", \
  "s101"
#define SPMM_LDS 163840

__global__ __launch_bounds__(256, 1) void spmm_main(const uint16_t* x, const uint16_t* w, const uint16_t* b, uint16_t* out, char* ws) {
  __shared__ __attribute__((aligned(128))) char smem[SPMM_LDS];
  int tid = threadIdx.x;
  asm volatile(SPMM_ASM
               : "+{v0}"(tid)
               : [sKA] "s"(__builtin_amdgcn_kernarg_segment_ptr()), [sBID] "s"(blockIdx.x),
                 [sL0] "s"((unsigned)(size_t)(__attribute__((address_space(3))) char*)smem)
               : SPMM_CLOBBERS);
}

}

extern "C" void kernel_launch(void* const* d_in, const int* in_sizes, int n_in, void* d_out, int out_size,
                              void* d_ws, size_t ws_size, hipStream_t stream) {
  static_assert(SPMM_LDS == 160 * 1024, "LDS budget");
  spmm_main<<<256, 256, 0, stream>>>((const uint16_t*)d_in[0], (const uint16_t*)d_in[1], (const uint16_t*)d_in[2], (uint16_t*)d_out, (char*)d_ws);
}
```

```cpp
#include <hip/hip_runtime.h>
#include <hip/hip_bf16.h>
#include <stdint.h>

namespace {
#define SPMM_ASM \
  ".Lstart%=:\n\t" \
  "s_load_dwordx8 s[4:11], %[sKA], 0\n\t" \
  "s_load_dwordx2 s[12:13], %[sKA], 0x20\n\t" \
  "s_memrealtime s[72:73]\n\t" \
  "s_getpc_b64 s[68:69]\n\t" \
  ".Lpc%=:\n\t" \
  "v_readfirstlane_b32 s24, v0\n\t" \
  "s_lshr_b32 s59, %[sBID], 6\n\t" \
  "s_and_b32 s25, %[sBID], 31\n\t" \
  "s_lshl_b32 s59, s59, 5\n\t" \
  "s_or_b32 s59, s59, s25\n\t" \
  "s_and_b32 s25, %[sBID], 32\n\t" \
  "s_lshl2_add_u32 s59, s25, s59\n\t" \
  "s_lshr_b32 s24, s24, 6\n\t" \
  "v_mov_b32 v1, 0\n\t" \
  "s_cmp_gt_u32 s59, 81\n\t" \
  "s_cbranch_scc1 .Le1%=\n\t" \
  "s_lshr_b32 s25, s59, 1\n\t" \
  "s_mul_i32 s38, s25, 57\n\t" \
  "s_lshr_b32 s38, s38, 9\n\t" \
  "s_mul_i32 s29, s38, -9\n\t" \
  "s_add_u32 s29, s29, s25\n\t" \
  "s_and_b32 s25, s59, 1\n\t" \
  "s_and_b32 s36, s24, 1\n\t" \
  "s_lshl1_add_u32 s36, s25, s36\n\t" \
  "s_cmp_gt_u32 s59, 79\n\t" \
  "s_cselect_b32 s42, 0, 73728\n\t" \
  "s_cmp_gt_u32 s59, 71\n\t" \
  "s_cselect_b32 s37, 64, 128\n\t" \
  "s_cselect_b32 s40, 8, 64\n\t" \
  "s_cselect_b32 s42, s42, 8\n\t" \
  "s_cselect_b32 s25, 3, 2\n\t" \
  "s_lshl_b32 s25, s29, s25\n\t" \
  "s_add_u32 s25, s25, s36\n\t" \
  "s_mul_i32 s25, s25, 18432\n\t" \
  "s_lshr_b32 s26, s24, 1\n\t" \
  "s_lshl3_add_u32 s26, s38, s26\n\t" \
  "s_lshl_b32 s26, s26, 5\n\t" \
  "s_add_u32 s25, s25, s26\n\t" \
  "s_add_u32 s25, s25, 4032\n\t" \
  "v_and_b32 v2, 2, v0\n\t" \
  "v_and_b32 v3, 4, v0\n\t" \
  "v_lshl_add_u32 v2, v3, 1, v2\n\t" \
  "v_lshl_add_u32 v2, v2, 1, s25\n\t" \
  "v_and_b32 v3, 1, v0\n\t" \
  "s_movk_i32 s26, 9216\n\t" \
  "v_mad_u32_u24 v2, v3, s26, v2\n\t" \
  "v_bfe_u32 v3, v0, 5, 1\n\t" \
  "v_mad_u32_u24 v2, v3, s37, v2\n\t" \
  "v_bfe_u32 v3, v0, 4, 1\n\t" \
  "v_mad_u32_u24 v2, v3, s40, v2\n\t" \
  "v_bfe_u32 v3, v0, 3, 1\n\t" \
  "v_mad_u32_u24 v2, v3, s42, v2\n\t" \
  "s_waitcnt lgkmcnt(0)\n\t" \
  "global_load_dwordx2 v[142:143], v1, s[12:13] sc1\n\t" \
  "global_load_dword v72, v2, s[6:7] offset:-4032\n\t" \
  "global_load_dword v73, v2, s[6:7] offset:-2880\n\t" \
  "global_load_dword v74, v2, s[6:7] offset:-1728\n\t" \
  "global_load_dword v75, v2, s[6:7] offset:-576\n\t" \
  "global_load_dword v76, v2, s[6:7] offset:576\n\t" \
  "global_load_dword v77, v2, s[6:7] offset:1728\n\t" \
  "global_load_dword v78, v2, s[6:7] offset:2880\n\t" \
  "global_load_dword v79, v2, s[6:7] offset:4032\n\t" \
  "s_mul_i32 s25, s38, 81\n\t" \
  "s_mul_i32 s26, s29, 9\n\t" \
  "s_add_u32 s25, s25, s26\n\t" \
  "s_lshl1_add_u32 s26, s26, 8\n\t" \
  "s_mul_i32 s27, s38, 10\n\t" \
  "s_add_u32 s27, s27, s29\n\t" \
  "s_lshl_b32 s27, s27, 10\n\t" \
  "s_add_u32 s27, s27, 1024\n\t" \
  "s_mul_i32 s28, s29, 10240\n\t" \
  "s_cmp_gt_u32 s59, 71\n\t" \
  "s_cselect_b32 s25, s26, s25\n\t" \
  "s_cselect_b32 s27, s28, s27\n\t" \
  "s_movk_i32 s37, 2048\n\t" \
  "s_cmov_b32 s37, 82944\n\t" \
  "s_movk_i32 s42, 1024\n\t" \
  "s_cmovk_i32 s42, 9216\n\t" \
  "s_cmp_gt_u32 s59, 79\n\t" \
  "s_cmovk_i32 s42, 0\n\t" \
  "s_lshl_b32 s25, s25, 10\n\t" \
  "s_lshr_b32 s26, s24, 1\n\t" \
  "s_lshl1_add_u32 s26, s36, s26\n\t" \
  "s_lshl_b32 s26, s26, 7\n\t" \
  "v_and_b32 v2, 7, v0\n\t" \
  "v_lshl_add_u32 v2, v2, 4, s26\n\t" \
  "v_bfe_u32 v4, v0, 4, 2\n\t" \
  "v_mad_u32_u24 v146, v4, s37, v2\n\t" \
  "v_mad_u32_u24 v146, v3, s42, v146\n\t" \
  "v_lshl_add_u32 v147, v4, 2, v2\n\t" \
  "s_add_u32 s25, s25, 30720\n\t" \
  "s_add_u32 s27, s27, 362496\n\t" \
  "v_add_u32 v146, s25, v146\n\t" \
  "v_add_u32 v147, s27, v147\n\t" \
  "s_branch .Le3%=\n\t" \
  ".Le1%=:\n\t" \
  "s_cmp_ge_u32 s59, 109\n\t" \
  "s_cbranch_scc1 .Le2%=\n\t" \
  "s_sub_u32 s25, s59, 82\n\t" \
  "s_mul_i32 s55, s25, 86\n\t" \
  "s_lshr_b32 s55, s55, 8\n\t" \
  "s_mul_i32 s56, s55, -3\n\t" \
  "s_add_u32 s56, s56, s25\n\t" \
  "s_lshl2_add_u32 s25, s56, s24\n\t" \
  "s_mul_i32 s25, s25, 55296\n\t" \
  "s_lshl_b32 s26, s55, 7\n\t" \
  "s_add_u32 s25, s25, s26\n\t" \
  "v_and_b32 v2, 7, v0\n\t" \
  "v_lshl_add_u32 v2, v2, 4, s25\n\t" \
  "v_bfe_u32 v3, v0, 3, 3\n\t" \
  "s_movk_i32 s26, 1152\n\t" \
  "v_mad_u32_u24 v2, v3, s26, v2\n\t" \
  "s_mul_i32 s25, s24, 6528\n\t" \
  "s_waitcnt lgkmcnt(0)\n\t" \
  "global_load_dwordx2 v[142:143], v1, s[12:13] sc1\n\t" \
  "s_add_u32 m0, s25, 131584\n\t" \
  "s_nop 0\n\t" \
  "global_load_lds_dwordx4 v2, s[6:7]\n\t" \
  "s_add_u32 m0, s25, 132672\n\t" \
  "v_add_u32 v2, 9216, v2\n\t" \
  "global_load_lds_dwordx4 v2, s[6:7]\n\t" \
  "s_add_u32 m0, s25, 133760\n\t" \
  "v_add_u32 v2, 9216, v2\n\t" \
  "global_load_lds_dwordx4 v2, s[6:7]\n\t" \
  "s_add_u32 m0, s25, 134848\n\t" \
  "v_add_u32 v2, 9216, v2\n\t" \
  "global_load_lds_dwordx4 v2, s[6:7]\n\t" \
  "s_add_u32 m0, s25, 135936\n\t" \
  "v_add_u32 v2, 9216, v2\n\t" \
  "global_load_lds_dwordx4 v2, s[6:7]\n\t" \
  "s_add_u32 m0, s25, 137024\n\t" \
  "v_add_u32 v2, 9216, v2\n\t" \
  "global_load_lds_dwordx4 v2, s[6:7]\n\t" \
  "s_branch .Le3%=\n\t" \
  ".Le2%=:\n\t" \
  "s_bfe_u32 s25, s59, 0x40003\n\t" \
  "s_lshl2_add_u32 s25, s25, s24\n\t" \
  "v_and_b32 v9, 63, v0\n\t" \
  "v_lshl_add_u32 v9, v9, 6, s25\n\t" \
  "v_lshlrev_b32 v9, 7, v9\n\t" \
  "v_min_u32 v9, .Lstop%=-.Lpc%=-16, v9\n\t" \
  "s_mov_b64 exec, 511\n\t" \
  "global_load_dword v9, v9, s[68:69]\n\t" \
  "s_mov_b64 exec, -1\n\t" \
  ".Lnp%=:\n\t" \
  "s_waitcnt lgkmcnt(0)\n\t" \
  "global_load_dwordx2 v[142:143], v1, s[12:13] sc1\n\t" \
  ".Le3%=:\n\t" \
  "s_memrealtime s[70:71]\n\t" \
  "v_and_b32 v6, 63, v0\n\t" \
  "v_lshlrev_b32 v187, 4, v6\n\t" \
  "s_lshr_b32 s25, s24, 1\n\t" \
  "s_and_b32 s26, s24, 1\n\t" \
  "s_lshl3_add_u32 s41, s25, s26\n\t" \
  "s_lshl_b32 s41, s41, 3\n\t" \
  "s_lshl_b32 s39, s24, 8\n\t" \
  "v_and_b32 v7, 56, v0\n\t" \
  "v_and_b32 v4, 4, v0\n\t" \
  "v_lshl_add_u32 v7, v7, 2, v4\n\t" \
  "v_lshl_add_u32 v7, v7, 1, s39\n\t" \
  "s_add_u32 s27, s41, 1024\n\t" \
  "s_mov_b32 exec_lo, 0\n\t" \
  "v_mov_b32 v7, s27\n\t" \
  "s_mov_b32 exec_lo, -1\n\t" \
  "v_and_b32 v4, 48, v187\n\t" \
  "v_add_u32 v7, v7, v4\n\t" \
  "global_load_dwordx2 v[144:145], v7, s[8:9]\n\t" \
  "v_bfe_u32 v7, v0, 3, 3\n\t" \
  "v_lshl_add_u32 v7, s25, 3, v7\n\t" \
  "v_lshrrev_b32 v4, 1, v7\n\t" \
  "v_xor_b32 v4, v4, v0\n\t" \
  "v_and_b32 v4, 7, v4\n\t" \
  "s_mul_i32 s27, s26, 640\n\t" \
  "v_lshl_add_u32 v4, v4, 4, s27\n\t" \
  "s_movk_i32 s28, 1152\n\t" \
  "v_mad_u32_u24 v186, v7, s28, v4\n\t" \
  "s_lshl_b32 s32, s25, 10\n\t" \
  "s_lshl4_add_u32 s32, s27, s32\n\t" \
  "s_mov_b32 s33, s32\n\t" \
  "s_mov_b32 s31, 0\n\t" \
  "s_mov_b32 s16, s4\n\t" \
  "s_and_b32 s17, s5, 0xffff\n\t" \
  "s_mov_b32 s18, 75497472\n\t" \
  "s_mov_b32 s19, 0x20000\n\t" \
  "s_mul_i32 s34, %[sBID], 18432\n\t" \
  "s_mov_b32 s35, s34\n\t" \
  "s_and_b32 s13, s13, 0xffff\n\t" \
  "s_mov_b32 s14, 740352\n\t" \
  "s_mov_b32 s15, s19\n\t" \
  "s_waitcnt lgkmcnt(0)\n\t" \
  "s_add_u32 s53, s70, 190\n\t" \
  "s_cmp_lt_u32 s59, 82\n\t" \
  "s_cbranch_scc1 .Le5%=\n\t" \
  "s_cmp_lt_u32 s59, 109\n\t" \
  "s_cselect_b32 s61, 1, 2\n\t" \
  "s_add_u32 s74, s68, .Le5%=-.Lpc%=\n\t" \
  "s_addc_u32 s75, s69, 0\n\t" \
  ".Lxs%=:\n\t" \
  "s_add_u32 m0, s33, 0\n\t" \
  "s_nop 0\n\t" \
  "buffer_load_dwordx4 v186, s[16:19], s34 offen offset:0 nt lds\n\t" \
  "s_add_u32 m0, s33, 1920\n\t" \
  "s_nop 0\n\t" \
  "buffer_load_dwordx4 v186, s[16:19], s34 offen offset:128 nt lds\n\t" \
  "s_add_u32 m0, s33, 3840\n\t" \
  "s_nop 0\n\t" \
  "buffer_load_dwordx4 v186, s[16:19], s34 offen offset:256 nt lds\n\t" \
  "s_add_u32 m0, s33, 5760\n\t" \
  "s_nop 0\n\t" \
  "buffer_load_dwordx4 v186, s[16:19], s34 offen offset:384 nt lds\n\t" \
  "s_bitcmp1_b32 s24, 0\n\t" \
  "s_cbranch_scc1 .Lx5%=\n\t" \
  "s_add_u32 m0, s33, 7680\n\t" \
  "s_nop 0\n\t" \
  "buffer_load_dwordx4 v186, s[16:19], s34 offen offset:512 nt lds\n\t" \
  ".Lx5%=:\n\t" \
  "s_add_u32 s31, s31, 18432\n\t" \
  "s_sub_u32 s26, s31, 129024\n\t" \
  "s_min_u32 s31, s31, s26\n\t" \
  "s_add_u32 s33, s31, s32\n\t" \
  "s_add_u32 s34, s34, 4718592\n\t" \
  "s_sub_u32 s61, s61, 1\n\t" \
  "s_cmp_lg_u32 s61, 0\n\t" \
  "s_cbranch_scc1 .Lxs%=\n\t" \
  "s_setpc_b64 s[74:75]\n\t" \
  ".Le5%=:\n\t" \
  "s_memrealtime s[70:71]\n\t" \
  "s_waitcnt lgkmcnt(0)\n\t" \
  "s_add_u32 s25, s70, 170\n\t" \
  "s_max_u32 s53, s53, s25\n\t" \
  "v_mov_b64 v[204:205], 0\n\t" \
  "v_mov_b64 v[206:207], 0\n\t" \
  "s_mul_i32 s25, s24, 9216\n\t" \
  "s_add_u32 s25, s25, 55296\n\t" \
  "v_add_u32 v208, s25, v187\n\t" \
  "s_mul_i32 s26, s24, 640\n\t" \
  "s_add_u32 s26, s26, 129024\n\t" \
  "v_min_u32 v246, 624, v187\n\t" \
  "v_add_u32 v246, s26, v246\n\t" \
  "ds_write_b128 v208, v[204:207] offset:36864\n\t" \
  "ds_write_b128 v208, v[204:207] offset:37888\n\t" \
  "ds_write_b128 v208, v[204:207] offset:38912\n\t" \
  "ds_write_b128 v208, v[204:207] offset:39936\n\t" \
  "s_mov_b32 s25, 0x7fff7fff\n\t" \
  "s_sub_u32 s26, s59, 82\n\t" \
  "s_cmp_lt_u32 s26, 27\n\t" \
  "s_cbranch_scc1 .Lrc%=\n\t" \
  ".Le6%=:\n\t" \
  "ds_write_b128 v208, v[204:207] offset:7168\n\t" \
  "ds_write_b128 v208, v[204:207] offset:8192\n\t" \
  "ds_write_b128 v208, v[204:207] offset:40960\n\t" \
  "ds_write_b128 v208, v[204:207] offset:41984\n\t" \
  "ds_write_b128 v208, v[204:207] offset:43008\n\t" \
  "ds_write_b128 v208, v[204:207] offset:44032\n\t" \
  "ds_write_b128 v208, v[204:207] offset:45056\n\t" \
  "ds_write_b128 v246, v[204:207]\n\t" \
  "s_cmp_ge_u32 s59, 82\n\t" \
  "s_cbranch_scc1 .Lf3%=\n\t" \
  "v_mov_b32 v148, 0x00010001\n\t" \
  "v_mov_b32 v149, 0x00080008\n\t" \
  "v_mov_b32 v150, 0x000c000c\n\t" \
  "s_mov_b32 s26, 0x00020002\n\t" \
  "s_mov_b32 s27, 0x00040004\n\t" \
  "s_mov_b32 s70, 0x05040100\n\t" \
  "s_mov_b32 s71, 0x07060302\n\t" \
  "s_mov_b32 vcc_lo, 0x55555555\n\t" \
  "s_mov_b32 vcc_hi, 0x55555555\n\t" \
  "v_mov_b32 v151, s70\n\t" \
  "v_mov_b32 v152, s71\n\t" \
  "v_cndmask_b32 v151, v152, v151, vcc\n\t" \
  "v_and_b32 v152, 1, v0\n\t" \
  "v_lshlrev_b32 v152, 3, v152\n\t" \
  "s_waitcnt vmcnt(1)\n\t" \
  "v_and_b32 v154, s25, v72\n\t" \
  "v_and_b32 v170, s25, v76\n\t" \
  "v_and_b32 v155, s25, v73\n\t" \
  "v_and_b32 v171, s25, v77\n\t" \
  "v_and_b32 v156, s25, v74\n\t" \
  "v_and_b32 v172, s25, v78\n\t" \
  "v_and_b32 v157, s25, v75\n\t" \
  "v_and_b32 v173, s25, v79\n\t" \
  "v_pk_max_u16 v158, v154, v155\n\t" \
  "v_pk_max_u16 v174, v170, v171\n\t" \
  "v_pk_max_u16 v159, v156, v157\n\t" \
  "v_pk_max_u16 v175, v172, v173\n\t" \
  "v_pk_min_u16 v158, v158, v159\n\t" \
  "v_pk_min_u16 v174, v174, v175\n\t" \
  "v_pk_min_u16 v159, v154, v155\n\t" \
  "v_pk_min_u16 v175, v170, v171\n\t" \
  "v_pk_max_u16 v158, v158, v159\n\t" \
  "v_pk_max_u16 v174, v174, v175\n\t" \
  "v_pk_min_u16 v159, v156, v157\n\t" \
  "v_pk_min_u16 v175, v172, v173\n\t" \
  "v_pk_max_u16 v158, v158, v159\n\t" \
  "v_pk_max_u16 v174, v174, v175\n\t" \
  "v_pk_sub_i16 v154, v154, v158\n\t" \
  "v_pk_sub_i16 v170, v170, v174\n\t" \
  "v_pk_sub_i16 v155, v155, v158\n\t" \
  "v_pk_sub_i16 v171, v171, v174\n\t" \
  "v_pk_sub_i16 v156, v156, v158\n\t" \
  "v_pk_sub_i16 v172, v172, v174\n\t" \
  "v_pk_sub_i16 v157, v157, v158\n\t" \
  "v_pk_sub_i16 v173, v173, v174\n\t" \
  "v_pk_ashrrev_i16 v154, 15, v154 op_sel_hi:[0,1]\n\t" \
  "v_pk_ashrrev_i16 v170, 15, v170 op_sel_hi:[0,1]\n\t" \
  "v_pk_ashrrev_i16 v155, 15, v155 op_sel_hi:[0,1]\n\t" \
  "v_pk_ashrrev_i16 v171, 15, v171 op_sel_hi:[0,1]\n\t" \
  "v_pk_ashrrev_i16 v156, 15, v156 op_sel_hi:[0,1]\n\t" \
  "v_pk_ashrrev_i16 v172, 15, v172 op_sel_hi:[0,1]\n\t" \
  "v_pk_ashrrev_i16 v157, 15, v157 op_sel_hi:[0,1]\n\t" \
  "v_pk_ashrrev_i16 v173, 15, v173 op_sel_hi:[0,1]\n\t" \
  "v_bfi_b32 v158, v155, v74, v73\n\t" \
  "v_bfi_b32 v174, v171, v78, v77\n\t" \
  "v_bfi_b32 v159, v156, v73, v74\n\t" \
  "v_bfi_b32 v175, v172, v77, v78\n\t" \
  "v_bfi_b32 v158, v154, v158, v72\n\t" \
  "v_bfi_b32 v174, v170, v174, v76\n\t" \
  "v_bfi_b32 v159, v157, v159, v75\n\t" \
  "v_bfi_b32 v175, v173, v175, v79\n\t" \
  "v_perm_b32 v160, v159, v158, s70\n\t" \
  "v_perm_b32 v162, v175, v174, s70\n\t" \
  "v_perm_b32 v161, v159, v158, s71\n\t" \
  "v_perm_b32 v163, v175, v174, s71\n\t" \
  "v_bfi_b32 v158, v155, s26, v148\n\t" \
  "v_bfi_b32 v174, v171, s26, v148\n\t" \
  "v_cndmask_b32_dpp v166, v161, v160, vcc quad_perm:[1,0,3,2] row_mask:0xf bank_mask:0xf\n\t" \
  "v_cndmask_b32_dpp v167, v163, v162, vcc quad_perm:[1,0,3,2] row_mask:0xf bank_mask:0xf\n\t" \
  "s_not_b64 vcc, vcc\n\t" \
  "v_cndmask_b32_dpp v168, v160, v161, vcc quad_perm:[1,0,3,2] row_mask:0xf bank_mask:0xf\n\t" \
  "v_cndmask_b32_dpp v169, v162, v163, vcc quad_perm:[1,0,3,2] row_mask:0xf bank_mask:0xf\n\t" \
  "buffer_store_dwordx4 v[166:169], v146, s[12:15], 0 offen sc1\n\t" \
  "v_bfi_b32 v159, v156, s27, v149\n\t" \
  "v_bfi_b32 v175, v172, s27, v149\n\t" \
  "v_bfi_b32 v159, v157, v159, v150\n\t" \
  "v_bfi_b32 v175, v173, v175, v150\n\t" \
  "v_and_or_b32 v164, v158, v154, v159\n\t" \
  "v_and_or_b32 v165, v174, v170, v175\n\t" \
  "v_lshl_or_b32 v174, v165, 4, v164\n\t" \
  "v_lshlrev_b32 v174, v152, v174\n\t" \
  "s_nop 1\n\t" \
  "v_or_b32_dpp v174, v174, v174 quad_perm:[1,0,3,2] row_mask:0xf bank_mask:0xf\n\t" \
  "s_nop 1\n\t" \
  "v_mov_b32_dpp v175, v174 row_ror:8 row_mask:0xf bank_mask:0xf\n\t" \
  "s_mov_b32 exec_lo, 0x00ff00ff\n\t" \
  "s_mov_b32 exec_hi, 0x00ff00ff\n\t" \
  "v_perm_b32 v174, v175, v174, v151\n\t" \
  "buffer_store_dword v174, v147, s[12:15], 0 offen sc1\n\t" \
  "s_mov_b64 exec, -1\n\t" \
  ".Lf3%=:\n\t" \
  "s_mov_b32 s20, s10\n\t" \
  "s_and_b32 s21, s11, 0xffff\n\t" \
  "s_mov_b32 s22, s18\n\t" \
  "s_mov_b32 s23, s19\n\t" \
  "v_and_b32 v7, 15, v6\n\t" \
  "v_lshrrev_b32 v4, 4, v6\n\t" \
  "v_lshrrev_b32 v5, 1, v7\n\t" \
  "v_xor_b32 v5, v5, v4\n\t" \
  "v_lshlrev_b32 v5, 4, v5\n\t" \
  "v_lshl_add_u32 v183, v7, 7, v5\n\t" \
  "v_xor_b32 v184, 64, v183\n\t" \
  "v_and_b32 v5, 7, v6\n\t" \
  "v_xor_b32 v4, v4, v5\n\t" \
  "v_lshlrev_b32 v4, 4, v4\n\t" \
  "v_lshl_add_u32 v4, v7, 8, v4\n\t" \
  "s_lshl_b32 s49, s24, 12\n\t" \
  "v_add_u32 v185, s49, v4\n\t" \
  "s_mul_i32 s45, s24, 576\n\t" \
  "s_mul_i32 s36, s24, 3456\n\t" \
  "s_add_u32 s45, s45, 161536\n\t" \
  "s_add_u32 s36, s36, 4096\n\t" \
  "s_mul_i32 s46, s24, 82944\n\t" \
  "s_add_u32 s46, s46, 30720\n\t" \
  "s_mul_i32 s47, s24, 10240\n\t" \
  "s_add_u32 s47, s47, 362496\n\t" \
  "s_lshl_b32 s40, s24, 2\n\t" \
  "s_add_u32 s40, s40, 403456\n\t" \
  "s_mul_i32 s60, s24, 6400\n\t" \
  "s_add_u32 s60, s60, 135680\n\t" \
  "s_mov_b32 s62, 0\n\t" \
  "s_movk_i32 s50, 0x7fff\n\t" \
  "s_mul_i32 s25, s24, 12288\n\t" \
  "s_add_u32 s25, s25, .Lvar%=-.Lpc%=-4096\n\t" \
  "s_add_u32 s68, s68, s25\n\t" \
  "s_addc_u32 s67, s69, 0\n\t" \
  "s_bfe_u32 s25, %[sBID], 0x20003\n\t" \
  "s_cmp_eq_u32 s25, 0\n\t" \
  "s_cmov_b32 s64, 0xeeeddc9a\n\t" \
  "s_cmov_b32 s65, 0x566eee9\n\t" \
  "s_cmp_eq_u32 s25, 1\n\t" \
  "s_cmov_b32 s64, 0xeeddd9ba\n\t" \
  "s_cmov_b32 s65, 0x566eee9\n\t" \
  "s_cmp_eq_u32 s25, 2\n\t" \
  "s_cmov_b32 s64, 0xeddd9cba\n\t" \
  "s_cmov_b32 s65, 0x566eee9\n\t" \
  "s_cmp_eq_u32 s25, 3\n\t" \
  "s_cmov_b32 s64, 0xddd9ccba\n\t" \
  "s_cmov_b32 s65, 0x566eee9\n\t" \
  "s_sub_u32 s54, 255, s59\n\t" \
  "s_getreg_b32 s25, hwreg(HW_REG_XCC_ID, 0, 1)\n\t" \
  "s_mul_i32 s25, s25, -55\n\t" \
  "s_lshr_b32 s26, s59, 1\n\t" \
  "s_add_u32 s25, s25, s26\n\t" \
  "s_add_u32 s25, s25, 202\n\t" \
  "s_cmp_lt_u32 s59, 109\n\t" \
  "s_cmov_b32 s54, s25\n\t" \
  "s_getreg_b32 s25, hwreg(HW_REG_XCC_ID, 0, 1)\n\t" \
  "s_mul_i32 s25, s25, 74\n\t" \
  "s_lshr_b32 s26, s54, 1\n\t" \
  "s_add_u32 s25, s25, s26\n\t" \
  "s_cmp_ge_u32 s59, 109\n\t" \
  "s_cmov_b32 s54, s25\n\t" \
  "s_cmp_lt_u32 s54, 72\n\t" \
  "s_cselect_b32 s55, 0, 1\n\t" \
  "s_cmov_b32 s64, 0x9cccccba\n\t" \
  "s_cmov_b32 s65, 0x44cccc\n\t" \
  "s_lshl2_add_u32 s25, %[sBID], s24\n\t" \
  "s_cmp_eq_u32 s25, 1018\n\t" \
  "s_cselect_b32 s63, 1, 0\n\t" \
  "s_mul_i32 s25, s24, 9216\n\t" \
  "s_add_u32 s25, s25, 92160\n\t" \
  "s_mul_i32 s26, s24, 640\n\t" \
  "s_add_u32 s26, s26, 129024\n\t" \
  "s_lshr_b32 s30, %[sBID], 3\n\t" \
  "s_lshl2_add_u32 s30, s30, s24\n\t" \
  "s_mul_i32 s30, s30, 2944\n\t" \
  "s_add_u32 s30, s30, 30720\n\t" \
  "v_lshlrev_b32 v213, 3, v187\n\t" \
  "v_min_u32 v203, 992, v187\n\t" \
  "v_lshrrev_b32 v246, 8, v187\n\t" \
  "v_lshlrev_b32 v247, 4, v246\n\t" \
  "v_xor_b32 v247, v247, v183\n\t" \
  "v_lshl_or_b32 v247, v247, 16, v247\n\t" \
  "v_lshl_add_u32 v246, v246, 2, s26\n\t" \
  "s_lshl2_add_u32 s27, s39, 131584\n\t" \
  "v_add_u32 v210, s27, v187\n\t" \
  "s_lshl2_add_u32 s27, s24, 131584\n\t" \
  "v_add_u32 v211, s27, v187\n\t" \
  "v_mul_u32_u24 v209, 37, v203\n\t" \
  "v_lshrrev_b32 v209, 12, v209\n\t" \
  "v_lshl_add_u32 v202, v209, 6, s26\n\t" \
  "v_lshl_add_u32 v203, v209, 4, v203\n\t" \
  "v_bfe_u32 v200, v203, 3, 4\n\t" \
  "v_lshl_add_u32 v214, v200, 2, 16\n\t" \
  "v_lshl_add_u32 v209, v209, 10, s25\n\t" \
  "s_mov_b32 s44, s25\n\t" \
  "s_mov_b32 s37, -37888\n\t" \
  "s_mov_b32 s42, 0x10000\n\t" \
  "s_movk_i32 s38, 240\n\t" \
  "s_mov_b32 s43, 0x78787878\n\t" \
  "s_mov_b32 s61, 1\n\t" \
  "s_cmp_ge_u32 s59, 82\n\t" \
  "s_cbranch_scc1 .Lf2%=\n\t" \
  "s_mul_i32 s28, s59, 64\n\t" \
  "s_add_u32 s28, s28, 18432\n\t" \
  "v_readfirstlane_b32 s9, v142\n\t" \
  "v_readfirstlane_b32 s58, v143\n\t" \
  "s_mul_i32 s57, s9, 0x9E3779B1\n\t" \
  "s_add_u32 s57, s57, 0x632BE5AB\n\t" \
  "s_and_b32 s57, s57, 0xffffff00\n\t" \
  "s_mul_i32 s70, s9, 0x85EBCA6B\n\t" \
  "s_xor_b32 s58, s58, s70\n\t" \
  "s_add_u32 s58, s58, 0x3C6EF372\n\t" \
  "s_memrealtime s[70:71]\n\t" \
  "v_mov_b32 v134, s57\n\t" \
  "v_mov_b32 v135, s58\n\t" \
  "v_mov_b32 v136, s57\n\t" \
  "v_mov_b32 v137, s58\n\t" \
  "s_mov_b32 s61, 3\n\t" \
  "s_cmp_lg_u32 s24, 1\n\t" \
  "s_waitcnt vmcnt(0) lgkmcnt(0)\n\t" \
  "s_barrier\n\t" \
  "s_cbranch_scc1 .Lf2%=\n\t" \
  "s_mov_b64 exec, 15\n\t" \
  "buffer_store_dwordx4 v[134:137], v187, s[12:15], s28 offen sc1\n\t" \
  "s_cmp_lg_u32 s59, 0\n\t" \
  "s_cbranch_scc1 .Lp3%=\n\t" \
  "s_mov_b64 exec, 255\n\t" \
  "s_add_u32 s28, s28, 5248\n\t" \
  "v_mov_b32 v134, s70\n\t" \
  "v_mov_b32 v136, s70\n\t" \
  "buffer_store_dwordx4 v[134:137], v187, s[12:15], s28 offen sc1\n\t" \
  ".Lp3%=:\n\t" \
  "s_mov_b64 exec, -1\n\t" \
  "s_memrealtime s[70:71]\n\t" \
  "s_waitcnt lgkmcnt(0)\n\t" \
  "s_add_u32 s53, s70, 40\n\t" \
  ".Lf2%=:\n\t" \
  "s_call_b64 s[74:75], .Lxs%=\n\t" \
  "ds_write_b128 v208, v[204:207] offset:0\n\t" \
  "ds_write_b128 v208, v[204:207] offset:1024\n\t" \
  "ds_write_b128 v208, v[204:207] offset:2048\n\t" \
  "ds_write_b128 v208, v[204:207] offset:3072\n\t" \
  "ds_write_b128 v208, v[204:207] offset:4096\n\t" \
  "ds_write_b128 v208, v[204:207] offset:5120\n\t" \
  "ds_write_b128 v208, v[204:207] offset:6144\n\t" \
  "s_waitcnt vmcnt(12)\n\t" \
  "v_readfirstlane_b32 s9, v142\n\t" \
  "v_readfirstlane_b32 s58, v143\n\t" \
  "s_mul_i32 s57, s9, 0x9E3779B1\n\t" \
  "s_add_u32 s57, s57, 0x632BE5AB\n\t" \
  "s_and_b32 s57, s57, 0xffffff00\n\t" \
  "s_mul_i32 s70, s9, 0x85EBCA6B\n\t" \
  "s_xor_b32 s58, s58, s70\n\t" \
  "s_add_u32 s58, s58, 0x3C6EF372\n\t" \
  "v_add_u32 v8, s45, v187\n\t" \
  "v_lshlrev_b32 v4, 16, v144\n\t" \
  "v_and_b32 v5, 0xffff0000, v144\n\t" \
  "v_lshlrev_b32 v6, 16, v145\n\t" \
  "v_and_b32 v7, 0xffff0000, v145\n\t" \
  "s_mov_b32 exec_hi, 15\n\t" \
  "ds_write_b128 v8, v[4:7]\n\t" \
  "s_mov_b32 exec_hi, -1\n\t" \
  "v_lshrrev_b32 v126, 4, v187\n\t" \
  "v_and_or_b32 v126, v126, 48, s45\n\t" \
  "ds_read_b128 v[0:3], v126 offset:0\n\t" \
  "ds_read_b128 v[4:7], v126 offset:64\n\t" \
  "ds_read_b128 v[8:11], v126 offset:128\n\t" \
  "ds_read_b128 v[12:15], v126 offset:192\n\t" \
  "ds_read_b128 v[16:19], v126 offset:256\n\t" \
  "ds_read_b128 v[20:23], v126 offset:320\n\t" \
  "ds_read_b128 v[24:27], v126 offset:384\n\t" \
  "ds_read_b128 v[28:31], v126 offset:448\n\t" \
  "ds_read_b128 v[32:35], v126 offset:512\n\t" \
  "ds_read_b128 v[72:75], v126 offset:0\n\t" \
  "ds_read_b128 v[76:79], v126 offset:64\n\t" \
  "ds_read_b128 v[80:83], v126 offset:128\n\t" \
  "ds_read_b128 v[84:87], v126 offset:192\n\t" \
  "ds_read_b128 v[88:91], v126 offset:256\n\t" \
  "ds_read_b128 v[92:95], v126 offset:320\n\t" \
  "ds_read_b128 v[96:99], v126 offset:384\n\t" \
  "ds_read_b128 v[100:103], v126 offset:448\n\t" \
  "ds_read_b128 v[104:107], v126 offset:512\n\t" \
  "ds_read_b128 v[36:39], v126 offset:0\n\t" \
  "ds_read_b128 v[40:43], v126 offset:64\n\t" \
  "ds_read_b128 v[44:47], v126 offset:128\n\t" \
  "ds_read_b128 v[48:51], v126 offset:192\n\t" \
  "ds_read_b128 v[52:55], v126 offset:256\n\t" \
  "ds_read_b128 v[56:59], v126 offset:320\n\t" \
  "ds_read_b128 v[60:63], v126 offset:384\n\t" \
  "ds_read_b128 v[64:67], v126 offset:448\n\t" \
  "ds_read_b128 v[68:71], v126 offset:512\n\t" \
  "s_mov_b64 exec, 0\n\t" \
  "s_cmp_eq_u32 s24, 0\n\t" \
  "s_cbranch_scc1 .Lhop0%=\n\t" \
  "s_cmp_eq_u32 s24, 1\n\t" \
  "s_cbranch_scc1 .Lhop1%=\n\t" \
  "s_cmp_eq_u32 s24, 2\n\t" \
  "s_cbranch_scc1 .Lhop2%=\n\t" \
  "s_branch .Lhop3%=\n\t" \
  ".Lhopy%=:\n\t" \
  ".Lhopx%=:\n\t" \
  "s_mov_b64 exec, -1\n\t" \
  "v_min_u32 v138, 656, v187\n\t" \
  "v_mul_u32_u24 v138, 8, v138\n\t" \
  "s_movk_i32 s26, 18488\n\t" \
  "s_mul_i32 s54, s54, 92\n\t" \
  "s_lshr_b32 s54, s54, 4\n\t" \
  "s_mul_i32 s25, s54, 0\n\t" \
  "s_lshr_b32 s25, s25, 8\n\t" \
  "s_bitcmp0_b32 s55, 0\n\t" \
  "s_cmov_b32 s25, 0\n\t" \
  "s_sub_u32 s27, 2064, s25\n\t" \
  "s_add_u32 s51, s27, 432\n\t" \
  "s_add_u32 s54, s54, 480\n\t" \
  "s_lshr_b32 s52, s54, 4\n\t" \
  "s_cmp_lt_u32 s59, 109\n\t" \
  "s_cselect_b32 s29, 50, 0\n\t" \
  "s_cselect_b32 s101, 14, 6\n\t" \
  "s_cmp_lt_u32 s59, 82\n\t" \
  "s_cmov_b32 s29, 50\n\t" \
  "s_bitcmp0_b32 s55, 0\n\t" \
  "s_cmov_b32 s101, 8\n\t" \
  ".Lpw%=:\n\t" \
  "s_memrealtime s[70:71]\n\t" \
  "s_waitcnt lgkmcnt(0)\n\t" \
  "s_sub_i32 s25, s70, s53\n\t" \
  "s_cmp_lt_i32 s25, 0\n\t" \
  "s_cbranch_scc1 .Lpw%=\n\t" \
  "s_mov_b32 s53, s27\n\t" \
  "s_movk_i32 s27, 230\n\t" \
  "s_cmp_lt_u32 s59, 109\n\t" \
  "s_cmovk_i32 s27, 130\n\t" \
  "s_cmp_lg_u32 s24, 1\n\t" \
  "s_cbranch_scc1 .Lp2%=\n\t" \
  ".Lpp%=:\n\t" \
  "buffer_load_dwordx4 v[134:137], v138, s[12:15], s26 offen sc1\n\t" \
  "s_waitcnt vmcnt(0)\n\t" \
  "v_xor_b32 v135, s58, v135\n\t" \
  "v_bitop3_b32 v135, v135, s58, v137 bitop3:0xf6\n\t" \
  "s_mov_b32 exec_hi, 0x1ff\n\t" \
  "v_bitop3_b32 v135, v135, s57, v136 bitop3:0xf6\n\t" \
  "v_bitop3_b32 v135, v135, s57, v134 bitop3:0xf6\n\t" \
  "s_mov_b32 exec_hi, -1\n\t" \
  "v_cmp_ne_u32 vcc, 0, v135\n\t" \
  "v_readlane_b32 s56, v134, 63\n\t" \
  "s_cbranch_vccz .Lp2%=\n\t" \
  "s_mov_b32 s29, 0\n\t" \
  "s_movk_i32 s27, 0x4000\n\t" \
  "s_sleep 2\n\t" \
  "s_sub_u32 s50, s50, 1\n\t" \
  "s_cbranch_scc0 .Lpp%=\n\t" \
  ".Lp2%=:\n\t" \
  "s_movk_i32 s50, 0x7fff\n\t" \
  "s_sub_u32 s25, s59, 82\n\t" \
  "s_cmp_lg_u32 s24, 3\n\t" \
  "s_cselect_b32 s25, 99, s25\n\t" \
  "s_cmp_lt_u32 s25, 27\n\t" \
  "s_waitcnt vmcnt(0) lgkmcnt(0)\n\t" \
  "s_barrier\n\t" \
  "s_cbranch_scc1 .Lp5%=\n\t" \
  ".Lp6%=:\n\t" \
  "s_memrealtime s[72:73]\n\t" \
  "s_add_u32 s56, s56, s27\n\t" \
  "ds_read_b128 v[126:129], v183 offset:0\n\t" \
  "ds_read_b128 v[130:133], v184 offset:0\n\t" \
  "buffer_load_dwordx4 v[188:191], v203, s[12:15], s36 offen offset:0 sc1\n\t" \
  "buffer_load_dwordx4 v[192:195], v203, s[12:15], s36 offen offset:1152 sc1\n\t" \
  "buffer_load_dwordx4 v[196:199], v203, s[12:15], s36 offen offset:2304 sc1\n\t" \
  "s_mov_b64 exec, 0x7fffff\n\t" \
  "buffer_load_dword v213, v213, s[12:15], s30 offen sc1\n\t" \
  "s_mov_b64 exec, -1\n\t" \
  "buffer_load_dwordx4 v[178:181], v187, s[12:15], s47 offen sc1\n\t" \
  "buffer_load_dword v182, v187, s[12:15], s40 offen sc1\n\t" \
  "buffer_load_dwordx4 v[142:145], v187, s[12:15], s47 offen offset:1024 sc1\n\t" \
  "s_add_u32 s48, s46, 0\n\t" \
  "buffer_load_dwordx4 a[0:3], v187, s[12:15], s48 offen offset:0 sc1\n\t" \
  "buffer_load_dwordx4 a[4:7], v187, s[12:15], s48 offen offset:1024 sc1\n\t" \
  "s_waitcnt vmcnt(6)\n\t" \
  ".Lrk%=:\n\t" \
  "v_xor_b32 v204, s57, v190\n\t" \
  "v_xor_b32 v205, s58, v191\n\t" \
  "v_and_b32 v248, 63, v190\n\t" \
  "v_bitop3_b32 v204, v204, s57, v194 bitop3:0xf6\n\t" \
  "v_bitop3_b32 v205, v205, s58, v195 bitop3:0xf6\n\t" \
  "v_and_b32 v249, 63, v194\n\t" \
  "v_bitop3_b32 v204, v204, s57, v198 bitop3:0xf6\n\t" \
  "v_bitop3_b32 v205, v205, s58, v199 bitop3:0xf6\n\t" \
  "v_and_b32 v250, 63, v198\n\t" \
  "v_lshrrev_b32 v204, 8, v204\n\t" \
  "v_add3_u32 v206, v248, v249, v250\n\t" \
  "v_max3_u32 v207, v248, v249, v250\n\t" \
  "v_or_b32 v204, v204, v205\n\t" \
  "v_cmp_lt_u32 s[4:5], v214, v206\n\t" \
  "v_add_u32 v207, 18, v207\n\t" \
  "v_cmp_ne_u32 vcc, 0, v204\n\t" \
  "v_max_u32 v207, v207, v206\n\t" \
  "v_add_u32 v222, v200, v248\n\t" \
  "v_cmp_lt_u32 s[10:11], 32, v207\n\t" \
  "v_mov_b32 v220, v200\n\t" \
  "v_add_u32 v221, 1, v200\n\t" \
  "v_add_u32 v224, v222, v249\n\t" \
  "v_add_u32 v223, 1, v222\n\t" \
  "v_add_u32 v225, 1, v224\n\t" \
  "v_lshrrev_b32 v226, 4, v220\n\t" \
  "v_lshrrev_b32 v227, 4, v221\n\t" \
  "v_lshrrev_b32 v228, 4, v222\n\t" \
  "v_lshrrev_b32 v229, 4, v223\n\t" \
  "v_lshrrev_b32 v230, 4, v224\n\t" \
  "v_lshrrev_b32 v231, 4, v225\n\t" \
  "s_cbranch_vccnz .Lrr%=\n\t" \
  ".Lrg%=:\n\t" \
  "s_bcnt1_i32_b64 s61, s[4:5]\n\t" \
  "v_mad_i32_i24 v226, v226, s37, v209\n\t" \
  "v_mad_i32_i24 v227, v227, s37, v209\n\t" \
  "v_mad_i32_i24 v228, v228, s37, v209\n\t" \
  "v_mad_i32_i24 v229, v229, s37, v209\n\t" \
  "v_mad_i32_i24 v230, v230, s37, v209\n\t" \
  "v_mad_i32_i24 v231, v231, s37, v209\n\t" \
  "s_cmp_gt_u32 s61, 5\n\t" \
  "s_cselect_b64 vcc, s[4:5], s[10:11]\n\t" \
  "s_cmp_lg_u64 vcc, 0\n\t" \
  "s_cbranch_scc1 .Lss%=\n\t" \
  ".Lsg%=:\n\t" \
  "buffer_load_dwordx4 a[8:11], v187, s[12:15], s48 offen offset:2048 sc1\n\t" \
  "v_sub_u32 v232, v220, v248\n\t" \
  "v_sub_u32 v233, v221, v248\n\t" \
  "v_sub_u32 v234, v220, v249\n\t" \
  "v_sub_u32 v235, v221, v249\n\t" \
  "v_sub_u32 v236, v220, v250\n\t" \
  "v_sub_u32 v237, v221, v250\n\t" \
  "buffer_load_dwordx4 a[12:15], v187, s[12:15], s48 offen offset:3072 sc1\n\t" \
  "v_bitop3_b32 v238, v220, v232, s42 bitop3:0xf2\n\t" \
  "v_bitop3_b32 v239, v221, v233, s42 bitop3:0xf2\n\t" \
  "v_bitop3_b32 v222, v222, v234, s42 bitop3:0xf2\n\t" \
  "v_bitop3_b32 v223, v223, v235, s42 bitop3:0xf2\n\t" \
  "v_bitop3_b32 v224, v224, v236, s42 bitop3:0xf2\n\t" \
  "v_bitop3_b32 v225, v225, v237, s42 bitop3:0xf2\n\t" \
  "s_add_u32 s48, s46, 4096\n\t" \
  "buffer_load_dwordx4 a[16:19], v187, s[12:15], s48 offen offset:0 sc1\n\t" \
  "v_bfe_u32 v232, v188, 7, 4\n\t" \
  "v_bfe_u32 v233, v189, 7, 4\n\t" \
  "v_bfe_u32 v234, v192, 7, 4\n\t" \
  "v_bfe_u32 v235, v193, 7, 4\n\t" \
  "v_bfe_u32 v236, v196, 7, 4\n\t" \
  "v_bfe_u32 v237, v197, 7, 4\n\t" \
  "buffer_load_dwordx4 a[20:23], v187, s[12:15], s48 offen offset:1024 sc1\n\t" \
  "v_lshl_add_u32 v226, v232, 4, v226\n\t" \
  "v_lshl_add_u32 v227, v233, 4, v227\n\t" \
  "v_lshl_add_u32 v228, v234, 4, v228\n\t" \
  "v_lshl_add_u32 v229, v235, 4, v229\n\t" \
  "v_lshl_add_u32 v230, v236, 4, v230\n\t" \
  "v_lshl_add_u32 v231, v237, 4, v231\n\t" \
  "buffer_load_dwordx4 a[24:27], v187, s[12:15], s48 offen offset:2048 sc1\n\t" \
  "v_lshrrev_b32 v232, 2, v238\n\t" \
  "v_lshrrev_b32 v233, 2, v239\n\t" \
  "v_lshrrev_b32 v234, 2, v222\n\t" \
  "v_lshrrev_b32 v235, 2, v223\n\t" \
  "v_lshrrev_b32 v236, 2, v224\n\t" \
  "v_lshrrev_b32 v237, 2, v225\n\t" \
  "buffer_load_dwordx4 a[28:31], v187, s[12:15], s48 offen offset:3072 sc1\n\t" \
  "v_and_or_b32 v226, v188, 2, v226\n\t" \
  "v_and_or_b32 v227, v189, 2, v227\n\t" \
  "v_and_or_b32 v228, v192, 2, v228\n\t" \
  "v_and_or_b32 v229, v193, 2, v229\n\t" \
  "v_and_or_b32 v230, v196, 2, v230\n\t" \
  "v_and_or_b32 v231, v197, 2, v231\n\t" \
  "v_mad_u32_u24 v226, v232, s38, v226\n\t" \
  "v_mad_u32_u24 v227, v233, s38, v227\n\t" \
  "v_mad_u32_u24 v228, v234, s38, v228\n\t" \
  "v_mad_u32_u24 v229, v235, s38, v229\n\t" \
  "v_mad_u32_u24 v230, v236, s38, v230\n\t" \
  "v_mad_u32_u24 v231, v237, s38, v231\n\t" \
  "s_add_u32 s48, s46, 8192\n\t" \
  "buffer_load_dwordx4 a[32:35], v187, s[12:15], s48 offen offset:0 sc1\n\t" \
  "v_lshl_add_u32 v232, v238, 1, v202\n\t" \
  "v_lshl_add_u32 v233, v239, 1, v202\n\t" \
  "v_lshl_add_u32 v234, v222, 1, v202\n\t" \
  "v_lshl_add_u32 v235, v223, 1, v202\n\t" \
  "v_lshl_add_u32 v236, v224, 1, v202\n\t" \
  "v_lshl_add_u32 v237, v225, 1, v202\n\t" \
  "buffer_load_dwordx4 v[146:149], v187, s[12:15], s47 offen offset:2048 sc1\n\t" \
  "ds_write_b16 v232, v188\n\t" \
  "v_lshl_add_u32 v226, v238, 2, v226\n\t" \
  "ds_write_b16 v233, v189\n\t" \
  "v_lshl_add_u32 v227, v239, 2, v227\n\t" \
  "ds_write_b16 v234, v192\n\t" \
  "v_lshl_add_u32 v228, v222, 2, v228\n\t" \
  "buffer_load_dwordx4 a[36:39], v187, s[12:15], s48 offen offset:1024 sc1\n\t" \
  "ds_write_b16 v235, v193\n\t" \
  "v_lshl_add_u32 v229, v223, 2, v229\n\t" \
  "ds_write_b16 v236, v196\n\t" \
  "v_lshl_add_u32 v230, v224, 2, v230\n\t" \
  "ds_write_b16 v237, v197\n\t" \
  "v_lshl_add_u32 v231, v225, 2, v231\n\t" \
  "buffer_load_dwordx4 a[40:43], v187, s[12:15], s48 offen offset:2048 sc1\n\t" \
  "ds_write_b16_d16_hi v226, v188\n\t" \
  "ds_write_b16_d16_hi v227, v189\n\t" \
  "ds_write_b16_d16_hi v228, v192\n\t" \
  "ds_write_b16_d16_hi v229, v193\n\t" \
  "ds_write_b16_d16_hi v230, v196\n\t" \
  "ds_write_b16_d16_hi v231, v197\n\t" \
  "buffer_load_dwordx4 a[44:47], v187, s[12:15], s48 offen offset:3072 sc1\n\t" \
  "ds_read2_b32 v[108:109], v246 offset0:0 offset1:4\n\t" \
  "ds_read2_b32 v[110:111], v246 offset0:16 offset1:20\n\t" \
  "ds_read2_b32 v[112:113], v246 offset0:32 offset1:36\n\t" \
  "ds_read2_b32 v[114:115], v246 offset0:48 offset1:52\n\t" \
  "ds_read2_b32 v[116:117], v246 offset0:64 offset1:68\n\t" \
  "ds_read2_b32 v[118:119], v246 offset0:80 offset1:84\n\t" \
  "s_add_u32 s48, s46, 12288\n\t" \
  "buffer_load_dwordx4 a[48:51], v187, s[12:15], s48 offen offset:0 sc1\n\t" \
  "ds_read2_b32 v[120:121], v246 offset0:96 offset1:100\n\t" \
  "ds_read2_b32 v[122:123], v246 offset0:112 offset1:116\n\t" \
  "ds_read2_b32 v[124:125], v246 offset0:128 offset1:132\n\t" \
  "v_add_u32 v236, s44, v187\n\t" \
  "ds_read_b128 v[236:239], v236 offset:0\n\t" \
  "s_waitcnt vmcnt(16)\n\t" \
  "ds_write_b128 v210, v[178:181]\n\t" \
  "s_waitcnt vmcnt(15)\n\t" \
  "v_and_b32 v182, 0xffff, v182\n\t" \
  "v_or_b32 v182, 0x99990000, v182\n\t" \
  "s_waitcnt lgkmcnt(0)\n\t" \
  "v_bitop3_b32 v108, v108, s43, v247 bitop3:0x6a\n\t" \
  "v_bitop3_b32 v109, v109, s43, v247 bitop3:0x6a\n\t" \
  "s_barrier\n\t" \
  "ds_read2st64_b32 v[178:179], v211 offset0:0 offset1:4\n\t" \
  "ds_read2st64_b32 v[180:181], v211 offset0:8 offset1:12\n\t" \
  "s_waitcnt vmcnt(13)\n\t" \
  "v_smfmac_f32_16x16x64_bf16 v[0:3], a[0:3], v[126:133], v142\n\t" \
  "s_waitcnt vmcnt(12)\n\t" \
  "buffer_load_dwordx4 a[52:55], v187, s[12:15], s48 offen offset:1024 sc1\n\t" \
  "ds_read_b128 v[134:137], v183 offset:18432\n\t" \
  "v_add_u32_sdwa v248, 0, v108 dst_sel:DWORD dst_unused:UNUSED_PAD src0_sel:DWORD src1_sel:WORD_0\n\t" \
  "v_smfmac_f32_16x16x64_bf16 v[4:7], a[4:7], v[126:133], v142 abid:1\n\t" \
  "s_waitcnt vmcnt(12)\n\t" \
  "buffer_load_dwordx4 a[56:59], v187, s[12:15], s48 offen offset:2048 sc1\n\t" \
  "ds_read_b128 v[138:141], v184 offset:18432\n\t" \
  "v_add_u32_sdwa v249, 0, v108 dst_sel:DWORD dst_unused:UNUSED_PAD src0_sel:DWORD src1_sel:WORD_1\n\t" \
  "v_smfmac_f32_16x16x64_bf16 v[8:11], a[8:11], v[126:133], v143\n\t" \
  "s_bfe_u32 s30, %[sBID], 0x50003\n\t" \
  "s_lshl2_add_u32 s30, s30, s24\n\t" \
  "s_lshr_b32 s30, s30, 1\n\t" \
  "s_lshl_b32 s30, s30, 21\n\t" \
  "s_min_u32 s30, s30, 75497468\n\t" \
  "s_bitcmp1_b32 s24, 0\n\t" \
  "s_cselect_b64 s[74:75], s[16:17], s[20:21]\n\t" \
  "s_load_dword s30, s[74:75], s30\n\t" \
  "s_waitcnt vmcnt(12)\n\t" \
  "buffer_load_dwordx4 a[60:63], v187, s[12:15], s48 offen offset:3072 sc1\n\t" \
  "ds_read_b64 v[220:221], v248 offset:0\n\t" \
  "v_add_u32_sdwa v250, 0, v109 dst_sel:DWORD dst_unused:UNUSED_PAD src0_sel:DWORD src1_sel:WORD_0\n\t" \
  "v_smfmac_f32_16x16x64_bf16 v[12:15], a[12:15], v[126:133], v143 abid:1\n\t" \
  "s_add_u32 s48, s46, 16384\n\t" \
  "s_waitcnt vmcnt(12)\n\t" \
  "buffer_load_dwordx4 a[64:67], v187, s[12:15], s48 offen offset:0 sc1\n\t" \
  "ds_read_b64 v[222:223], v249 offset:0\n\t" \
  "v_add_u32_sdwa v251, 0, v109 dst_sel:DWORD dst_unused:UNUSED_PAD src0_sel:DWORD src1_sel:WORD_1\n\t" \
  "v_smfmac_f32_16x16x64_bf16 v[16:19], a[16:19], v[126:133], v144\n\t" \
  "s_waitcnt vmcnt(12)\n\t" \
  "buffer_load_dwordx4 a[68:71], v187, s[12:15], s48 offen offset:1024 sc1\n\t" \
  "ds_read_b64 v[224:225], v250 offset:0\n\t" \
  "v_bitop3_b32 v110, v110, s43, v247 bitop3:0x6a\n\t" \
  "v_smfmac_f32_16x16x64_bf16 v[20:23], a[20:23], v[126:133], v144 abid:1\n\t" \
  "s_waitcnt vmcnt(12)\n\t" \
  "ds_read_b64 v[226:227], v251 offset:0\n\t" \
  "v_bitop3_b32 v111, v111, s43, v247 bitop3:0x6a\n\t" \
  "v_smfmac_f32_16x16x64_bf16 v[24:27], a[24:27], v[126:133], v145\n\t" \
  "s_waitcnt vmcnt(11)\n\t" \
  "buffer_load_dwordx4 v[150:153], v187, s[12:15], s47 offen offset:3072 sc1\n\t" \
  "ds_read_b64 v[228:229], v248 offset:18432\n\t" \
  "v_bitop3_b32 v112, v112, s43, v247 bitop3:0x6a\n\t" \
  "v_smfmac_f32_16x16x64_bf16 v[28:31], a[28:31], v[126:133], v145 abid:1\n\t" \
  "s_waitcnt vmcnt(11) lgkmcnt(7)\n\t" \
  "ds_read_b64 v[230:231], v249 offset:18432\n\t" \
  "v_bitop3_b32 v113, v113, s43, v247 bitop3:0x6a\n\t" \
  "v_smfmac_f32_16x16x64_bf16 v[32:35], a[32:35], v[126:133], v178\n\t" \
  "s_waitcnt lgkmcnt(2)\n\t" \
  "ds_read_b64 v[232:233], v250 offset:18432\n\t" \
  "v_bitop3_b32 v114, v114, s43, v247 bitop3:0x6a\n\t" \
  "v_smfmac_f32_16x16x64_bf16 v[0:3], v[236:239], v[220:227], v182 abid:1\n\t" \
  "s_ff1_i32_b64 s25, s[4:5]\n\t" \
  "ds_read_b64 v[234:235], v251 offset:18432\n\t" \
  "v_add_u32 v240, s44, v187\n\t" \
  "v_smfmac_f32_16x16x64_bf16 v[72:75], a[0:3], v[134:141], v142\n\t" \
  "s_bitset0_b64 s[4:5], s25\n\t" \
  "buffer_load_dwordx4 a[72:75], v187, s[12:15], s48 offen offset:2048 sc1\n\t" \
  "ds_read_b128 v[126:129], v183 offset:36864\n\t" \
  "v_bitop3_b32 v115, v115, s43, v247 bitop3:0x6a\n\t" \
  "v_smfmac_f32_16x16x64_bf16 v[76:79], a[4:7], v[134:141], v142 abid:1\n\t" \
  "s_max_i32 s25, s25, 0\n\t" \
  "ds_read_b128 v[130:133], v184 offset:36864\n\t" \
  "v_bitop3_b32 v116, v116, s43, v247 bitop3:0x6a\n\t" \
  "v_smfmac_f32_16x16x64_bf16 v[80:83], a[8:11], v[134:141], v143\n\t" \
  "s_mul_i32 s26, s25, 37\n\t" \
  "s_waitcnt lgkmcnt(2)\n\t" \
  "ds_read_b128 v[240:243], v240 offset:1024\n\t" \
  "ds_read_b64 v[220:221], v248 offset:36864\n\t" \
  "v_add_u32_sdwa v248, 0, v110 dst_sel:DWORD dst_unused:UNUSED_PAD src0_sel:DWORD src1_sel:WORD_0\n\t" \
  "v_smfmac_f32_16x16x64_bf16 v[72:75], v[236:239], v[228:235], v182 abid:1\n\t" \
  "s_lshr_b32 s26, s26, 8\n\t" \
  "ds_read_b64 v[222:223], v249 offset:36864\n\t" \
  "v_add_u32_sdwa v249, 0, v110 dst_sel:DWORD dst_unused:UNUSED_PAD src0_sel:DWORD src1_sel:WORD_1\n\t" \
  "v_smfmac_f32_16x16x64_bf16 v[84:87], a[12:15], v[134:141], v143 abid:1\n\t" \
  "s_mul_i32 s70, s26, -3\n\t" \
  "buffer_load_dwordx4 a[76:79], v187, s[12:15], s48 offen offset:3072 sc1\n\t" \
  "ds_read_b64 v[224:225], v250 offset:36864\n\t" \
  "v_add_u32_sdwa v250, 0, v111 dst_sel:DWORD dst_unused:UNUSED_PAD src0_sel:DWORD src1_sel:WORD_0\n\t" \
  "v_smfmac_f32_16x16x64_bf16 v[88:91], a[16:19], v[134:141], v144\n\t" \
  "s_add_u32 s70, s70, s25\n\t" \
  "ds_read_b64 v[226:227], v251 offset:36864\n\t" \
  "v_add_u32_sdwa v251, 0, v111 dst_sel:DWORD dst_unused:UNUSED_PAD src0_sel:DWORD src1_sel:WORD_1\n\t" \
  "v_smfmac_f32_16x16x64_bf16 v[92:95], a[20:23], v[134:141], v144 abid:1\n\t" \
  "s_mul_i32 s71, s26, -5\n\t" \
  "ds_read_b64 v[228:229], v248 offset:0\n\t" \
  "v_bitop3_b32 v117, v117, s43, v247 bitop3:0x6a\n\t" \
  "v_smfmac_f32_16x16x64_bf16 v[96:99], a[24:27], v[134:141], v145\n\t" \
  "s_add_u32 s48, s46, 20480\n\t" \
  "buffer_load_dwordx4 a[80:83], v187, s[12:15], s48 offen offset:0 sc1\n\t" \
  "ds_read_b64 v[230:231], v249 offset:0\n\t" \
  "v_bitop3_b32 v118, v118, s43, v247 bitop3:0x6a\n\t" \
  "v_smfmac_f32_16x16x64_bf16 v[100:103], a[28:31], v[134:141], v145 abid:1\n\t" \
  "s_add_u32 s71, s71, s25\n\t" \
  "s_waitcnt lgkmcnt(2)\n\t" \
  "ds_read_b64 v[232:233], v250 offset:0\n\t" \
  "v_bitop3_b32 v119, v119, s43, v247 bitop3:0x6a\n\t" \
  "v_smfmac_f32_16x16x64_bf16 v[36:39], v[236:239], v[220:227], v182 abid:1\n\t" \
  "s_lshl_b32 s71, s71, 9\n\t" \
  "ds_read_b64 v[234:235], v251 offset:0\n\t" \
  "v_bitop3_b32 v120, v120, s43, v247 bitop3:0x6a\n\t" \
  "v_smfmac_f32_16x16x64_bf16 v[104:107], a[32:35], v[134:141], v178\n\t" \
  "s_add_u32 s71, s71, s44\n\t" \
  "ds_read_b64 v[220:221], v248 offset:18432\n\t" \
  "v_bitop3_b32 v121, v121, s43, v247 bitop3:0x6a\n\t" \
  "v_smfmac_f32_16x16x64_bf16 v[36:39], a[0:3], v[126:133], v142\n\t" \
  "s_sub_u32 s71, s71, 36864\n\t" \
  "buffer_load_dwordx4 a[84:87], v187, s[12:15], s48 offen offset:1024 sc1\n\t" \
  "ds_read_b128 v[134:137], v183 offset:2048\n\t" \
  "v_bitop3_b32 v122, v122, s43, v247 bitop3:0x6a\n\t" \
  "v_smfmac_f32_16x16x64_bf16 v[40:43], a[4:7], v[126:133], v142 abid:1\n\t" \
  "s_lshl_b32 s6, s26, 2\n\t" \
  "s_waitcnt lgkmcnt(2)\n\t" \
  "ds_read_b128 v[138:141], v184 offset:2048\n\t" \
  "v_bitop3_b32 v123, v123, s43, v247 bitop3:0x6a\n\t" \
  "v_smfmac_f32_16x16x64_bf16 v[4:7], v[240:243], v[228:235], v182 abid:1\n\t" \
  "s_max_u32 s27, s61, 1\n\t" \
  "ds_read_b64 v[222:223], v249 offset:18432\n\t" \
  "v_add_u32 v236, s44, v187\n\t" \
  "v_smfmac_f32_16x16x64_bf16 v[44:47], a[8:11], v[126:133], v143\n\t" \
  "s_min_u32 s27, s27, 3\n\t" \
  "ds_read_b128 v[236:239], v236 offset:2048\n\t" \
  "v_bitop3_b32 v124, v124, s43, v247 bitop3:0x6a\n\t" \
  "v_smfmac_f32_16x16x64_bf16 v[48:51], a[12:15], v[126:133], v143 abid:1\n\t" \
  "s_mul_i32 s27, s27, 4096\n\t" \
  "buffer_load_dwordx4 a[88:91], v187, s[12:15], s48 offen offset:2048 sc1\n\t" \
  "ds_read_b64 v[224:225], v250 offset:18432\n\t" \
  "v_bitop3_b32 v125, v125, s43, v247 bitop3:0x6a\n\t" \
  "v_smfmac_f32_16x16x64_bf16 v[52:55], a[16:19], v[126:133], v144\n\t" \
  "s_add_u32 s68, s68, s27\n\t" \
  "ds_read_b64 v[226:227], v251 offset:18432\n\t" \
  "v_smfmac_f32_16x16x64_bf16 v[56:59], a[20:23], v[126:133], v144 abid:1\n\t" \
  "ds_read_b64 v[228:229], v248 offset:36864\n\t" \
  "v_add_u32_sdwa v248, 0, v112 dst_sel:DWORD dst_unused:UNUSED_PAD src0_sel:DWORD src1_sel:WORD_0\n\t" \
  "v_smfmac_f32_16x16x64_bf16 v[60:63], a[24:27], v[126:133], v145\n\t" \
  "buffer_load_dwordx4 a[92:95], v187, s[12:15], s48 offen offset:3072 sc1\n\t" \
  "ds_read_b64 v[230:231], v249 offset:36864\n\t" \
  "v_add_u32_sdwa v249, 0, v112 dst_sel:DWORD dst_unused:UNUSED_PAD src0_sel:DWORD src1_sel:WORD_1\n\t" \
  "v_smfmac_f32_16x16x64_bf16 v[64:67], a[28:31], v[126:133], v145 abid:1\n\t" \
  "s_waitcnt lgkmcnt(2)\n\t" \
  "ds_read_b64 v[232:233], v250 offset:36864\n\t" \
  "v_add_u32_sdwa v250, 0, v113 dst_sel:DWORD dst_unused:UNUSED_PAD src0_sel:DWORD src1_sel:WORD_0\n\t" \
  "v_smfmac_f32_16x16x64_bf16 v[76:79], v[240:243], v[220:227], v182 abid:1\n\t" \
  "ds_read_b64 v[234:235], v251 offset:36864\n\t" \
  "v_add_u32_sdwa v251, 0, v113 dst_sel:DWORD dst_unused:UNUSED_PAD src0_sel:DWORD src1_sel:WORD_1\n\t" \
  "v_smfmac_f32_16x16x64_bf16 v[68:71], a[32:35], v[126:133], v178\n\t" \
  "s_waitcnt vmcnt(15)\n\t" \
  "ds_read_b64 v[220:221], v248 offset:0\n\t" \
  "v_lshl_add_u32 v244, s70, 4, v246\n\t" \
  "v_smfmac_f32_16x16x64_bf16 v[0:3], a[36:39], v[134:141], v146\n\t" \
  "s_add_u32 s48, s46, 24576\n\t" \
  "s_waitcnt vmcnt(14)\n\t" \
  "buffer_load_dwordx4 a[96:99], v187, s[12:15], s48 offen offset:0 sc1\n\t" \
  "ds_read_b128 v[126:129], v183 offset:20480\n\t" \
  "ds_read_b32 v244, v244 offset:32\n\t" \
  "v_smfmac_f32_16x16x64_bf16 v[4:7], a[40:43], v[134:141], v146 abid:1\n\t" \
  "s_waitcnt lgkmcnt(3)\n\t" \
  "buffer_load_dwordx4 a[100:103], v187, s[12:15], s48 offen offset:1024 sc1\n\t" \
  "ds_read_b128 v[130:133], v184 offset:20480\n\t" \
  "v_lshrrev_b32 v245, 2, v187\n\t" \
  "v_smfmac_f32_16x16x64_bf16 v[40:43], v[240:243], v[228:235], v182 abid:1\n\t" \
  "s_waitcnt vmcnt(15)\n\t" \
  "ds_read_b64 v[222:223], v249 offset:0\n\t" \
  "v_add_u32 v245, s60, v245\n\t" \
  "v_smfmac_f32_16x16x64_bf16 v[8:11], a[44:47], v[134:141], v147\n\t" \
  "s_waitcnt vmcnt(14)\n\t" \
  "ds_read_b64 v[224:225], v250 offset:0\n\t" \
  "v_smfmac_f32_16x16x64_bf16 v[12:15], a[48:51], v[134:141], v147 abid:1\n\t" \
  "s_waitcnt vmcnt(13)\n\t" \
  "buffer_load_dwordx4 a[104:107], v187, s[12:15], s48 offen offset:2048 sc1\n\t" \
  "ds_read_b64 v[226:227], v251 offset:0\n\t" \
  "v_smfmac_f32_16x16x64_bf16 v[16:19], a[52:55], v[134:141], v148\n\t" \
  "s_waitcnt vmcnt(13)\n\t" \
  "ds_read_b64 v[228:229], v248 offset:18432\n\t" \
  "s_waitcnt lgkmcnt(5)\n\t" \
  "v_bitop3_b32 v244, v244, s43, v247 bitop3:0x6a\n\t" \
  "v_smfmac_f32_16x16x64_bf16 v[20:23], a[56:59], v[134:141], v148 abid:1\n\t" \
  "s_waitcnt vmcnt(12)\n\t" \
  "ds_read_b64 v[230:231], v249 offset:18432\n\t" \
  "ds_write_b32 v245, v244 offset:1024\n\t" \
  "v_smfmac_f32_16x16x64_bf16 v[24:27], a[60:63], v[134:141], v149\n\t" \
  "s_add_u32 s47, s47, 4096\n\t" \
  "s_waitcnt lgkmcnt(3)\n\t" \
  "buffer_load_dwordx4 v[154:157], v187, s[12:15], s47 offen offset:0 sc1\n\t" \
  "ds_read_b64 v[232:233], v250 offset:18432\n\t" \
  "v_smfmac_f32_16x16x64_bf16 v[8:11], v[236:239], v[220:227], v182 abid:1\n\t" \
  "s_waitcnt vmcnt(12)\n\t" \
  "ds_read_b64 v[234:235], v251 offset:18432\n\t" \
  "v_add_u32 v240, s44, v187\n\t" \
  "v_smfmac_f32_16x16x64_bf16 v[28:31], a[64:67], v[134:141], v149 abid:1\n\t" \
  "s_waitcnt vmcnt(11)\n\t" \
  "ds_read_b128 v[240:243], v240 offset:3072\n\t" \
  "v_smfmac_f32_16x16x64_bf16 v[32:35], a[68:71], v[134:141], v178 abid:1\n\t" \
  "ds_read_b64 v[220:221], v248 offset:36864\n\t" \
  "v_add_u32_sdwa v248, 0, v114 dst_sel:DWORD dst_unused:UNUSED_PAD src0_sel:DWORD src1_sel:WORD_0\n\t" \
  "v_smfmac_f32_16x16x64_bf16 v[72:75], a[36:39], v[126:133], v146\n\t" \
  "s_waitcnt lgkmcnt(2)\n\t" \
  "buffer_load_dwordx4 a[108:111], v187, s[12:15], s48 offen offset:3072 sc1\n\t" \
  "ds_read_b128 v[134:137], v183 offset:38912\n\t" \
  "v_smfmac_f32_16x16x64_bf16 v[80:83], v[236:239], v[228:235], v182 abid:1\n\t" \
  "ds_read_b128 v[138:141], v184 offset:38912\n\t" \
  "v_smfmac_f32_16x16x64_bf16 v[76:79], a[40:43], v[126:133], v146 abid:1\n\t" \
  "ds_read_b64 v[222:223], v249 offset:36864\n\t" \
  "v_add_u32_sdwa v249, 0, v114 dst_sel:DWORD dst_unused:UNUSED_PAD src0_sel:DWORD src1_sel:WORD_1\n\t" \
  "v_smfmac_f32_16x16x64_bf16 v[80:83], a[44:47], v[126:133], v147\n\t" \
  "ds_read_b64 v[224:225], v250 offset:36864\n\t" \
  "v_add_u32_sdwa v250, 0, v115 dst_sel:DWORD dst_unused:UNUSED_PAD src0_sel:DWORD src1_sel:WORD_0\n\t" \
  "v_smfmac_f32_16x16x64_bf16 v[84:87], a[48:51], v[126:133], v147 abid:1\n\t" \
  "s_add_u32 s48, s46, 28672\n\t" \
  "buffer_load_dwordx4 a[112:115], v187, s[12:15], s48 offen offset:0 sc1\n\t" \
  "ds_read_b64 v[226:227], v251 offset:36864\n\t" \
  "v_add_u32_sdwa v251, 0, v115 dst_sel:DWORD dst_unused:UNUSED_PAD src0_sel:DWORD src1_sel:WORD_1\n\t" \
  "v_smfmac_f32_16x16x64_bf16 v[88:91], a[52:55], v[126:133], v148\n\t" \
  "ds_read_b64 v[228:229], v248 offset:0\n\t" \
  "v_smfmac_f32_16x16x64_bf16 v[92:95], a[56:59], v[126:133], v148 abid:1\n\t" \
  "ds_read_b64 v[230:231], v249 offset:0\n\t" \
  "v_smfmac_f32_16x16x64_bf16 v[96:99], a[60:63], v[126:133], v149\n\t" \
  "s_waitcnt lgkmcnt(2)\n\t" \
  "buffer_load_dwordx4 a[116:119], v187, s[12:15], s48 offen offset:1024 sc1\n\t" \
  "ds_read_b64 v[232:233], v250 offset:0\n\t" \
  "v_smfmac_f32_16x16x64_bf16 v[44:47], v[236:239], v[220:227], v182 abid:1\n\t" \
  "ds_read_b64 v[234:235], v251 offset:0\n\t" \
  "v_smfmac_f32_16x16x64_bf16 v[100:103], a[64:67], v[126:133], v149 abid:1\n\t" \
  "ds_read_b64 v[220:221], v248 offset:18432\n\t" \
  "v_smfmac_f32_16x16x64_bf16 v[104:107], a[68:71], v[126:133], v178 abid:1\n\t" \
  "ds_read_b64 v[222:223], v249 offset:18432\n\t" \
  "v_smfmac_f32_16x16x64_bf16 v[36:39], a[36:39], v[134:141], v146\n\t" \
  "s_waitcnt lgkmcnt(2)\n\t" \
  "buffer_load_dwordx4 a[120:123], v187, s[12:15], s48 offen offset:2048 sc1\n\t" \
  "ds_read_b128 v[126:129], v183 offset:4096\n\t" \
  "v_smfmac_f32_16x16x64_bf16 v[12:15], v[240:243], v[228:235], v182 abid:1\n\t" \
  "ds_read_b128 v[130:133], v184 offset:4096\n\t" \
  "v_add_u32 v236, s44, v187\n\t" \
  "v_smfmac_f32_16x16x64_bf16 v[40:43], a[40:43], v[134:141], v146 abid:1\n\t" \
  "ds_read_b128 v[236:239], v236 offset:4096\n\t" \
  "v_smfmac_f32_16x16x64_bf16 v[44:47], a[44:47], v[134:141], v147\n\t" \
  "ds_read_b64 v[224:225], v250 offset:18432\n\t" \
  "v_smfmac_f32_16x16x64_bf16 v[48:51], a[48:51], v[134:141], v147 abid:1\n\t" \
  "buffer_load_dwordx4 a[124:127], v187, s[12:15], s48 offen offset:3072 sc1\n\t" \
  "ds_read_b64 v[226:227], v251 offset:18432\n\t" \
  "v_smfmac_f32_16x16x64_bf16 v[52:55], a[52:55], v[134:141], v148\n\t" \
  "ds_read_b64 v[228:229], v248 offset:36864\n\t" \
  "v_add_u32_sdwa v248, 0, v116 dst_sel:DWORD dst_unused:UNUSED_PAD src0_sel:DWORD src1_sel:WORD_0\n\t" \
  "v_smfmac_f32_16x16x64_bf16 v[56:59], a[56:59], v[134:141], v148 abid:1\n\t" \
  "ds_read_b64 v[230:231], v249 offset:36864\n\t" \
  "v_add_u32_sdwa v249, 0, v116 dst_sel:DWORD dst_unused:UNUSED_PAD src0_sel:DWORD src1_sel:WORD_1\n\t" \
  "v_smfmac_f32_16x16x64_bf16 v[60:63], a[60:63], v[134:141], v149\n\t" \
  "s_add_u32 s48, s46, 32768\n\t" \
  "s_waitcnt lgkmcnt(2)\n\t" \
  "buffer_load_dwordx4 a[128:131], v187, s[12:15], s48 offen offset:0 sc1\n\t" \
  "ds_read_b64 v[232:233], v250 offset:36864\n\t" \
  "v_add_u32_sdwa v250, 0, v117 dst_sel:DWORD dst_unused:UNUSED_PAD src0_sel:DWORD src1_sel:WORD_0\n\t" \
  "v_smfmac_f32_16x16x64_bf16 v[84:87], v[240:243], v[220:227], v182 abid:1\n\t" \
  "ds_read_b64 v[234:235], v251 offset:36864\n\t" \
  "v_add_u32_sdwa v251, 0, v117 dst_sel:DWORD dst_unused:UNUSED_PAD src0_sel:DWORD src1_sel:WORD_1\n\t" \
  "v_smfmac_f32_16x16x64_bf16 v[64:67], a[64:67], v[134:141], v149 abid:1\n\t" \
  "ds_read_b64 v[220:221], v248 offset:0\n\t" \
  "v_smfmac_f32_16x16x64_bf16 v[68:71], a[68:71], v[134:141], v178 abid:1\n\t" \
  "s_waitcnt vmcnt(15)\n\t" \
  "ds_read_b64 v[222:223], v249 offset:0\n\t" \
  "v_smfmac_f32_16x16x64_bf16 v[0:3], a[72:75], v[126:133], v150\n\t" \
  "s_waitcnt lgkmcnt(2)\n\t" \
  "buffer_load_dwordx4 a[132:135], v187, s[12:15], s48 offen offset:1024 sc1\n\t" \
  "ds_read_b128 v[134:137], v183 offset:22528\n\t" \
  "v_smfmac_f32_16x16x64_bf16 v[48:51], v[240:243], v[228:235], v182 abid:1\n\t" \
  "s_waitcnt vmcnt(15)\n\t" \
  "buffer_load_dwordx4 a[136:139], v187, s[12:15], s48 offen offset:2048 sc1\n\t" \
  "ds_read_b128 v[138:141], v184 offset:22528\n\t" \
  "v_smfmac_f32_16x16x64_bf16 v[4:7], a[76:79], v[126:133], v150 abid:1\n\t" \
  "s_waitcnt vmcnt(15)\n\t" \
  "ds_read_b64 v[224:225], v250 offset:0\n\t" \
  "v_smfmac_f32_16x16x64_bf16 v[8:11], a[80:83], v[126:133], v151\n\t" \
  "s_waitcnt vmcnt(14)\n\t" \
  "ds_read_b64 v[226:227], v251 offset:0\n\t" \
  "v_smfmac_f32_16x16x64_bf16 v[12:15], a[84:87], v[126:133], v151 abid:1\n\t" \
  "s_waitcnt vmcnt(13)\n\t" \
  "buffer_load_dwordx4 a[140:143], v187, s[12:15], s48 offen offset:3072 sc1\n\t" \
  "ds_read_b64 v[228:229], v248 offset:18432\n\t" \
  "v_smfmac_f32_16x16x64_bf16 v[16:19], a[88:91], v[126:133], v152\n\t" \
  "s_waitcnt vmcnt(13)\n\t" \
  "ds_read_b64 v[230:231], v249 offset:18432\n\t" \
  "v_smfmac_f32_16x16x64_bf16 v[20:23], a[92:95], v[126:133], v152 abid:1\n\t" \
  "s_waitcnt lgkmcnt(2)\n\t" \
  "ds_read_b64 v[232:233], v250 offset:18432\n\t" \
  "v_smfmac_f32_16x16x64_bf16 v[16:19], v[236:239], v[220:227], v182 abid:1\n\t" \
  "s_waitcnt vmcnt(12)\n\t" \
  "ds_read_b64 v[234:235], v251 offset:18432\n\t" \
  "v_add_u32 v240, s44, v187\n\t" \
  "v_smfmac_f32_16x16x64_bf16 v[24:27], a[96:99], v[126:133], v153\n\t" \
  "s_waitcnt vmcnt(11)\n\t" \
  "buffer_load_dwordx4 v[158:161], v187, s[12:15], s47 offen offset:1024 sc1\n\t" \
  "ds_read_b128 v[240:243], v240 offset:5120\n\t" \
  "v_smfmac_f32_16x16x64_bf16 v[28:31], a[100:103], v[126:133], v153 abid:1\n\t" \
  "s_waitcnt vmcnt(11)\n\t" \
  "ds_read_b64 v[220:221], v248 offset:36864\n\t" \
  "v_add_u32_sdwa v248, 0, v118 dst_sel:DWORD dst_unused:UNUSED_PAD src0_sel:DWORD src1_sel:WORD_0\n\t" \
  "v_smfmac_f32_16x16x64_bf16 v[32:35], a[104:107], v[126:133], v179\n\t" \
  "s_waitcnt lgkmcnt(2)\n\t" \
  "ds_read_b64 v[222:223], v249 offset:36864\n\t" \
  "v_add_u32_sdwa v249, 0, v118 dst_sel:DWORD dst_unused:UNUSED_PAD src0_sel:DWORD src1_sel:WORD_1\n\t" \
  "v_smfmac_f32_16x16x64_bf16 v[88:91], v[236:239], v[228:235], v182 abid:1\n\t" \
  "ds_read_b64 v[224:225], v250 offset:36864\n\t" \
  "v_add_u32_sdwa v250, 0, v119 dst_sel:DWORD dst_unused:UNUSED_PAD src0_sel:DWORD src1_sel:WORD_0\n\t" \
  "v_smfmac_f32_16x16x64_bf16 v[72:75], a[72:75], v[134:141], v150\n\t" \
  "s_add_u32 s48, s46, 36864\n\t" \
  "buffer_load_dwordx4 a[144:147], v187, s[12:15], s48 offen offset:0 sc1\n\t" \
  "ds_read_b128 v[126:129], v183 offset:40960\n\t" \
  "v_smfmac_f32_16x16x64_bf16 v[76:79], a[76:79], v[134:141], v150 abid:1\n\t" \
  "ds_read_b128 v[130:133], v184 offset:40960\n\t" \
  "v_smfmac_f32_16x16x64_bf16 v[80:83], a[80:83], v[134:141], v151\n\t" \
  "ds_read_b64 v[226:227], v251 offset:36864\n\t" \
  "v_add_u32_sdwa v251, 0, v119 dst_sel:DWORD dst_unused:UNUSED_PAD src0_sel:DWORD src1_sel:WORD_1\n\t" \
  "v_smfmac_f32_16x16x64_bf16 v[84:87], a[84:87], v[134:141], v151 abid:1\n\t" \
  "buffer_load_dwordx4 a[148:151], v187, s[12:15], s48 offen offset:1024 sc1\n\t" \
  "ds_read_b64 v[228:229], v248 offset:0\n\t" \
  "v_smfmac_f32_16x16x64_bf16 v[88:91], a[88:91], v[134:141], v152\n\t" \
  "ds_read_b64 v[230:231], v249 offset:0\n\t" \
  "v_smfmac_f32_16x16x64_bf16 v[92:95], a[92:95], v[134:141], v152 abid:1\n\t" \
  "s_waitcnt lgkmcnt(2)\n\t" \
  "ds_read_b64 v[232:233], v250 offset:0\n\t" \
  "v_smfmac_f32_16x16x64_bf16 v[52:55], v[236:239], v[220:227], v182 abid:1\n\t" \
  "ds_read_b64 v[234:235], v251 offset:0\n\t" \
  "v_smfmac_f32_16x16x64_bf16 v[96:99], a[96:99], v[134:141], v153\n\t" \
  "buffer_load_dwordx4 a[152:155], v187, s[12:15], s48 offen offset:2048 sc1\n\t" \
  "ds_read_b64 v[220:221], v248 offset:18432\n\t" \
  "v_smfmac_f32_16x16x64_bf16 v[100:103], a[100:103], v[134:141], v153 abid:1\n\t" \
  "ds_read_b64 v[222:223], v249 offset:18432\n\t" \
  "v_smfmac_f32_16x16x64_bf16 v[104:107], a[104:107], v[134:141], v179\n\t" \
  "s_waitcnt lgkmcnt(2)\n\t" \
  "ds_read_b64 v[224:225], v250 offset:18432\n\t" \
  "v_smfmac_f32_16x16x64_bf16 v[20:23], v[240:243], v[228:235], v182 abid:1\n\t" \
  "ds_read_b64 v[226:227], v251 offset:18432\n\t" \
  "v_add_u32 v236, s44, v187\n\t" \
  "v_smfmac_f32_16x16x64_bf16 v[36:39], a[72:75], v[126:133], v150\n\t" \
  "buffer_load_dwordx4 a[156:159], v187, s[12:15], s48 offen offset:3072 sc1\n\t" \
  "ds_read_b128 v[134:137], v183 offset:6144\n\t" \
  "v_smfmac_f32_16x16x64_bf16 v[40:43], a[76:79], v[126:133], v150 abid:1\n\t" \
  "ds_read_b128 v[138:141], v184 offset:6144\n\t" \
  "v_smfmac_f32_16x16x64_bf16 v[44:47], a[80:83], v[126:133], v151\n\t" \
  "s_waitcnt lgkmcnt(2)\n\t" \
  "ds_read_b128 v[236:239], v236 offset:6144\n\t" \
  "v_smfmac_f32_16x16x64_bf16 v[92:95], v[240:243], v[220:227], v182 abid:1\n\t" \
  "ds_read_b64 v[228:229], v248 offset:36864\n\t" \
  "v_add_u32_sdwa v248, 0, v120 dst_sel:DWORD dst_unused:UNUSED_PAD src0_sel:DWORD src1_sel:WORD_0\n\t" \
  "v_smfmac_f32_16x16x64_bf16 v[48:51], a[84:87], v[126:133], v151 abid:1\n\t" \
  "s_add_u32 s48, s46, 40960\n\t" \
  "buffer_load_dwordx4 a[160:163], v187, s[12:15], s48 offen offset:0 sc1\n\t" \
  "ds_read_b64 v[230:231], v249 offset:36864\n\t" \
  "v_add_u32_sdwa v249, 0, v120 dst_sel:DWORD dst_unused:UNUSED_PAD src0_sel:DWORD src1_sel:WORD_1\n\t" \
  "v_smfmac_f32_16x16x64_bf16 v[52:55], a[88:91], v[126:133], v152\n\t" \
  "ds_read_b64 v[232:233], v250 offset:36864\n\t" \
  "v_add_u32_sdwa v250, 0, v121 dst_sel:DWORD dst_unused:UNUSED_PAD src0_sel:DWORD src1_sel:WORD_0\n\t" \
  "v_smfmac_f32_16x16x64_bf16 v[56:59], a[92:95], v[126:133], v152 abid:1\n\t" \
  "ds_read_b64 v[234:235], v251 offset:36864\n\t" \
  "v_add_u32_sdwa v251, 0, v121 dst_sel:DWORD dst_unused:UNUSED_PAD src0_sel:DWORD src1_sel:WORD_1\n\t" \
  "v_smfmac_f32_16x16x64_bf16 v[60:63], a[96:99], v[126:133], v153\n\t" \
  "buffer_load_dwordx4 a[164:167], v187, s[12:15], s48 offen offset:1024 sc1\n\t" \
  "ds_read_b64 v[220:221], v248 offset:0\n\t" \
  "v_smfmac_f32_16x16x64_bf16 v[64:67], a[100:103], v[126:133], v153 abid:1\n\t" \
  "ds_read_b64 v[222:223], v249 offset:0\n\t" \
  "v_smfmac_f32_16x16x64_bf16 v[68:71], a[104:107], v[126:133], v179\n\t" \
  "s_waitcnt lgkmcnt(2)\n\t" \
  "ds_read_b64 v[224:225], v250 offset:0\n\t" \
  "v_smfmac_f32_16x16x64_bf16 v[56:59], v[240:243], v[228:235], v182 abid:1\n\t" \
  "s_waitcnt vmcnt(15)\n\t" \
  "ds_read_b64 v[226:227], v251 offset:0\n\t" \
  "v_smfmac_f32_16x16x64_bf16 v[0:3], a[108:111], v[134:141], v154\n\t" \
  "s_waitcnt vmcnt(14)\n\t" \
  "buffer_load_dwordx4 a[168:171], v187, s[12:15], s48 offen offset:2048 sc1\n\t" \
  "ds_read_b128 v[126:129], v183 offset:24576\n\t" \
  "v_smfmac_f32_16x16x64_bf16 v[4:7], a[112:115], v[134:141], v154 abid:1\n\t" \
  "s_waitcnt vmcnt(14)\n\t" \
  "buffer_load_dwordx4 a[172:175], v187, s[12:15], s48 offen offset:3072 sc1\n\t" \
  "ds_read_b128 v[130:133], v184 offset:24576\n\t" \
  "v_smfmac_f32_16x16x64_bf16 v[8:11], a[116:119], v[134:141], v155\n\t" \
  "s_waitcnt lgkmcnt(2)\n\t" \
  "ds_read_b64 v[228:229], v248 offset:18432\n\t" \
  "v_smfmac_f32_16x16x64_bf16 v[24:27], v[236:239], v[220:227], v182 abid:1\n\t" \
  "s_waitcnt vmcnt(14)\n\t" \
  "ds_read_b64 v[230:231], v249 offset:18432\n\t" \
  "v_add_u32 v240, s44, v187\n\t" \
  "v_smfmac_f32_16x16x64_bf16 v[12:15], a[120:123], v[134:141], v155 abid:1\n\t" \
  "s_add_u32 s48, s46, 45056\n\t" \
  "s_waitcnt vmcnt(13)\n\t" \
  "buffer_load_dwordx4 a[176:179], v187, s[12:15], s48 offen offset:0 sc1\n\t" \
  "ds_read_b128 v[240:243], v240 offset:7168\n\t" \
  "v_smfmac_f32_16x16x64_bf16 v[16:19], a[124:127], v[134:141], v156\n\t" \
  "s_waitcnt vmcnt(13)\n\t" \
  "ds_read_b64 v[232:233], v250 offset:18432\n\t" \
  "v_smfmac_f32_16x16x64_bf16 v[20:23], a[128:131], v[134:141], v156 abid:1\n\t" \
  "s_waitcnt vmcnt(12)\n\t" \
  "ds_read_b64 v[234:235], v251 offset:18432\n\t" \
  "v_smfmac_f32_16x16x64_bf16 v[24:27], a[132:135], v[134:141], v157\n\t" \
  "s_waitcnt vmcnt(11)\n\t" \
  "buffer_load_dwordx4 v[162:165], v187, s[12:15], s47 offen offset:2048 sc1\n\t" \
  "ds_read_b64 v[220:221], v248 offset:36864\n\t" \
  "v_add_u32_sdwa v248, 0, v122 dst_sel:DWORD dst_unused:UNUSED_PAD src0_sel:DWORD src1_sel:WORD_0\n\t" \
  "v_smfmac_f32_16x16x64_bf16 v[28:31], a[136:139], v[134:141], v157 abid:1\n\t" \
  "s_waitcnt vmcnt(11)\n\t" \
  "ds_read_b64 v[222:223], v249 offset:36864\n\t" \
  "v_add_u32_sdwa v249, 0, v122 dst_sel:DWORD dst_unused:UNUSED_PAD src0_sel:DWORD src1_sel:WORD_1\n\t" \
  "v_smfmac_f32_16x16x64_bf16 v[32:35], a[140:143], v[134:141], v179 abid:1\n\t" \
  "s_waitcnt lgkmcnt(2)\n\t" \
  "ds_read_b64 v[224:225], v250 offset:36864\n\t" \
  "v_add_u32_sdwa v250, 0, v123 dst_sel:DWORD dst_unused:UNUSED_PAD src0_sel:DWORD src1_sel:WORD_0\n\t" \
  "v_smfmac_f32_16x16x64_bf16 v[96:99], v[236:239], v[228:235], v182 abid:1\n\t" \
  "ds_read_b64 v[226:227], v251 offset:36864\n\t" \
  "v_add_u32_sdwa v251, 0, v123 dst_sel:DWORD dst_unused:UNUSED_PAD src0_sel:DWORD src1_sel:WORD_1\n\t" \
  "v_smfmac_f32_16x16x64_bf16 v[72:75], a[108:111], v[126:133], v154\n\t" \
  "buffer_load_dwordx4 a[180:183], v187, s[12:15], s48 offen offset:1024 sc1\n\t" \
  "ds_read_b128 v[134:137], v183 offset:43008\n\t" \
  "v_smfmac_f32_16x16x64_bf16 v[76:79], a[112:115], v[126:133], v154 abid:1\n\t" \
  "ds_read_b128 v[138:141], v184 offset:43008\n\t" \
  "v_smfmac_f32_16x16x64_bf16 v[80:83], a[116:119], v[126:133], v155\n\t" \
  "s_waitcnt lgkmcnt(2)\n\t" \
  "ds_read_b64 v[228:229], v248 offset:0\n\t" \
  "v_smfmac_f32_16x16x64_bf16 v[60:63], v[236:239], v[220:227], v182 abid:1\n\t" \
  "ds_read_b64 v[230:231], v249 offset:0\n\t" \
  "v_smfmac_f32_16x16x64_bf16 v[84:87], a[120:123], v[126:133], v155 abid:1\n\t" \
  "buffer_load_dwordx4 a[184:187], v187, s[12:15], s48 offen offset:2048 sc1\n\t" \
  "ds_read_b64 v[232:233], v250 offset:0\n\t" \
  "v_smfmac_f32_16x16x64_bf16 v[88:91], a[124:127], v[126:133], v156\n\t" \
  "ds_read_b64 v[234:235], v251 offset:0\n\t" \
  "v_smfmac_f32_16x16x64_bf16 v[92:95], a[128:131], v[126:133], v156 abid:1\n\t" \
  "ds_read_b64 v[220:221], v248 offset:18432\n\t" \
  "v_smfmac_f32_16x16x64_bf16 v[96:99], a[132:135], v[126:133], v157\n\t" \
  "buffer_load_dwordx4 a[188:191], v187, s[12:15], s48 offen offset:3072 sc1\n\t" \
  "ds_read_b64 v[222:223], v249 offset:18432\n\t" \
  "v_smfmac_f32_16x16x64_bf16 v[100:103], a[136:139], v[126:133], v157 abid:1\n\t" \
  "s_waitcnt lgkmcnt(2)\n\t" \
  "ds_read_b64 v[224:225], v250 offset:18432\n\t" \
  "v_smfmac_f32_16x16x64_bf16 v[28:31], v[240:243], v[228:235], v182 abid:1\n\t" \
  "ds_read_b64 v[226:227], v251 offset:18432\n\t" \
  "v_add_u32 v236, s44, v187\n\t" \
  "v_smfmac_f32_16x16x64_bf16 v[104:107], a[140:143], v[126:133], v179 abid:1\n\t" \
  "ds_read_b128 v[236:239], v236 offset:8192\n\t" \
  "v_smfmac_f32_16x16x64_bf16 v[36:39], a[108:111], v[134:141], v154\n\t" \
  "s_add_u32 s48, s46, 49152\n\t" \
  "buffer_load_dwordx4 a[192:195], v187, s[12:15], s48 offen offset:0 sc1\n\t" \
  "ds_read_b128 v[126:129], v183 offset:8192\n\t" \
  "v_smfmac_f32_16x16x64_bf16 v[40:43], a[112:115], v[134:141], v154 abid:1\n\t" \
  "s_waitcnt lgkmcnt(2)\n\t" \
  "ds_read_b128 v[130:133], v184 offset:8192\n\t" \
  "v_smfmac_f32_16x16x64_bf16 v[100:103], v[240:243], v[220:227], v182 abid:1\n\t" \
  "ds_read_b64 v[228:229], v248 offset:36864\n\t" \
  "v_add_u32_sdwa v248, 0, v124 dst_sel:DWORD dst_unused:UNUSED_PAD src0_sel:DWORD src1_sel:WORD_0\n\t" \
  "v_smfmac_f32_16x16x64_bf16 v[44:47], a[116:119], v[134:141], v155\n\t" \
  "ds_read_b64 v[230:231], v249 offset:36864\n\t" \
  "v_add_u32_sdwa v249, 0, v124 dst_sel:DWORD dst_unused:UNUSED_PAD src0_sel:DWORD src1_sel:WORD_1\n\t" \
  "v_smfmac_f32_16x16x64_bf16 v[48:51], a[120:123], v[134:141], v155 abid:1\n\t" \
  "buffer_load_dwordx4 a[196:199], v187, s[12:15], s48 offen offset:1024 sc1\n\t" \
  "ds_read_b64 v[232:233], v250 offset:36864\n\t" \
  "v_add_u32_sdwa v250, 0, v125 dst_sel:DWORD dst_unused:UNUSED_PAD src0_sel:DWORD src1_sel:WORD_0\n\t" \
  "v_smfmac_f32_16x16x64_bf16 v[52:55], a[124:127], v[134:141], v156\n\t" \
  "ds_read_b64 v[234:235], v251 offset:36864\n\t" \
  "v_add_u32_sdwa v251, 0, v125 dst_sel:DWORD dst_unused:UNUSED_PAD src0_sel:DWORD src1_sel:WORD_1\n\t" \
  "v_smfmac_f32_16x16x64_bf16 v[56:59], a[128:131], v[134:141], v156 abid:1\n\t" \
  "ds_read_b64 v[220:221], v248 offset:0\n\t" \
  "v_smfmac_f32_16x16x64_bf16 v[60:63], a[132:135], v[134:141], v157\n\t" \
  "buffer_load_dwordx4 a[200:203], v187, s[12:15], s48 offen offset:2048 sc1\n\t" \
  "ds_read_b64 v[222:223], v249 offset:0\n\t" \
  "v_smfmac_f32_16x16x64_bf16 v[64:67], a[136:139], v[134:141], v157 abid:1\n\t" \
  "s_waitcnt lgkmcnt(2)\n\t" \
  "ds_read_b64 v[224:225], v250 offset:0\n\t" \
  "v_smfmac_f32_16x16x64_bf16 v[64:67], v[240:243], v[228:235], v182 abid:1\n\t" \
  "ds_read_b64 v[226:227], v251 offset:0\n\t" \
  "v_smfmac_f32_16x16x64_bf16 v[68:71], a[140:143], v[134:141], v179 abid:1\n\t" \
  "s_waitcnt vmcnt(15)\n\t" \
  "ds_read_b64 v[228:229], v248 offset:18432\n\t" \
  "v_smfmac_f32_16x16x64_bf16 v[0:3], a[144:147], v[126:133], v158\n\t" \
  "s_waitcnt vmcnt(14)\n\t" \
  "buffer_load_dwordx4 a[204:207], v187, s[12:15], s48 offen offset:3072 sc1\n\t" \
  "ds_read_b128 v[134:137], v183 offset:26624\n\t" \
  "v_smfmac_f32_16x16x64_bf16 v[4:7], a[148:151], v[126:133], v158 abid:1\n\t" \
  "s_add_u32 s48, s46, 53248\n\t" \
  "s_waitcnt lgkmcnt(2)\n\t" \
  "buffer_load_dwordx4 a[208:211], v187, s[12:15], s48 offen offset:0 sc1\n\t" \
  "ds_read_b128 v[138:141], v184 offset:26624\n\t" \
  "v_smfmac_f32_16x16x64_bf16 v[32:35], v[236:239], v[220:227], v182 abid:1\n\t" \
  "s_waitcnt vmcnt(15)\n\t" \
  "ds_read_b64 v[230:231], v249 offset:18432\n\t" \
  "v_mov_b64 v[240:241], 0\n\t" \
  "v_mov_b64 v[242:243], 0\n\t" \
  "v_smfmac_f32_16x16x64_bf16 v[8:11], a[152:155], v[126:133], v159\n\t" \
  "s_waitcnt vmcnt(14)\n\t" \
  "s_mov_b32 exec_hi, 0\n\t" \
  "v_add_u32 v240, s71, v187\n\t" \
  "ds_read_b128 v[240:243], v240\n\t" \
  "s_mov_b32 exec_hi, -1\n\t" \
  "v_smfmac_f32_16x16x64_bf16 v[12:15], a[156:159], v[126:133], v159 abid:1\n\t" \
  "s_waitcnt vmcnt(13)\n\t" \
  "buffer_load_dwordx4 a[212:215], v187, s[12:15], s48 offen offset:1024 sc1\n\t" \
  "ds_read_b64 v[232:233], v250 offset:18432\n\t" \
  "v_smfmac_f32_16x16x64_bf16 v[16:19], a[160:163], v[126:133], v160\n\t" \
  "s_waitcnt vmcnt(13)\n\t" \
  "ds_read_b64 v[234:235], v251 offset:18432\n\t" \
  "v_smfmac_f32_16x16x64_bf16 v[20:23], a[164:167], v[126:133], v160 abid:1\n\t" \
  "s_waitcnt vmcnt(12)\n\t" \
  "ds_read_b64 v[220:221], v248 offset:36864\n\t" \
  "v_add_u32_sdwa v248, 0, v244 dst_sel:DWORD dst_unused:UNUSED_PAD src0_sel:DWORD src1_sel:WORD_0\n\t" \
  "v_smfmac_f32_16x16x64_bf16 v[24:27], a[168:171], v[126:133], v161\n\t" \
  "s_waitcnt vmcnt(11)\n\t" \
  "buffer_load_dwordx4 v[166:169], v187, s[12:15], s47 offen offset:3072 sc1\n\t" \
  "ds_read_b64 v[222:223], v249 offset:36864\n\t" \
  "v_add_u32_sdwa v249, 0, v244 dst_sel:DWORD dst_unused:UNUSED_PAD src0_sel:DWORD src1_sel:WORD_1\n\t" \
  "v_smfmac_f32_16x16x64_bf16 v[28:31], a[172:175], v[126:133], v161 abid:1\n\t" \
  "s_waitcnt lgkmcnt(2)\n\t" \
  "ds_read_b64 v[224:225], v250 offset:36864\n\t" \
  "v_add_u32_sdwa v250, 0, v244 dst_sel:DWORD dst_unused:UNUSED_PAD src0_sel:DWORD src1_sel:WORD_0\n\t" \
  "v_smfmac_f32_16x16x64_bf16 v[104:107], v[236:239], v[228:235], v182 abid:1\n\t" \
  "s_waitcnt vmcnt(11)\n\t" \
  "ds_read_b64 v[226:227], v251 offset:36864\n\t" \
  "v_add_u32_sdwa v251, 0, v244 dst_sel:DWORD dst_unused:UNUSED_PAD src0_sel:DWORD src1_sel:WORD_1\n\t" \
  "v_smfmac_f32_16x16x64_bf16 v[32:35], a[176:179], v[126:133], v180\n\t" \
  "ds_read_b64 v[228:229], v248 offset:0\n\t" \
  "v_smfmac_f32_16x16x64_bf16 v[72:75], a[144:147], v[134:141], v158\n\t" \
  "buffer_load_dwordx4 a[216:219], v187, s[12:15], s48 offen offset:2048 sc1\n\t" \
  "ds_read_b128 v[126:129], v183 offset:45056\n\t" \
  "v_smfmac_f32_16x16x64_bf16 v[76:79], a[148:151], v[134:141], v158 abid:1\n\t" \
  "s_waitcnt lgkmcnt(2)\n\t" \
  "ds_read_b128 v[130:133], v184 offset:45056\n\t" \
  "v_smfmac_f32_16x16x64_bf16 v[68:71], v[236:239], v[220:227], v182 abid:1\n\t" \
  "ds_read_b64 v[230:231], v249 offset:0\n\t" \
  "v_smfmac_f32_16x16x64_bf16 v[80:83], a[152:155], v[134:141], v159\n\t" \
  "ds_read_b64 v[232:233], v250 offset:0\n\t" \
  "v_smfmac_f32_16x16x64_bf16 v[84:87], a[156:159], v[134:141], v159 abid:1\n\t" \
  "buffer_load_dwordx4 a[220:223], v187, s[12:15], s48 offen offset:3072 sc1\n\t" \
  "ds_read_b64 v[234:235], v251 offset:0\n\t" \
  "v_smfmac_f32_16x16x64_bf16 v[88:91], a[160:163], v[134:141], v160\n\t" \
  "ds_read_b64 v[220:221], v248 offset:18432\n\t" \
  "v_smfmac_f32_16x16x64_bf16 v[92:95], a[164:167], v[134:141], v160 abid:1\n\t" \
  "ds_read_b64 v[222:223], v249 offset:18432\n\t" \
  "v_smfmac_f32_16x16x64_bf16 v[96:99], a[168:171], v[134:141], v161\n\t" \
  "s_add_u32 s48, s46, 57344\n\t" \
  "s_waitcnt lgkmcnt(2)\n\t" \
  "buffer_load_dwordx4 a[224:227], v187, s[12:15], s48 offen offset:0 sc1\n\t" \
  "ds_read_b64 v[224:225], v250 offset:18432\n\t" \
  "s_set_gpr_idx_on s6, gpr_idx(DST)\n\t" \
  "v_smfmac_f32_16x16x64_bf16 v[0:3], v[240:243], v[228:235], v182 abid:1\n\t" \
  "s_set_gpr_idx_off\n\t" \
  "ds_read_b64 v[226:227], v251 offset:18432\n\t" \
  "v_add_u32 v236, s60, v187\n\t" \
  "v_smfmac_f32_16x16x64_bf16 v[100:103], a[172:175], v[134:141], v161 abid:1\n\t" \
  "ds_write_b128 v236, v[240:243]\n\t" \
  "v_smfmac_f32_16x16x64_bf16 v[104:107], a[176:179], v[134:141], v180\n\t" \
  "ds_read_b64 v[228:229], v248 offset:36864\n\t" \
  "v_smfmac_f32_16x16x64_bf16 v[36:39], a[144:147], v[126:133], v158\n\t" \
  "s_waitcnt lgkmcnt(2)\n\t" \
  "buffer_load_dwordx4 a[228:231], v187, s[12:15], s48 offen offset:1024 sc1\n\t" \
  "ds_read_b128 v[134:137], v183 offset:10240\n\t" \
  "s_set_gpr_idx_on s6, gpr_idx(DST)\n\t" \
  "v_smfmac_f32_16x16x64_bf16 v[72:75], v[240:243], v[220:227], v182 abid:1\n\t" \
  "s_set_gpr_idx_off\n\t" \
  "ds_read_b128 v[138:141], v184 offset:10240\n\t" \
  "v_smfmac_f32_16x16x64_bf16 v[40:43], a[148:151], v[126:133], v158 abid:1\n\t" \
  "ds_read_b64 v[230:231], v249 offset:36864\n\t" \
  "v_smfmac_f32_16x16x64_bf16 v[44:47], a[152:155], v[126:133], v159\n\t" \
  "ds_read_b64 v[232:233], v250 offset:36864\n\t" \
  "v_smfmac_f32_16x16x64_bf16 v[48:51], a[156:159], v[126:133], v159 abid:1\n\t" \
  "buffer_load_dwordx4 a[232:235], v187, s[12:15], s48 offen offset:2048 sc1\n\t" \
  "ds_read_b64 v[234:235], v251 offset:36864\n\t" \
  "v_smfmac_f32_16x16x64_bf16 v[52:55], a[160:163], v[126:133], v160\n\t" \
  "v_smfmac_f32_16x16x64_bf16 v[56:59], a[164:167], v[126:133], v160 abid:1\n\t" \
  "v_smfmac_f32_16x16x64_bf16 v[60:63], a[168:171], v[126:133], v161\n\t" \
  "s_waitcnt lgkmcnt(0)\n\t" \
  "buffer_load_dwordx4 a[236:239], v187, s[12:15], s48 offen offset:3072 sc1\n\t" \
  "s_set_gpr_idx_on s6, gpr_idx(DST)\n\t" \
  "v_smfmac_f32_16x16x64_bf16 v[36:39], v[240:243], v[228:235], v182 abid:1\n\t" \
  "s_set_gpr_idx_off\n\t" \
  "s_cmp_gt_u32 s61, 1\n\t" \
  "s_cbranch_scc1 .Lmf%=\n\t" \
  ".Lmg%=:\n\t" \
  "s_and_b32 s25, s62, 0x1ff\n\t" \
  "s_cbranch_scc1 .Lsl%=\n\t" \
  ".Lsh%=:\n\t" \
  "v_smfmac_f32_16x16x64_bf16 v[64:67], a[172:175], v[126:133], v161 abid:1\n\t" \
  "s_sub_u32 s56, s72, s56\n\t" \
  "v_smfmac_f32_16x16x64_bf16 v[68:71], a[176:179], v[126:133], v180\n\t" \
  "s_max_i32 s56, s56, s29\n\t" \
  "s_waitcnt vmcnt(15)\n\t" \
  "v_smfmac_f32_16x16x64_bf16 v[0:3], a[180:183], v[134:141], v162\n\t" \
  "s_add_u32 s48, s46, 61440\n\t" \
  "s_waitcnt vmcnt(14)\n\t" \
  "buffer_load_dwordx4 a[240:243], v187, s[12:15], s48 offen offset:0 sc1\n\t" \
  "ds_read_b128 v[126:129], v183 offset:28672\n\t" \
  "v_smfmac_f32_16x16x64_bf16 v[4:7], a[184:187], v[134:141], v162 abid:1\n\t" \
  "s_mov_b32 s29, 55296\n\t" \
  "s_waitcnt vmcnt(14)\n\t" \
  "buffer_load_dwordx4 a[244:247], v187, s[12:15], s48 offen offset:1024 sc1\n\t" \
  "ds_read_b128 v[130:133], v184 offset:28672\n\t" \
  "v_smfmac_f32_16x16x64_bf16 v[8:11], a[188:191], v[134:141], v163\n\t" \
  "s_add_u32 s42, s72, 425\n\t" \
  "s_waitcnt vmcnt(14)\n\t" \
  "v_smfmac_f32_16x16x64_bf16 v[12:15], a[192:195], v[134:141], v163 abid:1\n\t" \
  "s_and_b32 s27, s64, 7\n\t" \
  "s_waitcnt vmcnt(13)\n\t" \
  "buffer_load_dwordx4 a[248:251], v187, s[12:15], s48 offen offset:2048 sc1\n\t" \
  "v_smfmac_f32_16x16x64_bf16 v[16:19], a[196:199], v[134:141], v164\n\t" \
  "s_lshl2_add_u32 s66, s27, s68\n\t" \
  "s_waitcnt vmcnt(13)\n\t" \
  "v_smfmac_f32_16x16x64_bf16 v[20:23], a[200:203], v[134:141], v164 abid:1\n\t" \
  "s_and_b32 s27, s64, 8\n\t" \
  "s_waitcnt vmcnt(12)\n\t" \
  "v_smfmac_f32_16x16x64_bf16 v[24:27], a[204:207], v[134:141], v165\n\t" \
  "s_add_u32 s47, s47, 4096\n\t" \
  "s_waitcnt vmcnt(11)\n\t" \
  "buffer_load_dwordx4 v[170:173], v187, s[12:15], s47 offen offset:0 sc1\n\t" \
  "v_smfmac_f32_16x16x64_bf16 v[28:31], a[208:211], v[134:141], v165 abid:1\n\t" \
  "s_mul_i32 s27, s27, 176\n\t" \
  "s_waitcnt vmcnt(11)\n\t" \
  "v_smfmac_f32_16x16x64_bf16 v[32:35], a[212:215], v[134:141], v180 abid:1\n\t" \
  "s_add_u32 s66, s66, s27\n\t" \
  "s_waitcnt lgkmcnt(0)\n\t" \
  "v_smfmac_f32_16x16x64_bf16 v[72:75], a[180:183], v[126:133], v162\n\t" \
  "s_lshr_b64 s[64:65], s[64:65], 4\n\t" \
  "buffer_load_dwordx4 a[252:255], v187, s[12:15], s48 offen offset:3072 sc1\n\t" \
  "ds_read_b128 v[134:137], v183 offset:47104\n\t" \
  "v_smfmac_f32_16x16x64_bf16 v[76:79], a[184:187], v[126:133], v162 abid:1\n\t" \
  "ds_read_b128 v[138:141], v184 offset:47104\n\t" \
  "v_smfmac_f32_16x16x64_bf16 v[80:83], a[188:191], v[126:133], v163\n\t" \
  "v_smfmac_f32_16x16x64_bf16 v[84:87], a[192:195], v[126:133], v163 abid:1\n\t" \
  "s_add_u32 s48, s46, 65536\n\t" \
  "buffer_load_dwordx4 v[188:191], v187, s[12:15], s48 offen offset:0 sc1\n\t" \
  "v_smfmac_f32_16x16x64_bf16 v[88:91], a[196:199], v[126:133], v164\n\t" \
  "v_smfmac_f32_16x16x64_bf16 v[92:95], a[200:203], v[126:133], v164 abid:1\n\t" \
  "v_smfmac_f32_16x16x64_bf16 v[96:99], a[204:207], v[126:133], v165\n\t" \
  "buffer_load_dwordx4 v[192:195], v187, s[12:15], s48 offen offset:1024 sc1\n\t" \
  "v_smfmac_f32_16x16x64_bf16 v[100:103], a[208:211], v[126:133], v165 abid:1\n\t" \
  "v_smfmac_f32_16x16x64_bf16 v[104:107], a[212:215], v[126:133], v180 abid:1\n\t" \
  "s_waitcnt lgkmcnt(0)\n\t" \
  "v_smfmac_f32_16x16x64_bf16 v[36:39], a[180:183], v[134:141], v162\n\t" \
  "buffer_load_dwordx4 v[196:199], v187, s[12:15], s48 offen offset:2048 sc1\n\t" \
  "ds_read_b128 v[126:129], v183 offset:12288\n\t" \
  "v_smfmac_f32_16x16x64_bf16 v[40:43], a[184:187], v[134:141], v162 abid:1\n\t" \
  "ds_read_b128 v[130:133], v184 offset:12288\n\t" \
  "v_smfmac_f32_16x16x64_bf16 v[44:47], a[188:191], v[134:141], v163\n\t" \
  "v_smfmac_f32_16x16x64_bf16 v[48:51], a[192:195], v[134:141], v163 abid:1\n\t" \
  "buffer_load_dwordx4 v[200:203], v187, s[12:15], s48 offen offset:3072 sc1\n\t" \
  "v_smfmac_f32_16x16x64_bf16 v[52:55], a[196:199], v[134:141], v164\n\t" \
  "v_smfmac_f32_16x16x64_bf16 v[56:59], a[200:203], v[134:141], v164 abid:1\n\t" \
  "v_smfmac_f32_16x16x64_bf16 v[60:63], a[204:207], v[134:141], v165\n\t" \
  "s_add_u32 s48, s46, 69632\n\t" \
  "buffer_load_dwordx4 v[204:207], v187, s[12:15], s48 offen offset:0 sc1\n\t" \
  "v_smfmac_f32_16x16x64_bf16 v[64:67], a[208:211], v[134:141], v165 abid:1\n\t" \
  "v_smfmac_f32_16x16x64_bf16 v[68:71], a[212:215], v[134:141], v180 abid:1\n\t" \
  "s_waitcnt vmcnt(15) lgkmcnt(0)\n\t" \
  "v_smfmac_f32_16x16x64_bf16 v[0:3], a[216:219], v[126:133], v166\n\t" \
  "s_waitcnt vmcnt(14)\n\t" \
  "buffer_load_dwordx4 v[208:211], v187, s[12:15], s48 offen offset:1024 sc1\n\t" \
  "ds_read_b128 v[134:137], v183 offset:30720\n\t" \
  "v_smfmac_f32_16x16x64_bf16 v[4:7], a[220:223], v[126:133], v166 abid:1\n\t" \
  "s_waitcnt vmcnt(14)\n\t" \
  "buffer_load_dwordx4 v[212:215], v187, s[12:15], s48 offen offset:2048 sc1\n\t" \
  "ds_read_b128 v[138:141], v184 offset:30720\n\t" \
  "v_smfmac_f32_16x16x64_bf16 v[8:11], a[224:227], v[126:133], v167\n\t" \
  "s_waitcnt vmcnt(14)\n\t" \
  "v_smfmac_f32_16x16x64_bf16 v[12:15], a[228:231], v[126:133], v167 abid:1\n\t" \
  "s_waitcnt vmcnt(13)\n\t" \
  "buffer_load_dwordx4 v[216:219], v187, s[12:15], s48 offen offset:3072 sc1\n\t" \
  "v_smfmac_f32_16x16x64_bf16 v[16:19], a[232:235], v[126:133], v168\n\t" \
  "s_waitcnt vmcnt(13)\n\t" \
  "v_smfmac_f32_16x16x64_bf16 v[20:23], a[236:239], v[126:133], v168 abid:1\n\t" \
  "s_waitcnt vmcnt(12)\n\t" \
  "v_smfmac_f32_16x16x64_bf16 v[24:27], a[240:243], v[126:133], v169\n\t" \
  "s_waitcnt vmcnt(11)\n\t" \
  "buffer_load_dwordx4 v[174:177], v187, s[12:15], s47 offen offset:1024 sc1\n\t" \
  "v_smfmac_f32_16x16x64_bf16 v[28:31], a[244:247], v[126:133], v169 abid:1\n\t" \
  "s_waitcnt vmcnt(11)\n\t" \
  "v_smfmac_f32_16x16x64_bf16 v[32:35], a[248:251], v[126:133], v181\n\t" \
  "s_waitcnt lgkmcnt(0)\n\t" \
  "v_smfmac_f32_16x16x64_bf16 v[72:75], a[216:219], v[134:141], v166\n\t" \
  "s_add_u32 s48, s46, 81920\n\t" \
  "buffer_load_dwordx4 v[252:255], v187, s[12:15], s48 offen offset:0 sc1\n\t" \
  "ds_read_b128 v[126:129], v183 offset:49152\n\t" \
  "v_smfmac_f32_16x16x64_bf16 v[76:79], a[220:223], v[134:141], v166 abid:1\n\t" \
  "ds_read_b128 v[130:133], v184 offset:49152\n\t" \
  "v_smfmac_f32_16x16x64_bf16 v[80:83], a[224:227], v[134:141], v167\n\t" \
  "v_smfmac_f32_16x16x64_bf16 v[84:87], a[228:231], v[134:141], v167 abid:1\n\t" \
  "s_add_u32 s48, s46, 73728\n\t" \
  "buffer_load_dwordx4 v[220:223], v187, s[12:15], s48 offen offset:0 sc1\n\t" \
  "v_smfmac_f32_16x16x64_bf16 v[88:91], a[232:235], v[134:141], v168\n\t" \
  "v_smfmac_f32_16x16x64_bf16 v[92:95], a[236:239], v[134:141], v168 abid:1\n\t" \
  "v_smfmac_f32_16x16x64_bf16 v[96:99], a[240:243], v[134:141], v169\n\t" \
  "buffer_load_dwordx4 v[224:227], v187, s[12:15], s48 offen offset:1024 sc1\n\t" \
  "v_smfmac_f32_16x16x64_bf16 v[100:103], a[244:247], v[134:141], v169 abid:1\n\t" \
  "v_smfmac_f32_16x16x64_bf16 v[104:107], a[248:251], v[134:141], v181\n\t" \
  "s_waitcnt lgkmcnt(0)\n\t" \
  "v_smfmac_f32_16x16x64_bf16 v[36:39], a[216:219], v[126:133], v166\n\t" \
  "buffer_load_dwordx4 v[228:231], v187, s[12:15], s48 offen offset:2048 sc1\n\t" \
  "ds_read_b128 v[134:137], v183 offset:14336\n\t" \
  "v_smfmac_f32_16x16x64_bf16 v[40:43], a[220:223], v[126:133], v166 abid:1\n\t" \
  "ds_read_b128 v[138:141], v184 offset:14336\n\t" \
  "v_smfmac_f32_16x16x64_bf16 v[44:47], a[224:227], v[126:133], v167\n\t" \
  "v_smfmac_f32_16x16x64_bf16 v[48:51], a[228:231], v[126:133], v167 abid:1\n\t" \
  "buffer_load_dwordx4 v[232:235], v187, s[12:15], s48 offen offset:3072 sc1\n\t" \
  "v_smfmac_f32_16x16x64_bf16 v[52:55], a[232:235], v[126:133], v168\n\t" \
  "v_smfmac_f32_16x16x64_bf16 v[56:59], a[236:239], v[126:133], v168 abid:1\n\t" \
  "v_smfmac_f32_16x16x64_bf16 v[60:63], a[240:243], v[126:133], v169\n\t" \
  "s_add_u32 s48, s46, 77824\n\t" \
  "buffer_load_dwordx4 v[236:239], v187, s[12:15], s48 offen offset:0 sc1\n\t" \
  "v_smfmac_f32_16x16x64_bf16 v[64:67], a[244:247], v[126:133], v169 abid:1\n\t" \
  "v_smfmac_f32_16x16x64_bf16 v[68:71], a[248:251], v[126:133], v181\n\t" \
  "s_waitcnt vmcnt(15) lgkmcnt(0)\n\t" \
  "v_smfmac_f32_16x16x64_bf16 v[0:3], a[252:255], v[134:141], v170\n\t" \
  "s_waitcnt vmcnt(14)\n\t" \
  "buffer_load_dwordx4 v[240:243], v187, s[12:15], s48 offen offset:1024 sc1\n\t" \
  "ds_read_b128 v[126:129], v183 offset:32768\n\t" \
  "v_smfmac_f32_16x16x64_bf16 v[4:7], v[188:191], v[134:141], v170 abid:1\n\t" \
  "s_waitcnt vmcnt(14)\n\t" \
  "buffer_load_dwordx4 v[244:247], v187, s[12:15], s48 offen offset:2048 sc1\n\t" \
  "ds_read_b128 v[130:133], v184 offset:32768\n\t" \
  "v_smfmac_f32_16x16x64_bf16 v[8:11], v[192:195], v[134:141], v171\n\t" \
  "s_waitcnt vmcnt(14)\n\t" \
  "buffer_load_dwordx4 v[248:251], v187, s[12:15], s48 offen offset:3072 sc1\n\t" \
  "v_smfmac_f32_16x16x64_bf16 v[12:15], v[196:199], v[134:141], v171 abid:1\n\t" \
  "s_waitcnt vmcnt(14)\n\t" \
  "v_smfmac_f32_16x16x64_bf16 v[16:19], v[200:203], v[134:141], v172\n\t" \
  "s_waitcnt vmcnt(13)\n\t" \
  "v_smfmac_f32_16x16x64_bf16 v[20:23], v[204:207], v[134:141], v172 abid:1\n\t" \
  "s_waitcnt vmcnt(12)\n\t" \
  "v_smfmac_f32_16x16x64_bf16 v[24:27], v[208:211], v[134:141], v173\n\t" \
  "s_barrier\n\t" \
  "s_add_u32 m0, s33, 0\n\t" \
  "s_waitcnt vmcnt(11)\n\t" \
  "buffer_load_dwordx4 v186, s[16:19], s34 offen offset:0 nt lds\n\t" \
  "v_smfmac_f32_16x16x64_bf16 v[28:31], v[212:215], v[134:141], v173 abid:1\n\t" \
  "s_add_u32 m0, s33, 1920\n\t" \
  "s_waitcnt vmcnt(11)\n\t" \
  "buffer_load_dwordx4 v186, s[16:19], s34 offen offset:128 nt lds\n\t" \
  "v_smfmac_f32_16x16x64_bf16 v[32:35], v[216:219], v[134:141], v181 abid:1\n\t" \
  "s_add_u32 m0, s33, 3840\n\t" \
  "s_waitcnt lgkmcnt(0)\n\t" \
  "buffer_load_dwordx4 v186, s[16:19], s34 offen offset:256 nt lds\n\t" \
  "v_smfmac_f32_16x16x64_bf16 v[72:75], a[252:255], v[126:133], v170\n\t" \
  "ds_read_b128 v[134:137], v183 offset:16384\n\t" \
  "v_smfmac_f32_16x16x64_bf16 v[76:79], v[188:191], v[126:133], v170 abid:1\n\t" \
  "ds_read_b128 v[138:141], v184 offset:16384\n\t" \
  "v_smfmac_f32_16x16x64_bf16 v[80:83], v[192:195], v[126:133], v171\n\t" \
  "v_smfmac_f32_16x16x64_bf16 v[84:87], v[196:199], v[126:133], v171 abid:1\n\t" \
  "v_smfmac_f32_16x16x64_bf16 v[88:91], v[200:203], v[126:133], v172\n\t" \
  "v_smfmac_f32_16x16x64_bf16 v[92:95], v[204:207], v[126:133], v172 abid:1\n\t" \
  "v_smfmac_f32_16x16x64_bf16 v[96:99], v[208:211], v[126:133], v173\n\t" \
  "v_smfmac_f32_16x16x64_bf16 v[100:103], v[212:215], v[126:133], v173 abid:1\n\t" \
  "v_smfmac_f32_16x16x64_bf16 v[104:107], v[216:219], v[126:133], v181 abid:1\n\t" \
  "s_memrealtime s[72:73]\n\t" \
  "ds_read_b128 v[126:129], v183 offset:34816\n\t" \
  "ds_read_b128 v[130:133], v184 offset:34816\n\t" \
  "s_waitcnt vmcnt(11) lgkmcnt(2)\n\t" \
  "v_smfmac_f32_16x16x64_bf16 v[32:35], v[252:255], v[134:141], v182\n\t" \
  "s_add_u32 m0, s33, 5760\n\t" \
  "s_nop 0\n\t" \
  "buffer_load_dwordx4 v186, s[16:19], s34 offen offset:384 nt lds\n\t" \
  "s_waitcnt vmcnt(11)\n\t" \
  "v_smfmac_f32_16x16x64_bf16 v[0:3], v[220:223], v[134:141], v174\n\t" \
  "s_add_u32 m0, s33, 7680\n\t" \
  "s_bitcmp1_b32 s24, 0\n\t" \
  "s_cmov_b32 m0, 0x30000\n\t" \
  "s_cselect_b32 s25, 0x40000000, s34\n\t" \
  "buffer_load_dwordx4 v186, s[16:19], s25 offen offset:512 nt lds\n\t" \
  "s_add_u32 s31, s31, 18432\n\t" \
  "s_sub_u32 s26, s31, 129024\n\t" \
  "s_min_u32 s31, s31, s26\n\t" \
  "s_add_u32 s33, s31, s32\n\t" \
  "s_add_u32 s34, s34, 4718592\n\t" \
  "s_waitcnt lgkmcnt(0)\n\t" \
  "v_smfmac_f32_16x16x64_bf16 v[104:107], v[252:255], v[126:133], v182\n\t" \
  "v_smfmac_f32_16x16x64_bf16 v[72:75], v[220:223], v[126:133], v174\n\t" \
  "s_barrier\n\t" \
  "s_mov_b32 s37, 36864\n\t" \
  "s_movk_i32 s30, 18432\n\t" \
  "s_mov_b32 s38, 0\n\t" \
  "s_waitcnt vmcnt(11)\n\t" \
  "v_smfmac_f32_16x16x64_bf16 v[4:7], v[224:227], v[134:141], v174 abid:1\n\t" \
  "s_mov_b32 s43, 0\n\t" \
  "s_mov_b32 s40, s35\n\t" \
  "s_add_u32 s46, s72, 0x4000\n\t" \
  "v_cvt_pk_bf16_f32 v32, v32, v33\n\t" \
  "v_cvt_pk_bf16_f32 v33, v34, v35\n\t" \
  "v_xor_b32 v34, s41, v183\n\t" \
  "v_smfmac_f32_16x16x64_bf16 v[76:79], v[224:227], v[126:133], v174 abid:1\n\t" \
  "ds_write_b64 v34, v[32:33] offset:16384\n\t" \
  "v_cvt_pk_bf16_f32 v104, v104, v105\n\t" \
  "v_cvt_pk_bf16_f32 v105, v106, v107\n\t" \
  "v_cvt_pk_bf16_f32 v0, v0, v1\n\t" \
  "v_cvt_pk_bf16_f32 v1, v2, v3\n\t" \
  "s_waitcnt vmcnt(10)\n\t" \
  "v_smfmac_f32_16x16x64_bf16 v[8:11], v[228:231], v[134:141], v175\n\t" \
  "ds_write_b64 v34, v[104:105] offset:34816\n\t" \
  "v_cvt_pk_bf16_f32 v2, v4, v5\n\t" \
  "v_cvt_pk_bf16_f32 v3, v6, v7\n\t" \
  "v_smfmac_f32_16x16x64_bf16 v[80:83], v[228:231], v[126:133], v175\n\t" \
  "ds_write_b128 v185, v[0:3] offset:0\n\t" \
  "v_xor_b32 v106, 64, v185\n\t" \
  "v_add_u32 v107, s44, v187\n\t" \
  "v_lshrrev_b32 v35, 4, v187\n\t" \
  "v_and_b32 v35, 48, v35\n\t" \
  "s_waitcnt vmcnt(9)\n\t" \
  "v_smfmac_f32_16x16x64_bf16 v[12:15], v[232:235], v[134:141], v175 abid:1\n\t" \
  "ds_read_b128 v[0:3], v183 offset:53248\n\t" \
  "ds_read_b128 v[4:7], v184 offset:53248\n\t" \
  "v_xor_b32 v34, v35, v187\n\t" \
  "v_and_b32 v34, 0xf0, v34\n\t" \
  "v_smfmac_f32_16x16x64_bf16 v[84:87], v[232:235], v[126:133], v175 abid:1\n\t" \
  "s_add_u32 m0, s33, 0\n\t" \
  "s_nop 0\n\t" \
  "buffer_load_dwordx4 v186, s[16:19], s34 offen offset:0 nt lds\n\t" \
  "s_movk_i32 s27, 72\n\t" \
  "v_mad_u32_u24 v34, v35, s27, v34\n\t" \
  "v_or_b32 v35, s45, v35\n\t" \
  "v_cvt_pk_bf16_f32 v8, v8, v9\n\t" \
  "v_cvt_pk_bf16_f32 v9, v10, v11\n\t" \
  "s_waitcnt vmcnt(9)\n\t" \
  "v_smfmac_f32_16x16x64_bf16 v[16:19], v[236:239], v[134:141], v176\n\t" \
  "s_max_u32 s42, s42, s72\n\t" \
  "s_sub_u32 s56, s42, s56\n\t" \
  "s_add_u32 s52, s52, s56\n\t" \
  "s_cmp_eq_u32 s55, 0\n\t" \
  "s_cselect_b32 s48, 7, 6\n\t" \
  "v_cvt_pk_bf16_f32 v10, v12, v13\n\t" \
  "v_cvt_pk_bf16_f32 v11, v14, v15\n\t" \
  "v_smfmac_f32_16x16x64_bf16 v[88:91], v[236:239], v[126:133], v176\n\t" \
  "ds_write_b128 v106, v[8:11] offset:0\n\t" \
  "v_cvt_pk_bf16_f32 v72, v72, v73\n\t" \
  "v_cvt_pk_bf16_f32 v73, v74, v75\n\t" \
  "v_cvt_pk_bf16_f32 v74, v76, v77\n\t" \
  "v_cvt_pk_bf16_f32 v75, v78, v79\n\t" \
  "s_waitcnt vmcnt(8)\n\t" \
  "v_smfmac_f32_16x16x64_bf16 v[20:23], v[240:243], v[134:141], v176 abid:1\n\t" \
  "ds_read_b128 v[8:11], v183 offset:51200\n\t" \
  "ds_read_b128 v[12:15], v184 offset:51200\n\t" \
  "v_smfmac_f32_16x16x64_bf16 v[92:95], v[240:243], v[126:133], v176 abid:1\n\t" \
  "ds_write_b128 v185, v[72:75] offset:18432\n\t" \
  "v_cvt_pk_bf16_f32 v16, v16, v17\n\t" \
  "v_cvt_pk_bf16_f32 v17, v18, v19\n\t" \
  "v_add_u32 v183, 55296, v183\n\t" \
  "v_add_u32 v184, 55296, v184\n\t" \
  "s_waitcnt vmcnt(7)\n\t" \
  "v_smfmac_f32_16x16x64_bf16 v[24:27], v[244:247], v[134:141], v177\n\t" \
  "s_add_u32 m0, s33, 1920\n\t" \
  "s_nop 0\n\t" \
  "buffer_load_dwordx4 v186, s[16:19], s34 offen offset:128 nt lds\n\t" \
  "v_cvt_pk_bf16_f32 v18, v20, v21\n\t" \
  "v_cvt_pk_bf16_f32 v19, v22, v23\n\t" \
  "v_smfmac_f32_16x16x64_bf16 v[96:99], v[244:247], v[126:133], v177\n\t" \
  "ds_write_b128 v185, v[16:19] offset:128\n\t" \
  "v_cvt_pk_bf16_f32 v80, v80, v81\n\t" \
  "v_cvt_pk_bf16_f32 v81, v82, v83\n\t" \
  "v_cvt_pk_bf16_f32 v82, v84, v85\n\t" \
  "v_cvt_pk_bf16_f32 v83, v86, v87\n\t" \
  "s_waitcnt vmcnt(7)\n\t" \
  "v_smfmac_f32_16x16x64_bf16 v[28:31], v[248:251], v[134:141], v177 abid:1\n\t" \
  "ds_write_b128 v106, v[80:83] offset:18432\n\t" \
  "v_smfmac_f32_16x16x64_bf16 v[100:103], v[248:251], v[126:133], v177 abid:1\n\t" \
  "v_lshrrev_b32 v32, 2, v187\n\t" \
  "s_cmp_gt_u32 s61, 2\n\t" \
  "v_add_u32 v32, s60, v32\n\t" \
  "s_cbranch_scc1 .Ln4%=\n\t" \
  ".Ln3%=:\n\t" \
  "ds_read2st64_b32 v[32:33], v32 offset0:4 offset1:9\n\t" \
  "v_cvt_pk_bf16_f32 v88, v88, v89\n\t" \
  "v_cvt_pk_bf16_f32 v89, v90, v91\n\t" \
  "s_waitcnt lgkmcnt(4)\n\t" \
  "v_smfmac_f32_16x16x64_bf16 v[36:39], a[252:255], v[8:15], v170\n\t" \
  "s_lshl_b32 s28, %[sBID], 3\n\t" \
  "s_memrealtime s[72:73]\n\t" \
  "ds_read_b128 v[72:75], v107 offset:0\n\t" \
  "v_cvt_pk_bf16_f32 v90, v92, v93\n\t" \
  "v_cvt_pk_bf16_f32 v91, v94, v95\n\t" \
  "v_smfmac_f32_16x16x64_bf16 v[40:43], v[188:191], v[8:15], v170 abid:1\n\t" \
  "ds_write_b128 v185, v[88:91] offset:18560\n\t" \
  "v_cvt_pk_bf16_f32 v24, v24, v25\n\t" \
  "v_cvt_pk_bf16_f32 v25, v26, v27\n\t" \
  "v_smfmac_f32_16x16x64_bf16 v[44:47], v[192:195], v[8:15], v171\n\t" \
  "s_cmp_lg_u32 s24, 1\n\t" \
  "s_cmov_b32 s28, 0x40000000\n\t" \
  "ds_read_b128 v[76:79], v107 offset:1024\n\t" \
  "v_cvt_pk_bf16_f32 v26, v28, v29\n\t" \
  "v_cvt_pk_bf16_f32 v27, v30, v31\n\t" \
  "v_smfmac_f32_16x16x64_bf16 v[48:51], v[196:199], v[8:15], v171 abid:1\n\t" \
  "ds_write_b128 v106, v[24:27] offset:128\n\t" \
  "v_cvt_pk_bf16_f32 v96, v96, v97\n\t" \
  "v_cvt_pk_bf16_f32 v97, v98, v99\n\t" \
  "v_smfmac_f32_16x16x64_bf16 v[52:55], v[200:203], v[8:15], v172\n\t" \
  "ds_read_b128 v[80:83], v107 offset:2048\n\t" \
  "v_cvt_pk_bf16_f32 v98, v100, v101\n\t" \
  "v_cvt_pk_bf16_f32 v99, v102, v103\n\t" \
  "v_smfmac_f32_16x16x64_bf16 v[56:59], v[204:207], v[8:15], v172 abid:1\n\t" \
  "ds_write_b128 v106, v[96:99] offset:18560\n\t" \
  "s_add_u32 m0, s33, 3840\n\t" \
  "s_nop 0\n\t" \
  "buffer_load_dwordx4 v186, s[16:19], s34 offen offset:256 nt lds\n\t" \
  "v_mov_b32 v126, s57\n\t" \
  "v_mov_b32 v127, s58\n\t" \
  "v_smfmac_f32_16x16x64_bf16 v[60:63], v[208:211], v[8:15], v173\n\t" \
  "ds_read_b128 v[84:87], v107 offset:3072\n\t" \
  "ds_read_b128 v[88:91], v107 offset:4096\n\t" \
  "s_add_u32 m0, s33, 5760\n\t" \
  "s_nop 0\n\t" \
  "buffer_load_dwordx4 v186, s[16:19], s34 offen offset:384 nt lds\n\t" \
  "v_smfmac_f32_16x16x64_bf16 v[64:67], v[212:215], v[8:15], v173 abid:1\n\t" \
  "ds_read_b128 v[92:95], v107 offset:5120\n\t" \
  "ds_read_b128 v[96:99], v107 offset:6144\n\t" \
  "s_add_u32 m0, s33, 7680\n\t" \
  "s_bitcmp1_b32 s24, 0\n\t" \
  "s_cmov_b32 m0, 0x30000\n\t" \
  "s_cselect_b32 s25, 0x40000000, s34\n\t" \
  "buffer_load_dwordx4 v186, s[16:19], s25 offen offset:512 nt lds\n\t" \
  "v_smfmac_f32_16x16x64_bf16 v[68:71], v[216:219], v[8:15], v181 abid:1\n\t" \
  "ds_read_b128 v[100:103], v107 offset:7168\n\t" \
  "ds_read_b128 v[104:107], v107 offset:8192\n\t" \
  "s_mov_b64 exec, 1\n\t" \
  "buffer_store_dwordx2 v[126:127], off, s[12:15], s28 offset:256 sc1\n\t" \
  "s_mov_b64 exec, -1\n\t" \
  "s_add_u32 s31, s31, 18432\n\t" \
  "s_sub_u32 s26, s31, 129024\n\t" \
  "s_min_u32 s31, s31, s26\n\t" \
  "s_add_u32 s33, s31, s32\n\t" \
  "s_add_u32 s34, s34, 4718592\n\t" \
  "s_or_b32 s25, s62, s63\n\t" \
  "s_cbranch_scc1 .Lq2%=\n\t" \
  ".Lq3%=:\n\t" \
  "s_setpc_b64 s[66:67]\n\t" \
  ".Lhop0%=:\n\t" \
  "s_cbranch_execz .Lhop4%=\n\t" \
  ".Lfin%=:\n\t" \
  "s_waitcnt lgkmcnt(0)\n\t" \
  "s_barrier\n\t" \
  "v_smfmac_f32_16x16x64_bf16 v[68:71], v[252:255], v[0:7], v182\n\t" \
  "s_and_b32 s25, s32, 1024\n\t" \
  "v_add_u32 v134, s25, v187\n\t" \
  "v_smfmac_f32_16x16x64_bf16 v[36:39], v[220:223], v[0:7], v174\n\t" \
  "v_add_u32 v24, s43, v134\n\t" \
  "ds_read_b128 v[24:27], v24 offset:16384\n\t" \
  ".Lhop1%=:\n\t" \
  "s_cbranch_execz .Lhop5%=\n\t" \
  "s_add_u32 s43, s43, 18432\n\t" \
  "s_sub_u32 s42, s43, 129024\n\t" \
  "s_min_u32 s43, s43, s42\n\t" \
  "v_smfmac_f32_16x16x64_bf16 v[40:43], v[224:227], v[0:7], v174 abid:1\n\t" \
  "v_add_u32 v28, s43, v134\n\t" \
  "ds_read_b128 v[28:31], v28 offset:16384\n\t" \
  "s_add_u32 s43, s43, 18432\n\t" \
  "s_sub_u32 s42, s43, 129024\n\t" \
  ".Lhop2%=:\n\t" \
  "s_cbranch_execz .Lhop6%=\n\t" \
  "s_min_u32 s43, s43, s42\n\t" \
  "v_smfmac_f32_16x16x64_bf16 v[44:47], v[228:231], v[0:7], v175\n\t" \
  "s_bitcmp0_b32 s24, 0\n\t" \
  "s_cmov_b32 s40, 0x40000000\n\t" \
  "v_xor_b32 v127, s41, v183\n\t" \
  "v_subrev_u32 v127, s30, v127\n\t" \
  "v_smfmac_f32_16x16x64_bf16 v[48:51], v[232:235], v[0:7], v175 abid:1\n\t" \
  "v_cvt_pk_bf16_f32 v68, v68, v69\n\t" \
  ".Lhop3%=:\n\t" \
  "s_cbranch_execz .Lhop7%=\n\t" \
  "v_cvt_pk_bf16_f32 v69, v70, v71\n\t" \
  "ds_write_b64 v127, v[68:69] offset:16384\n\t" \
  "v_add_u32 v126, s37, v185\n\t" \
  "v_xor_b32 v128, 64, v126\n\t" \
  "v_smfmac_f32_16x16x64_bf16 v[52:55], v[236:239], v[0:7], v176\n\t" \
  "v_smfmac_f32_16x16x64_bf16 v[56:59], v[240:243], v[0:7], v176 abid:1\n\t" \
  "v_cvt_pk_bf16_f32 v36, v36, v37\n\t" \
  ".Lhop4%=:\n\t" \
  "s_cbranch_execz .Lhop8%=\n\t" \
  "v_cvt_pk_bf16_f32 v37, v38, v39\n\t" \
  "v_cvt_pk_bf16_f32 v38, v40, v41\n\t" \
  "v_cvt_pk_bf16_f32 v39, v42, v43\n\t" \
  "ds_write_b128 v126, v[36:39] offset:0\n\t" \
  "v_smfmac_f32_16x16x64_bf16 v[60:63], v[244:247], v[0:7], v177\n\t" \
  "v_smfmac_f32_16x16x64_bf16 v[64:67], v[248:251], v[0:7], v177 abid:1\n\t" \
  "v_cvt_pk_bf16_f32 v44, v44, v45\n\t" \
  ".Lhop5%=:\n\t" \
  "s_cbranch_execz .Lhop9%=\n\t" \
  "v_cvt_pk_bf16_f32 v45, v46, v47\n\t" \
  "v_cvt_pk_bf16_f32 v46, v48, v49\n\t" \
  "v_cvt_pk_bf16_f32 v47, v50, v51\n\t" \
  "ds_write_b128 v128, v[44:47] offset:0\n\t" \
  "s_waitcnt lgkmcnt(4)\n\t" \
  "buffer_store_dwordx4 v[24:27], v186, s[20:23], s40 offen offset:384 sc1\n\t" \
  "s_add_u32 s38, s38, s49\n\t" \
  "s_add_u32 s40, s40, 4718592\n\t" \
  ".Lhop6%=:\n\t" \
  "s_cbranch_execz .Lhop10%=\n\t" \
  "v_cvt_pk_bf16_f32 v52, v52, v53\n\t" \
  "v_cvt_pk_bf16_f32 v53, v54, v55\n\t" \
  "v_cvt_pk_bf16_f32 v54, v56, v57\n\t" \
  "v_cvt_pk_bf16_f32 v55, v58, v59\n\t" \
  "ds_write_b128 v126, v[52:55] offset:128\n\t" \
  "v_add_u32 v135, s38, v187\n\t" \
  "v_xad_u32 v136, v187, 64, s38\n\t" \
  ".Lhop7%=:\n\t" \
  "s_cbranch_execz .Lhe%=\n\t" \
  "v_cvt_pk_bf16_f32 v60, v60, v61\n\t" \
  "v_cvt_pk_bf16_f32 v61, v62, v63\n\t" \
  "v_cvt_pk_bf16_f32 v62, v64, v65\n\t" \
  "v_cvt_pk_bf16_f32 v63, v66, v67\n\t" \
  "ds_write_b128 v128, v[60:63] offset:128\n\t" \
  "ds_read_b128 v[8:11], v135 offset:0\n\t" \
  "ds_read_b128 v[12:15], v136 offset:1024\n\t" \
  ".Lhop8%=:\n\t" \
  "s_cbranch_execz .Lmf%=\n\t" \
  "ds_read_b128 v[16:19], v135 offset:2048\n\t" \
  "ds_read_b128 v[20:23], v136 offset:3072\n\t" \
  "s_waitcnt lgkmcnt(9)\n\t" \
  "buffer_store_dwordx4 v[28:31], v186, s[20:23], s40 offen offset:384 sc1\n\t" \
  "s_add_u32 s40, s40, 4718592\n\t" \
  "v_add_u32 v24, s43, v134\n\t" \
  "s_add_u32 s36, s35, s39\n\t" \
  "s_waitcnt lgkmcnt(3)\n\t" \
  "buffer_store_dwordx4 v[8:11], v34, s[20:23], s36 offen sc1\n\t" \
  ".Lhop9%=:\n\t" \
  "s_cbranch_execz .Lhopy%=\n\t" \
  "s_barrier\n\t" \
  "ds_read_b128 v[24:27], v24 offset:16384\n\t" \
  "s_add_u32 s36, s36, 4608\n\t" \
  "s_waitcnt lgkmcnt(3)\n\t" \
  "buffer_store_dwordx4 v[12:15], v34, s[20:23], s36 offen sc1\n\t" \
  "s_add_u32 s36, s36, 4608\n\t" \
  "s_waitcnt lgkmcnt(2)\n\t" \
  "buffer_store_dwordx4 v[16:19], v34, s[20:23], s36 offen sc1\n\t" \
  "s_add_u32 s36, s36, 4608\n\t" \
  ".Lhop10%=:\n\t" \
  "s_cbranch_execz .Lhopy%=\n\t" \
  "s_waitcnt lgkmcnt(1)\n\t" \
  "buffer_store_dwordx4 v[20:23], v34, s[20:23], s36 offen sc1\n\t" \
  "s_waitcnt lgkmcnt(0)\n\t" \
  "buffer_store_dwordx4 v[24:27], v186, s[20:23], s40 offen offset:384 sc1\n\t" \
  "s_cmp_lg_u32 s63, 0\n\t" \
  "s_cbranch_scc1 .Lez%=\n\t" \
  ".Ley%=:\n\t" \
  "s_endpgm\n\t" \
  ".Lrc%=:\n\t" \
  "s_lshr_b32 s26, s55, 1\n\t" \
  "s_and_b32 s52, s55, 1\n\t" \
  "s_lshl2_add_u32 s52, s52, s24\n\t" \
  "s_cmp_eq_u32 s55, 8\n\t" \
  "s_cmov_b32 s26, s24\n\t" \
  "s_cmov_b32 s52, 8\n\t" \
  "s_mul_i32 s54, s26, 3\n\t" \
  "s_add_u32 s54, s54, s56\n\t" \
  "s_mul_i32 s51, s54, 9\n\t" \
  "s_add_u32 s51, s51, s52\n\t" \
  "s_lshl_b32 s51, s51, 7\n\t" \
  "s_add_u32 s51, s51, 4096\n\t" \
  "v_bfe_u32 v163, v0, 3, 3\n\t" \
  "s_movk_i32 s26, 1088\n\t" \
  "v_and_b32 v158, 4, v0\n\t" \
  "s_add_u32 s27, s41, 131584\n\t" \
  "v_lshl_add_u32 v158, v158, 7, s27\n\t" \
  "v_mad_u32_u24 v163, v163, s26, v158\n\t" \
  "v_and_b32 v158, 3, v0\n\t" \
  "v_lshl_add_u32 v163, v158, 4, v163\n\t" \
  "v_mov_b32 v160, 0\n\t" \
  "s_mul_i32 s27, s56, 6144\n\t" \
  "v_and_b32 v159, 60, v0\n\t" \
  "v_lshl_add_u32 v164, v158, 9, s27\n\t" \
  "v_lshl_add_u32 v164, v159, 1, v164\n\t" \
  "s_mul_i32 s28, s24, 9216\n\t" \
  "s_add_u32 s28, s28, 92160\n\t" \
  "v_lshl_add_u32 v165, v158, 6, s28\n\t" \
  "v_and_b32 v159, 48, v0\n\t" \
  "v_lshl_add_u32 v165, v159, 4, v165\n\t" \
  "v_and_b32 v159, 12, v0\n\t" \
  "v_add_u32 v165, v165, v159\n\t" \
  "v_add_u32 v153, s28, v187\n\t" \
  "s_add_u32 s28, s28, 3072\n\t" \
  "s_bitcmp0_b32 s24, 0\n\t" \
  "s_waitcnt vmcnt(6)\n\t" \
  "s_cbranch_scc1 .Lr1%=\n\t" \
  "s_waitcnt vmcnt(5)\n\t" \
  ".Lr1%=:\n\t" \
  "s_barrier\n\t" \
  "ds_read_b64 v[72:73], v163 offset:0\n\t" \
  "ds_read_b64 v[74:75], v163 offset:128\n\t" \
  "ds_read_b64 v[76:77], v163 offset:256\n\t" \
  "ds_read_b64 v[78:79], v163 offset:384\n\t" \
  "ds_read_b64 v[80:81], v163 offset:8704\n\t" \
  "ds_read_b64 v[82:83], v163 offset:8832\n\t" \
  "ds_read_b64 v[84:85], v163 offset:8960\n\t" \
  "ds_read_b64 v[86:87], v163 offset:9088\n\t" \
  "ds_read_b64 v[88:89], v163 offset:17408\n\t" \
  "ds_read_b64 v[90:91], v163 offset:17536\n\t" \
  "ds_read_b64 v[92:93], v163 offset:17664\n\t" \
  "ds_read_b64 v[94:95], v163 offset:17792\n\t" \
  "v_readfirstlane_b32 s9, v142\n\t" \
  "v_readfirstlane_b32 s58, v143\n\t" \
  "s_mul_i32 s57, s9, 0x9E3779B1\n\t" \
  "s_add_u32 s57, s57, 0x632BE5AB\n\t" \
  "s_and_b32 s57, s57, 0xffffff00\n\t" \
  "s_mul_i32 s70, s9, 0x85EBCA6B\n\t" \
  "s_xor_b32 s58, s58, s70\n\t" \
  "s_add_u32 s58, s58, 0x3C6EF372\n\t" \
  "s_mov_b32 s27, 0x80008000\n\t" \
  "s_waitcnt lgkmcnt(8)\n\t" \
  "v_and_b32 v167, s25, v72\n\t" \
  "v_and_b32 v175, s25, v73\n\t" \
  "v_and_b32 v168, s25, v74\n\t" \
  "v_and_b32 v176, s25, v75\n\t" \
  "v_and_b32 v169, s25, v76\n\t" \
  "v_and_b32 v177, s25, v77\n\t" \
  "v_and_b32 v170, s25, v78\n\t" \
  "v_and_b32 v178, s25, v79\n\t" \
  "v_pk_max_u16 v171, v167, v168\n\t" \
  "v_pk_max_u16 v179, v175, v176\n\t" \
  "v_pk_max_u16 v172, v169, v170\n\t" \
  "v_pk_max_u16 v180, v177, v178\n\t" \
  "v_pk_min_u16 v171, v171, v172\n\t" \
  "v_pk_min_u16 v179, v179, v180\n\t" \
  "v_pk_min_u16 v172, v167, v168\n\t" \
  "v_pk_min_u16 v180, v175, v176\n\t" \
  "v_pk_max_u16 v171, v171, v172\n\t" \
  "v_pk_max_u16 v179, v179, v180\n\t" \
  "v_pk_min_u16 v172, v169, v170\n\t" \
  "v_pk_min_u16 v180, v177, v178\n\t" \
  "v_pk_max_u16 v171, v171, v172\n\t" \
  "v_pk_max_u16 v179, v179, v180\n\t" \
  "v_pk_add_u16 v172, v168, -1 op_sel_hi:[1,0]\n\t" \
  "v_pk_add_u16 v180, v176, -1 op_sel_hi:[1,0]\n\t" \
  "v_pk_add_u16 v173, v169, -1 op_sel_hi:[1,0]\n\t" \
  "v_pk_add_u16 v181, v177, -1 op_sel_hi:[1,0]\n\t" \
  "v_pk_sub_i16 v167, v167, v171\n\t" \
  "v_pk_sub_i16 v175, v175, v179\n\t" \
  "v_pk_sub_i16 v168, v168, v171\n\t" \
  "v_pk_sub_i16 v176, v176, v179\n\t" \
  "v_pk_sub_i16 v169, v169, v171\n\t" \
  "v_pk_sub_i16 v177, v177, v179\n\t" \
  "v_pk_sub_i16 v170, v170, v171\n\t" \
  "v_pk_sub_i16 v178, v178, v179\n\t" \
  "v_and_or_b32 v171, v169, v170, v167\n\t" \
  "v_and_or_b32 v179, v177, v178, v175\n\t" \
  "v_and_or_b32 v174, v167, v168, v170\n\t" \
  "v_and_or_b32 v182, v175, v176, v178\n\t" \
  "v_or3_b32 v171, v171, v168, v172\n\t" \
  "v_or3_b32 v179, v179, v176, v180\n\t" \
  "v_or3_b32 v174, v174, v169, v173\n\t" \
  "v_or3_b32 v182, v182, v177, v181\n\t" \
  "v_lshrrev_b32 v160, 1, v160\n\t" \
  "v_bitop3_b32 v160, v160, v171, s27 bitop3:0xf2\n\t" \
  "v_lshrrev_b32 v160, 1, v160\n\t" \
  "v_bitop3_b32 v160, v160, v179, s27 bitop3:0xf2\n\t" \
  "v_lshrrev_b32 v160, 1, v160\n\t" \
  "v_bitop3_b32 v160, v160, v174, s27 bitop3:0xf2\n\t" \
  "v_lshrrev_b32 v160, 1, v160\n\t" \
  "v_bitop3_b32 v160, v160, v182, s27 bitop3:0xf2\n\t" \
  "s_waitcnt lgkmcnt(4)\n\t" \
  "v_and_b32 v167, s25, v80\n\t" \
  "v_and_b32 v175, s25, v81\n\t" \
  "v_and_b32 v168, s25, v82\n\t" \
  "v_and_b32 v176, s25, v83\n\t" \
  "v_and_b32 v169, s25, v84\n\t" \
  "v_and_b32 v177, s25, v85\n\t" \
  "v_and_b32 v170, s25, v86\n\t" \
  "v_and_b32 v178, s25, v87\n\t" \
  "v_pk_max_u16 v171, v167, v168\n\t" \
  "v_pk_max_u16 v179, v175, v176\n\t" \
  "v_pk_max_u16 v172, v169, v170\n\t" \
  "v_pk_max_u16 v180, v177, v178\n\t" \
  "v_pk_min_u16 v171, v171, v172\n\t" \
  "v_pk_min_u16 v179, v179, v180\n\t" \
  "v_pk_min_u16 v172, v167, v168\n\t" \
  "v_pk_min_u16 v180, v175, v176\n\t" \
  "v_pk_max_u16 v171, v171, v172\n\t" \
  "v_pk_max_u16 v179, v179, v180\n\t" \
  "v_pk_min_u16 v172, v169, v170\n\t" \
  "v_pk_min_u16 v180, v177, v178\n\t" \
  "v_pk_max_u16 v171, v171, v172\n\t" \
  "v_pk_max_u16 v179, v179, v180\n\t" \
  "v_pk_add_u16 v172, v168, -1 op_sel_hi:[1,0]\n\t" \
  "v_pk_add_u16 v180, v176, -1 op_sel_hi:[1,0]\n\t" \
  "v_pk_add_u16 v173, v169, -1 op_sel_hi:[1,0]\n\t" \
  "v_pk_add_u16 v181, v177, -1 op_sel_hi:[1,0]\n\t" \
  "v_pk_sub_i16 v167, v167, v171\n\t" \
  "v_pk_sub_i16 v175, v175, v179\n\t" \
  "v_pk_sub_i16 v168, v168, v171\n\t" \
  "v_pk_sub_i16 v176, v176, v179\n\t" \
  "v_pk_sub_i16 v169, v169, v171\n\t" \
  "v_pk_sub_i16 v177, v177, v179\n\t" \
  "v_pk_sub_i16 v170, v170, v171\n\t" \
  "v_pk_sub_i16 v178, v178, v179\n\t" \
  "v_and_or_b32 v171, v169, v170, v167\n\t" \
  "v_and_or_b32 v179, v177, v178, v175\n\t" \
  "v_and_or_b32 v174, v167, v168, v170\n\t" \
  "v_and_or_b32 v182, v175, v176, v178\n\t" \
  "v_or3_b32 v171, v171, v168, v172\n\t" \
  "v_or3_b32 v179, v179, v176, v180\n\t" \
  "v_or3_b32 v174, v174, v169, v173\n\t" \
  "v_or3_b32 v182, v182, v177, v181\n\t" \
  "v_lshrrev_b32 v160, 1, v160\n\t" \
  "v_bitop3_b32 v160, v160, v171, s27 bitop3:0xf2\n\t" \
  "v_lshrrev_b32 v160, 1, v160\n\t" \
  "v_bitop3_b32 v160, v160, v179, s27 bitop3:0xf2\n\t" \
  "v_lshrrev_b32 v160, 1, v160\n\t" \
  "v_bitop3_b32 v160, v160, v174, s27 bitop3:0xf2\n\t" \
  "v_lshrrev_b32 v160, 1, v160\n\t" \
  "v_bitop3_b32 v160, v160, v182, s27 bitop3:0xf2\n\t" \
  "s_waitcnt lgkmcnt(0)\n\t" \
  "v_and_b32 v167, s25, v88\n\t" \
  "v_and_b32 v175, s25, v89\n\t" \
  "v_and_b32 v168, s25, v90\n\t" \
  "v_and_b32 v176, s25, v91\n\t" \
  "v_and_b32 v169, s25, v92\n\t" \
  "v_and_b32 v177, s25, v93\n\t" \
  "v_and_b32 v170, s25, v94\n\t" \
  "v_and_b32 v178, s25, v95\n\t" \
  "v_pk_max_u16 v171, v167, v168\n\t" \
  "v_pk_max_u16 v179, v175, v176\n\t" \
  "v_pk_max_u16 v172, v169, v170\n\t" \
  "v_pk_max_u16 v180, v177, v178\n\t" \
  "v_pk_min_u16 v171, v171, v172\n\t" \
  "v_pk_min_u16 v179, v179, v180\n\t" \
  "v_pk_min_u16 v172, v167, v168\n\t" \
  "v_pk_min_u16 v180, v175, v176\n\t" \
  "v_pk_max_u16 v171, v171, v172\n\t" \
  "v_pk_max_u16 v179, v179, v180\n\t" \
  "v_pk_min_u16 v172, v169, v170\n\t" \
  "v_pk_min_u16 v180, v177, v178\n\t" \
  "v_pk_max_u16 v171, v171, v172\n\t" \
  "v_pk_max_u16 v179, v179, v180\n\t" \
  "v_pk_add_u16 v172, v168, -1 op_sel_hi:[1,0]\n\t" \
  "v_pk_add_u16 v180, v176, -1 op_sel_hi:[1,0]\n\t" \
  "v_pk_add_u16 v173, v169, -1 op_sel_hi:[1,0]\n\t" \
  "v_pk_add_u16 v181, v177, -1 op_sel_hi:[1,0]\n\t" \
  "v_pk_sub_i16 v167, v167, v171\n\t" \
  "v_pk_sub_i16 v175, v175, v179\n\t" \
  "v_pk_sub_i16 v168, v168, v171\n\t" \
  "v_pk_sub_i16 v176, v176, v179\n\t" \
  "v_pk_sub_i16 v169, v169, v171\n\t" \
  "v_pk_sub_i16 v177, v177, v179\n\t" \
  "v_pk_sub_i16 v170, v170, v171\n\t" \
  "v_pk_sub_i16 v178, v178, v179\n\t" \
  "v_and_or_b32 v171, v169, v170, v167\n\t" \
  "v_and_or_b32 v179, v177, v178, v175\n\t" \
  "v_and_or_b32 v174, v167, v168, v170\n\t" \
  "v_and_or_b32 v182, v175, v176, v178\n\t" \
  "v_or3_b32 v171, v171, v168, v172\n\t" \
  "v_or3_b32 v179, v179, v176, v180\n\t" \
  "v_or3_b32 v174, v174, v169, v173\n\t" \
  "v_or3_b32 v182, v182, v177, v181\n\t" \
  "v_lshrrev_b32 v160, 1, v160\n\t" \
  "v_bitop3_b32 v160, v160, v171, s27 bitop3:0xf2\n\t" \
  "v_lshrrev_b32 v160, 1, v160\n\t" \
  "v_bitop3_b32 v160, v160, v179, s27 bitop3:0xf2\n\t" \
  "v_lshrrev_b32 v160, 1, v160\n\t" \
  "v_bitop3_b32 v160, v160, v174, s27 bitop3:0xf2\n\t" \
  "v_lshrrev_b32 v160, 1, v160\n\t" \
  "v_bitop3_b32 v160, v160, v182, s27 bitop3:0xf2\n\t" \
  "v_lshrrev_b32 v160, 4, v160\n\t" \
  "v_bcnt_u32_b32 v161, v160, 0\n\t" \
  "v_mov_b32 v162, v161\n\t" \
  "s_nop 1\n\t" \
  "v_add_u32_dpp v162, v162, v162 row_shr:1 row_mask:0xf bank_mask:0xf bound_ctrl:0\n\t" \
  "s_nop 1\n\t" \
  "v_add_u32_dpp v162, v162, v162 row_shr:2 row_mask:0xf bank_mask:0xf bound_ctrl:0\n\t" \
  "s_nop 1\n\t" \
  "v_add_u32_dpp v162, v162, v162 row_shr:4 row_mask:0xf bank_mask:0xf bound_ctrl:0\n\t" \
  "s_nop 1\n\t" \
  "v_add_u32_dpp v162, v162, v162 row_shr:8 row_mask:0xf bank_mask:0xf bound_ctrl:0\n\t" \
  "s_nop 1\n\t" \
  "v_add_u32_dpp v162, v162, v162 row_bcast:15 row_mask:0xa bank_mask:0xf\n\t" \
  "s_nop 1\n\t" \
  "v_add_u32_dpp v162, v162, v162 row_bcast:31 row_mask:0xc bank_mask:0xf\n\t" \
  "s_mul_i32 s25, s54, 27\n\t" \
  "v_readlane_b32 s27, v162, 63\n\t" \
  "v_sub_u32 v162, v162, v161\n\t" \
  "s_movk_i32 s70, 8704\n\t" \
  "v_cmpx_ne_u32 vcc, 0, v160\n\t" \
  "s_cbranch_execz .Lr3%=\n\t" \
  ".Lr2%=:\n\t" \
  "v_ffbl_b32 v154, v160\n\t" \
  "v_add_u32 v158, -1, v160\n\t" \
  "v_and_b32 v160, v160, v158\n\t" \
  "v_lshrrev_b32 v155, 4, v154\n\t" \
  "v_and_b32 v157, 1, v154\n\t" \
  "v_lshl_add_u32 v157, v157, 1, v155\n\t" \
  "v_bfe_u32 v155, v154, 2, 2\n\t" \
  "v_and_b32 v156, 2, v154\n\t" \
  "v_mad_u32_u24 v158, v155, s70, v163\n\t" \
  "v_lshl_add_u32 v158, v156, 6, v158\n\t" \
  "v_lshl_add_u32 v158, v157, 1, v158\n\t" \
  "ds_read_u16 v166, v158 offset:128\n\t" \
  "v_lshl_add_u32 v158, v155, 11, v164\n\t" \
  "v_lshl_or_b32 v158, v157, 7, v158\n\t" \
  "v_or_b32 v158, v158, v156\n\t" \
  "v_lshl_add_u32 v159, v155, 10, v165\n\t" \
  "v_lshl_add_u32 v159, v157, 4, v159\n\t" \
  "v_add_u32 v159, v159, v156\n\t" \
  "v_min_u32 v154, 14, v162\n\t" \
  "v_lshl_add_u32 v154, v154, 2, s28\n\t" \
  "v_add_u32 v162, 1, v162\n\t" \
  "s_waitcnt lgkmcnt(0)\n\t" \
  "ds_write_b16 v159, v166\n\t" \
  "v_lshl_or_b32 v158, v166, 16, v158\n\t" \
  "ds_write_b32 v154, v158\n\t" \
  "v_cmpx_ne_u32 vcc, 0, v160\n\t" \
  "s_cbranch_execnz .Lr2%=\n\t" \
  ".Lr3%=:\n\t" \
  "s_mov_b64 exec, -1\n\t" \
  "v_lshrrev_b32 v158, 1, v187\n\t" \
  "v_add_u32 v158, s28, v158\n\t" \
  "ds_read_b64 v[154:155], v158\n\t" \
  "ds_read_b128 v[72:75], v153 offset:0\n\t" \
  "ds_read_b128 v[76:79], v153 offset:1024\n\t" \
  "ds_read_b128 v[80:83], v153 offset:2048\n\t" \
  "s_min_u32 s27, s27, 63\n\t" \
  "s_or_b32 s27, s27, s57\n\t" \
  "v_mov_b32 v156, s27\n\t" \
  "v_mov_b32 v157, s58\n\t" \
  "s_waitcnt lgkmcnt(3)\n\t" \
  "s_mov_b64 exec, 0xff\n\t" \
  "buffer_store_dwordx4 v[154:157], v187, s[12:15], s51 offen sc1\n\t" \
  "s_mov_b64 exec, -1\n\t" \
  "s_add_u32 s25, s25, s52\n\t" \
  "s_lshl_b32 s25, s25, 10\n\t" \
  "ds_write_b128 v153, v[204:207] offset:0\n\t" \
  "ds_write_b128 v153, v[204:207] offset:1024\n\t" \
  "ds_write_b128 v153, v[204:207] offset:2048\n\t" \
  "ds_write_b128 v153, v[204:207] offset:3072\n\t" \
  "s_add_u32 s25, s25, 408576\n\t" \
  "s_waitcnt lgkmcnt(6)\n\t" \
  "buffer_store_dwordx4 v[72:75], v187, s[12:15], s25 offen sc1\n\t" \
  "s_add_u32 s25, s25, 9216\n\t" \
  "s_waitcnt lgkmcnt(5)\n\t" \
  "buffer_store_dwordx4 v[76:79], v187, s[12:15], s25 offen sc1\n\t" \
  "s_add_u32 s25, s25, 9216\n\t" \
  "s_waitcnt lgkmcnt(4)\n\t" \
  "buffer_store_dwordx4 v[80:83], v187, s[12:15], s25 offen sc1\n\t" \
  "s_mov_b32 s61, 1\n\t" \
  "s_call_b64 s[74:75], .Lxs%=\n\t" \
  "s_branch .Le6%=\n\t" \
  ".Lp5%=:\n\t" \
  "s_lshl_b32 s25, s25, 3\n\t" \
  "v_mov_b32 v108, s57\n\t" \
  "v_mov_b32 v109, s58\n\t" \
  "s_mov_b64 exec, 1\n\t" \
  "buffer_store_dwordx2 v[108:109], off, s[12:15], s25 offset:2304 sc1\n\t" \
  "s_mov_b64 exec, -1\n\t" \
  "s_branch .Lp6%=\n\t" \
  ".Lrr%=:\n\t" \
  "s_sub_u32 s50, s50, 1\n\t" \
  "s_cbranch_scc1 .Lrg%=\n\t" \
  "s_sleep 2\n\t" \
  "buffer_load_dwordx4 v[188:191], v203, s[12:15], s36 offen offset:0 sc1\n\t" \
  "buffer_load_dwordx4 v[192:195], v203, s[12:15], s36 offen offset:1152 sc1\n\t" \
  "buffer_load_dwordx4 v[196:199], v203, s[12:15], s36 offen offset:2304 sc1\n\t" \
  "s_waitcnt vmcnt(0)\n\t" \
  "s_branch .Lrk%=\n\t" \
  ".Lss%=:\n\t" \
  "s_movk_i32 s50, 0x7fff\n\t" \
  "v_cmp_lt_u32 vcc, 16, v206\n\t" \
  "s_or_b64 exec, s[10:11], vcc\n\t" \
  "s_mov_b64 s[10:11], exec\n\t" \
  "v_mov_b32 v248, 0\n\t" \
  "v_mov_b32 v249, 0\n\t" \
  "v_mov_b32 v250, 0\n\t" \
  "s_mov_b64 exec, -1\n\t" \
  "s_mov_b64 s[4:5], 0\n\t" \
  "s_mov_b32 s61, 0\n\t" \
  "s_movk_i32 s25, 56\n\t" \
  ".Lsc%=:\n\t" \
  "s_bitcmp1_b64 s[10:11], s25\n\t" \
  "s_addc_u32 s62, s62, s62\n\t" \
  "s_sub_u32 s25, s25, 7\n\t" \
  "s_cbranch_scc0 .Lsc%=\n\t" \
  "v_lshrrev_b32 v204, 1, v187\n\t" \
  "v_min_u32 v204, 0xd0, v204\n\t" \
  ".Lsp%=:\n\t" \
  "buffer_load_dwordx2 v[206:207], v204, s[12:15], 0 offen offset:2304 sc1\n\t" \
  "s_waitcnt vmcnt(0)\n\t" \
  "v_xor_b32 v206, s57, v206\n\t" \
  "v_bitop3_b32 v206, v206, s58, v207 bitop3:0xf6\n\t" \
  "v_cmp_ne_u32 vcc, 0, v206\n\t" \
  "s_cbranch_vccz .Lsg%=\n\t" \
  "s_sleep 4\n\t" \
  "s_sub_u32 s50, s50, 1\n\t" \
  "s_cbranch_scc0 .Lsp%=\n\t" \
  "s_branch .Lsg%=\n\t" \
  ".Ln4%=:\n\t" \
  "ds_read_b32 v35, v32 offset:3584\n\t" \
  "s_branch .Ln3%=\n\t" \
  ".Lmf%=:\n\t" \
  "s_cbranch_execz .Lmw1%=\n\t" \
  "s_mov_b32 s27, 1\n\t" \
  ".Lml%=:\n\t" \
  "s_ff1_i32_b64 s25, s[4:5]\n\t" \
  "s_bitset0_b64 s[4:5], s25\n\t" \
  "s_max_i32 s25, s25, 0\n\t" \
  "s_mul_i32 s26, s25, 37\n\t" \
  "s_lshr_b32 s26, s26, 8\n\t" \
  "s_mul_i32 s70, s26, -3\n\t" \
  "s_add_u32 s70, s70, s25\n\t" \
  "s_mul_i32 s71, s26, -5\n\t" \
  "s_add_u32 s71, s71, s25\n\t" \
  "s_lshl_b32 s71, s71, 9\n\t" \
  "s_add_u32 s71, s71, s44\n\t" \
  "s_sub_u32 s71, s71, 36864\n\t" \
  "v_lshl_add_u32 v244, s70, 4, v246\n\t" \
  "ds_read_b32 v244, v244 offset:32\n\t" \
  "s_mov_b32 exec_hi, 0\n\t" \
  "v_add_u32 v240, s71, v187\n\t" \
  "ds_read_b128 v[240:243], v240\n\t" \
  "s_mov_b32 exec_hi, -1\n\t" \
  "s_lshl_b32 s26, s26, 2\n\t" \
  ".Lmw1%=:\n\t" \
  "s_cbranch_execz .Lmw2%=\n\t" \
  "s_cmp_eq_u32 s27, 1\n\t" \
  "s_cmov_b32 s7, s26\n\t" \
  "s_cmp_eq_u32 s27, 2\n\t" \
  "s_cmov_b32 s8, s26\n\t" \
  "v_add_u32 v236, 1280, v236\n\t" \
  "v_add_u32 v245, 1280, v245\n\t" \
  "s_waitcnt lgkmcnt(1)\n\t" \
  "v_bitop3_b32 v244, v244, s43, v247 bitop3:0x6a\n\t" \
  "v_add_u32_sdwa v248, 0, v244 dst_sel:DWORD dst_unused:UNUSED_PAD src0_sel:DWORD src1_sel:WORD_0\n\t" \
  "v_add_u32_sdwa v249, 0, v244 dst_sel:DWORD dst_unused:UNUSED_PAD src0_sel:DWORD src1_sel:WORD_1\n\t" \
  ".Lmw2%=:\n\t" \
  "s_cbranch_execz .Lmw3%=\n\t" \
  "ds_read_b64 v[220:221], v248 offset:0\n\t" \
  "ds_read_b64 v[222:223], v249 offset:0\n\t" \
  "ds_read_b64 v[224:225], v248 offset:0\n\t" \
  "ds_read_b64 v[226:227], v249 offset:0\n\t" \
  "ds_read_b64 v[228:229], v248 offset:18432\n\t" \
  "ds_read_b64 v[230:231], v249 offset:18432\n\t" \
  "ds_read_b64 v[232:233], v248 offset:18432\n\t" \
  "ds_read_b64 v[234:235], v249 offset:18432\n\t" \
  "ds_read_b64 v[212:213], v248 offset:36864\n\t" \
  "ds_read_b64 v[214:215], v249 offset:36864\n\t" \
  "ds_read_b64 v[216:217], v248 offset:36864\n\t" \
  "ds_read_b64 v[218:219], v249 offset:36864\n\t" \
  ".Lmw3%=:\n\t" \
  "s_cbranch_execz .Lhopy%=\n\t" \
  "ds_write_b32 v245, v244 offset:1024\n\t" \
  "s_waitcnt lgkmcnt(1)\n\t" \
  "ds_write_b128 v236, v[240:243]\n\t" \
  "s_set_gpr_idx_on s26, gpr_idx(DST)\n\t" \
  "v_smfmac_f32_16x16x64_bf16 v[0:3], v[240:243], v[220:227], v182 abid:1\n\t" \
  "v_smfmac_f32_16x16x64_bf16 v[72:75], v[240:243], v[228:235], v182 abid:1\n\t" \
  "v_smfmac_f32_16x16x64_bf16 v[36:39], v[240:243], v[212:219], v182 abid:1\n\t" \
  "s_set_gpr_idx_off\n\t" \
  "s_add_u32 s27, s27, 1\n\t" \
  "s_cmp_lt_u32 s27, 4\n\t" \
  "s_cbranch_scc1 .Lmn%=\n\t" \
  "s_lshl_b32 s55, s55, 8\n\t" \
  "s_or_b32 s55, s55, s26\n\t" \
  "s_bitset1_b32 s62, 16\n\t" \
  ".Lmn%=:\n\t" \
  "s_cmp_lt_u32 s27, s61\n\t" \
  "s_cbranch_scc1 .Lml%=\n\t" \
  "s_branch .Lmg%=\n\t" \
  ".Lsl%=:\n\t" \
  "s_mov_b32 s36, 0\n\t" \
  "s_mov_b32 s11, 0\n\t" \
  ".Lsa%=:\n\t" \
  "s_waitcnt lgkmcnt(0)\n\t" \
  "s_mov_b32 s25, 0\n\t" \
  ".Lsaj%=:\n\t" \
  "s_bitcmp0_b32 s62, s25\n\t" \
  "s_cbranch_scc1 .Lsan%=\n\t" \
  "s_mul_i32 s26, s24, 81\n\t" \
  "s_add_u32 s26, s26, s25\n\t" \
  "s_lshl_b32 s26, s26, 10\n\t" \
  "s_add_u32 s26, s26, 408576\n\t" \
  "v_add_u32 v228, s36, v183\n\t" \
  "v_add_u32 v229, s36, v184\n\t" \
  "s_lshl2_add_u32 s28, s25, s11\n\t" \
  "s_movk_i32 s27, 9\n\t" \
  ".Lsak%=:\n\t" \
  "buffer_load_dwordx4 v[240:243], v187, s[12:15], s26 offen sc1\n\t" \
  "ds_read_b128 v[220:223], v228\n\t" \
  "ds_read_b128 v[224:227], v229\n\t" \
  "v_add_u32 v228, 0x800, v228\n\t" \
  "v_add_u32 v229, 0x800, v229\n\t" \
  "s_add_u32 s26, s26, 9216\n\t" \
  "s_waitcnt vmcnt(0) lgkmcnt(0)\n\t" \
  "s_set_gpr_idx_on s28, gpr_idx(DST)\n\t" \
  "v_smfmac_f32_16x16x64_bf16 v[0:3], v[240:243], v[220:227], v182 abid:1\n\t" \
  "s_set_gpr_idx_off\n\t" \
  "s_sub_u32 s27, s27, 1\n\t" \
  "s_cmp_lg_u32 s27, 0\n\t" \
  "s_cbranch_scc1 .Lsak%=\n\t" \
  ".Lsan%=:\n\t" \
  "s_add_u32 s25, s25, 1\n\t" \
  "s_cmp_lt_u32 s25, 9\n\t" \
  "s_cbranch_scc1 .Lsaj%=\n\t" \
  "s_nop 7\n\t" \
  "s_cmp_eq_u32 s11, 36\n\t" \
  "s_cbranch_scc1 .Lsh%=\n\t" \
  "s_add_u32 s36, s36, 18432\n\t" \
  "s_cmp_eq_u32 s11, 0\n\t" \
  "s_cselect_b32 s11, 72, 36\n\t" \
  "s_branch .Lsa%=\n\t" \
  ".Lq2%=:\n\t" \
  "s_cmp_lg_u32 s62, 0\n\t" \
  "s_cbranch_scc1 .Lq4%=\n\t" \
  "s_mov_b32 s64, 0xc9ccc9cb\n\t" \
  "s_mov_b32 s65, 0x44cccc9\n\t" \
  "s_mov_b32 s55, 44\n\t" \
  "s_mov_b32 s48, 7\n\t" \
  "s_branch .Lq3%=\n\t" \
  ".Lq4%=:\n\t" \
  "s_mov_b32 s48, 7\n\t" \
  "s_mov_b32 s4, 0xcccccccb\n\t" \
  "s_mov_b32 s5, 0x44cc\n\t" \
  "s_mov_b32 s10, s68\n\t" \
  "s_mov_b64 s[64:65], 0\n\t" \
  "s_getpc_b64 s[68:69]\n\t" \
  ".Lq5%=:\n\t" \
  "s_add_u32 s68, s68, .Lhs%=-.Lq5%=\n\t" \
  "s_branch .Lq3%=\n\t" \
  ".Lhs%=:\n\t" \
  "s_bitcmp1_b32 s62, 16\n\t" \
  "s_cbranch_scc1 .Lhm%=\n\t" \
  "s_sub_u32 s36, 0, s30\n\t" \
  "s_mov_b32 s11, 36\n\t" \
  ".Lsb%=:\n\t" \
  "s_waitcnt lgkmcnt(0)\n\t" \
  "s_mov_b32 s25, 0\n\t" \
  ".Lsbj%=:\n\t" \
  "s_bitcmp0_b32 s62, s25\n\t" \
  "s_cbranch_scc1 .Lsbn%=\n\t" \
  "s_mul_i32 s26, s24, 81\n\t" \
  "s_add_u32 s26, s26, s25\n\t" \
  "s_lshl_b32 s26, s26, 10\n\t" \
  "s_add_u32 s26, s26, 408576\n\t" \
  "v_add_u32 v16, s36, v183\n\t" \
  "v_add_u32 v17, s36, v184\n\t" \
  "s_lshl2_add_u32 s28, s25, s11\n\t" \
  "s_movk_i32 s27, 9\n\t" \
  ".Lsbk%=:\n\t" \
  "buffer_load_dwordx4 v[28:31], v187, s[12:15], s26 offen sc1\n\t" \
  "ds_read_b128 v[8:11], v16\n\t" \
  "ds_read_b128 v[12:15], v17\n\t" \
  "v_add_u32 v16, 0x800, v16\n\t" \
  "v_add_u32 v17, 0x800, v17\n\t" \
  "s_add_u32 s26, s26, 9216\n\t" \
  "s_waitcnt vmcnt(0) lgkmcnt(0)\n\t" \
  "s_set_gpr_idx_on s28, gpr_idx(DST)\n\t" \
  "v_smfmac_f32_16x16x64_bf16 v[0:3], v[28:31], v[8:15], v182 abid:1\n\t" \
  "s_set_gpr_idx_off\n\t" \
  "s_sub_u32 s27, s27, 1\n\t" \
  "s_cmp_lg_u32 s27, 0\n\t" \
  "s_cbranch_scc1 .Lsbk%=\n\t" \
  ".Lsbn%=:\n\t" \
  "s_add_u32 s25, s25, 1\n\t" \
  "s_cmp_lt_u32 s25, 9\n\t" \
  "s_cbranch_scc1 .Lsbj%=\n\t" \
  "s_nop 7\n\t" \
  ".Lht%=:\n\t" \
  "s_and_b32 s27, s4, 7\n\t" \
  "s_lshl2_add_u32 s66, s27, s10\n\t" \
  "s_and_b32 s27, s4, 8\n\t" \
  "s_mul_i32 s27, s27, 176\n\t" \
  "s_add_u32 s66, s66, s27\n\t" \
  "s_lshr_b64 s[4:5], s[4:5], 4\n\t" \
  "s_setpc_b64 s[66:67]\n\t" \
  ".Lhm%=:\n\t" \
  "s_mov_b32 s25, s61\n\t" \
  "s_mov_b32 s26, s55\n\t" \
  ".Lhn%=:\n\t" \
  "s_sub_u32 s25, s25, 1\n\t" \
  "s_mul_i32 s27, s25, 1280\n\t" \
  "s_add_u32 s27, s27, s60\n\t" \
  "v_lshrrev_b32 v16, 2, v187\n\t" \
  "v_add_u32 v16, s27, v16\n\t" \
  "v_add_u32 v28, s27, v187\n\t" \
  "ds_read_b32 v16, v16 offset:1024\n\t" \
  "ds_read_b128 v[28:31], v28\n\t" \
  "s_and_b32 s27, s26, 0xff\n\t" \
  "s_lshr_b32 s26, s26, 8\n\t" \
  "s_waitcnt lgkmcnt(1)\n\t" \
  "v_add_u32_sdwa v8, s37, v16 dst_sel:DWORD dst_unused:UNUSED_PAD src0_sel:DWORD src1_sel:WORD_0\n\t" \
  "v_add_u32_sdwa v10, s37, v16 dst_sel:DWORD dst_unused:UNUSED_PAD src0_sel:DWORD src1_sel:WORD_1\n\t" \
  "ds_read_b64 v[8:9], v8 offset:0\n\t" \
  "ds_read_b64 v[10:11], v10 offset:0\n\t" \
  "s_waitcnt lgkmcnt(0)\n\t" \
  "s_set_gpr_idx_on s27, gpr_idx(DST)\n\t" \
  "v_smfmac_f32_16x16x64_bf16 v[36:39], v[28:31], v[8:15], v182 abid:1\n\t" \
  "s_set_gpr_idx_off\n\t" \
  "s_cmp_gt_u32 s25, 3\n\t" \
  "s_cbranch_scc1 .Lhn%=\n\t" \
  "s_branch .Lht%=\n\t" \
  ".Lhe%=:\n\t" \
  "s_cbranch_execz .Lhh%=\n\t" \
  "s_and_b32 s61, s55, 7\n\t" \
  "s_lshr_b32 s55, s55, 3\n\t" \
  "s_cmp_lg_u32 s61, 0\n\t" \
  "s_cbranch_scc1 .Lh0%=\n\t" \
  "v_lshlrev_b32 v16, 1, v187\n\t" \
  "s_mov_b32 m0, 131584\n\t" \
  "s_mov_b32 s69, s67\n\t" \
  "global_load_lds_dword v16, s[68:69]\n\t" \
  ".Lh3%=:\n\t" \
  "s_and_b32 s27, s64, 7\n\t" \
  "s_lshl2_add_u32 s66, s27, s68\n\t" \
  "s_and_b32 s27, s64, 8\n\t" \
  "s_mul_i32 s27, s27, 176\n\t" \
  "s_add_u32 s66, s66, s27\n\t" \
  "s_lshr_b64 s[64:65], s[64:65], 4\n\t" \
  "s_setpc_b64 s[66:67]\n\t" \
  ".Lhh%=:\n\t" \
  "s_cbranch_execz .Lhi%=\n\t" \
  ".Lh0%=:\n\t" \
  "s_cmp_lg_u32 s61, 1\n\t" \
  "s_cbranch_scc1 .Lh1%=\n\t" \
  "s_call_b64 s[74:75], .Lxs%=\n\t" \
  "s_branch .Lh3%=\n\t" \
  ".Lh1%=:\n\t" \
  "s_cmp_lg_u32 s61, 2\n\t" \
  "s_cbranch_scc1 .Lh5%=\n\t" \
  "s_add_u32 s25, s38, s49\n\t" \
  "v_add_u32 v24, s25, v187\n\t" \
  "v_xad_u32 v25, v187, 64, s25\n\t" \
  "ds_read_b128 v[8:11], v24 offset:0\n\t" \
  "ds_read_b128 v[12:15], v25 offset:1024\n\t" \
  "ds_read_b128 v[16:19], v24 offset:2048\n\t" \
  "ds_read_b128 v[20:23], v25 offset:3072\n\t" \
  ".Lhi%=:\n\t" \
  "s_cbranch_execz .Lhj%=\n\t" \
  "s_add_u32 s38, s38, 18432\n\t" \
  "s_sub_u32 s44, s38, 129024\n\t" \
  "s_min_u32 s38, s38, s44\n\t" \
  "s_add_u32 s36, s35, s39\n\t" \
  "s_waitcnt lgkmcnt(3)\n\t" \
  "buffer_store_dwordx4 v[8:11], v34, s[20:23], s36 offen sc1\n\t" \
  "s_add_u32 s36, s36, 4608\n\t" \
  "s_waitcnt lgkmcnt(2)\n\t" \
  "buffer_store_dwordx4 v[12:15], v34, s[20:23], s36 offen sc1\n\t" \
  "s_add_u32 s36, s36, 4608\n\t" \
  "s_waitcnt lgkmcnt(1)\n\t" \
  "buffer_store_dwordx4 v[16:19], v34, s[20:23], s36 offen sc1\n\t" \
  "s_add_u32 s36, s36, 4608\n\t" \
  "s_waitcnt lgkmcnt(0)\n\t" \
  "buffer_store_dwordx4 v[20:23], v34, s[20:23], s36 offen sc1\n\t" \
  ".Lhj%=:\n\t" \
  "s_cbranch_execz .Lhopy%=\n\t" \
  "s_add_u32 s35, s35, 4718592\n\t" \
  "s_branch .Lh3%=\n\t" \
  ".Lh5%=:\n\t" \
  "s_cmp_eq_u32 s61, 5\n\t" \
  "s_cbranch_scc1 .Lh2%=\n\t" \
  "s_mov_b32 m0, 132352\n\t" \
  "s_nop 0\n\t" \
  "buffer_load_dwordx4 v187, s[12:15], 0 offen offset:256 sc1 lds\n\t" \
  "buffer_load_dwordx4 v187, s[12:15], 0 offen offset:1280 sc1 lds\n\t" \
  "s_branch .Lh3%=\n\t" \
  ".Lh2%=:\n\t" \
  "v_add_u32 v16, 132608, v187\n\t" \
  "ds_read_b128 v[8:11], v16\n\t" \
  "ds_read_b128 v[12:15], v16 offset:1024\n\t" \
  "s_waitcnt lgkmcnt(0)\n\t" \
  "v_xor_b32 v8, s57, v8\n\t" \
  "v_bitop3_b32 v8, v8, s58, v9 bitop3:0xf6\n\t" \
  "v_bitop3_b32 v8, v8, s57, v10 bitop3:0xf6\n\t" \
  "v_bitop3_b32 v8, v8, s58, v11 bitop3:0xf6\n\t" \
  "v_bitop3_b32 v8, v8, s57, v12 bitop3:0xf6\n\t" \
  "v_bitop3_b32 v8, v8, s58, v13 bitop3:0xf6\n\t" \
  "v_bitop3_b32 v8, v8, s57, v14 bitop3:0xf6\n\t" \
  "v_bitop3_b32 v8, v8, s58, v15 bitop3:0xf6\n\t" \
  "v_cmp_ne_u32 vcc, 0, v8\n\t" \
  "s_add_u32 s25, s9, 1\n\t" \
  "v_mov_b32 v8, s25\n\t" \
  "s_cbranch_vccnz .Lh4%=\n\t" \
  "s_mov_b64 exec, 1\n\t" \
  "buffer_store_dword v8, off, s[12:15], 0 sc1\n\t" \
  "s_mov_b64 exec, -1\n\t" \
  "s_mov_b32 s63, 0\n\t" \
  ".Lh4%=:\n\t" \
  "s_branch .Lh3%=\n\t" \
  ".Lez%=:\n\t" \
  "s_mov_b32 s26, 0x7fff\n\t" \
  ".Lex%=:\n\t" \
  "buffer_load_dwordx4 v[126:129], v187, s[12:15], 0 offen offset:256 sc1\n\t" \
  "buffer_load_dwordx4 v[130:133], v187, s[12:15], 0 offen offset:1280 sc1\n\t" \
  "s_waitcnt vmcnt(0)\n\t" \
  "v_xor_b32 v126, s57, v126\n\t" \
  "v_bitop3_b32 v126, v126, s58, v127 bitop3:0xf6\n\t" \
  "v_bitop3_b32 v126, v126, s57, v128 bitop3:0xf6\n\t" \
  "v_bitop3_b32 v126, v126, s58, v129 bitop3:0xf6\n\t" \
  "v_bitop3_b32 v126, v126, s57, v130 bitop3:0xf6\n\t" \
  "v_bitop3_b32 v126, v126, s58, v131 bitop3:0xf6\n\t" \
  "v_bitop3_b32 v126, v126, s57, v132 bitop3:0xf6\n\t" \
  "v_bitop3_b32 v126, v126, s58, v133 bitop3:0xf6\n\t" \
  "v_cmp_ne_u32 vcc, 0, v126\n\t" \
  "s_add_u32 s25, s9, 1\n\t" \
  "v_mov_b32 v126, s25\n\t" \
  "s_cbranch_vccnz .Lew%=\n\t" \
  "s_mov_b64 exec, 1\n\t" \
  "buffer_store_dword v126, off, s[12:15], 0 sc1\n\t" \
  "s_mov_b64 exec, -1\n\t" \
  "s_mov_b32 s63, 0\n\t" \
  ".Lew%=:\n\t" \
  "s_cmp_eq_u32 s63, 0\n\t" \
  "s_cbranch_scc1 .Ley%=\n\t" \
  "s_sleep 8\n\t" \
  "s_sub_u32 s26, s26, 1\n\t" \
  "s_cbranch_scc0 .Lex%=\n\t" \
  "s_branch .Ley%=\n\t" \
  ".p2align 6\n\t" \
  ".Lvar%=:\n\t" \
  ".fill (.Lvar%=+0-.)/4, 4, 0xbf800000\n\t" \
  "s_branch .Lfin%=\n\t" \
  "s_branch .Lhe%=\n\t" \
  "s_waitcnt vmcnt(6)\n\t" \
  "s_waitcnt vmcnt(10)\n\t" \
  "s_waitcnt vmcnt(12)\n\t" \
  "s_waitcnt vmcnt(17)\n\t" \
  "s_waitcnt vmcnt(21) lgkmcnt(0)\n\t" \
  "s_barrier\n\t" \
  "v_smfmac_f32_16x16x64_bf16 v[36:39], v[220:223], v[0:7], v174\n\t" \
  "ds_read_b128 v[126:129], v183 offset:0\n\t" \
  "v_add_u32 v24, s37, v185\n\t" \
  "v_smfmac_f32_16x16x64_bf16 v[40:43], v[224:227], v[0:7], v174 abid:1\n\t" \
  "ds_read_b128 v[130:133], v184 offset:0\n\t" \
  "v_xor_b32 v25, s41, v183\n\t" \
  "v_smfmac_f32_16x16x64_bf16 v[44:47], v[228:231], v[0:7], v175\n\t" \
  "ds_read_b128 v[134:137], v183 offset:2048\n\t" \
  "v_subrev_u32 v25, s30, v25\n\t" \
  "v_smfmac_f32_16x16x64_bf16 v[48:51], v[232:235], v[0:7], v175 abid:1\n\t" \
  "s_add_u32 s30, s29, 18432\n\t" \
  "ds_read_b128 v[138:141], v184 offset:2048\n\t" \
  "v_cvt_pk_bf16_f32 v8, v36, v37\n\t" \
  "v_cvt_pk_bf16_f32 v9, v38, v39\n\t" \
  "v_smfmac_f32_16x16x64_bf16 v[52:55], v[236:239], v[0:7], v176\n\t" \
  "ds_read_b128 v[36:39], v35 offset:0\n\t" \
  "v_cvt_pk_bf16_f32 v10, v40, v41\n\t" \
  "v_cvt_pk_bf16_f32 v11, v42, v43\n\t" \
  "v_smfmac_f32_16x16x64_bf16 v[56:59], v[240:243], v[0:7], v176 abid:1\n\t" \
  "s_sub_u32 s25, s30, 129024\n\t" \
  "ds_read_b128 v[40:43], v35 offset:64\n\t" \
  "v_cvt_pk_bf16_f32 v16, v44, v45\n\t" \
  "v_cvt_pk_bf16_f32 v17, v46, v47\n\t" \
  "v_smfmac_f32_16x16x64_bf16 v[60:63], v[244:247], v[0:7], v177\n\t" \
  "ds_read_b128 v[44:47], v35 offset:128\n\t" \
  "v_cvt_pk_bf16_f32 v18, v48, v49\n\t" \
  "v_cvt_pk_bf16_f32 v19, v50, v51\n\t" \
  "v_smfmac_f32_16x16x64_bf16 v[64:67], v[248:251], v[0:7], v177 abid:1\n\t" \
  "s_min_u32 s30, s30, s25\n\t" \
  "ds_read_b128 v[48:51], v35 offset:192\n\t" \
  "v_cvt_pk_bf16_f32 v12, v52, v53\n\t" \
  "v_cvt_pk_bf16_f32 v13, v54, v55\n\t" \
  "v_smfmac_f32_16x16x64_bf16 v[68:71], v[252:255], v[0:7], v182\n\t" \
  "s_waitcnt lgkmcnt(3)\n\t" \
  "ds_read_b128 v[52:55], v35 offset:256\n\t" \
  "v_cvt_pk_bf16_f32 v14, v56, v57\n\t" \
  "v_cvt_pk_bf16_f32 v15, v58, v59\n\t" \
  "v_smfmac_f32_16x16x64_bf16 v[36:39], a[0:3], v[126:133], v142\n\t" \
  "s_waitcnt lgkmcnt(3)\n\t" \
  "ds_read_b128 v[56:59], v35 offset:320\n\t" \
  "v_cvt_pk_bf16_f32 v20, v60, v61\n\t" \
  "v_cvt_pk_bf16_f32 v21, v62, v63\n\t" \
  "v_smfmac_f32_16x16x64_bf16 v[40:43], a[4:7], v[126:133], v142 abid:1\n\t" \
  "s_waitcnt lgkmcnt(3)\n\t" \
  "ds_read_b128 v[60:63], v35 offset:384\n\t" \
  "v_cvt_pk_bf16_f32 v22, v64, v65\n\t" \
  "v_cvt_pk_bf16_f32 v23, v66, v67\n\t" \
  "v_smfmac_f32_16x16x64_bf16 v[44:47], a[8:11], v[126:133], v143\n\t" \
  "s_waitcnt lgkmcnt(3)\n\t" \
  "ds_read_b128 v[64:67], v35 offset:448\n\t" \
  "v_cvt_pk_bf16_f32 v26, v68, v69\n\t" \
  "v_cvt_pk_bf16_f32 v27, v70, v71\n\t" \
  "v_smfmac_f32_16x16x64_bf16 v[48:51], a[12:15], v[126:133], v143 abid:1\n\t" \
  "s_waitcnt lgkmcnt(3)\n\t" \
  "ds_read_b128 v[68:71], v35 offset:512\n\t" \
  "v_add_u32_sdwa v0, s29, v108 dst_sel:DWORD dst_unused:UNUSED_PAD src0_sel:DWORD src1_sel:WORD_0\n\t" \
  "v_smfmac_f32_16x16x64_bf16 v[52:55], a[16:19], v[126:133], v144\n\t" \
  "s_waitcnt lgkmcnt(3)\n\t" \
  "s_bfe_i32 s47, s48, 0x10000\n\t" \
  "ds_write_b64 v24, v[8:9] offset:0\n\t" \
  "v_add_u32_sdwa v2, s29, v108 dst_sel:DWORD dst_unused:UNUSED_PAD src0_sel:DWORD src1_sel:WORD_1\n\t" \
  "v_add_u32 v28, s38, v187\n\t" \
  "v_smfmac_f32_16x16x64_bf16 v[56:59], a[20:23], v[126:133], v144 abid:1\n\t" \
  "s_waitcnt lgkmcnt(3)\n\t" \
  "s_lshr_b32 s48, s48, 1\n\t" \
  "ds_read_b64 v[0:1], v0 offset:0\n\t" \
  "ds_read_b128 v[28:31], v28 offset:0\n\t" \
  "v_add_u32_sdwa v4, s29, v109 dst_sel:DWORD dst_unused:UNUSED_PAD src0_sel:DWORD src1_sel:WORD_0\n\t" \
  "v_smfmac_f32_16x16x64_bf16 v[60:63], a[24:27], v[126:133], v145\n\t" \
  "s_waitcnt lgkmcnt(3)\n\t" \
  "s_add_u32 s36, s35, s39\n\t" \
  "ds_write_b64 v24, v[10:11] offset:8\n\t" \
  "v_add_u32_sdwa v6, s29, v109 dst_sel:DWORD dst_unused:UNUSED_PAD src0_sel:DWORD src1_sel:WORD_1\n\t" \
  "v_smfmac_f32_16x16x64_bf16 v[64:67], a[28:31], v[126:133], v145 abid:1\n\t" \
  "s_waitcnt lgkmcnt(3)\n\t" \
  "s_andn2_b32 s44, 0x40000000, s47\n\t" \
  "ds_read_b64 v[2:3], v2 offset:0\n\t" \
  "v_smfmac_f32_16x16x64_bf16 v[68:71], a[32:35], v[126:133], v178\n\t" \
  "s_sub_u32 s30, s30, s29\n\t" \
  "s_or_b32 s36, s36, s44\n\t" \
  "ds_write_b64 v24, v[12:13] offset:128\n\t" \
  "v_smfmac_f32_16x16x64_bf16 v[36:39], a[36:39], v[134:141], v146\n\t" \
  "s_mov_b32 s37, s29\n\t" \
  "ds_read_b128 v[126:129], v183 offset:4096\n\t" \
  "s_waitcnt lgkmcnt(4)\n\t" \
  "buffer_store_dwordx4 v[28:31], v34, s[20:23], s36 offen sc1\n\t" \
  "v_smfmac_f32_16x16x64_bf16 v[40:43], a[40:43], v[134:141], v146 abid:1\n\t" \
  "s_and_b32 s44, s47, 4718592\n\t" \
  "ds_write_b64 v24, v[14:15] offset:136\n\t" \
  "v_xor_b32 v24, 64, v24\n\t" \
  "v_xad_u32 v28, v187, 64, s38\n\t" \
  "v_smfmac_f32_16x16x64_bf16 v[44:47], a[44:47], v[134:141], v147\n\t" \
  "s_add_u32 s35, s35, s44\n\t" \
  "ds_read_b128 v[130:133], v184 offset:4096\n\t" \
  "ds_read_b128 v[28:31], v28 offset:1024\n\t" \
  "v_add_u32_sdwa v8, s29, v110 dst_sel:DWORD dst_unused:UNUSED_PAD src0_sel:DWORD src1_sel:WORD_0\n\t" \
  "v_smfmac_f32_16x16x64_bf16 v[48:51], a[48:51], v[134:141], v147 abid:1\n\t" \
  "s_add_u32 s36, s36, 4608\n\t" \
  "ds_write_b64 v24, v[16:17] offset:0\n\t" \
  "v_add_u32_sdwa v10, s29, v110 dst_sel:DWORD dst_unused:UNUSED_PAD src0_sel:DWORD src1_sel:WORD_1\n\t" \
  "v_smfmac_f32_16x16x64_bf16 v[52:55], a[52:55], v[134:141], v148\n\t" \
  "ds_read_b64 v[4:5], v4 offset:0\n\t" \
  "v_add_u32_sdwa v12, s29, v111 dst_sel:DWORD dst_unused:UNUSED_PAD src0_sel:DWORD src1_sel:WORD_0\n\t" \
  "v_smfmac_f32_16x16x64_bf16 v[56:59], a[56:59], v[134:141], v148 abid:1\n\t" \
  "ds_write_b64 v24, v[18:19] offset:8\n\t" \
  "v_add_u32_sdwa v14, s29, v111 dst_sel:DWORD dst_unused:UNUSED_PAD src0_sel:DWORD src1_sel:WORD_1\n\t" \
  "v_smfmac_f32_16x16x64_bf16 v[60:63], a[60:63], v[134:141], v149\n\t" \
  "s_and_b32 s27, s64, 7\n\t" \
  "ds_read_b64 v[6:7], v6 offset:0\n\t" \
  "s_waitcnt lgkmcnt(4)\n\t" \
  "buffer_store_dwordx4 v[28:31], v34, s[20:23], s36 offen sc1\n\t" \
  "v_smfmac_f32_16x16x64_bf16 v[64:67], a[64:67], v[134:141], v149 abid:1\n\t" \
  "s_lshl2_add_u32 s66, s27, s68\n\t" \
  "s_and_b32 s44, s47, 18432\n\t" \
  "ds_write_b64 v24, v[20:21] offset:128\n\t" \
  "v_add_u32 v28, s38, v187\n\t" \
  "v_smfmac_f32_16x16x64_bf16 v[68:71], a[68:71], v[134:141], v178 abid:1\n\t" \
  "s_waitcnt lgkmcnt(5)\n\t" \
  "ds_read_b128 v[134:137], v183 offset:6144\n\t" \
  "ds_read_b128 v[28:31], v28 offset:2048\n\t" \
  "v_smfmac_f32_16x16x64_bf16 v[36:39], a[72:75], v[126:133], v150\n\t" \
  "s_and_b32 s27, s64, 8\n\t" \
  "s_add_u32 s36, s36, 4608\n\t" \
  "ds_write_b64 v24, v[22:23] offset:136\n\t" \
  "v_add_u32_sdwa v16, s29, v112 dst_sel:DWORD dst_unused:UNUSED_PAD src0_sel:DWORD src1_sel:WORD_0\n\t" \
  "v_smfmac_f32_16x16x64_bf16 v[40:43], a[76:79], v[126:133], v150 abid:1\n\t" \
  "s_waitcnt lgkmcnt(3)\n\t" \
  "ds_read_b128 v[138:141], v184 offset:6144\n\t" \
  "v_add_u32_sdwa v18, s29, v112 dst_sel:DWORD dst_unused:UNUSED_PAD src0_sel:DWORD src1_sel:WORD_1\n\t" \
  "v_smfmac_f32_16x16x64_bf16 v[36:39], v[72:75], v[0:7], v182 abid:1\n\t" \
  "s_mul_i32 s27, s27, 176\n\t" \
  "ds_write_b64 v25, v[26:27] offset:16384\n\t" \
  "v_add_u32_sdwa v20, s29, v113 dst_sel:DWORD dst_unused:UNUSED_PAD src0_sel:DWORD src1_sel:WORD_0\n\t" \
  "v_smfmac_f32_16x16x64_bf16 v[44:47], a[80:83], v[126:133], v151\n\t" \
  "s_add_u32 s66, s66, s27\n\t" \
  "ds_read_b64 v[8:9], v8 offset:0\n\t" \
  "s_waitcnt lgkmcnt(4)\n\t" \
  "buffer_store_dwordx4 v[28:31], v34, s[20:23], s36 offen sc1\n\t" \
  "v_add_u32_sdwa v22, s29, v113 dst_sel:DWORD dst_unused:UNUSED_PAD src0_sel:DWORD src1_sel:WORD_1\n\t" \
  "v_smfmac_f32_16x16x64_bf16 v[48:51], a[84:87], v[126:133], v151 abid:1\n\t" \
  "s_lshr_b64 s[64:65], s[64:65], 4\n\t" \
  "ds_read_b64 v[10:11], v10 offset:0\n\t" \
  "v_add_u32_sdwa v0, s29, v114 dst_sel:DWORD dst_unused:UNUSED_PAD src0_sel:DWORD src1_sel:WORD_0\n\t" \
  "v_xad_u32 v28, v187, 64, s38\n\t" \
  "v_smfmac_f32_16x16x64_bf16 v[52:55], a[88:91], v[126:133], v152\n\t" \
  "s_add_u32 s38, s38, s44\n\t" \
  "ds_read_b64 v[12:13], v12 offset:0\n\t" \
  "ds_read_b128 v[28:31], v28 offset:3072\n\t" \
  "v_add_u32_sdwa v2, s29, v114 dst_sel:DWORD dst_unused:UNUSED_PAD src0_sel:DWORD src1_sel:WORD_1\n\t" \
  "v_smfmac_f32_16x16x64_bf16 v[56:59], a[92:95], v[126:133], v152 abid:1\n\t" \
  "s_add_u32 s36, s36, 4608\n\t" \
  "ds_read_b64 v[14:15], v14 offset:0\n\t" \
  "v_add_u32_sdwa v4, s29, v115 dst_sel:DWORD dst_unused:UNUSED_PAD src0_sel:DWORD src1_sel:WORD_0\n\t" \
  "v_smfmac_f32_16x16x64_bf16 v[60:63], a[96:99], v[126:133], v153\n\t" \
  "s_sub_u32 s44, s38, 129024\n\t" \
  "ds_read_b64 v[16:17], v16 offset:0\n\t" \
  "v_add_u32_sdwa v6, s29, v115 dst_sel:DWORD dst_unused:UNUSED_PAD src0_sel:DWORD src1_sel:WORD_1\n\t" \
  "v_smfmac_f32_16x16x64_bf16 v[64:67], a[100:103], v[126:133], v153 abid:1\n\t" \
  "s_min_u32 s38, s38, s44\n\t" \
  "ds_read_b64 v[18:19], v18 offset:0\n\t" \
  "v_smfmac_f32_16x16x64_bf16 v[68:71], a[104:107], v[126:133], v179\n\t" \
  "s_waitcnt lgkmcnt(7)\n\t" \
  "ds_read_b128 v[126:129], v183 offset:8192\n\t" \
  "s_waitcnt lgkmcnt(4)\n\t" \
  "buffer_store_dwordx4 v[28:31], v34, s[20:23], s36 offen sc1\n\t" \
  "v_smfmac_f32_16x16x64_bf16 v[36:39], a[108:111], v[134:141], v154\n\t" \
  "s_waitcnt lgkmcnt(3)\n\t" \
  "ds_read_b128 v[130:133], v184 offset:8192\n\t" \
  "v_smfmac_f32_16x16x64_bf16 v[40:43], v[76:79], v[8:15], v182 abid:1\n\t" \
  "s_branch .Ljn01%=\n\t" \
  ".fill (.Lvar%=+1404-.)/4, 4, 0xbf800000\n\t" \
  "s_nop 0\n\t" \
  "s_branch .Lfin%=\n\t" \
  "s_branch .Lhe%=\n\t" \
  "s_waitcnt vmcnt(6)\n\t" \
  "s_waitcnt vmcnt(10)\n\t" \
  "s_waitcnt vmcnt(12)\n\t" \
  "s_waitcnt vmcnt(17)\n\t" \
  "s_waitcnt vmcnt(21) lgkmcnt(0)\n\t" \
  "s_barrier\n\t" \
  "v_smfmac_f32_16x16x64_bf16 v[36:39], v[220:223], v[0:7], v174\n\t" \
  "s_add_u32 m0, s33, 0\n\t" \
  "ds_read_b128 v[126:129], v183 offset:0\n\t" \
  "v_add_u32 v24, s37, v185\n\t" \
  "v_smfmac_f32_16x16x64_bf16 v[40:43], v[224:227], v[0:7], v174 abid:1\n\t" \
  "buffer_load_dwordx4 v186, s[16:19], s34 offen offset:0 nt lds\n\t" \
  "ds_read_b128 v[130:133], v184 offset:0\n\t" \
  "v_xor_b32 v25, s41, v183\n\t" \
  "v_smfmac_f32_16x16x64_bf16 v[44:47], v[228:231], v[0:7], v175\n\t" \
  "s_add_u32 m0, s33, 1920\n\t" \
  "ds_read_b128 v[134:137], v183 offset:2048\n\t" \
  "v_subrev_u32 v25, s30, v25\n\t" \
  "v_smfmac_f32_16x16x64_bf16 v[48:51], v[232:235], v[0:7], v175 abid:1\n\t" \
  "s_add_u32 s30, s29, 18432\n\t" \
  "buffer_load_dwordx4 v186, s[16:19], s34 offen offset:128 nt lds\n\t" \
  "ds_read_b128 v[138:141], v184 offset:2048\n\t" \
  "v_cvt_pk_bf16_f32 v8, v36, v37\n\t" \
  "v_cvt_pk_bf16_f32 v9, v38, v39\n\t" \
  "v_smfmac_f32_16x16x64_bf16 v[52:55], v[236:239], v[0:7], v176\n\t" \
  "s_add_u32 m0, s33, 3840\n\t" \
  "ds_read_b128 v[36:39], v35 offset:0\n\t" \
  "v_cvt_pk_bf16_f32 v10, v40, v41\n\t" \
  "v_cvt_pk_bf16_f32 v11, v42, v43\n\t" \
  "v_smfmac_f32_16x16x64_bf16 v[56:59], v[240:243], v[0:7], v176 abid:1\n\t" \
  "s_sub_u32 s25, s30, 129024\n\t" \
  "buffer_load_dwordx4 v186, s[16:19], s34 offen offset:256 nt lds\n\t" \
  "ds_read_b128 v[40:43], v35 offset:64\n\t" \
  "v_cvt_pk_bf16_f32 v16, v44, v45\n\t" \
  "v_cvt_pk_bf16_f32 v17, v46, v47\n\t" \
  "v_smfmac_f32_16x16x64_bf16 v[60:63], v[244:247], v[0:7], v177\n\t" \
  "s_add_u32 m0, s33, 5760\n\t" \
  "ds_read_b128 v[44:47], v35 offset:128\n\t" \
  "v_cvt_pk_bf16_f32 v18, v48, v49\n\t" \
  "v_cvt_pk_bf16_f32 v19, v50, v51\n\t" \
  "v_smfmac_f32_16x16x64_bf16 v[64:67], v[248:251], v[0:7], v177 abid:1\n\t" \
  "s_min_u32 s30, s30, s25\n\t" \
  "buffer_load_dwordx4 v186, s[16:19], s34 offen offset:384 nt lds\n\t" \
  "ds_read_b128 v[48:51], v35 offset:192\n\t" \
  "v_cvt_pk_bf16_f32 v12, v52, v53\n\t" \
  "v_cvt_pk_bf16_f32 v13, v54, v55\n\t" \
  "v_smfmac_f32_16x16x64_bf16 v[68:71], v[252:255], v[0:7], v182\n\t" \
  "s_add_u32 m0, s33, 7680\n\t" \
  "s_waitcnt lgkmcnt(3)\n\t" \
  "ds_read_b128 v[52:55], v35 offset:256\n\t" \
  "v_cvt_pk_bf16_f32 v14, v56, v57\n\t" \
  "v_cvt_pk_bf16_f32 v15, v58, v59\n\t" \
  "v_smfmac_f32_16x16x64_bf16 v[36:39], a[0:3], v[126:133], v142\n\t" \
  "s_waitcnt lgkmcnt(3)\n\t" \
  "buffer_load_dwordx4 v186, s[16:19], s34 offen offset:512 nt lds\n\t" \
  "ds_read_b128 v[56:59], v35 offset:320\n\t" \
  "v_cvt_pk_bf16_f32 v20, v60, v61\n\t" \
  "v_cvt_pk_bf16_f32 v21, v62, v63\n\t" \
  "v_smfmac_f32_16x16x64_bf16 v[40:43], a[4:7], v[126:133], v142 abid:1\n\t" \
  "s_waitcnt lgkmcnt(3)\n\t" \
  "ds_read_b128 v[60:63], v35 offset:384\n\t" \
  "v_cvt_pk_bf16_f32 v22, v64, v65\n\t" \
  "v_cvt_pk_bf16_f32 v23, v66, v67\n\t" \
  "v_smfmac_f32_16x16x64_bf16 v[44:47], a[8:11], v[126:133], v143\n\t" \
  "s_waitcnt lgkmcnt(3)\n\t" \
  "ds_read_b128 v[64:67], v35 offset:448\n\t" \
  "v_cvt_pk_bf16_f32 v26, v68, v69\n\t" \
  "v_cvt_pk_bf16_f32 v27, v70, v71\n\t" \
  "v_smfmac_f32_16x16x64_bf16 v[48:51], a[12:15], v[126:133], v143 abid:1\n\t" \
  "s_waitcnt lgkmcnt(3)\n\t" \
  "ds_read_b128 v[68:71], v35 offset:512\n\t" \
  "v_add_u32_sdwa v0, s29, v108 dst_sel:DWORD dst_unused:UNUSED_PAD src0_sel:DWORD src1_sel:WORD_0\n\t" \
  "v_smfmac_f32_16x16x64_bf16 v[52:55], a[16:19], v[126:133], v144\n\t" \
  "s_waitcnt lgkmcnt(3)\n\t" \
  "ds_write_b64 v24, v[8:9] offset:0\n\t" \
  "v_add_u32_sdwa v2, s29, v108 dst_sel:DWORD dst_unused:UNUSED_PAD src0_sel:DWORD src1_sel:WORD_1\n\t" \
  "v_smfmac_f32_16x16x64_bf16 v[56:59], a[20:23], v[126:133], v144 abid:1\n\t" \
  "s_waitcnt lgkmcnt(3)\n\t" \
  "ds_read_b64 v[0:1], v0 offset:0\n\t" \
  "v_add_u32_sdwa v4, s29, v109 dst_sel:DWORD dst_unused:UNUSED_PAD src0_sel:DWORD src1_sel:WORD_0\n\t" \
  "v_smfmac_f32_16x16x64_bf16 v[60:63], a[24:27], v[126:133], v145\n\t" \
  "s_waitcnt lgkmcnt(3)\n\t" \
  "ds_write_b64 v24, v[10:11] offset:8\n\t" \
  "v_add_u32_sdwa v6, s29, v109 dst_sel:DWORD dst_unused:UNUSED_PAD src0_sel:DWORD src1_sel:WORD_1\n\t" \
  "v_smfmac_f32_16x16x64_bf16 v[64:67], a[28:31], v[126:133], v145 abid:1\n\t" \
  "s_waitcnt lgkmcnt(3)\n\t" \
  "ds_read_b64 v[2:3], v2 offset:0\n\t" \
  "v_smfmac_f32_16x16x64_bf16 v[68:71], a[32:35], v[126:133], v178\n\t" \
  "s_sub_u32 s30, s30, s29\n\t" \
  "ds_write_b64 v24, v[12:13] offset:128\n\t" \
  "v_smfmac_f32_16x16x64_bf16 v[36:39], a[36:39], v[134:141], v146\n\t" \
  "s_mov_b32 s37, s29\n\t" \
  "ds_read_b128 v[126:129], v183 offset:4096\n\t" \
  "v_smfmac_f32_16x16x64_bf16 v[40:43], a[40:43], v[134:141], v146 abid:1\n\t" \
  "s_add_u32 s31, s31, 18432\n\t" \
  "ds_write_b64 v24, v[14:15] offset:136\n\t" \
  "v_xor_b32 v24, 64, v24\n\t" \
  "v_smfmac_f32_16x16x64_bf16 v[44:47], a[44:47], v[134:141], v147\n\t" \
  "s_sub_u32 s26, s31, 129024\n\t" \
  "ds_read_b128 v[130:133], v184 offset:4096\n\t" \
  "v_add_u32_sdwa v8, s29, v110 dst_sel:DWORD dst_unused:UNUSED_PAD src0_sel:DWORD src1_sel:WORD_0\n\t" \
  "v_smfmac_f32_16x16x64_bf16 v[48:51], a[48:51], v[134:141], v147 abid:1\n\t" \
  "s_min_u32 s31, s31, s26\n\t" \
  "ds_write_b64 v24, v[16:17] offset:0\n\t" \
  "v_add_u32_sdwa v10, s29, v110 dst_sel:DWORD dst_unused:UNUSED_PAD src0_sel:DWORD src1_sel:WORD_1\n\t" \
  "v_smfmac_f32_16x16x64_bf16 v[52:55], a[52:55], v[134:141], v148\n\t" \
  "s_add_u32 s33, s31, s32\n\t" \
  "ds_read_b64 v[4:5], v4 offset:0\n\t" \
  "v_add_u32_sdwa v12, s29, v111 dst_sel:DWORD dst_unused:UNUSED_PAD src0_sel:DWORD src1_sel:WORD_0\n\t" \
  "v_smfmac_f32_16x16x64_bf16 v[56:59], a[56:59], v[134:141], v148 abid:1\n\t" \
  "s_add_u32 s34, s34, 4718592\n\t" \
  "ds_write_b64 v24, v[18:19] offset:8\n\t" \
  "v_add_u32_sdwa v14, s29, v111 dst_sel:DWORD dst_unused:UNUSED_PAD src0_sel:DWORD src1_sel:WORD_1\n\t" \
  "v_smfmac_f32_16x16x64_bf16 v[60:63], a[60:63], v[134:141], v149\n\t" \
  "s_and_b32 s27, s64, 7\n\t" \
  "ds_read_b64 v[6:7], v6 offset:0\n\t" \
  "v_smfmac_f32_16x16x64_bf16 v[64:67], a[64:67], v[134:141], v149 abid:1\n\t" \
  "s_lshl2_add_u32 s66, s27, s68\n\t" \
  "ds_write_b64 v24, v[20:21] offset:128\n\t" \
  "v_smfmac_f32_16x16x64_bf16 v[68:71], a[68:71], v[134:141], v178 abid:1\n\t" \
  "s_waitcnt lgkmcnt(5)\n\t" \
  "ds_read_b128 v[134:137], v183 offset:6144\n\t" \
  "v_smfmac_f32_16x16x64_bf16 v[36:39], a[72:75], v[126:133], v150\n\t" \
  "s_and_b32 s27, s64, 8\n\t" \
  "ds_write_b64 v24, v[22:23] offset:136\n\t" \
  "v_add_u32_sdwa v16, s29, v112 dst_sel:DWORD dst_unused:UNUSED_PAD src0_sel:DWORD src1_sel:WORD_0\n\t" \
  "v_smfmac_f32_16x16x64_bf16 v[40:43], a[76:79], v[126:133], v150 abid:1\n\t" \
  "s_waitcnt lgkmcnt(3)\n\t" \
  "ds_read_b128 v[138:141], v184 offset:6144\n\t" \
  "v_add_u32_sdwa v18, s29, v112 dst_sel:DWORD dst_unused:UNUSED_PAD src0_sel:DWORD src1_sel:WORD_1\n\t" \
  "v_smfmac_f32_16x16x64_bf16 v[36:39], v[72:75], v[0:7], v182 abid:1\n\t" \
  "s_mul_i32 s27, s27, 176\n\t" \
  "ds_write_b64 v25, v[26:27] offset:16384\n\t" \
  "v_add_u32_sdwa v20, s29, v113 dst_sel:DWORD dst_unused:UNUSED_PAD src0_sel:DWORD src1_sel:WORD_0\n\t" \
  "v_smfmac_f32_16x16x64_bf16 v[44:47], a[80:83], v[126:133], v151\n\t" \
  "s_add_u32 s66, s66, s27\n\t" \
  "ds_read_b64 v[8:9], v8 offset:0\n\t" \
  "v_add_u32_sdwa v22, s29, v113 dst_sel:DWORD dst_unused:UNUSED_PAD src0_sel:DWORD src1_sel:WORD_1\n\t" \
  "v_smfmac_f32_16x16x64_bf16 v[48:51], a[84:87], v[126:133], v151 abid:1\n\t" \
  "s_lshr_b64 s[64:65], s[64:65], 4\n\t" \
  "ds_read_b64 v[10:11], v10 offset:0\n\t" \
  "v_add_u32_sdwa v0, s29, v114 dst_sel:DWORD dst_unused:UNUSED_PAD src0_sel:DWORD src1_sel:WORD_0\n\t" \
  "v_smfmac_f32_16x16x64_bf16 v[52:55], a[88:91], v[126:133], v152\n\t" \
  "ds_read_b64 v[12:13], v12 offset:0\n\t" \
  "v_add_u32_sdwa v2, s29, v114 dst_sel:DWORD dst_unused:UNUSED_PAD src0_sel:DWORD src1_sel:WORD_1\n\t" \
  "v_smfmac_f32_16x16x64_bf16 v[56:59], a[92:95], v[126:133], v152 abid:1\n\t" \
  "ds_read_b64 v[14:15], v14 offset:0\n\t" \
  "v_add_u32_sdwa v4, s29, v115 dst_sel:DWORD dst_unused:UNUSED_PAD src0_sel:DWORD src1_sel:WORD_0\n\t" \
  "v_smfmac_f32_16x16x64_bf16 v[60:63], a[96:99], v[126:133], v153\n\t" \
  "ds_read_b64 v[16:17], v16 offset:0\n\t" \
  "v_add_u32_sdwa v6, s29, v115 dst_sel:DWORD dst_unused:UNUSED_PAD src0_sel:DWORD src1_sel:WORD_1\n\t" \
  "v_smfmac_f32_16x16x64_bf16 v[64:67], a[100:103], v[126:133], v153 abid:1\n\t" \
  "ds_read_b64 v[18:19], v18 offset:0\n\t" \
  "v_smfmac_f32_16x16x64_bf16 v[68:71], a[104:107], v[126:133], v179\n\t" \
  "s_waitcnt lgkmcnt(7)\n\t" \
  "ds_read_b128 v[126:129], v183 offset:8192\n\t" \
  "v_smfmac_f32_16x16x64_bf16 v[36:39], a[108:111], v[134:141], v154\n\t" \
  "s_waitcnt lgkmcnt(3)\n\t" \
  "ds_read_b128 v[130:133], v184 offset:8192\n\t" \
  "v_smfmac_f32_16x16x64_bf16 v[40:43], v[76:79], v[8:15], v182 abid:1\n\t" \
  ".Ljn01%=:\n\t" \
  "ds_read_b64 v[20:21], v20 offset:0\n\t" \
  "v_add_u32_sdwa v8, s29, v32 dst_sel:DWORD dst_unused:UNUSED_PAD src0_sel:DWORD src1_sel:WORD_0\n\t" \
  "v_smfmac_f32_16x16x64_bf16 v[40:43], a[112:115], v[134:141], v154 abid:1\n\t" \
  "ds_read_b64 v[22:23], v22 offset:0\n\t" \
  "v_add_u32_sdwa v10, s29, v32 dst_sel:DWORD dst_unused:UNUSED_PAD src0_sel:DWORD src1_sel:WORD_1\n\t" \
  "v_smfmac_f32_16x16x64_bf16 v[44:47], a[116:119], v[134:141], v155\n\t" \
  "ds_read_b64 v[0:1], v0 offset:0\n\t" \
  "v_add_u32 v28, s60, v187\n\t" \
  "v_smfmac_f32_16x16x64_bf16 v[48:51], a[120:123], v[134:141], v155 abid:1\n\t" \
  "ds_read_b64 v[2:3], v2 offset:0\n\t" \
  "v_smfmac_f32_16x16x64_bf16 v[52:55], a[124:127], v[134:141], v156\n\t" \
  "ds_read_b64 v[4:5], v4 offset:0\n\t" \
  "v_smfmac_f32_16x16x64_bf16 v[56:59], a[128:131], v[134:141], v156 abid:1\n\t" \
  "s_waitcnt lgkmcnt(3)\n\t" \
  "ds_read_b64 v[6:7], v6 offset:0\n\t" \
  "v_smfmac_f32_16x16x64_bf16 v[44:47], v[80:83], v[16:23], v182 abid:1\n\t" \
  "ds_read_b64 v[8:9], v8 offset:0\n\t" \
  "v_add_u32_sdwa v16, s29, v116 dst_sel:DWORD dst_unused:UNUSED_PAD src0_sel:DWORD src1_sel:WORD_0\n\t" \
  "v_smfmac_f32_16x16x64_bf16 v[60:63], a[132:135], v[134:141], v157\n\t" \
  "ds_read_b64 v[10:11], v10 offset:0\n\t" \
  "v_add_u32 v24, s38, v187\n\t" \
  "v_smfmac_f32_16x16x64_bf16 v[64:67], a[136:139], v[134:141], v157 abid:1\n\t" \
  "ds_read_b128 v[24:27], v24 offset:0\n\t" \
  "v_add_u32_sdwa v18, s29, v116 dst_sel:DWORD dst_unused:UNUSED_PAD src0_sel:DWORD src1_sel:WORD_1\n\t" \
  "v_smfmac_f32_16x16x64_bf16 v[68:71], a[140:143], v[134:141], v179 abid:1\n\t" \
  "s_waitcnt lgkmcnt(3)\n\t" \
  "ds_read_b128 v[134:137], v183 offset:10240\n\t" \
  "v_add_u32_sdwa v20, s29, v117 dst_sel:DWORD dst_unused:UNUSED_PAD src0_sel:DWORD src1_sel:WORD_0\n\t" \
  "v_smfmac_f32_16x16x64_bf16 v[48:51], v[84:87], v[0:7], v182 abid:1\n\t" \
  "ds_read_b128 v[138:141], v184 offset:10240\n\t" \
  "v_add_u32_sdwa v22, s29, v117 dst_sel:DWORD dst_unused:UNUSED_PAD src0_sel:DWORD src1_sel:WORD_1\n\t" \
  "v_smfmac_f32_16x16x64_bf16 v[36:39], a[144:147], v[126:133], v158\n\t" \
  "s_add_u32 s36, s35, s39\n\t" \
  "ds_read_b128 v[28:31], v28 offset:0\n\t" \
  "v_add_u32_sdwa v0, s29, v118 dst_sel:DWORD dst_unused:UNUSED_PAD src0_sel:DWORD src1_sel:WORD_0\n\t" \
  "v_smfmac_f32_16x16x64_bf16 v[40:43], a[148:151], v[126:133], v158 abid:1\n\t" \
  "s_waitcnt lgkmcnt(3)\n\t" \
  "buffer_store_dwordx4 v[24:27], v34, s[20:23], s36 offen sc1\n\t" \
  "v_add_u32_sdwa v2, s29, v118 dst_sel:DWORD dst_unused:UNUSED_PAD src0_sel:DWORD src1_sel:WORD_1\n\t" \
  "v_smfmac_f32_16x16x64_bf16 v[44:47], a[152:155], v[126:133], v159\n\t" \
  "ds_read_b64 v[16:17], v16 offset:0\n\t" \
  "v_xad_u32 v24, v187, 64, s38\n\t" \
  "v_smfmac_f32_16x16x64_bf16 v[48:51], a[156:159], v[126:133], v159 abid:1\n\t" \
  "ds_read_b128 v[24:27], v24 offset:1024\n\t" \
  "v_add_u32_sdwa v4, s29, v119 dst_sel:DWORD dst_unused:UNUSED_PAD src0_sel:DWORD src1_sel:WORD_0\n\t" \
  "v_smfmac_f32_16x16x64_bf16 v[52:55], a[160:163], v[126:133], v160\n\t" \
  "s_waitcnt lgkmcnt(2)\n\t" \
  "ds_read_b64 v[18:19], v18 offset:0\n\t" \
  "v_add_u32_sdwa v6, s29, v119 dst_sel:DWORD dst_unused:UNUSED_PAD src0_sel:DWORD src1_sel:WORD_1\n\t" \
  "s_set_gpr_idx_on s6, gpr_idx(DST)\n\t" \
  "v_smfmac_f32_16x16x64_bf16 v[36:39], v[28:31], v[8:15], v182 abid:1\n\t" \
  "s_set_gpr_idx_off\n\t" \
  "ds_read_b64 v[20:21], v20 offset:0\n\t" \
  "v_add_u32_sdwa v8, s29, v120 dst_sel:DWORD dst_unused:UNUSED_PAD src0_sel:DWORD src1_sel:WORD_0\n\t" \
  "v_smfmac_f32_16x16x64_bf16 v[56:59], a[164:167], v[126:133], v160 abid:1\n\t" \
  "s_add_u32 s36, s36, 4608\n\t" \
  "ds_read_b64 v[22:23], v22 offset:0\n\t" \
  "v_add_u32_sdwa v10, s29, v120 dst_sel:DWORD dst_unused:UNUSED_PAD src0_sel:DWORD src1_sel:WORD_1\n\t" \
  "v_smfmac_f32_16x16x64_bf16 v[60:63], a[168:171], v[126:133], v161\n\t" \
  "s_waitcnt lgkmcnt(3)\n\t" \
  "buffer_store_dwordx4 v[24:27], v34, s[20:23], s36 offen sc1\n\t" \
  "v_add_u32_sdwa v12, s29, v121 dst_sel:DWORD dst_unused:UNUSED_PAD src0_sel:DWORD src1_sel:WORD_0\n\t" \
  "v_smfmac_f32_16x16x64_bf16 v[64:67], a[172:175], v[126:133], v161 abid:1\n\t" \
  "ds_read_b64 v[0:1], v0 offset:0\n\t" \
  "v_add_u32 v24, s38, v187\n\t" \
  "v_smfmac_f32_16x16x64_bf16 v[68:71], a[176:179], v[126:133], v180\n\t" \
  "ds_read_b128 v[24:27], v24 offset:2048\n\t" \
  "v_add_u32_sdwa v14, s29, v121 dst_sel:DWORD dst_unused:UNUSED_PAD src0_sel:DWORD src1_sel:WORD_1\n\t" \
  "v_smfmac_f32_16x16x64_bf16 v[36:39], a[180:183], v[134:141], v162\n\t" \
  "s_waitcnt lgkmcnt(2)\n\t" \
  "ds_read_b128 v[126:129], v183 offset:12288\n\t" \
  "v_smfmac_f32_16x16x64_bf16 v[52:55], v[88:91], v[16:23], v182 abid:1\n\t" \
  "ds_read_b128 v[130:133], v184 offset:12288\n\t" \
  "v_add_u32_sdwa v16, s29, v122 dst_sel:DWORD dst_unused:UNUSED_PAD src0_sel:DWORD src1_sel:WORD_0\n\t" \
  "v_smfmac_f32_16x16x64_bf16 v[40:43], a[184:187], v[134:141], v162 abid:1\n\t" \
  "s_add_u32 s36, s36, 4608\n\t" \
  "ds_read_b64 v[2:3], v2 offset:0\n\t" \
  "v_add_u32_sdwa v18, s29, v122 dst_sel:DWORD dst_unused:UNUSED_PAD src0_sel:DWORD src1_sel:WORD_1\n\t" \
  "v_smfmac_f32_16x16x64_bf16 v[44:47], a[188:191], v[134:141], v163\n\t" \
  "s_waitcnt lgkmcnt(3)\n\t" \
  "buffer_store_dwordx4 v[24:27], v34, s[20:23], s36 offen sc1\n\t" \
  "v_add_u32_sdwa v20, s29, v123 dst_sel:DWORD dst_unused:UNUSED_PAD src0_sel:DWORD src1_sel:WORD_0\n\t" \
  "v_smfmac_f32_16x16x64_bf16 v[48:51], a[192:195], v[134:141], v163 abid:1\n\t" \
  "ds_read_b64 v[4:5], v4 offset:0\n\t" \
  "v_xad_u32 v24, v187, 64, s38\n\t" \
  "v_smfmac_f32_16x16x64_bf16 v[52:55], a[196:199], v[134:141], v164\n\t" \
  "s_add_u32 s38, s38, 18432\n\t" \
  "ds_read_b128 v[24:27], v24 offset:3072\n\t" \
  "v_add_u32_sdwa v22, s29, v123 dst_sel:DWORD dst_unused:UNUSED_PAD src0_sel:DWORD src1_sel:WORD_1\n\t" \
  "v_smfmac_f32_16x16x64_bf16 v[56:59], a[200:203], v[134:141], v164 abid:1\n\t" \
  "s_sub_u32 s44, s38, 129024\n\t" \
  "ds_read_b64 v[6:7], v6 offset:0\n\t" \
  "v_smfmac_f32_16x16x64_bf16 v[60:63], a[204:207], v[134:141], v165\n\t" \
  "s_min_u32 s38, s38, s44\n\t" \
  "ds_read_b64 v[8:9], v8 offset:0\n\t" \
  "v_smfmac_f32_16x16x64_bf16 v[64:67], a[208:211], v[134:141], v165 abid:1\n\t" \
  "s_add_u32 s36, s36, 4608\n\t" \
  "ds_read_b64 v[10:11], v10 offset:0\n\t" \
  "v_smfmac_f32_16x16x64_bf16 v[68:71], a[212:215], v[134:141], v180 abid:1\n\t" \
  "s_waitcnt lgkmcnt(3)\n\t" \
  "buffer_store_dwordx4 v[24:27], v34, s[20:23], s36 offen sc1\n\t" \
  "v_smfmac_f32_16x16x64_bf16 v[36:39], a[216:219], v[126:133], v166\n\t" \
  "s_waitcnt lgkmcnt(2)\n\t" \
  "ds_read_b128 v[134:137], v183 offset:14336\n\t" \
  "v_smfmac_f32_16x16x64_bf16 v[56:59], v[92:95], v[0:7], v182 abid:1\n\t" \
  "s_add_u32 s35, s35, 4718592\n\t" \
  "ds_read_b128 v[138:141], v184 offset:14336\n\t" \
  "v_smfmac_f32_16x16x64_bf16 v[40:43], a[220:223], v[126:133], v166 abid:1\n\t" \
  "ds_read_b64 v[12:13], v12 offset:0\n\t" \
  "v_smfmac_f32_16x16x64_bf16 v[44:47], a[224:227], v[126:133], v167\n\t" \
  "ds_read_b64 v[14:15], v14 offset:0\n\t" \
  "v_smfmac_f32_16x16x64_bf16 v[48:51], a[228:231], v[126:133], v167 abid:1\n\t" \
  "ds_read_b64 v[16:17], v16 offset:0\n\t" \
  "v_smfmac_f32_16x16x64_bf16 v[52:55], a[232:235], v[126:133], v168\n\t" \
  "ds_read_b64 v[18:19], v18 offset:0\n\t" \
  "v_smfmac_f32_16x16x64_bf16 v[56:59], a[236:239], v[126:133], v168 abid:1\n\t" \
  "ds_read_b64 v[20:21], v20 offset:0\n\t" \
  "v_smfmac_f32_16x16x64_bf16 v[60:63], a[240:243], v[126:133], v169\n\t" \
  "s_waitcnt lgkmcnt(3)\n\t" \
  "ds_read_b64 v[22:23], v22 offset:0\n\t" \
  "v_smfmac_f32_16x16x64_bf16 v[60:63], v[96:99], v[8:15], v182 abid:1\n\t" \
  "v_add_u32_sdwa v8, s29, v124 dst_sel:DWORD dst_unused:UNUSED_PAD src0_sel:DWORD src1_sel:WORD_0\n\t" \
  "v_smfmac_f32_16x16x64_bf16 v[64:67], a[244:247], v[126:133], v169 abid:1\n\t" \
  "ds_read_b64 v[8:9], v8 offset:0\n\t" \
  "v_add_u32_sdwa v10, s29, v124 dst_sel:DWORD dst_unused:UNUSED_PAD src0_sel:DWORD src1_sel:WORD_1\n\t" \
  "v_smfmac_f32_16x16x64_bf16 v[68:71], a[248:251], v[126:133], v181\n\t" \
  "ds_read_b128 v[0:3], v183 offset:16384\n\t" \
  "v_add_u32_sdwa v12, s29, v125 dst_sel:DWORD dst_unused:UNUSED_PAD src0_sel:DWORD src1_sel:WORD_0\n\t" \
  "v_smfmac_f32_16x16x64_bf16 v[36:39], a[252:255], v[134:141], v170\n\t" \
  "s_waitcnt lgkmcnt(2)\n\t" \
  "ds_read_b128 v[4:7], v184 offset:16384\n\t" \
  "v_add_u32_sdwa v14, s29, v125 dst_sel:DWORD dst_unused:UNUSED_PAD src0_sel:DWORD src1_sel:WORD_1\n\t" \
  "v_smfmac_f32_16x16x64_bf16 v[64:67], v[100:103], v[16:23], v182 abid:1\n\t" \
  "s_add_u32 s29, s29, s30\n\t" \
  "ds_read_b64 v[10:11], v10 offset:0\n\t" \
  "v_add_u32 v183, s30, v183\n\t" \
  "v_smfmac_f32_16x16x64_bf16 v[40:43], v[188:191], v[134:141], v170 abid:1\n\t" \
  "ds_read_b64 v[12:13], v12 offset:0\n\t" \
  "v_add_u32 v184, s30, v184\n\t" \
  "v_smfmac_f32_16x16x64_bf16 v[44:47], v[192:195], v[134:141], v171\n\t" \
  "ds_read_b64 v[14:15], v14 offset:0\n\t" \
  "v_smfmac_f32_16x16x64_bf16 v[48:51], v[196:199], v[134:141], v171 abid:1\n\t" \
  "v_smfmac_f32_16x16x64_bf16 v[52:55], v[200:203], v[134:141], v172\n\t" \
  "v_smfmac_f32_16x16x64_bf16 v[56:59], v[204:207], v[134:141], v172 abid:1\n\t" \
  "v_smfmac_f32_16x16x64_bf16 v[60:63], v[208:211], v[134:141], v173\n\t" \
  "s_waitcnt lgkmcnt(0)\n\t" \
  "v_smfmac_f32_16x16x64_bf16 v[68:71], v[104:107], v[8:15], v182 abid:1\n\t" \
  "v_smfmac_f32_16x16x64_bf16 v[64:67], v[212:215], v[134:141], v173 abid:1\n\t" \
  "v_smfmac_f32_16x16x64_bf16 v[68:71], v[216:219], v[134:141], v181 abid:1\n\t" \
  "s_setpc_b64 s[66:67]\n\t" \
  ".fill (.Lvar%=+4096-.)/4, 4, 0xbf800000\n\t" \
  "s_branch .Lfin%=\n\t" \
  "s_branch .Lhe%=\n\t" \
  "s_waitcnt vmcnt(6)\n\t" \
  "s_waitcnt vmcnt(10)\n\t" \
  "s_waitcnt vmcnt(12)\n\t" \
  "s_waitcnt vmcnt(17)\n\t" \
  "s_waitcnt vmcnt(21) lgkmcnt(0)\n\t" \
  "s_barrier\n\t" \
  "v_smfmac_f32_16x16x64_bf16 v[36:39], v[220:223], v[0:7], v174\n\t" \
  "ds_read_b128 v[126:129], v183 offset:0\n\t" \
  "v_add_u32 v24, s37, v185\n\t" \
  "v_smfmac_f32_16x16x64_bf16 v[40:43], v[224:227], v[0:7], v174 abid:1\n\t" \
  "ds_read_b128 v[130:133], v184 offset:0\n\t" \
  "v_xor_b32 v25, s41, v183\n\t" \
  "v_smfmac_f32_16x16x64_bf16 v[44:47], v[228:231], v[0:7], v175\n\t" \
  "ds_read_b128 v[134:137], v183 offset:2048\n\t" \
  "v_subrev_u32 v25, s30, v25\n\t" \
  "v_smfmac_f32_16x16x64_bf16 v[48:51], v[232:235], v[0:7], v175 abid:1\n\t" \
  "s_add_u32 s30, s29, 18432\n\t" \
  "ds_read_b128 v[138:141], v184 offset:2048\n\t" \
  "v_cvt_pk_bf16_f32 v8, v36, v37\n\t" \
  "v_cvt_pk_bf16_f32 v9, v38, v39\n\t" \
  "v_smfmac_f32_16x16x64_bf16 v[52:55], v[236:239], v[0:7], v176\n\t" \
  "ds_read_b128 v[36:39], v35 offset:0\n\t" \
  "v_cvt_pk_bf16_f32 v10, v40, v41\n\t" \
  "v_cvt_pk_bf16_f32 v11, v42, v43\n\t" \
  "v_smfmac_f32_16x16x64_bf16 v[56:59], v[240:243], v[0:7], v176 abid:1\n\t" \
  "s_sub_u32 s25, s30, 129024\n\t" \
  "ds_read_b128 v[40:43], v35 offset:64\n\t" \
  "v_cvt_pk_bf16_f32 v16, v44, v45\n\t" \
  "v_cvt_pk_bf16_f32 v17, v46, v47\n\t" \
  "v_smfmac_f32_16x16x64_bf16 v[60:63], v[244:247], v[0:7], v177\n\t" \
  "ds_read_b128 v[44:47], v35 offset:128\n\t" \
  "v_cvt_pk_bf16_f32 v18, v48, v49\n\t" \
  "v_cvt_pk_bf16_f32 v19, v50, v51\n\t" \
  "v_smfmac_f32_16x16x64_bf16 v[64:67], v[248:251], v[0:7], v177 abid:1\n\t" \
  "s_min_u32 s30, s30, s25\n\t" \
  "ds_read_b128 v[48:51], v35 offset:192\n\t" \
  "v_cvt_pk_bf16_f32 v12, v52, v53\n\t" \
  "v_cvt_pk_bf16_f32 v13, v54, v55\n\t" \
  "v_smfmac_f32_16x16x64_bf16 v[68:71], v[252:255], v[0:7], v182\n\t" \
  "s_waitcnt lgkmcnt(3)\n\t" \
  "ds_read_b128 v[52:55], v35 offset:256\n\t" \
  "v_cvt_pk_bf16_f32 v14, v56, v57\n\t" \
  "v_cvt_pk_bf16_f32 v15, v58, v59\n\t" \
  "v_smfmac_f32_16x16x64_bf16 v[36:39], a[0:3], v[126:133], v142\n\t" \
  "s_waitcnt lgkmcnt(3)\n\t" \
  "ds_read_b128 v[56:59], v35 offset:320\n\t" \
  "v_cvt_pk_bf16_f32 v20, v60, v61\n\t" \
  "v_cvt_pk_bf16_f32 v21, v62, v63\n\t" \
  "v_smfmac_f32_16x16x64_bf16 v[40:43], a[4:7], v[126:133], v142 abid:1\n\t" \
  "s_waitcnt lgkmcnt(3)\n\t" \
  "ds_read_b128 v[60:63], v35 offset:384\n\t" \
  "v_cvt_pk_bf16_f32 v22, v64, v65\n\t" \
  "v_cvt_pk_bf16_f32 v23, v66, v67\n\t" \
  "v_smfmac_f32_16x16x64_bf16 v[44:47], a[8:11], v[126:133], v143\n\t" \
  "s_waitcnt lgkmcnt(3)\n\t" \
  "ds_read_b128 v[64:67], v35 offset:448\n\t" \
  "v_cvt_pk_bf16_f32 v26, v68, v69\n\t" \
  "v_cvt_pk_bf16_f32 v27, v70, v71\n\t" \
  "v_smfmac_f32_16x16x64_bf16 v[48:51], a[12:15], v[126:133], v143 abid:1\n\t" \
  "s_waitcnt lgkmcnt(3)\n\t" \
  "ds_read_b128 v[68:71], v35 offset:512\n\t" \
  "v_add_u32_sdwa v0, s29, v108 dst_sel:DWORD dst_unused:UNUSED_PAD src0_sel:DWORD src1_sel:WORD_0\n\t" \
  "v_smfmac_f32_16x16x64_bf16 v[52:55], a[16:19], v[126:133], v144\n\t" \
  "s_waitcnt lgkmcnt(3)\n\t" \
  "s_bfe_i32 s47, s48, 0x10000\n\t" \
  "ds_write_b64 v24, v[8:9] offset:0\n\t" \
  "v_add_u32_sdwa v2, s29, v108 dst_sel:DWORD dst_unused:UNUSED_PAD src0_sel:DWORD src1_sel:WORD_1\n\t" \
  "v_add_u32 v28, s38, v187\n\t" \
  "v_smfmac_f32_16x16x64_bf16 v[56:59], a[20:23], v[126:133], v144 abid:1\n\t" \
  "s_waitcnt lgkmcnt(3)\n\t" \
  "s_lshr_b32 s48, s48, 1\n\t" \
  "ds_read_b64 v[0:1], v0 offset:0\n\t" \
  "ds_read_b128 v[28:31], v28 offset:0\n\t" \
  "v_add_u32_sdwa v4, s29, v109 dst_sel:DWORD dst_unused:UNUSED_PAD src0_sel:DWORD src1_sel:WORD_0\n\t" \
  "v_smfmac_f32_16x16x64_bf16 v[60:63], a[24:27], v[126:133], v145\n\t" \
  "s_waitcnt lgkmcnt(3)\n\t" \
  "s_add_u32 s36, s35, s39\n\t" \
  "ds_write_b64 v24, v[10:11] offset:8\n\t" \
  "v_add_u32_sdwa v6, s29, v109 dst_sel:DWORD dst_unused:UNUSED_PAD src0_sel:DWORD src1_sel:WORD_1\n\t" \
  "v_smfmac_f32_16x16x64_bf16 v[64:67], a[28:31], v[126:133], v145 abid:1\n\t" \
  "s_waitcnt lgkmcnt(3)\n\t" \
  "s_andn2_b32 s44, 0x40000000, s47\n\t" \
  "ds_read_b64 v[2:3], v2 offset:0\n\t" \
  "v_smfmac_f32_16x16x64_bf16 v[68:71], a[32:35], v[126:133], v178\n\t" \
  "s_sub_u32 s30, s30, s29\n\t" \
  "s_or_b32 s36, s36, s44\n\t" \
  "ds_write_b64 v24, v[12:13] offset:128\n\t" \
  "v_smfmac_f32_16x16x64_bf16 v[36:39], a[36:39], v[134:141], v146\n\t" \
  "s_mov_b32 s37, s29\n\t" \
  "ds_read_b128 v[126:129], v183 offset:4096\n\t" \
  "s_waitcnt lgkmcnt(4)\n\t" \
  "buffer_store_dwordx4 v[28:31], v34, s[20:23], s36 offen sc1\n\t" \
  "v_smfmac_f32_16x16x64_bf16 v[40:43], a[40:43], v[134:141], v146 abid:1\n\t" \
  "s_and_b32 s44, s47, 4718592\n\t" \
  "ds_write_b64 v24, v[14:15] offset:136\n\t" \
  "v_xor_b32 v24, 64, v24\n\t" \
  "v_xad_u32 v28, v187, 64, s38\n\t" \
  "v_smfmac_f32_16x16x64_bf16 v[44:47], a[44:47], v[134:141], v147\n\t" \
  "s_add_u32 s35, s35, s44\n\t" \
  "ds_read_b128 v[130:133], v184 offset:4096\n\t" \
  "ds_read_b128 v[28:31], v28 offset:1024\n\t" \
  "v_add_u32_sdwa v8, s29, v110 dst_sel:DWORD dst_unused:UNUSED_PAD src0_sel:DWORD src1_sel:WORD_0\n\t" \
  "v_smfmac_f32_16x16x64_bf16 v[48:51], a[48:51], v[134:141], v147 abid:1\n\t" \
  "s_add_u32 s36, s36, 4608\n\t" \
  "ds_write_b64 v24, v[16:17] offset:0\n\t" \
  "v_add_u32_sdwa v10, s29, v110 dst_sel:DWORD dst_unused:UNUSED_PAD src0_sel:DWORD src1_sel:WORD_1\n\t" \
  "v_smfmac_f32_16x16x64_bf16 v[52:55], a[52:55], v[134:141], v148\n\t" \
  "ds_read_b64 v[4:5], v4 offset:0\n\t" \
  "v_add_u32_sdwa v12, s29, v111 dst_sel:DWORD dst_unused:UNUSED_PAD src0_sel:DWORD src1_sel:WORD_0\n\t" \
  "v_smfmac_f32_16x16x64_bf16 v[56:59], a[56:59], v[134:141], v148 abid:1\n\t" \
  "ds_write_b64 v24, v[18:19] offset:8\n\t" \
  "v_add_u32_sdwa v14, s29, v111 dst_sel:DWORD dst_unused:UNUSED_PAD src0_sel:DWORD src1_sel:WORD_1\n\t" \
  "v_smfmac_f32_16x16x64_bf16 v[60:63], a[60:63], v[134:141], v149\n\t" \
  "s_and_b32 s27, s64, 7\n\t" \
  "ds_read_b64 v[6:7], v6 offset:0\n\t" \
  "s_waitcnt lgkmcnt(4)\n\t" \
  "buffer_store_dwordx4 v[28:31], v34, s[20:23], s36 offen sc1\n\t" \
  "v_smfmac_f32_16x16x64_bf16 v[64:67], a[64:67], v[134:141], v149 abid:1\n\t" \
  "s_lshl2_add_u32 s66, s27, s68\n\t" \
  "s_and_b32 s44, s47, 18432\n\t" \
  "ds_write_b64 v24, v[20:21] offset:128\n\t" \
  "v_add_u32 v28, s38, v187\n\t" \
  "v_smfmac_f32_16x16x64_bf16 v[68:71], a[68:71], v[134:141], v178 abid:1\n\t" \
  "s_waitcnt lgkmcnt(5)\n\t" \
  "ds_read_b128 v[134:137], v183 offset:6144\n\t" \
  "ds_read_b128 v[28:31], v28 offset:2048\n\t" \
  "v_smfmac_f32_16x16x64_bf16 v[36:39], a[72:75], v[126:133], v150\n\t" \
  "s_and_b32 s27, s64, 8\n\t" \
  "s_add_u32 s36, s36, 4608\n\t" \
  "ds_write_b64 v24, v[22:23] offset:136\n\t" \
  "v_add_u32_sdwa v16, s29, v112 dst_sel:DWORD dst_unused:UNUSED_PAD src0_sel:DWORD src1_sel:WORD_0\n\t" \
  "v_smfmac_f32_16x16x64_bf16 v[40:43], a[76:79], v[126:133], v150 abid:1\n\t" \
  "s_waitcnt lgkmcnt(3)\n\t" \
  "ds_read_b128 v[138:141], v184 offset:6144\n\t" \
  "v_add_u32_sdwa v18, s29, v112 dst_sel:DWORD dst_unused:UNUSED_PAD src0_sel:DWORD src1_sel:WORD_1\n\t" \
  "v_smfmac_f32_16x16x64_bf16 v[36:39], v[72:75], v[0:7], v182 abid:1\n\t" \
  "s_mul_i32 s27, s27, 176\n\t" \
  "ds_write_b64 v25, v[26:27] offset:16384\n\t" \
  "v_add_u32_sdwa v20, s29, v113 dst_sel:DWORD dst_unused:UNUSED_PAD src0_sel:DWORD src1_sel:WORD_0\n\t" \
  "v_smfmac_f32_16x16x64_bf16 v[44:47], a[80:83], v[126:133], v151\n\t" \
  "s_add_u32 s66, s66, s27\n\t" \
  "ds_read_b64 v[8:9], v8 offset:0\n\t" \
  "s_waitcnt lgkmcnt(4)\n\t" \
  "buffer_store_dwordx4 v[28:31], v34, s[20:23], s36 offen sc1\n\t" \
  "v_add_u32_sdwa v22, s29, v113 dst_sel:DWORD dst_unused:UNUSED_PAD src0_sel:DWORD src1_sel:WORD_1\n\t" \
  "v_smfmac_f32_16x16x64_bf16 v[48:51], a[84:87], v[126:133], v151 abid:1\n\t" \
  "s_lshr_b64 s[64:65], s[64:65], 4\n\t" \
  "ds_read_b64 v[10:11], v10 offset:0\n\t" \
  "v_add_u32_sdwa v0, s29, v114 dst_sel:DWORD dst_unused:UNUSED_PAD src0_sel:DWORD src1_sel:WORD_0\n\t" \
  "v_xad_u32 v28, v187, 64, s38\n\t" \
  "v_smfmac_f32_16x16x64_bf16 v[52:55], a[88:91], v[126:133], v152\n\t" \
  "s_add_u32 s38, s38, s44\n\t" \
  "ds_read_b64 v[12:13], v12 offset:0\n\t" \
  "ds_read_b128 v[28:31], v28 offset:3072\n\t" \
  "v_add_u32_sdwa v2, s29, v114 dst_sel:DWORD dst_unused:UNUSED_PAD src0_sel:DWORD src1_sel:WORD_1\n\t" \
  "v_smfmac_f32_16x16x64_bf16 v[56:59], a[92:95], v[126:133], v152 abid:1\n\t" \
  "s_add_u32 s36, s36, 4608\n\t" \
  "ds_read_b64 v[14:15], v14 offset:0\n\t" \
  "v_add_u32_sdwa v4, s29, v115 dst_sel:DWORD dst_unused:UNUSED_PAD src0_sel:DWORD src1_sel:WORD_0\n\t" \
  "v_smfmac_f32_16x16x64_bf16 v[60:63], a[96:99], v[126:133], v153\n\t" \
  "s_sub_u32 s44, s38, 129024\n\t" \
  "ds_read_b64 v[16:17], v16 offset:0\n\t" \
  "v_add_u32_sdwa v6, s29, v115 dst_sel:DWORD dst_unused:UNUSED_PAD src0_sel:DWORD src1_sel:WORD_1\n\t" \
  "v_smfmac_f32_16x16x64_bf16 v[64:67], a[100:103], v[126:133], v153 abid:1\n\t" \
  "s_min_u32 s38, s38, s44\n\t" \
  "ds_read_b64 v[18:19], v18 offset:0\n\t" \
  "v_smfmac_f32_16x16x64_bf16 v[68:71], a[104:107], v[126:133], v179\n\t" \
  "s_waitcnt lgkmcnt(7)\n\t" \
  "ds_read_b128 v[126:129], v183 offset:8192\n\t" \
  "s_waitcnt lgkmcnt(4)\n\t" \
  "buffer_store_dwordx4 v[28:31], v34, s[20:23], s36 offen sc1\n\t" \
  "v_smfmac_f32_16x16x64_bf16 v[36:39], a[108:111], v[134:141], v154\n\t" \
  "s_waitcnt lgkmcnt(3)\n\t" \
  "ds_read_b128 v[130:133], v184 offset:8192\n\t" \
  "v_smfmac_f32_16x16x64_bf16 v[40:43], v[76:79], v[8:15], v182 abid:1\n\t" \
  "s_branch .Ljn02%=\n\t" \
  ".fill (.Lvar%=+5500-.)/4, 4, 0xbf800000\n\t" \
  "s_nop 0\n\t" \
  "s_branch .Lfin%=\n\t" \
  "s_branch .Lhe%=\n\t" \
  "s_waitcnt vmcnt(6)\n\t" \
  "s_waitcnt vmcnt(10)\n\t" \
  "s_waitcnt vmcnt(12)\n\t" \
  "s_waitcnt vmcnt(17)\n\t" \
  "s_waitcnt vmcnt(21) lgkmcnt(0)\n\t" \
  "s_barrier\n\t" \
  "v_smfmac_f32_16x16x64_bf16 v[36:39], v[220:223], v[0:7], v174\n\t" \
  "s_add_u32 m0, s33, 0\n\t" \
  "ds_read_b128 v[126:129], v183 offset:0\n\t" \
  "v_add_u32 v24, s37, v185\n\t" \
  "v_smfmac_f32_16x16x64_bf16 v[40:43], v[224:227], v[0:7], v174 abid:1\n\t" \
  "buffer_load_dwordx4 v186, s[16:19], s34 offen offset:0 nt lds\n\t" \
  "ds_read_b128 v[130:133], v184 offset:0\n\t" \
  "v_xor_b32 v25, s41, v183\n\t" \
  "v_smfmac_f32_16x16x64_bf16 v[44:47], v[228:231], v[0:7], v175\n\t" \
  "s_add_u32 m0, s33, 1920\n\t" \
  "ds_read_b128 v[134:137], v183 offset:2048\n\t" \
  "v_subrev_u32 v25, s30, v25\n\t" \
  "v_smfmac_f32_16x16x64_bf16 v[48:51], v[232:235], v[0:7], v175 abid:1\n\t" \
  "s_add_u32 s30, s29, 18432\n\t" \
  "buffer_load_dwordx4 v186, s[16:19], s34 offen offset:128 nt lds\n\t" \
  "ds_read_b128 v[138:141], v184 offset:2048\n\t" \
  "v_cvt_pk_bf16_f32 v8, v36, v37\n\t" \
  "v_cvt_pk_bf16_f32 v9, v38, v39\n\t" \
  "v_smfmac_f32_16x16x64_bf16 v[52:55], v[236:239], v[0:7], v176\n\t" \
  "s_add_u32 m0, s33, 3840\n\t" \
  "ds_read_b128 v[36:39], v35 offset:0\n\t" \
  "v_cvt_pk_bf16_f32 v10, v40, v41\n\t" \
  "v_cvt_pk_bf16_f32 v11, v42, v43\n\t" \
  "v_smfmac_f32_16x16x64_bf16 v[56:59], v[240:243], v[0:7], v176 abid:1\n\t" \
  "s_sub_u32 s25, s30, 129024\n\t" \
  "buffer_load_dwordx4 v186, s[16:19], s34 offen offset:256 nt lds\n\t" \
  "ds_read_b128 v[40:43], v35 offset:64\n\t" \
  "v_cvt_pk_bf16_f32 v16, v44, v45\n\t" \
  "v_cvt_pk_bf16_f32 v17, v46, v47\n\t" \
  "v_smfmac_f32_16x16x64_bf16 v[60:63], v[244:247], v[0:7], v177\n\t" \
  "s_add_u32 m0, s33, 5760\n\t" \
  "ds_read_b128 v[44:47], v35 offset:128\n\t" \
  "v_cvt_pk_bf16_f32 v18, v48, v49\n\t" \
  "v_cvt_pk_bf16_f32 v19, v50, v51\n\t" \
  "v_smfmac_f32_16x16x64_bf16 v[64:67], v[248:251], v[0:7], v177 abid:1\n\t" \
  "s_min_u32 s30, s30, s25\n\t" \
  "buffer_load_dwordx4 v186, s[16:19], s34 offen offset:384 nt lds\n\t" \
  "ds_read_b128 v[48:51], v35 offset:192\n\t" \
  "v_cvt_pk_bf16_f32 v12, v52, v53\n\t" \
  "v_cvt_pk_bf16_f32 v13, v54, v55\n\t" \
  "v_smfmac_f32_16x16x64_bf16 v[68:71], v[252:255], v[0:7], v182\n\t" \
  "s_add_u32 m0, s33, 7680\n\t" \
  "s_waitcnt lgkmcnt(3)\n\t" \
  "ds_read_b128 v[52:55], v35 offset:256\n\t" \
  "v_cvt_pk_bf16_f32 v14, v56, v57\n\t" \
  "v_cvt_pk_bf16_f32 v15, v58, v59\n\t" \
  "v_smfmac_f32_16x16x64_bf16 v[36:39], a[0:3], v[126:133], v142\n\t" \
  "s_waitcnt lgkmcnt(3)\n\t" \
  "buffer_load_dwordx4 v186, s[16:19], s34 offen offset:512 nt lds\n\t" \
  "ds_read_b128 v[56:59], v35 offset:320\n\t" \
  "v_cvt_pk_bf16_f32 v20, v60, v61\n\t" \
  "v_cvt_pk_bf16_f32 v21, v62, v63\n\t" \
  "v_smfmac_f32_16x16x64_bf16 v[40:43], a[4:7], v[126:133], v142 abid:1\n\t" \
  "s_waitcnt lgkmcnt(3)\n\t" \
  "ds_read_b128 v[60:63], v35 offset:384\n\t" \
  "v_cvt_pk_bf16_f32 v22, v64, v65\n\t" \
  "v_cvt_pk_bf16_f32 v23, v66, v67\n\t" \
  "v_smfmac_f32_16x16x64_bf16 v[44:47], a[8:11], v[126:133], v143\n\t" \
  "s_waitcnt lgkmcnt(3)\n\t" \
  "ds_read_b128 v[64:67], v35 offset:448\n\t" \
  "v_cvt_pk_bf16_f32 v26, v68, v69\n\t" \
  "v_cvt_pk_bf16_f32 v27, v70, v71\n\t" \
  "v_smfmac_f32_16x16x64_bf16 v[48:51], a[12:15], v[126:133], v143 abid:1\n\t" \
  "s_waitcnt lgkmcnt(3)\n\t" \
  "ds_read_b128 v[68:71], v35 offset:512\n\t" \
  "v_add_u32_sdwa v0, s29, v108 dst_sel:DWORD dst_unused:UNUSED_PAD src0_sel:DWORD src1_sel:WORD_0\n\t" \
  "v_smfmac_f32_16x16x64_bf16 v[52:55], a[16:19], v[126:133], v144\n\t" \
  "s_waitcnt lgkmcnt(3)\n\t" \
  "ds_write_b64 v24, v[8:9] offset:0\n\t" \
  "v_add_u32_sdwa v2, s29, v108 dst_sel:DWORD dst_unused:UNUSED_PAD src0_sel:DWORD src1_sel:WORD_1\n\t" \
  "v_smfmac_f32_16x16x64_bf16 v[56:59], a[20:23], v[126:133], v144 abid:1\n\t" \
  "s_waitcnt lgkmcnt(3)\n\t" \
  "ds_read_b64 v[0:1], v0 offset:0\n\t" \
  "v_add_u32_sdwa v4, s29, v109 dst_sel:DWORD dst_unused:UNUSED_PAD src0_sel:DWORD src1_sel:WORD_0\n\t" \
  "v_smfmac_f32_16x16x64_bf16 v[60:63], a[24:27], v[126:133], v145\n\t" \
  "s_waitcnt lgkmcnt(3)\n\t" \
  "ds_write_b64 v24, v[10:11] offset:8\n\t" \
  "v_add_u32_sdwa v6, s29, v109 dst_sel:DWORD dst_unused:UNUSED_PAD src0_sel:DWORD src1_sel:WORD_1\n\t" \
  "v_smfmac_f32_16x16x64_bf16 v[64:67], a[28:31], v[126:133], v145 abid:1\n\t" \
  "s_waitcnt lgkmcnt(3)\n\t" \
  "ds_read_b64 v[2:3], v2 offset:0\n\t" \
  "v_smfmac_f32_16x16x64_bf16 v[68:71], a[32:35], v[126:133], v178\n\t" \
  "s_sub_u32 s30, s30, s29\n\t" \
  "ds_write_b64 v24, v[12:13] offset:128\n\t" \
  "v_smfmac_f32_16x16x64_bf16 v[36:39], a[36:39], v[134:141], v146\n\t" \
  "s_mov_b32 s37, s29\n\t" \
  "ds_read_b128 v[126:129], v183 offset:4096\n\t" \
  "v_smfmac_f32_16x16x64_bf16 v[40:43], a[40:43], v[134:141], v146 abid:1\n\t" \
  "s_add_u32 s31, s31, 18432\n\t" \
  "ds_write_b64 v24, v[14:15] offset:136\n\t" \
  "v_xor_b32 v24, 64, v24\n\t" \
  "v_smfmac_f32_16x16x64_bf16 v[44:47], a[44:47], v[134:141], v147\n\t" \
  "s_sub_u32 s26, s31, 129024\n\t" \
  "ds_read_b128 v[130:133], v184 offset:4096\n\t" \
  "v_add_u32_sdwa v8, s29, v110 dst_sel:DWORD dst_unused:UNUSED_PAD src0_sel:DWORD src1_sel:WORD_0\n\t" \
  "v_smfmac_f32_16x16x64_bf16 v[48:51], a[48:51], v[134:141], v147 abid:1\n\t" \
  "s_min_u32 s31, s31, s26\n\t" \
  "ds_write_b64 v24, v[16:17] offset:0\n\t" \
  "v_add_u32_sdwa v10, s29, v110 dst_sel:DWORD dst_unused:UNUSED_PAD src0_sel:DWORD src1_sel:WORD_1\n\t" \
  "v_smfmac_f32_16x16x64_bf16 v[52:55], a[52:55], v[134:141], v148\n\t" \
  "s_add_u32 s33, s31, s32\n\t" \
  "ds_read_b64 v[4:5], v4 offset:0\n\t" \
  "v_add_u32_sdwa v12, s29, v111 dst_sel:DWORD dst_unused:UNUSED_PAD src0_sel:DWORD src1_sel:WORD_0\n\t" \
  "v_smfmac_f32_16x16x64_bf16 v[56:59], a[56:59], v[134:141], v148 abid:1\n\t" \
  "s_add_u32 s34, s34, 4718592\n\t" \
  "ds_write_b64 v24, v[18:19] offset:8\n\t" \
  "v_add_u32_sdwa v14, s29, v111 dst_sel:DWORD dst_unused:UNUSED_PAD src0_sel:DWORD src1_sel:WORD_1\n\t" \
  "v_smfmac_f32_16x16x64_bf16 v[60:63], a[60:63], v[134:141], v149\n\t" \
  "s_and_b32 s27, s64, 7\n\t" \
  "ds_read_b64 v[6:7], v6 offset:0\n\t" \
  "v_smfmac_f32_16x16x64_bf16 v[64:67], a[64:67], v[134:141], v149 abid:1\n\t" \
  "s_lshl2_add_u32 s66, s27, s68\n\t" \
  "ds_write_b64 v24, v[20:21] offset:128\n\t" \
  "v_smfmac_f32_16x16x64_bf16 v[68:71], a[68:71], v[134:141], v178 abid:1\n\t" \
  "s_waitcnt lgkmcnt(5)\n\t" \
  "ds_read_b128 v[134:137], v183 offset:6144\n\t" \
  "v_smfmac_f32_16x16x64_bf16 v[36:39], a[72:75], v[126:133], v150\n\t" \
  "s_and_b32 s27, s64, 8\n\t" \
  "ds_write_b64 v24, v[22:23] offset:136\n\t" \
  "v_add_u32_sdwa v16, s29, v112 dst_sel:DWORD dst_unused:UNUSED_PAD src0_sel:DWORD src1_sel:WORD_0\n\t" \
  "v_smfmac_f32_16x16x64_bf16 v[40:43], a[76:79], v[126:133], v150 abid:1\n\t" \
  "s_waitcnt lgkmcnt(3)\n\t" \
  "ds_read_b128 v[138:141], v184 offset:6144\n\t" \
  "v_add_u32_sdwa v18, s29, v112 dst_sel:DWORD dst_unused:UNUSED_PAD src0_sel:DWORD src1_sel:WORD_1\n\t" \
  "v_smfmac_f32_16x16x64_bf16 v[36:39], v[72:75], v[0:7], v182 abid:1\n\t" \
  "s_mul_i32 s27, s27, 176\n\t" \
  "ds_write_b64 v25, v[26:27] offset:16384\n\t" \
  "v_add_u32_sdwa v20, s29, v113 dst_sel:DWORD dst_unused:UNUSED_PAD src0_sel:DWORD src1_sel:WORD_0\n\t" \
  "v_smfmac_f32_16x16x64_bf16 v[44:47], a[80:83], v[126:133], v151\n\t" \
  "s_add_u32 s66, s66, s27\n\t" \
  "ds_read_b64 v[8:9], v8 offset:0\n\t" \
  "v_add_u32_sdwa v22, s29, v113 dst_sel:DWORD dst_unused:UNUSED_PAD src0_sel:DWORD src1_sel:WORD_1\n\t" \
  "v_smfmac_f32_16x16x64_bf16 v[48:51], a[84:87], v[126:133], v151 abid:1\n\t" \
  "s_lshr_b64 s[64:65], s[64:65], 4\n\t" \
  "ds_read_b64 v[10:11], v10 offset:0\n\t" \
  "v_add_u32_sdwa v0, s29, v114 dst_sel:DWORD dst_unused:UNUSED_PAD src0_sel:DWORD src1_sel:WORD_0\n\t" \
  "v_smfmac_f32_16x16x64_bf16 v[52:55], a[88:91], v[126:133], v152\n\t" \
  "ds_read_b64 v[12:13], v12 offset:0\n\t" \
  "v_add_u32_sdwa v2, s29, v114 dst_sel:DWORD dst_unused:UNUSED_PAD src0_sel:DWORD src1_sel:WORD_1\n\t" \
  "v_smfmac_f32_16x16x64_bf16 v[56:59], a[92:95], v[126:133], v152 abid:1\n\t" \
  "ds_read_b64 v[14:15], v14 offset:0\n\t" \
  "v_add_u32_sdwa v4, s29, v115 dst_sel:DWORD dst_unused:UNUSED_PAD src0_sel:DWORD src1_sel:WORD_0\n\t" \
  "v_smfmac_f32_16x16x64_bf16 v[60:63], a[96:99], v[126:133], v153\n\t" \
  "ds_read_b64 v[16:17], v16 offset:0\n\t" \
  "v_add_u32_sdwa v6, s29, v115 dst_sel:DWORD dst_unused:UNUSED_PAD src0_sel:DWORD src1_sel:WORD_1\n\t" \
  "v_smfmac_f32_16x16x64_bf16 v[64:67], a[100:103], v[126:133], v153 abid:1\n\t" \
  "ds_read_b64 v[18:19], v18 offset:0\n\t" \
  "v_smfmac_f32_16x16x64_bf16 v[68:71], a[104:107], v[126:133], v179\n\t" \
  "s_waitcnt lgkmcnt(7)\n\t" \
  "ds_read_b128 v[126:129], v183 offset:8192\n\t" \
  "v_smfmac_f32_16x16x64_bf16 v[36:39], a[108:111], v[134:141], v154\n\t" \
  "s_waitcnt lgkmcnt(3)\n\t" \
  "ds_read_b128 v[130:133], v184 offset:8192\n\t" \
  "v_smfmac_f32_16x16x64_bf16 v[40:43], v[76:79], v[8:15], v182 abid:1\n\t" \
  ".Ljn02%=:\n\t" \
  "ds_read_b64 v[20:21], v20 offset:0\n\t" \
  "v_add_u32_sdwa v8, s29, v32 dst_sel:DWORD dst_unused:UNUSED_PAD src0_sel:DWORD src1_sel:WORD_0\n\t" \
  "v_smfmac_f32_16x16x64_bf16 v[40:43], a[112:115], v[134:141], v154 abid:1\n\t" \
  "ds_read_b64 v[22:23], v22 offset:0\n\t" \
  "v_add_u32_sdwa v10, s29, v32 dst_sel:DWORD dst_unused:UNUSED_PAD src0_sel:DWORD src1_sel:WORD_1\n\t" \
  "v_smfmac_f32_16x16x64_bf16 v[44:47], a[116:119], v[134:141], v155\n\t" \
  "ds_read_b64 v[0:1], v0 offset:0\n\t" \
  "v_add_u32 v28, s60, v187\n\t" \
  "v_smfmac_f32_16x16x64_bf16 v[48:51], a[120:123], v[134:141], v155 abid:1\n\t" \
  "ds_read_b64 v[2:3], v2 offset:0\n\t" \
  "v_smfmac_f32_16x16x64_bf16 v[52:55], a[124:127], v[134:141], v156\n\t" \
  "ds_read_b64 v[4:5], v4 offset:0\n\t" \
  "v_smfmac_f32_16x16x64_bf16 v[56:59], a[128:131], v[134:141], v156 abid:1\n\t" \
  "s_waitcnt lgkmcnt(3)\n\t" \
  "ds_read_b64 v[6:7], v6 offset:0\n\t" \
  "v_smfmac_f32_16x16x64_bf16 v[44:47], v[80:83], v[16:23], v182 abid:1\n\t" \
  "ds_read_b64 v[8:9], v8 offset:0\n\t" \
  "v_add_u32_sdwa v16, s29, v116 dst_sel:DWORD dst_unused:UNUSED_PAD src0_sel:DWORD src1_sel:WORD_0\n\t" \
  "v_smfmac_f32_16x16x64_bf16 v[60:63], a[132:135], v[134:141], v157\n\t" \
  "ds_read_b64 v[10:11], v10 offset:0\n\t" \
  "v_add_u32 v24, s38, v187\n\t" \
  "v_smfmac_f32_16x16x64_bf16 v[64:67], a[136:139], v[134:141], v157 abid:1\n\t" \
  "ds_read_b128 v[24:27], v24 offset:0\n\t" \
  "v_add_u32_sdwa v18, s29, v116 dst_sel:DWORD dst_unused:UNUSED_PAD src0_sel:DWORD src1_sel:WORD_1\n\t" \
  "v_smfmac_f32_16x16x64_bf16 v[68:71], a[140:143], v[134:141], v179 abid:1\n\t" \
  "s_waitcnt lgkmcnt(3)\n\t" \
  "ds_read_b128 v[134:137], v183 offset:10240\n\t" \
  "v_add_u32_sdwa v20, s29, v117 dst_sel:DWORD dst_unused:UNUSED_PAD src0_sel:DWORD src1_sel:WORD_0\n\t" \
  "v_smfmac_f32_16x16x64_bf16 v[48:51], v[84:87], v[0:7], v182 abid:1\n\t" \
  "ds_read_b128 v[138:141], v184 offset:10240\n\t" \
  "v_add_u32_sdwa v22, s29, v117 dst_sel:DWORD dst_unused:UNUSED_PAD src0_sel:DWORD src1_sel:WORD_1\n\t" \
  "v_smfmac_f32_16x16x64_bf16 v[36:39], a[144:147], v[126:133], v158\n\t" \
  "s_add_u32 s36, s35, s39\n\t" \
  "ds_read_b128 v[28:31], v28 offset:0\n\t" \
  "v_add_u32_sdwa v0, s29, v118 dst_sel:DWORD dst_unused:UNUSED_PAD src0_sel:DWORD src1_sel:WORD_0\n\t" \
  "v_smfmac_f32_16x16x64_bf16 v[40:43], a[148:151], v[126:133], v158 abid:1\n\t" \
  "s_waitcnt lgkmcnt(3)\n\t" \
  "buffer_store_dwordx4 v[24:27], v34, s[20:23], s36 offen sc1\n\t" \
  "v_add_u32_sdwa v2, s29, v118 dst_sel:DWORD dst_unused:UNUSED_PAD src0_sel:DWORD src1_sel:WORD_1\n\t" \
  "v_smfmac_f32_16x16x64_bf16 v[44:47], a[152:155], v[126:133], v159\n\t" \
  "ds_read_b64 v[16:17], v16 offset:0\n\t" \
  "v_xad_u32 v24, v187, 64, s38\n\t" \
  "v_smfmac_f32_16x16x64_bf16 v[48:51], a[156:159], v[126:133], v159 abid:1\n\t" \
  "ds_read_b128 v[24:27], v24 offset:1024\n\t" \
  "v_add_u32_sdwa v4, s29, v119 dst_sel:DWORD dst_unused:UNUSED_PAD src0_sel:DWORD src1_sel:WORD_0\n\t" \
  "v_smfmac_f32_16x16x64_bf16 v[52:55], a[160:163], v[126:133], v160\n\t" \
  "s_waitcnt lgkmcnt(2)\n\t" \
  "ds_read_b64 v[18:19], v18 offset:0\n\t" \
  "v_add_u32_sdwa v6, s29, v119 dst_sel:DWORD dst_unused:UNUSED_PAD src0_sel:DWORD src1_sel:WORD_1\n\t" \
  "s_set_gpr_idx_on s6, gpr_idx(DST)\n\t" \
  "v_smfmac_f32_16x16x64_bf16 v[36:39], v[28:31], v[8:15], v182 abid:1\n\t" \
  "s_set_gpr_idx_off\n\t" \
  "ds_read_b64 v[20:21], v20 offset:0\n\t" \
  "v_add_u32_sdwa v8, s29, v33 dst_sel:DWORD dst_unused:UNUSED_PAD src0_sel:DWORD src1_sel:WORD_0\n\t" \
  "v_smfmac_f32_16x16x64_bf16 v[56:59], a[164:167], v[126:133], v160 abid:1\n\t" \
  "s_add_u32 s36, s36, 4608\n\t" \
  "ds_read_b64 v[22:23], v22 offset:0\n\t" \
  "v_add_u32_sdwa v10, s29, v33 dst_sel:DWORD dst_unused:UNUSED_PAD src0_sel:DWORD src1_sel:WORD_1\n\t" \
  "v_smfmac_f32_16x16x64_bf16 v[60:63], a[168:171], v[126:133], v161\n\t" \
  "s_waitcnt lgkmcnt(3)\n\t" \
  "buffer_store_dwordx4 v[24:27], v34, s[20:23], s36 offen sc1\n\t" \
  "v_add_u32 v28, s60, v187\n\t" \
  "v_smfmac_f32_16x16x64_bf16 v[64:67], a[172:175], v[126:133], v161 abid:1\n\t" \
  "ds_read_b64 v[0:1], v0 offset:0\n\t" \
  "v_add_u32 v24, s38, v187\n\t" \
  "v_smfmac_f32_16x16x64_bf16 v[68:71], a[176:179], v[126:133], v180\n\t" \
  "ds_read_b128 v[24:27], v24 offset:2048\n\t" \
  "v_smfmac_f32_16x16x64_bf16 v[36:39], a[180:183], v[134:141], v162\n\t" \
  "s_waitcnt lgkmcnt(2)\n\t" \
  "ds_read_b128 v[126:129], v183 offset:12288\n\t" \
  "v_smfmac_f32_16x16x64_bf16 v[52:55], v[88:91], v[16:23], v182 abid:1\n\t" \
  "ds_read_b128 v[130:133], v184 offset:12288\n\t" \
  "v_add_u32_sdwa v16, s29, v120 dst_sel:DWORD dst_unused:UNUSED_PAD src0_sel:DWORD src1_sel:WORD_0\n\t" \
  "v_smfmac_f32_16x16x64_bf16 v[40:43], a[184:187], v[134:141], v162 abid:1\n\t" \
  "s_add_u32 s36, s36, 4608\n\t" \
  "ds_read_b64 v[2:3], v2 offset:0\n\t" \
  "v_add_u32_sdwa v18, s29, v120 dst_sel:DWORD dst_unused:UNUSED_PAD src0_sel:DWORD src1_sel:WORD_1\n\t" \
  "v_smfmac_f32_16x16x64_bf16 v[44:47], a[188:191], v[134:141], v163\n\t" \
  "s_waitcnt lgkmcnt(3)\n\t" \
  "buffer_store_dwordx4 v[24:27], v34, s[20:23], s36 offen sc1\n\t" \
  "v_add_u32_sdwa v20, s29, v121 dst_sel:DWORD dst_unused:UNUSED_PAD src0_sel:DWORD src1_sel:WORD_0\n\t" \
  "v_smfmac_f32_16x16x64_bf16 v[48:51], a[192:195], v[134:141], v163 abid:1\n\t" \
  "ds_read_b64 v[4:5], v4 offset:0\n\t" \
  "v_xad_u32 v24, v187, 64, s38\n\t" \
  "v_smfmac_f32_16x16x64_bf16 v[52:55], a[196:199], v[134:141], v164\n\t" \
  "s_add_u32 s38, s38, 18432\n\t" \
  "ds_read_b128 v[24:27], v24 offset:3072\n\t" \
  "v_add_u32_sdwa v22, s29, v121 dst_sel:DWORD dst_unused:UNUSED_PAD src0_sel:DWORD src1_sel:WORD_1\n\t" \
  "v_smfmac_f32_16x16x64_bf16 v[56:59], a[200:203], v[134:141], v164 abid:1\n\t" \
  "s_sub_u32 s44, s38, 129024\n\t" \
  "ds_read_b64 v[6:7], v6 offset:0\n\t" \
  "v_smfmac_f32_16x16x64_bf16 v[60:63], a[204:207], v[134:141], v165\n\t" \
  "s_min_u32 s38, s38, s44\n\t" \
  "ds_read_b64 v[8:9], v8 offset:0\n\t" \
  "v_smfmac_f32_16x16x64_bf16 v[64:67], a[208:211], v[134:141], v165 abid:1\n\t" \
  "s_add_u32 s36, s36, 4608\n\t" \
  "ds_read_b64 v[10:11], v10 offset:0\n\t" \
  "v_smfmac_f32_16x16x64_bf16 v[68:71], a[212:215], v[134:141], v180 abid:1\n\t" \
  "s_waitcnt lgkmcnt(3)\n\t" \
  "buffer_store_dwordx4 v[24:27], v34, s[20:23], s36 offen sc1\n\t" \
  "v_smfmac_f32_16x16x64_bf16 v[36:39], a[216:219], v[126:133], v166\n\t" \
  "s_waitcnt lgkmcnt(2)\n\t" \
  "ds_read_b128 v[134:137], v183 offset:14336\n\t" \
  "v_smfmac_f32_16x16x64_bf16 v[56:59], v[92:95], v[0:7], v182 abid:1\n\t" \
  "s_add_u32 s35, s35, 4718592\n\t" \
  "ds_read_b128 v[138:141], v184 offset:14336\n\t" \
  "v_smfmac_f32_16x16x64_bf16 v[40:43], a[220:223], v[126:133], v166 abid:1\n\t" \
  "ds_read_b128 v[28:31], v28 offset:1280\n\t" \
  "v_smfmac_f32_16x16x64_bf16 v[44:47], a[224:227], v[126:133], v167\n\t" \
  "ds_read_b64 v[16:17], v16 offset:0\n\t" \
  "v_smfmac_f32_16x16x64_bf16 v[48:51], a[228:231], v[126:133], v167 abid:1\n\t" \
  "ds_read_b64 v[18:19], v18 offset:0\n\t" \
  "v_smfmac_f32_16x16x64_bf16 v[52:55], a[232:235], v[126:133], v168\n\t" \
  "ds_read_b64 v[20:21], v20 offset:0\n\t" \
  "v_smfmac_f32_16x16x64_bf16 v[56:59], a[236:239], v[126:133], v168 abid:1\n\t" \
  "s_waitcnt lgkmcnt(3)\n\t" \
  "ds_read_b64 v[22:23], v22 offset:0\n\t" \
  "s_set_gpr_idx_on s7, gpr_idx(DST)\n\t" \
  "v_smfmac_f32_16x16x64_bf16 v[36:39], v[28:31], v[8:15], v182 abid:1\n\t" \
  "s_set_gpr_idx_off\n\t" \
  "v_add_u32_sdwa v8, s29, v122 dst_sel:DWORD dst_unused:UNUSED_PAD src0_sel:DWORD src1_sel:WORD_0\n\t" \
  "v_smfmac_f32_16x16x64_bf16 v[60:63], a[240:243], v[126:133], v169\n\t" \
  "ds_read_b64 v[8:9], v8 offset:0\n\t" \
  "v_add_u32_sdwa v10, s29, v122 dst_sel:DWORD dst_unused:UNUSED_PAD src0_sel:DWORD src1_sel:WORD_1\n\t" \
  "v_smfmac_f32_16x16x64_bf16 v[64:67], a[244:247], v[126:133], v169 abid:1\n\t" \
  "ds_read_b64 v[10:11], v10 offset:0\n\t" \
  "v_add_u32_sdwa v12, s29, v123 dst_sel:DWORD dst_unused:UNUSED_PAD src0_sel:DWORD src1_sel:WORD_0\n\t" \
  "v_smfmac_f32_16x16x64_bf16 v[68:71], a[248:251], v[126:133], v181\n\t" \
  "s_waitcnt lgkmcnt(2)\n\t" \
  "ds_read_b128 v[0:3], v183 offset:16384\n\t" \
  "ds_read_b64 v[12:13], v12 offset:0\n\t" \
  "v_add_u32_sdwa v14, s29, v123 dst_sel:DWORD dst_unused:UNUSED_PAD src0_sel:DWORD src1_sel:WORD_1\n\t" \
  "v_smfmac_f32_16x16x64_bf16 v[60:63], v[96:99], v[16:23], v182 abid:1\n\t" \
  "ds_read_b128 v[4:7], v184 offset:16384\n\t" \
  "v_add_u32_sdwa v16, s29, v124 dst_sel:DWORD dst_unused:UNUSED_PAD src0_sel:DWORD src1_sel:WORD_0\n\t" \
  "v_smfmac_f32_16x16x64_bf16 v[36:39], a[252:255], v[134:141], v170\n\t" \
  "ds_read_b64 v[14:15], v14 offset:0\n\t" \
  "v_add_u32_sdwa v18, s29, v124 dst_sel:DWORD dst_unused:UNUSED_PAD src0_sel:DWORD src1_sel:WORD_1\n\t" \
  "v_smfmac_f32_16x16x64_bf16 v[40:43], v[188:191], v[134:141], v170 abid:1\n\t" \
  "ds_read_b64 v[16:17], v16 offset:0\n\t" \
  "v_add_u32_sdwa v20, s29, v125 dst_sel:DWORD dst_unused:UNUSED_PAD src0_sel:DWORD src1_sel:WORD_0\n\t" \
  "v_smfmac_f32_16x16x64_bf16 v[44:47], v[192:195], v[134:141], v171\n\t" \
  "ds_read_b64 v[18:19], v18 offset:0\n\t" \
  "v_add_u32_sdwa v22, s29, v125 dst_sel:DWORD dst_unused:UNUSED_PAD src0_sel:DWORD src1_sel:WORD_1\n\t" \
  "v_smfmac_f32_16x16x64_bf16 v[48:51], v[196:199], v[134:141], v171 abid:1\n\t" \
  "s_add_u32 s29, s29, s30\n\t" \
  "ds_read_b64 v[20:21], v20 offset:0\n\t" \
  "v_add_u32 v183, s30, v183\n\t" \
  "v_smfmac_f32_16x16x64_bf16 v[52:55], v[200:203], v[134:141], v172\n\t" \
  "s_waitcnt lgkmcnt(3)\n\t" \
  "ds_read_b64 v[22:23], v22 offset:0\n\t" \
  "v_add_u32 v184, s30, v184\n\t" \
  "v_smfmac_f32_16x16x64_bf16 v[64:67], v[100:103], v[8:15], v182 abid:1\n\t" \
  "v_smfmac_f32_16x16x64_bf16 v[56:59], v[204:207], v[134:141], v172 abid:1\n\t" \
  "v_smfmac_f32_16x16x64_bf16 v[60:63], v[208:211], v[134:141], v173\n\t" \
  "v_smfmac_f32_16x16x64_bf16 v[64:67], v[212:215], v[134:141], v173 abid:1\n\t" \
  "s_waitcnt lgkmcnt(0)\n\t" \
  "v_smfmac_f32_16x16x64_bf16 v[68:71], v[104:107], v[16:23], v182 abid:1\n\t" \
  "v_smfmac_f32_16x16x64_bf16 v[68:71], v[216:219], v[134:141], v181 abid:1\n\t" \
  "s_setpc_b64 s[66:67]\n\t" \
  ".fill (.Lvar%=+8192-.)/4, 4, 0xbf800000\n\t" \
  "s_branch .Lfin%=\n\t" \
  "s_branch .Lhe%=\n\t" \
  "s_waitcnt vmcnt(6)\n\t" \
  "s_waitcnt vmcnt(10)\n\t" \
  "s_waitcnt vmcnt(12)\n\t" \
  "s_waitcnt vmcnt(17)\n\t" \
  "s_waitcnt vmcnt(21) lgkmcnt(0)\n\t" \
  "s_barrier\n\t" \
  "v_smfmac_f32_16x16x64_bf16 v[36:39], v[220:223], v[0:7], v174\n\t" \
  "ds_read_b128 v[126:129], v183 offset:0\n\t" \
  "v_add_u32 v24, s37, v185\n\t" \
  "v_lshrrev_b32 v28, 4, v187\n\t" \
  "v_smfmac_f32_16x16x64_bf16 v[40:43], v[224:227], v[0:7], v174 abid:1\n\t" \
  "ds_read_b128 v[130:133], v184 offset:0\n\t" \
  "v_xor_b32 v25, s41, v183\n\t" \
  "v_and_or_b32 v28, v28, 48, s45\n\t" \
  "v_smfmac_f32_16x16x64_bf16 v[44:47], v[228:231], v[0:7], v175\n\t" \
  "ds_read_b128 v[134:137], v183 offset:2048\n\t" \
  "v_subrev_u32 v25, s30, v25\n\t" \
  "v_smfmac_f32_16x16x64_bf16 v[48:51], v[232:235], v[0:7], v175 abid:1\n\t" \
  "s_add_u32 s30, s29, 18432\n\t" \
  "ds_read_b128 v[138:141], v184 offset:2048\n\t" \
  "v_cvt_pk_bf16_f32 v8, v36, v37\n\t" \
  "v_cvt_pk_bf16_f32 v9, v38, v39\n\t" \
  "v_smfmac_f32_16x16x64_bf16 v[52:55], v[236:239], v[0:7], v176\n\t" \
  "ds_read_b128 v[36:39], v28 offset:0\n\t" \
  "v_cvt_pk_bf16_f32 v10, v40, v41\n\t" \
  "v_cvt_pk_bf16_f32 v11, v42, v43\n\t" \
  "v_smfmac_f32_16x16x64_bf16 v[56:59], v[240:243], v[0:7], v176 abid:1\n\t" \
  "s_sub_u32 s25, s30, 129024\n\t" \
  "ds_read_b128 v[40:43], v28 offset:64\n\t" \
  "v_cvt_pk_bf16_f32 v16, v44, v45\n\t" \
  "v_cvt_pk_bf16_f32 v17, v46, v47\n\t" \
  "v_smfmac_f32_16x16x64_bf16 v[60:63], v[244:247], v[0:7], v177\n\t" \
  "ds_read_b128 v[44:47], v28 offset:128\n\t" \
  "v_cvt_pk_bf16_f32 v18, v48, v49\n\t" \
  "v_cvt_pk_bf16_f32 v19, v50, v51\n\t" \
  "v_smfmac_f32_16x16x64_bf16 v[64:67], v[248:251], v[0:7], v177 abid:1\n\t" \
  "s_min_u32 s30, s30, s25\n\t" \
  "ds_read_b128 v[48:51], v28 offset:192\n\t" \
  "v_cvt_pk_bf16_f32 v12, v52, v53\n\t" \
  "v_cvt_pk_bf16_f32 v13, v54, v55\n\t" \
  "v_smfmac_f32_16x16x64_bf16 v[68:71], v[252:255], v[0:7], v182\n\t" \
  "s_waitcnt lgkmcnt(3)\n\t" \
  "ds_read_b128 v[52:55], v28 offset:256\n\t" \
  "v_cvt_pk_bf16_f32 v14, v56, v57\n\t" \
  "v_cvt_pk_bf16_f32 v15, v58, v59\n\t" \
  "v_smfmac_f32_16x16x64_bf16 v[36:39], a[0:3], v[126:133], v142\n\t" \
  "s_waitcnt lgkmcnt(3)\n\t" \
  "ds_read_b128 v[56:59], v28 offset:320\n\t" \
  "v_cvt_pk_bf16_f32 v20, v60, v61\n\t" \
  "v_cvt_pk_bf16_f32 v21, v62, v63\n\t" \
  "v_smfmac_f32_16x16x64_bf16 v[40:43], a[4:7], v[126:133], v142 abid:1\n\t" \
  "s_waitcnt lgkmcnt(3)\n\t" \
  "ds_read_b128 v[60:63], v28 offset:384\n\t" \
  "v_cvt_pk_bf16_f32 v22, v64, v65\n\t" \
  "v_cvt_pk_bf16_f32 v23, v66, v67\n\t" \
  "v_smfmac_f32_16x16x64_bf16 v[44:47], a[8:11], v[126:133], v143\n\t" \
  "s_waitcnt lgkmcnt(3)\n\t" \
  "ds_read_b128 v[64:67], v28 offset:448\n\t" \
  "v_cvt_pk_bf16_f32 v26, v68, v69\n\t" \
  "v_cvt_pk_bf16_f32 v27, v70, v71\n\t" \
  "v_smfmac_f32_16x16x64_bf16 v[48:51], a[12:15], v[126:133], v143 abid:1\n\t" \
  "s_waitcnt lgkmcnt(3)\n\t" \
  "ds_read_b128 v[68:71], v28 offset:512\n\t" \
  "v_add_u32_sdwa v0, s29, v108 dst_sel:DWORD dst_unused:UNUSED_PAD src0_sel:DWORD src1_sel:WORD_0\n\t" \
  "v_smfmac_f32_16x16x64_bf16 v[52:55], a[16:19], v[126:133], v144\n\t" \
  "s_waitcnt lgkmcnt(3)\n\t" \
  "s_bfe_i32 s47, s48, 0x10000\n\t" \
  "ds_write_b64 v24, v[8:9] offset:0\n\t" \
  "v_add_u32_sdwa v2, s29, v108 dst_sel:DWORD dst_unused:UNUSED_PAD src0_sel:DWORD src1_sel:WORD_1\n\t" \
  "v_add_u32 v28, s38, v187\n\t" \
  "v_smfmac_f32_16x16x64_bf16 v[56:59], a[20:23], v[126:133], v144 abid:1\n\t" \
  "s_waitcnt lgkmcnt(3)\n\t" \
  "s_lshr_b32 s48, s48, 1\n\t" \
  "ds_read_b64 v[0:1], v0 offset:0\n\t" \
  "ds_read_b128 v[28:31], v28 offset:0\n\t" \
  "v_add_u32_sdwa v4, s29, v109 dst_sel:DWORD dst_unused:UNUSED_PAD src0_sel:DWORD src1_sel:WORD_0\n\t" \
  "v_smfmac_f32_16x16x64_bf16 v[60:63], a[24:27], v[126:133], v145\n\t" \
  "s_waitcnt lgkmcnt(3)\n\t" \
  "s_add_u32 s36, s35, s39\n\t" \
  "ds_write_b64 v24, v[10:11] offset:8\n\t" \
  "v_add_u32_sdwa v6, s29, v109 dst_sel:DWORD dst_unused:UNUSED_PAD src0_sel:DWORD src1_sel:WORD_1\n\t" \
  "v_smfmac_f32_16x16x64_bf16 v[64:67], a[28:31], v[126:133], v145 abid:1\n\t" \
  "s_waitcnt lgkmcnt(3)\n\t" \
  "s_andn2_b32 s44, 0x40000000, s47\n\t" \
  "ds_read_b64 v[2:3], v2 offset:0\n\t" \
  "v_smfmac_f32_16x16x64_bf16 v[68:71], a[32:35], v[126:133], v178\n\t" \
  "s_sub_u32 s30, s30, s29\n\t" \
  "s_or_b32 s36, s36, s44\n\t" \
  "ds_write_b64 v24, v[12:13] offset:128\n\t" \
  "v_smfmac_f32_16x16x64_bf16 v[36:39], a[36:39], v[134:141], v146\n\t" \
  "s_mov_b32 s37, s29\n\t" \
  "ds_read_b128 v[126:129], v183 offset:4096\n\t" \
  "s_waitcnt lgkmcnt(4)\n\t" \
  "buffer_store_dwordx4 v[28:31], v34, s[20:23], s36 offen sc1\n\t" \
  "v_smfmac_f32_16x16x64_bf16 v[40:43], a[40:43], v[134:141], v146 abid:1\n\t" \
  "s_and_b32 s44, s47, 4718592\n\t" \
  "ds_write_b64 v24, v[14:15] offset:136\n\t" \
  "v_xor_b32 v24, 64, v24\n\t" \
  "v_xad_u32 v28, v187, 64, s38\n\t" \
  "v_smfmac_f32_16x16x64_bf16 v[44:47], a[44:47], v[134:141], v147\n\t" \
  "s_add_u32 s35, s35, s44\n\t" \
  "ds_read_b128 v[130:133], v184 offset:4096\n\t" \
  "ds_read_b128 v[28:31], v28 offset:1024\n\t" \
  "v_add_u32_sdwa v8, s29, v110 dst_sel:DWORD dst_unused:UNUSED_PAD src0_sel:DWORD src1_sel:WORD_0\n\t" \
  "v_smfmac_f32_16x16x64_bf16 v[48:51], a[48:51], v[134:141], v147 abid:1\n\t" \
  "s_add_u32 s36, s36, 4608\n\t" \
  "ds_write_b64 v24, v[16:17] offset:0\n\t" \
  "v_add_u32_sdwa v10, s29, v110 dst_sel:DWORD dst_unused:UNUSED_PAD src0_sel:DWORD src1_sel:WORD_1\n\t" \
  "v_smfmac_f32_16x16x64_bf16 v[52:55], a[52:55], v[134:141], v148\n\t" \
  "ds_read_b64 v[4:5], v4 offset:0\n\t" \
  "v_add_u32_sdwa v12, s29, v111 dst_sel:DWORD dst_unused:UNUSED_PAD src0_sel:DWORD src1_sel:WORD_0\n\t" \
  "v_smfmac_f32_16x16x64_bf16 v[56:59], a[56:59], v[134:141], v148 abid:1\n\t" \
  "ds_write_b64 v24, v[18:19] offset:8\n\t" \
  "v_add_u32_sdwa v14, s29, v111 dst_sel:DWORD dst_unused:UNUSED_PAD src0_sel:DWORD src1_sel:WORD_1\n\t" \
  "v_smfmac_f32_16x16x64_bf16 v[60:63], a[60:63], v[134:141], v149\n\t" \
  "s_and_b32 s27, s64, 7\n\t" \
  "ds_read_b64 v[6:7], v6 offset:0\n\t" \
  "s_waitcnt lgkmcnt(4)\n\t" \
  "buffer_store_dwordx4 v[28:31], v34, s[20:23], s36 offen sc1\n\t" \
  "v_smfmac_f32_16x16x64_bf16 v[64:67], a[64:67], v[134:141], v149 abid:1\n\t" \
  "s_lshl2_add_u32 s66, s27, s68\n\t" \
  "s_and_b32 s44, s47, 18432\n\t" \
  "ds_write_b64 v24, v[20:21] offset:128\n\t" \
  "v_add_u32 v28, s38, v187\n\t" \
  "v_smfmac_f32_16x16x64_bf16 v[68:71], a[68:71], v[134:141], v178 abid:1\n\t" \
  "s_waitcnt lgkmcnt(5)\n\t" \
  "ds_read_b128 v[134:137], v183 offset:6144\n\t" \
  "ds_read_b128 v[28:31], v28 offset:2048\n\t" \
  "v_smfmac_f32_16x16x64_bf16 v[36:39], a[72:75], v[126:133], v150\n\t" \
  "s_and_b32 s27, s64, 8\n\t" \
  "s_add_u32 s36, s36, 4608\n\t" \
  "ds_write_b64 v24, v[22:23] offset:136\n\t" \
  "v_add_u32_sdwa v16, s29, v112 dst_sel:DWORD dst_unused:UNUSED_PAD src0_sel:DWORD src1_sel:WORD_0\n\t" \
  "v_smfmac_f32_16x16x64_bf16 v[40:43], a[76:79], v[126:133], v150 abid:1\n\t" \
  "s_waitcnt lgkmcnt(3)\n\t" \
  "ds_read_b128 v[138:141], v184 offset:6144\n\t" \
  "v_add_u32_sdwa v18, s29, v112 dst_sel:DWORD dst_unused:UNUSED_PAD src0_sel:DWORD src1_sel:WORD_1\n\t" \
  "v_smfmac_f32_16x16x64_bf16 v[36:39], v[72:75], v[0:7], v182 abid:1\n\t" \
  "s_mul_i32 s27, s27, 176\n\t" \
  "ds_write_b64 v25, v[26:27] offset:16384\n\t" \
  "v_add_u32_sdwa v20, s29, v113 dst_sel:DWORD dst_unused:UNUSED_PAD src0_sel:DWORD src1_sel:WORD_0\n\t" \
  "v_smfmac_f32_16x16x64_bf16 v[44:47], a[80:83], v[126:133], v151\n\t" \
  "s_add_u32 s66, s66, s27\n\t" \
  "ds_read_b64 v[8:9], v8 offset:0\n\t" \
  "ds_read_b64 v[10:11], v10 offset:0\n\t" \
  "s_waitcnt lgkmcnt(5)\n\t" \
  "buffer_store_dwordx4 v[28:31], v34, s[20:23], s36 offen sc1\n\t" \
  "v_add_u32_sdwa v22, s29, v113 dst_sel:DWORD dst_unused:UNUSED_PAD src0_sel:DWORD src1_sel:WORD_1\n\t" \
  "v_smfmac_f32_16x16x64_bf16 v[48:51], a[84:87], v[126:133], v151 abid:1\n\t" \
  "s_lshr_b64 s[64:65], s[64:65], 4\n\t" \
  "ds_read_b64 v[12:13], v12 offset:0\n\t" \
  "ds_read_b64 v[14:15], v14 offset:0\n\t" \
  "v_add_u32_sdwa v0, s29, v114 dst_sel:DWORD dst_unused:UNUSED_PAD src0_sel:DWORD src1_sel:WORD_0\n\t" \
  "v_xad_u32 v28, v187, 64, s38\n\t" \
  "v_smfmac_f32_16x16x64_bf16 v[52:55], a[88:91], v[126:133], v152\n\t" \
  "s_add_u32 s38, s38, s44\n\t" \
  "ds_read_b64 v[16:17], v16 offset:0\n\t" \
  "ds_read_b128 v[28:31], v28 offset:3072\n\t" \
  "v_add_u32_sdwa v2, s29, v114 dst_sel:DWORD dst_unused:UNUSED_PAD src0_sel:DWORD src1_sel:WORD_1\n\t" \
  "v_smfmac_f32_16x16x64_bf16 v[56:59], a[92:95], v[126:133], v152 abid:1\n\t" \
  "s_add_u32 s36, s36, 4608\n\t" \
  "ds_read_b64 v[18:19], v18 offset:0\n\t" \
  "v_add_u32_sdwa v4, s29, v115 dst_sel:DWORD dst_unused:UNUSED_PAD src0_sel:DWORD src1_sel:WORD_0\n\t" \
  "v_smfmac_f32_16x16x64_bf16 v[60:63], a[96:99], v[126:133], v153\n\t" \
  "s_sub_u32 s44, s38, 129024\n\t" \
  "ds_read_b64 v[20:21], v20 offset:0\n\t" \
  "v_add_u32_sdwa v6, s29, v115 dst_sel:DWORD dst_unused:UNUSED_PAD src0_sel:DWORD src1_sel:WORD_1\n\t" \
  "v_smfmac_f32_16x16x64_bf16 v[64:67], a[100:103], v[126:133], v153 abid:1\n\t" \
  "s_waitcnt lgkmcnt(3)\n\t" \
  "s_min_u32 s38, s38, s44\n\t" \
  "ds_read_b64 v[22:23], v22 offset:0\n\t" \
  "v_smfmac_f32_16x16x64_bf16 v[40:43], v[76:79], v[8:15], v182 abid:1\n\t" \
  "ds_read_b64 v[0:1], v0 offset:0\n\t" \
  "s_waitcnt lgkmcnt(4)\n\t" \
  "buffer_store_dwordx4 v[28:31], v34, s[20:23], s36 offen sc1\n\t" \
  "v_smfmac_f32_16x16x64_bf16 v[68:71], a[104:107], v[126:133], v179\n\t" \
  "ds_read_b128 v[126:129], v183 offset:8192\n\t" \
  "v_add_u32_sdwa v8, s29, v32 dst_sel:DWORD dst_unused:UNUSED_PAD src0_sel:DWORD src1_sel:WORD_0\n\t" \
  "v_smfmac_f32_16x16x64_bf16 v[36:39], a[108:111], v[134:141], v154\n\t" \
  "s_branch .Ljn03%=\n\t" \
  ".fill (.Lvar%=+9596-.)/4, 4, 0xbf800000\n\t" \
  "s_nop 0\n\t" \
  "s_branch .Lfin%=\n\t" \
  "s_branch .Lhe%=\n\t" \
  "s_waitcnt vmcnt(6)\n\t" \
  "s_waitcnt vmcnt(10)\n\t" \
  "s_waitcnt vmcnt(12)\n\t" \
  "s_waitcnt vmcnt(17)\n\t" \
  "s_waitcnt vmcnt(21) lgkmcnt(0)\n\t" \
  "s_barrier\n\t" \
  "v_smfmac_f32_16x16x64_bf16 v[36:39], v[220:223], v[0:7], v174\n\t" \
  "s_add_u32 m0, s33, 0\n\t" \
  "ds_read_b128 v[126:129], v183 offset:0\n\t" \
  "v_add_u32 v24, s37, v185\n\t" \
  "v_lshrrev_b32 v28, 4, v187\n\t" \
  "v_smfmac_f32_16x16x64_bf16 v[40:43], v[224:227], v[0:7], v174 abid:1\n\t" \
  "buffer_load_dwordx4 v186, s[16:19], s34 offen offset:0 nt lds\n\t" \
  "ds_read_b128 v[130:133], v184 offset:0\n\t" \
  "v_xor_b32 v25, s41, v183\n\t" \
  "v_and_or_b32 v28, v28, 48, s45\n\t" \
  "v_smfmac_f32_16x16x64_bf16 v[44:47], v[228:231], v[0:7], v175\n\t" \
  "s_add_u32 m0, s33, 1920\n\t" \
  "ds_read_b128 v[134:137], v183 offset:2048\n\t" \
  "v_subrev_u32 v25, s30, v25\n\t" \
  "v_smfmac_f32_16x16x64_bf16 v[48:51], v[232:235], v[0:7], v175 abid:1\n\t" \
  "s_add_u32 s30, s29, 18432\n\t" \
  "buffer_load_dwordx4 v186, s[16:19], s34 offen offset:128 nt lds\n\t" \
  "ds_read_b128 v[138:141], v184 offset:2048\n\t" \
  "v_cvt_pk_bf16_f32 v8, v36, v37\n\t" \
  "v_cvt_pk_bf16_f32 v9, v38, v39\n\t" \
  "v_smfmac_f32_16x16x64_bf16 v[52:55], v[236:239], v[0:7], v176\n\t" \
  "s_add_u32 m0, s33, 3840\n\t" \
  "ds_read_b128 v[36:39], v28 offset:0\n\t" \
  "v_cvt_pk_bf16_f32 v10, v40, v41\n\t" \
  "v_cvt_pk_bf16_f32 v11, v42, v43\n\t" \
  "v_smfmac_f32_16x16x64_bf16 v[56:59], v[240:243], v[0:7], v176 abid:1\n\t" \
  "s_sub_u32 s25, s30, 129024\n\t" \
  "buffer_load_dwordx4 v186, s[16:19], s34 offen offset:256 nt lds\n\t" \
  "ds_read_b128 v[40:43], v28 offset:64\n\t" \
  "v_cvt_pk_bf16_f32 v16, v44, v45\n\t" \
  "v_cvt_pk_bf16_f32 v17, v46, v47\n\t" \
  "v_smfmac_f32_16x16x64_bf16 v[60:63], v[244:247], v[0:7], v177\n\t" \
  "s_add_u32 m0, s33, 5760\n\t" \
  "ds_read_b128 v[44:47], v28 offset:128\n\t" \
  "v_cvt_pk_bf16_f32 v18, v48, v49\n\t" \
  "v_cvt_pk_bf16_f32 v19, v50, v51\n\t" \
  "v_smfmac_f32_16x16x64_bf16 v[64:67], v[248:251], v[0:7], v177 abid:1\n\t" \
  "s_min_u32 s30, s30, s25\n\t" \
  "buffer_load_dwordx4 v186, s[16:19], s34 offen offset:384 nt lds\n\t" \
  "ds_read_b128 v[48:51], v28 offset:192\n\t" \
  "v_cvt_pk_bf16_f32 v12, v52, v53\n\t" \
  "v_cvt_pk_bf16_f32 v13, v54, v55\n\t" \
  "v_smfmac_f32_16x16x64_bf16 v[68:71], v[252:255], v[0:7], v182\n\t" \
  "s_add_u32 m0, s33, 7680\n\t" \
  "s_waitcnt lgkmcnt(3)\n\t" \
  "ds_read_b128 v[52:55], v28 offset:256\n\t" \
  "v_cvt_pk_bf16_f32 v14, v56, v57\n\t" \
  "v_cvt_pk_bf16_f32 v15, v58, v59\n\t" \
  "v_smfmac_f32_16x16x64_bf16 v[36:39], a[0:3], v[126:133], v142\n\t" \
  "s_waitcnt lgkmcnt(3)\n\t" \
  "buffer_load_dwordx4 v186, s[16:19], s34 offen offset:512 nt lds\n\t" \
  "ds_read_b128 v[56:59], v28 offset:320\n\t" \
  "v_cvt_pk_bf16_f32 v20, v60, v61\n\t" \
  "v_cvt_pk_bf16_f32 v21, v62, v63\n\t" \
  "v_smfmac_f32_16x16x64_bf16 v[40:43], a[4:7], v[126:133], v142 abid:1\n\t" \
  "s_waitcnt lgkmcnt(3)\n\t" \
  "ds_read_b128 v[60:63], v28 offset:384\n\t" \
  "v_cvt_pk_bf16_f32 v22, v64, v65\n\t" \
  "v_cvt_pk_bf16_f32 v23, v66, v67\n\t" \
  "v_smfmac_f32_16x16x64_bf16 v[44:47], a[8:11], v[126:133], v143\n\t" \
  "s_waitcnt lgkmcnt(3)\n\t" \
  "ds_read_b128 v[64:67], v28 offset:448\n\t" \
  "v_cvt_pk_bf16_f32 v26, v68, v69\n\t" \
  "v_cvt_pk_bf16_f32 v27, v70, v71\n\t" \
  "v_smfmac_f32_16x16x64_bf16 v[48:51], a[12:15], v[126:133], v143 abid:1\n\t" \
  "s_waitcnt lgkmcnt(3)\n\t" \
  "ds_read_b128 v[68:71], v28 offset:512\n\t" \
  "v_add_u32_sdwa v0, s29, v108 dst_sel:DWORD dst_unused:UNUSED_PAD src0_sel:DWORD src1_sel:WORD_0\n\t" \
  "v_smfmac_f32_16x16x64_bf16 v[52:55], a[16:19], v[126:133], v144\n\t" \
  "s_waitcnt lgkmcnt(3)\n\t" \
  "ds_write_b64 v24, v[8:9] offset:0\n\t" \
  "v_add_u32_sdwa v2, s29, v108 dst_sel:DWORD dst_unused:UNUSED_PAD src0_sel:DWORD src1_sel:WORD_1\n\t" \
  "v_smfmac_f32_16x16x64_bf16 v[56:59], a[20:23], v[126:133], v144 abid:1\n\t" \
  "s_waitcnt lgkmcnt(3)\n\t" \
  "ds_read_b64 v[0:1], v0 offset:0\n\t" \
  "v_add_u32_sdwa v4, s29, v109 dst_sel:DWORD dst_unused:UNUSED_PAD src0_sel:DWORD src1_sel:WORD_0\n\t" \
  "v_smfmac_f32_16x16x64_bf16 v[60:63], a[24:27], v[126:133], v145\n\t" \
  "s_waitcnt lgkmcnt(3)\n\t" \
  "ds_write_b64 v24, v[10:11] offset:8\n\t" \
  "v_add_u32_sdwa v6, s29, v109 dst_sel:DWORD dst_unused:UNUSED_PAD src0_sel:DWORD src1_sel:WORD_1\n\t" \
  "v_smfmac_f32_16x16x64_bf16 v[64:67], a[28:31], v[126:133], v145 abid:1\n\t" \
  "s_waitcnt lgkmcnt(3)\n\t" \
  "ds_read_b64 v[2:3], v2 offset:0\n\t" \
  "v_smfmac_f32_16x16x64_bf16 v[68:71], a[32:35], v[126:133], v178\n\t" \
  "s_sub_u32 s30, s30, s29\n\t" \
  "ds_write_b64 v24, v[12:13] offset:128\n\t" \
  "v_smfmac_f32_16x16x64_bf16 v[36:39], a[36:39], v[134:141], v146\n\t" \
  "s_mov_b32 s37, s29\n\t" \
  "ds_read_b128 v[126:129], v183 offset:4096\n\t" \
  "v_smfmac_f32_16x16x64_bf16 v[40:43], a[40:43], v[134:141], v146 abid:1\n\t" \
  "s_add_u32 s31, s31, 18432\n\t" \
  "ds_write_b64 v24, v[14:15] offset:136\n\t" \
  "v_xor_b32 v24, 64, v24\n\t" \
  "v_smfmac_f32_16x16x64_bf16 v[44:47], a[44:47], v[134:141], v147\n\t" \
  "s_sub_u32 s26, s31, 129024\n\t" \
  "ds_read_b128 v[130:133], v184 offset:4096\n\t" \
  "v_add_u32_sdwa v8, s29, v110 dst_sel:DWORD dst_unused:UNUSED_PAD src0_sel:DWORD src1_sel:WORD_0\n\t" \
  "v_smfmac_f32_16x16x64_bf16 v[48:51], a[48:51], v[134:141], v147 abid:1\n\t" \
  "s_min_u32 s31, s31, s26\n\t" \
  "ds_write_b64 v24, v[16:17] offset:0\n\t" \
  "v_add_u32_sdwa v10, s29, v110 dst_sel:DWORD dst_unused:UNUSED_PAD src0_sel:DWORD src1_sel:WORD_1\n\t" \
  "v_smfmac_f32_16x16x64_bf16 v[52:55], a[52:55], v[134:141], v148\n\t" \
  "s_add_u32 s33, s31, s32\n\t" \
  "ds_read_b64 v[4:5], v4 offset:0\n\t" \
  "v_add_u32_sdwa v12, s29, v111 dst_sel:DWORD dst_unused:UNUSED_PAD src0_sel:DWORD src1_sel:WORD_0\n\t" \
  "v_smfmac_f32_16x16x64_bf16 v[56:59], a[56:59], v[134:141], v148 abid:1\n\t" \
  "s_add_u32 s34, s34, 4718592\n\t" \
  "ds_write_b64 v24, v[18:19] offset:8\n\t" \
  "v_add_u32_sdwa v14, s29, v111 dst_sel:DWORD dst_unused:UNUSED_PAD src0_sel:DWORD src1_sel:WORD_1\n\t" \
  "v_smfmac_f32_16x16x64_bf16 v[60:63], a[60:63], v[134:141], v149\n\t" \
  "s_and_b32 s27, s64, 7\n\t" \
  "ds_read_b64 v[6:7], v6 offset:0\n\t" \
  "v_smfmac_f32_16x16x64_bf16 v[64:67], a[64:67], v[134:141], v149 abid:1\n\t" \
  "s_lshl2_add_u32 s66, s27, s68\n\t" \
  "ds_write_b64 v24, v[20:21] offset:128\n\t" \
  "v_smfmac_f32_16x16x64_bf16 v[68:71], a[68:71], v[134:141], v178 abid:1\n\t" \
  "s_waitcnt lgkmcnt(5)\n\t" \
  "ds_read_b128 v[134:137], v183 offset:6144\n\t" \
  "v_smfmac_f32_16x16x64_bf16 v[36:39], a[72:75], v[126:133], v150\n\t" \
  "s_and_b32 s27, s64, 8\n\t" \
  "ds_write_b64 v24, v[22:23] offset:136\n\t" \
  "v_add_u32_sdwa v16, s29, v112 dst_sel:DWORD dst_unused:UNUSED_PAD src0_sel:DWORD src1_sel:WORD_0\n\t" \
  "v_smfmac_f32_16x16x64_bf16 v[40:43], a[76:79], v[126:133], v150 abid:1\n\t" \
  "s_waitcnt lgkmcnt(3)\n\t" \
  "ds_read_b128 v[138:141], v184 offset:6144\n\t" \
  "v_add_u32_sdwa v18, s29, v112 dst_sel:DWORD dst_unused:UNUSED_PAD src0_sel:DWORD src1_sel:WORD_1\n\t" \
  "v_smfmac_f32_16x16x64_bf16 v[36:39], v[72:75], v[0:7], v182 abid:1\n\t" \
  "s_mul_i32 s27, s27, 176\n\t" \
  "ds_write_b64 v25, v[26:27] offset:16384\n\t" \
  "v_add_u32_sdwa v20, s29, v113 dst_sel:DWORD dst_unused:UNUSED_PAD src0_sel:DWORD src1_sel:WORD_0\n\t" \
  "v_smfmac_f32_16x16x64_bf16 v[44:47], a[80:83], v[126:133], v151\n\t" \
  "s_add_u32 s66, s66, s27\n\t" \
  "ds_read_b64 v[8:9], v8 offset:0\n\t" \
  "ds_read_b64 v[10:11], v10 offset:0\n\t" \
  "v_add_u32_sdwa v22, s29, v113 dst_sel:DWORD dst_unused:UNUSED_PAD src0_sel:DWORD src1_sel:WORD_1\n\t" \
  "v_smfmac_f32_16x16x64_bf16 v[48:51], a[84:87], v[126:133], v151 abid:1\n\t" \
  "s_lshr_b64 s[64:65], s[64:65], 4\n\t" \
  "ds_read_b64 v[12:13], v12 offset:0\n\t" \
  "ds_read_b64 v[14:15], v14 offset:0\n\t" \
  "v_add_u32_sdwa v0, s29, v114 dst_sel:DWORD dst_unused:UNUSED_PAD src0_sel:DWORD src1_sel:WORD_0\n\t" \
  "v_smfmac_f32_16x16x64_bf16 v[52:55], a[88:91], v[126:133], v152\n\t" \
  "ds_read_b64 v[16:17], v16 offset:0\n\t" \
  "v_add_u32_sdwa v2, s29, v114 dst_sel:DWORD dst_unused:UNUSED_PAD src0_sel:DWORD src1_sel:WORD_1\n\t" \
  "v_smfmac_f32_16x16x64_bf16 v[56:59], a[92:95], v[126:133], v152 abid:1\n\t" \
  "ds_read_b64 v[18:19], v18 offset:0\n\t" \
  "v_add_u32_sdwa v4, s29, v115 dst_sel:DWORD dst_unused:UNUSED_PAD src0_sel:DWORD src1_sel:WORD_0\n\t" \
  "v_smfmac_f32_16x16x64_bf16 v[60:63], a[96:99], v[126:133], v153\n\t" \
  "ds_read_b64 v[20:21], v20 offset:0\n\t" \
  "v_add_u32_sdwa v6, s29, v115 dst_sel:DWORD dst_unused:UNUSED_PAD src0_sel:DWORD src1_sel:WORD_1\n\t" \
  "v_smfmac_f32_16x16x64_bf16 v[64:67], a[100:103], v[126:133], v153 abid:1\n\t" \
  "s_waitcnt lgkmcnt(3)\n\t" \
  "ds_read_b64 v[22:23], v22 offset:0\n\t" \
  "v_smfmac_f32_16x16x64_bf16 v[40:43], v[76:79], v[8:15], v182 abid:1\n\t" \
  "ds_read_b64 v[0:1], v0 offset:0\n\t" \
  "v_smfmac_f32_16x16x64_bf16 v[68:71], a[104:107], v[126:133], v179\n\t" \
  "ds_read_b128 v[126:129], v183 offset:8192\n\t" \
  "v_add_u32_sdwa v8, s29, v32 dst_sel:DWORD dst_unused:UNUSED_PAD src0_sel:DWORD src1_sel:WORD_0\n\t" \
  "v_smfmac_f32_16x16x64_bf16 v[36:39], a[108:111], v[134:141], v154\n\t" \
  ".Ljn03%=:\n\t" \
  "ds_read_b128 v[130:133], v184 offset:8192\n\t" \
  "v_add_u32_sdwa v10, s29, v32 dst_sel:DWORD dst_unused:UNUSED_PAD src0_sel:DWORD src1_sel:WORD_1\n\t" \
  "v_smfmac_f32_16x16x64_bf16 v[40:43], a[112:115], v[134:141], v154 abid:1\n\t" \
  "s_waitcnt lgkmcnt(3)\n\t" \
  "ds_read_b64 v[2:3], v2 offset:0\n\t" \
  "v_add_u32 v28, s60, v187\n\t" \
  "v_smfmac_f32_16x16x64_bf16 v[44:47], v[80:83], v[16:23], v182 abid:1\n\t" \
  "ds_read_b64 v[4:5], v4 offset:0\n\t" \
  "v_add_u32_sdwa v16, s29, v116 dst_sel:DWORD dst_unused:UNUSED_PAD src0_sel:DWORD src1_sel:WORD_0\n\t" \
  "v_smfmac_f32_16x16x64_bf16 v[44:47], a[116:119], v[134:141], v155\n\t" \
  "ds_read_b64 v[6:7], v6 offset:0\n\t" \
  "v_add_u32_sdwa v18, s29, v116 dst_sel:DWORD dst_unused:UNUSED_PAD src0_sel:DWORD src1_sel:WORD_1\n\t" \
  "v_smfmac_f32_16x16x64_bf16 v[48:51], a[120:123], v[134:141], v155 abid:1\n\t" \
  "ds_read_b64 v[8:9], v8 offset:0\n\t" \
  "v_add_u32_sdwa v20, s29, v117 dst_sel:DWORD dst_unused:UNUSED_PAD src0_sel:DWORD src1_sel:WORD_0\n\t" \
  "v_smfmac_f32_16x16x64_bf16 v[52:55], a[124:127], v[134:141], v156\n\t" \
  "ds_read_b64 v[10:11], v10 offset:0\n\t" \
  "v_add_u32_sdwa v22, s29, v117 dst_sel:DWORD dst_unused:UNUSED_PAD src0_sel:DWORD src1_sel:WORD_1\n\t" \
  "v_smfmac_f32_16x16x64_bf16 v[56:59], a[128:131], v[134:141], v156 abid:1\n\t" \
  "ds_read_b128 v[28:31], v28 offset:0\n\t" \
  "v_smfmac_f32_16x16x64_bf16 v[60:63], a[132:135], v[134:141], v157\n\t" \
  "s_waitcnt lgkmcnt(3)\n\t" \
  "ds_read_b64 v[16:17], v16 offset:0\n\t" \
  "v_add_u32 v24, s38, v187\n\t" \
  "v_smfmac_f32_16x16x64_bf16 v[48:51], v[84:87], v[0:7], v182 abid:1\n\t" \
  "ds_read_b128 v[24:27], v24 offset:0\n\t" \
  "v_add_u32_sdwa v0, s29, v118 dst_sel:DWORD dst_unused:UNUSED_PAD src0_sel:DWORD src1_sel:WORD_0\n\t" \
  "v_smfmac_f32_16x16x64_bf16 v[64:67], a[136:139], v[134:141], v157 abid:1\n\t" \
  "ds_read_b64 v[18:19], v18 offset:0\n\t" \
  "v_add_u32_sdwa v2, s29, v118 dst_sel:DWORD dst_unused:UNUSED_PAD src0_sel:DWORD src1_sel:WORD_1\n\t" \
  "v_smfmac_f32_16x16x64_bf16 v[68:71], a[140:143], v[134:141], v179 abid:1\n\t" \
  "s_waitcnt lgkmcnt(3)\n\t" \
  "ds_read_b128 v[134:137], v183 offset:10240\n\t" \
  "v_add_u32_sdwa v4, s29, v119 dst_sel:DWORD dst_unused:UNUSED_PAD src0_sel:DWORD src1_sel:WORD_0\n\t" \
  "s_set_gpr_idx_on s6, gpr_idx(DST)\n\t" \
  "v_smfmac_f32_16x16x64_bf16 v[36:39], v[28:31], v[8:15], v182 abid:1\n\t" \
  "s_set_gpr_idx_off\n\t" \
  "s_add_u32 s36, s35, s39\n\t" \
  "ds_read_b128 v[138:141], v184 offset:10240\n\t" \
  "v_add_u32_sdwa v6, s29, v119 dst_sel:DWORD dst_unused:UNUSED_PAD src0_sel:DWORD src1_sel:WORD_1\n\t" \
  "v_smfmac_f32_16x16x64_bf16 v[36:39], a[144:147], v[126:133], v158\n\t" \
  "s_waitcnt lgkmcnt(3)\n\t" \
  "buffer_store_dwordx4 v[24:27], v34, s[20:23], s36 offen sc1\n\t" \
  "v_add_u32_sdwa v8, s29, v33 dst_sel:DWORD dst_unused:UNUSED_PAD src0_sel:DWORD src1_sel:WORD_0\n\t" \
  "v_smfmac_f32_16x16x64_bf16 v[40:43], a[148:151], v[126:133], v158 abid:1\n\t" \
  "ds_read_b64 v[20:21], v20 offset:0\n\t" \
  "v_xad_u32 v24, v187, 64, s38\n\t" \
  "v_smfmac_f32_16x16x64_bf16 v[44:47], a[152:155], v[126:133], v159\n\t" \
  "ds_read_b128 v[24:27], v24 offset:1024\n\t" \
  "v_add_u32_sdwa v10, s29, v33 dst_sel:DWORD dst_unused:UNUSED_PAD src0_sel:DWORD src1_sel:WORD_1\n\t" \
  "v_smfmac_f32_16x16x64_bf16 v[48:51], a[156:159], v[126:133], v159 abid:1\n\t" \
  "ds_read_b64 v[22:23], v22 offset:0\n\t" \
  "v_add_u32 v28, s60, v187\n\t" \
  "v_smfmac_f32_16x16x64_bf16 v[52:55], a[160:163], v[126:133], v160\n\t" \
  "ds_read_b64 v[0:1], v0 offset:0\n\t" \
  "v_smfmac_f32_16x16x64_bf16 v[56:59], a[164:167], v[126:133], v160 abid:1\n\t" \
  "s_add_u32 s36, s36, 4608\n\t" \
  "ds_read_b64 v[2:3], v2 offset:0\n\t" \
  "v_smfmac_f32_16x16x64_bf16 v[60:63], a[168:171], v[126:133], v161\n\t" \
  "s_waitcnt lgkmcnt(3)\n\t" \
  "buffer_store_dwordx4 v[24:27], v34, s[20:23], s36 offen sc1\n\t" \
  "v_smfmac_f32_16x16x64_bf16 v[64:67], a[172:175], v[126:133], v161 abid:1\n\t" \
  "s_waitcnt lgkmcnt(2)\n\t" \
  "ds_read_b64 v[4:5], v4 offset:0\n\t" \
  "v_add_u32 v24, s38, v187\n\t" \
  "v_smfmac_f32_16x16x64_bf16 v[52:55], v[88:91], v[16:23], v182 abid:1\n\t" \
  "ds_read_b128 v[24:27], v24 offset:2048\n\t" \
  "v_add_u32_sdwa v16, s29, v120 dst_sel:DWORD dst_unused:UNUSED_PAD src0_sel:DWORD src1_sel:WORD_0\n\t" \
  "v_smfmac_f32_16x16x64_bf16 v[68:71], a[176:179], v[126:133], v180\n\t" \
  "ds_read_b128 v[126:129], v183 offset:12288\n\t" \
  "v_add_u32_sdwa v18, s29, v120 dst_sel:DWORD dst_unused:UNUSED_PAD src0_sel:DWORD src1_sel:WORD_1\n\t" \
  "v_smfmac_f32_16x16x64_bf16 v[36:39], a[180:183], v[134:141], v162\n\t" \
  "ds_read_b128 v[130:133], v184 offset:12288\n\t" \
  "v_add_u32_sdwa v20, s29, v121 dst_sel:DWORD dst_unused:UNUSED_PAD src0_sel:DWORD src1_sel:WORD_0\n\t" \
  "v_smfmac_f32_16x16x64_bf16 v[40:43], a[184:187], v[134:141], v162 abid:1\n\t" \
  "s_add_u32 s36, s36, 4608\n\t" \
  "ds_read_b64 v[6:7], v6 offset:0\n\t" \
  "v_add_u32_sdwa v22, s29, v121 dst_sel:DWORD dst_unused:UNUSED_PAD src0_sel:DWORD src1_sel:WORD_1\n\t" \
  "v_smfmac_f32_16x16x64_bf16 v[44:47], a[188:191], v[134:141], v163\n\t" \
  "s_waitcnt lgkmcnt(3)\n\t" \
  "buffer_store_dwordx4 v[24:27], v34, s[20:23], s36 offen sc1\n\t" \
  "v_smfmac_f32_16x16x64_bf16 v[48:51], a[192:195], v[134:141], v163 abid:1\n\t" \
  "ds_read_b64 v[8:9], v8 offset:0\n\t" \
  "v_xad_u32 v24, v187, 64, s38\n\t" \
  "v_smfmac_f32_16x16x64_bf16 v[52:55], a[196:199], v[134:141], v164\n\t" \
  "s_add_u32 s38, s38, 18432\n\t" \
  "ds_read_b128 v[24:27], v24 offset:3072\n\t" \
  "v_smfmac_f32_16x16x64_bf16 v[56:59], a[200:203], v[134:141], v164 abid:1\n\t" \
  "s_waitcnt lgkmcnt(2)\n\t" \
  "ds_read_b64 v[10:11], v10 offset:0\n\t" \
  "v_smfmac_f32_16x16x64_bf16 v[56:59], v[92:95], v[0:7], v182 abid:1\n\t" \
  "s_sub_u32 s44, s38, 129024\n\t" \
  "ds_read_b128 v[28:31], v28 offset:1280\n\t" \
  "v_smfmac_f32_16x16x64_bf16 v[60:63], a[204:207], v[134:141], v165\n\t" \
  "s_add_u32 s36, s36, 4608\n\t" \
  "ds_read_b64 v[16:17], v16 offset:0\n\t" \
  "v_smfmac_f32_16x16x64_bf16 v[64:67], a[208:211], v[134:141], v165 abid:1\n\t" \
  "s_waitcnt lgkmcnt(3)\n\t" \
  "buffer_store_dwordx4 v[24:27], v34, s[20:23], s36 offen sc1\n\t" \
  "v_smfmac_f32_16x16x64_bf16 v[68:71], a[212:215], v[134:141], v180 abid:1\n\t" \
  "s_min_u32 s38, s38, s44\n\t" \
  "ds_read_b128 v[134:137], v183 offset:14336\n\t" \
  "v_smfmac_f32_16x16x64_bf16 v[36:39], a[216:219], v[126:133], v166\n\t" \
  "s_waitcnt lgkmcnt(2)\n\t" \
  "ds_read_b128 v[138:141], v184 offset:14336\n\t" \
  "s_set_gpr_idx_on s7, gpr_idx(DST)\n\t" \
  "v_smfmac_f32_16x16x64_bf16 v[36:39], v[28:31], v[8:15], v182 abid:1\n\t" \
  "s_set_gpr_idx_off\n\t" \
  "ds_read_b64 v[18:19], v18 offset:0\n\t" \
  "v_add_u32_sdwa v8, s29, v122 dst_sel:DWORD dst_unused:UNUSED_PAD src0_sel:DWORD src1_sel:WORD_0\n\t" \
  "v_smfmac_f32_16x16x64_bf16 v[40:43], a[220:223], v[126:133], v166 abid:1\n\t" \
  "s_add_u32 s35, s35, 4718592\n\t" \
  "ds_read_b64 v[20:21], v20 offset:0\n\t" \
  "v_add_u32_sdwa v10, s29, v122 dst_sel:DWORD dst_unused:UNUSED_PAD src0_sel:DWORD src1_sel:WORD_1\n\t" \
  "v_smfmac_f32_16x16x64_bf16 v[44:47], a[224:227], v[126:133], v167\n\t" \
  "ds_read_b64 v[22:23], v22 offset:0\n\t" \
  "v_add_u32_sdwa v12, s29, v123 dst_sel:DWORD dst_unused:UNUSED_PAD src0_sel:DWORD src1_sel:WORD_0\n\t" \
  "v_smfmac_f32_16x16x64_bf16 v[48:51], a[228:231], v[126:133], v167 abid:1\n\t" \
  "ds_read_b64 v[8:9], v8 offset:0\n\t" \
  "v_add_u32_sdwa v14, s29, v123 dst_sel:DWORD dst_unused:UNUSED_PAD src0_sel:DWORD src1_sel:WORD_1\n\t" \
  "v_smfmac_f32_16x16x64_bf16 v[52:55], a[232:235], v[126:133], v168\n\t" \
  "ds_read_b64 v[10:11], v10 offset:0\n\t" \
  "v_smfmac_f32_16x16x64_bf16 v[56:59], a[236:239], v[126:133], v168 abid:1\n\t" \
  "ds_read_b64 v[12:13], v12 offset:0\n\t" \
  "v_smfmac_f32_16x16x64_bf16 v[60:63], a[240:243], v[126:133], v169\n\t" \
  "s_waitcnt lgkmcnt(3)\n\t" \
  "ds_read_b64 v[14:15], v14 offset:0\n\t" \
  "v_smfmac_f32_16x16x64_bf16 v[60:63], v[96:99], v[16:23], v182 abid:1\n\t" \
  "v_add_u32_sdwa v16, s29, v35 dst_sel:DWORD dst_unused:UNUSED_PAD src0_sel:DWORD src1_sel:WORD_0\n\t" \
  "v_smfmac_f32_16x16x64_bf16 v[64:67], a[244:247], v[126:133], v169 abid:1\n\t" \
  "ds_read_b64 v[16:17], v16 offset:0\n\t" \
  "v_add_u32_sdwa v18, s29, v35 dst_sel:DWORD dst_unused:UNUSED_PAD src0_sel:DWORD src1_sel:WORD_1\n\t" \
  "v_smfmac_f32_16x16x64_bf16 v[68:71], a[248:251], v[126:133], v181\n\t" \
  "ds_read_b128 v[0:3], v183 offset:16384\n\t" \
  "ds_read_b64 v[18:19], v18 offset:0\n\t" \
  "v_add_u32 v28, s60, v187\n\t" \
  "v_smfmac_f32_16x16x64_bf16 v[36:39], a[252:255], v[134:141], v170\n\t" \
  "s_waitcnt lgkmcnt(3)\n\t" \
  "ds_read_b128 v[4:7], v184 offset:16384\n\t" \
  "v_add_u32 v183, s30, v183\n\t" \
  "v_smfmac_f32_16x16x64_bf16 v[64:67], v[100:103], v[8:15], v182 abid:1\n\t" \
  "ds_read_b128 v[28:31], v28 offset:2560\n\t" \
  "v_add_u32_sdwa v8, s29, v124 dst_sel:DWORD dst_unused:UNUSED_PAD src0_sel:DWORD src1_sel:WORD_0\n\t" \
  "v_smfmac_f32_16x16x64_bf16 v[40:43], v[188:191], v[134:141], v170 abid:1\n\t" \
  "ds_read_b64 v[8:9], v8 offset:0\n\t" \
  "v_add_u32_sdwa v10, s29, v124 dst_sel:DWORD dst_unused:UNUSED_PAD src0_sel:DWORD src1_sel:WORD_1\n\t" \
  "v_smfmac_f32_16x16x64_bf16 v[44:47], v[192:195], v[134:141], v171\n\t" \
  "ds_read_b64 v[10:11], v10 offset:0\n\t" \
  "v_add_u32_sdwa v12, s29, v125 dst_sel:DWORD dst_unused:UNUSED_PAD src0_sel:DWORD src1_sel:WORD_0\n\t" \
  "v_smfmac_f32_16x16x64_bf16 v[48:51], v[196:199], v[134:141], v171 abid:1\n\t" \
  "ds_read_b64 v[12:13], v12 offset:0\n\t" \
  "v_add_u32_sdwa v14, s29, v125 dst_sel:DWORD dst_unused:UNUSED_PAD src0_sel:DWORD src1_sel:WORD_1\n\t" \
  "v_smfmac_f32_16x16x64_bf16 v[52:55], v[200:203], v[134:141], v172\n\t" \
  "s_waitcnt lgkmcnt(3)\n\t" \
  "ds_read_b64 v[14:15], v14 offset:0\n\t" \
  "v_add_u32 v184, s30, v184\n\t" \
  "s_set_gpr_idx_on s8, gpr_idx(DST)\n\t" \
  "v_smfmac_f32_16x16x64_bf16 v[36:39], v[28:31], v[16:23], v182 abid:1\n\t" \
  "s_set_gpr_idx_off\n\t" \
  "v_smfmac_f32_16x16x64_bf16 v[56:59], v[204:207], v[134:141], v172 abid:1\n\t" \
  "s_add_u32 s29, s29, s30\n\t" \
  "v_smfmac_f32_16x16x64_bf16 v[60:63], v[208:211], v[134:141], v173\n\t" \
  "v_smfmac_f32_16x16x64_bf16 v[64:67], v[212:215], v[134:141], v173 abid:1\n\t" \
  "s_waitcnt lgkmcnt(0)\n\t" \
  "v_smfmac_f32_16x16x64_bf16 v[68:71], v[104:107], v[8:15], v182 abid:1\n\t" \
  "v_smfmac_f32_16x16x64_bf16 v[68:71], v[216:219], v[134:141], v181 abid:1\n\t" \
  "s_setpc_b64 s[66:67]\n\t" \
  ".fill (.Lvar%=+12288-.)/4, 4, 0xbf800000\n\t" \
  "s_branch .Lfin%=\n\t" \
  "s_branch .Lhe%=\n\t" \
  "s_waitcnt vmcnt(6)\n\t" \
  "s_waitcnt vmcnt(10)\n\t" \
  "s_waitcnt vmcnt(12)\n\t" \
  "s_waitcnt vmcnt(17)\n\t" \
  "s_waitcnt vmcnt(21) lgkmcnt(0)\n\t" \
  "s_cmp_lt_u32 s72, s52\n\t" \
  "s_cbranch_scc1 .Lns11%=\n\t" \
  ".Lng11%=:\n\t" \
  "s_barrier\n\t" \
  "v_smfmac_f32_16x16x64_bf16 v[36:39], v[220:223], v[0:7], v174\n\t" \
  "s_movk_i32 s53, 1952\n\t" \
  "ds_read_b128 v[126:129], v183 offset:0\n\t" \
  "v_add_u32 v24, s37, v185\n\t" \
  "v_smfmac_f32_16x16x64_bf16 v[40:43], v[224:227], v[0:7], v174 abid:1\n\t" \
  "ds_read_b128 v[130:133], v184 offset:0\n\t" \
  "v_xor_b32 v25, s41, v183\n\t" \
  "v_smfmac_f32_16x16x64_bf16 v[44:47], v[228:231], v[0:7], v175\n\t" \
  "s_mov_b32 s51, s53\n\t" \
  "ds_read_b128 v[134:137], v183 offset:2048\n\t" \
  "v_subrev_u32 v25, s30, v25\n\t" \
  "v_smfmac_f32_16x16x64_bf16 v[48:51], v[232:235], v[0:7], v175 abid:1\n\t" \
  "s_add_u32 s30, s29, 18432\n\t" \
  "ds_read_b128 v[138:141], v184 offset:2048\n\t" \
  "v_cvt_pk_bf16_f32 v8, v36, v37\n\t" \
  "v_cvt_pk_bf16_f32 v9, v38, v39\n\t" \
  "v_smfmac_f32_16x16x64_bf16 v[52:55], v[236:239], v[0:7], v176\n\t" \
  "ds_read_b128 v[36:39], v35 offset:0\n\t" \
  "v_cvt_pk_bf16_f32 v10, v40, v41\n\t" \
  "v_cvt_pk_bf16_f32 v11, v42, v43\n\t" \
  "v_smfmac_f32_16x16x64_bf16 v[56:59], v[240:243], v[0:7], v176 abid:1\n\t" \
  "s_sub_u32 s25, s30, 129024\n\t" \
  "ds_read_b128 v[40:43], v35 offset:64\n\t" \
  "v_cvt_pk_bf16_f32 v16, v44, v45\n\t" \
  "v_cvt_pk_bf16_f32 v17, v46, v47\n\t" \
  "v_smfmac_f32_16x16x64_bf16 v[60:63], v[244:247], v[0:7], v177\n\t" \
  "ds_read_b128 v[44:47], v35 offset:128\n\t" \
  "v_cvt_pk_bf16_f32 v18, v48, v49\n\t" \
  "v_cvt_pk_bf16_f32 v19, v50, v51\n\t" \
  "v_smfmac_f32_16x16x64_bf16 v[64:67], v[248:251], v[0:7], v177 abid:1\n\t" \
  "s_min_u32 s30, s30, s25\n\t" \
  "ds_read_b128 v[48:51], v35 offset:192\n\t" \
  "v_cvt_pk_bf16_f32 v12, v52, v53\n\t" \
  "v_cvt_pk_bf16_f32 v13, v54, v55\n\t" \
  "v_smfmac_f32_16x16x64_bf16 v[68:71], v[252:255], v[0:7], v182\n\t" \
  "s_waitcnt lgkmcnt(3)\n\t" \
  "ds_read_b128 v[52:55], v35 offset:256\n\t" \
  "v_cvt_pk_bf16_f32 v14, v56, v57\n\t" \
  "v_cvt_pk_bf16_f32 v15, v58, v59\n\t" \
  "v_smfmac_f32_16x16x64_bf16 v[36:39], a[0:3], v[126:133], v142\n\t" \
  "s_waitcnt lgkmcnt(3)\n\t" \
  "ds_read_b128 v[56:59], v35 offset:320\n\t" \
  "v_cvt_pk_bf16_f32 v20, v60, v61\n\t" \
  "v_cvt_pk_bf16_f32 v21, v62, v63\n\t" \
  "v_smfmac_f32_16x16x64_bf16 v[40:43], a[4:7], v[126:133], v142 abid:1\n\t" \
  "s_waitcnt lgkmcnt(3)\n\t" \
  "ds_read_b128 v[60:63], v35 offset:384\n\t" \
  "v_cvt_pk_bf16_f32 v22, v64, v65\n\t" \
  "v_cvt_pk_bf16_f32 v23, v66, v67\n\t" \
  "v_smfmac_f32_16x16x64_bf16 v[44:47], a[8:11], v[126:133], v143\n\t" \
  "s_waitcnt lgkmcnt(3)\n\t" \
  "ds_read_b128 v[64:67], v35 offset:448\n\t" \
  "v_cvt_pk_bf16_f32 v26, v68, v69\n\t" \
  "v_cvt_pk_bf16_f32 v27, v70, v71\n\t" \
  "v_smfmac_f32_16x16x64_bf16 v[48:51], a[12:15], v[126:133], v143 abid:1\n\t" \
  "s_waitcnt lgkmcnt(3)\n\t" \
  "ds_read_b128 v[68:71], v35 offset:512\n\t" \
  "v_add_u32_sdwa v0, s29, v108 dst_sel:DWORD dst_unused:UNUSED_PAD src0_sel:DWORD src1_sel:WORD_0\n\t" \
  "v_smfmac_f32_16x16x64_bf16 v[52:55], a[16:19], v[126:133], v144\n\t" \
  "s_waitcnt lgkmcnt(3)\n\t" \
  "ds_write_b64 v24, v[8:9] offset:0\n\t" \
  "v_add_u32_sdwa v2, s29, v108 dst_sel:DWORD dst_unused:UNUSED_PAD src0_sel:DWORD src1_sel:WORD_1\n\t" \
  "v_smfmac_f32_16x16x64_bf16 v[56:59], a[20:23], v[126:133], v144 abid:1\n\t" \
  "s_waitcnt lgkmcnt(3)\n\t" \
  "s_bfe_i32 s47, s48, 0x10000\n\t" \
  "ds_read_b64 v[0:1], v0 offset:0\n\t" \
  "v_add_u32_sdwa v4, s29, v109 dst_sel:DWORD dst_unused:UNUSED_PAD src0_sel:DWORD src1_sel:WORD_0\n\t" \
  "v_add_u32 v28, s38, v187\n\t" \
  "v_smfmac_f32_16x16x64_bf16 v[60:63], a[24:27], v[126:133], v145\n\t" \
  "s_waitcnt lgkmcnt(3)\n\t" \
  "s_lshr_b32 s48, s48, 1\n\t" \
  "ds_write_b64 v24, v[10:11] offset:8\n\t" \
  "ds_read_b128 v[28:31], v28 offset:4096\n\t" \
  "v_add_u32_sdwa v6, s29, v109 dst_sel:DWORD dst_unused:UNUSED_PAD src0_sel:DWORD src1_sel:WORD_1\n\t" \
  "v_smfmac_f32_16x16x64_bf16 v[64:67], a[28:31], v[126:133], v145 abid:1\n\t" \
  "s_waitcnt lgkmcnt(3)\n\t" \
  "s_add_u32 s36, s35, s39\n\t" \
  "ds_read_b64 v[2:3], v2 offset:0\n\t" \
  "v_smfmac_f32_16x16x64_bf16 v[68:71], a[32:35], v[126:133], v178\n\t" \
  "s_sub_u32 s30, s30, s29\n\t" \
  "s_andn2_b32 s44, 0x40000000, s47\n\t" \
  "ds_write_b64 v24, v[12:13] offset:128\n\t" \
  "v_smfmac_f32_16x16x64_bf16 v[36:39], a[36:39], v[134:141], v146\n\t" \
  "s_mov_b32 s37, s29\n\t" \
  "s_or_b32 s36, s36, s44\n\t" \
  "ds_read_b128 v[126:129], v183 offset:4096\n\t" \
  "v_smfmac_f32_16x16x64_bf16 v[40:43], a[40:43], v[134:141], v146 abid:1\n\t" \
  "ds_write_b64 v24, v[14:15] offset:136\n\t" \
  "s_waitcnt lgkmcnt(4)\n\t" \
  "buffer_store_dwordx4 v[28:31], v34, s[20:23], s36 offen sc1\n\t" \
  "v_xor_b32 v24, 64, v24\n\t" \
  "v_smfmac_f32_16x16x64_bf16 v[44:47], a[44:47], v[134:141], v147\n\t" \
  "s_and_b32 s44, s47, 4718592\n\t" \
  "ds_read_b128 v[130:133], v184 offset:4096\n\t" \
  "v_add_u32_sdwa v8, s29, v110 dst_sel:DWORD dst_unused:UNUSED_PAD src0_sel:DWORD src1_sel:WORD_0\n\t" \
  "v_xad_u32 v28, v187, 64, s38\n\t" \
  "v_smfmac_f32_16x16x64_bf16 v[48:51], a[48:51], v[134:141], v147 abid:1\n\t" \
  "s_add_u32 s35, s35, s44\n\t" \
  "ds_write_b64 v24, v[16:17] offset:0\n\t" \
  "ds_read_b128 v[28:31], v28 offset:5120\n\t" \
  "v_add_u32_sdwa v10, s29, v110 dst_sel:DWORD dst_unused:UNUSED_PAD src0_sel:DWORD src1_sel:WORD_1\n\t" \
  "v_smfmac_f32_16x16x64_bf16 v[52:55], a[52:55], v[134:141], v148\n\t" \
  "s_add_u32 s36, s36, 4608\n\t" \
  "ds_read_b64 v[4:5], v4 offset:0\n\t" \
  "v_add_u32_sdwa v12, s29, v111 dst_sel:DWORD dst_unused:UNUSED_PAD src0_sel:DWORD src1_sel:WORD_0\n\t" \
  "v_smfmac_f32_16x16x64_bf16 v[56:59], a[56:59], v[134:141], v148 abid:1\n\t" \
  "ds_write_b64 v24, v[18:19] offset:8\n\t" \
  "v_add_u32_sdwa v14, s29, v111 dst_sel:DWORD dst_unused:UNUSED_PAD src0_sel:DWORD src1_sel:WORD_1\n\t" \
  "v_smfmac_f32_16x16x64_bf16 v[60:63], a[60:63], v[134:141], v149\n\t" \
  "s_and_b32 s27, s64, 7\n\t" \
  "ds_read_b64 v[6:7], v6 offset:0\n\t" \
  "v_smfmac_f32_16x16x64_bf16 v[64:67], a[64:67], v[134:141], v149 abid:1\n\t" \
  "s_lshl2_add_u32 s66, s27, s68\n\t" \
  "ds_write_b64 v24, v[20:21] offset:128\n\t" \
  "s_waitcnt lgkmcnt(4)\n\t" \
  "buffer_store_dwordx4 v[28:31], v34, s[20:23], s36 offen sc1\n\t" \
  "v_smfmac_f32_16x16x64_bf16 v[68:71], a[68:71], v[134:141], v178 abid:1\n\t" \
  "s_waitcnt lgkmcnt(5)\n\t" \
  "s_and_b32 s44, s47, 18432\n\t" \
  "ds_read_b128 v[134:137], v183 offset:6144\n\t" \
  "v_add_u32 v28, s38, v187\n\t" \
  "v_smfmac_f32_16x16x64_bf16 v[36:39], a[72:75], v[126:133], v150\n\t" \
  "s_and_b32 s27, s64, 8\n\t" \
  "ds_write_b64 v24, v[22:23] offset:136\n\t" \
  "ds_read_b128 v[28:31], v28 offset:6144\n\t" \
  "v_add_u32_sdwa v16, s29, v112 dst_sel:DWORD dst_unused:UNUSED_PAD src0_sel:DWORD src1_sel:WORD_0\n\t" \
  "v_smfmac_f32_16x16x64_bf16 v[40:43], a[76:79], v[126:133], v150 abid:1\n\t" \
  "s_waitcnt lgkmcnt(3)\n\t" \
  "s_add_u32 s36, s36, 4608\n\t" \
  "ds_read_b128 v[138:141], v184 offset:6144\n\t" \
  "v_add_u32_sdwa v18, s29, v112 dst_sel:DWORD dst_unused:UNUSED_PAD src0_sel:DWORD src1_sel:WORD_1\n\t" \
  "v_smfmac_f32_16x16x64_bf16 v[36:39], v[72:75], v[0:7], v182 abid:1\n\t" \
  "s_mul_i32 s27, s27, 176\n\t" \
  "ds_write_b64 v25, v[26:27] offset:16384\n\t" \
  "v_add_u32_sdwa v20, s29, v113 dst_sel:DWORD dst_unused:UNUSED_PAD src0_sel:DWORD src1_sel:WORD_0\n\t" \
  "v_smfmac_f32_16x16x64_bf16 v[44:47], a[80:83], v[126:133], v151\n\t" \
  "s_add_u32 s66, s66, s27\n\t" \
  "ds_read_b64 v[8:9], v8 offset:0\n\t" \
  "v_add_u32_sdwa v22, s29, v113 dst_sel:DWORD dst_unused:UNUSED_PAD src0_sel:DWORD src1_sel:WORD_1\n\t" \
  "v_smfmac_f32_16x16x64_bf16 v[48:51], a[84:87], v[126:133], v151 abid:1\n\t" \
  "s_lshr_b64 s[64:65], s[64:65], 4\n\t" \
  "ds_read_b64 v[10:11], v10 offset:0\n\t" \
  "s_waitcnt lgkmcnt(4)\n\t" \
  "buffer_store_dwordx4 v[28:31], v34, s[20:23], s36 offen sc1\n\t" \
  "v_add_u32_sdwa v0, s29, v114 dst_sel:DWORD dst_unused:UNUSED_PAD src0_sel:DWORD src1_sel:WORD_0\n\t" \
  "v_smfmac_f32_16x16x64_bf16 v[52:55], a[88:91], v[126:133], v152\n\t" \
  "s_sub_u32 s28, s72, s46\n\t" \
  "ds_read_b64 v[12:13], v12 offset:0\n\t" \
  "v_add_u32_sdwa v2, s29, v114 dst_sel:DWORD dst_unused:UNUSED_PAD src0_sel:DWORD src1_sel:WORD_1\n\t" \
  "v_xad_u32 v28, v187, 64, s38\n\t" \
  "v_smfmac_f32_16x16x64_bf16 v[56:59], a[92:95], v[126:133], v152 abid:1\n\t" \
  "s_add_u32 s46, s72, 185\n\t" \
  "s_add_u32 s38, s38, s44\n\t" \
  "ds_read_b64 v[14:15], v14 offset:0\n\t" \
  "ds_read_b128 v[28:31], v28 offset:7168\n\t" \
  "v_add_u32_sdwa v4, s29, v115 dst_sel:DWORD dst_unused:UNUSED_PAD src0_sel:DWORD src1_sel:WORD_0\n\t" \
  "v_smfmac_f32_16x16x64_bf16 v[60:63], a[96:99], v[126:133], v153\n\t" \
  "s_max_i32 s28, s28, 0\n\t" \
  "s_add_u32 s36, s36, 4608\n\t" \
  "ds_read_b64 v[16:17], v16 offset:0\n\t" \
  "v_add_u32_sdwa v6, s29, v115 dst_sel:DWORD dst_unused:UNUSED_PAD src0_sel:DWORD src1_sel:WORD_1\n\t" \
  "v_smfmac_f32_16x16x64_bf16 v[64:67], a[100:103], v[126:133], v153 abid:1\n\t" \
  "s_min_u32 s28, s28, 6\n\t" \
  "s_sub_u32 s44, s38, 129024\n\t" \
  "ds_read_b64 v[18:19], v18 offset:0\n\t" \
  "v_smfmac_f32_16x16x64_bf16 v[68:71], a[104:107], v[126:133], v179\n\t" \
  "s_waitcnt lgkmcnt(7)\n\t" \
  "s_min_u32 s38, s38, s44\n\t" \
  "ds_read_b128 v[126:129], v183 offset:8192\n\t" \
  "v_smfmac_f32_16x16x64_bf16 v[36:39], a[108:111], v[134:141], v154\n\t" \
  "s_waitcnt lgkmcnt(3)\n\t" \
  "ds_read_b128 v[130:133], v184 offset:8192\n\t" \
  "s_waitcnt lgkmcnt(4)\n\t" \
  "buffer_store_dwordx4 v[28:31], v34, s[20:23], s36 offen sc1\n\t" \
  "v_smfmac_f32_16x16x64_bf16 v[40:43], v[76:79], v[8:15], v182 abid:1\n\t" \
  "s_lshl_b32 s28, s28, 6\n\t" \
  "ds_read_b64 v[20:21], v20 offset:0\n\t" \
  "v_add_u32_sdwa v8, s29, v32 dst_sel:DWORD dst_unused:UNUSED_PAD src0_sel:DWORD src1_sel:WORD_0\n\t" \
  "v_smfmac_f32_16x16x64_bf16 v[40:43], a[112:115], v[134:141], v154 abid:1\n\t" \
  "s_branch .Ljn11%=\n\t" \
  ".Lns11%=:\n\t" \
  "s_memrealtime s[72:73]\n\t" \
  "s_waitcnt lgkmcnt(0)\n\t" \
  "s_sub_i32 s28, s72, s52\n\t" \
  "s_cmp_lt_i32 s28, 0\n\t" \
  "s_cbranch_scc1 .Lns11%=\n\t" \
  "s_branch .Lng11%=\n\t" \
  ".fill (.Lvar%=+13692-.)/4, 4, 0xbf800000\n\t" \
  "s_nop 0\n\t" \
  "s_branch .Lfin%=\n\t" \
  "s_branch .Lhe%=\n\t" \
  "s_waitcnt vmcnt(6)\n\t" \
  "s_waitcnt vmcnt(10)\n\t" \
  "s_waitcnt vmcnt(12)\n\t" \
  "s_waitcnt vmcnt(17)\n\t" \
  "s_waitcnt vmcnt(21) lgkmcnt(0)\n\t" \
  "s_cmp_lt_u32 s72, s52\n\t" \
  "s_cbranch_scc1 .Lls11%=\n\t" \
  ".Llg11%=:\n\t" \
  "s_barrier\n\t" \
  "v_smfmac_f32_16x16x64_bf16 v[36:39], v[220:223], v[0:7], v174\n\t" \
  "s_add_u32 m0, s33, 0\n\t" \
  "ds_read_b128 v[126:129], v183 offset:0\n\t" \
  "v_add_u32 v24, s37, v185\n\t" \
  "v_smfmac_f32_16x16x64_bf16 v[40:43], v[224:227], v[0:7], v174 abid:1\n\t" \
  "buffer_load_dwordx4 v186, s[16:19], s34 offen offset:0 nt lds\n\t" \
  "ds_read_b128 v[130:133], v184 offset:0\n\t" \
  "v_xor_b32 v25, s41, v183\n\t" \
  "v_smfmac_f32_16x16x64_bf16 v[44:47], v[228:231], v[0:7], v175\n\t" \
  "s_add_u32 m0, s33, 1920\n\t" \
  "ds_read_b128 v[134:137], v183 offset:2048\n\t" \
  "v_subrev_u32 v25, s30, v25\n\t" \
  "v_smfmac_f32_16x16x64_bf16 v[48:51], v[232:235], v[0:7], v175 abid:1\n\t" \
  "s_add_u32 s30, s29, 18432\n\t" \
  "buffer_load_dwordx4 v186, s[16:19], s34 offen offset:128 nt lds\n\t" \
  "ds_read_b128 v[138:141], v184 offset:2048\n\t" \
  "v_cvt_pk_bf16_f32 v8, v36, v37\n\t" \
  "v_cvt_pk_bf16_f32 v9, v38, v39\n\t" \
  "v_smfmac_f32_16x16x64_bf16 v[52:55], v[236:239], v[0:7], v176\n\t" \
  "s_add_u32 m0, s33, 3840\n\t" \
  "ds_read_b128 v[36:39], v35 offset:0\n\t" \
  "v_cvt_pk_bf16_f32 v10, v40, v41\n\t" \
  "v_cvt_pk_bf16_f32 v11, v42, v43\n\t" \
  "v_smfmac_f32_16x16x64_bf16 v[56:59], v[240:243], v[0:7], v176 abid:1\n\t" \
  "s_sub_u32 s25, s30, 129024\n\t" \
  "buffer_load_dwordx4 v186, s[16:19], s34 offen offset:256 nt lds\n\t" \
  "ds_read_b128 v[40:43], v35 offset:64\n\t" \
  "v_cvt_pk_bf16_f32 v16, v44, v45\n\t" \
  "v_cvt_pk_bf16_f32 v17, v46, v47\n\t" \
  "v_smfmac_f32_16x16x64_bf16 v[60:63], v[244:247], v[0:7], v177\n\t" \
  "s_add_u32 m0, s33, 5760\n\t" \
  "ds_read_b128 v[44:47], v35 offset:128\n\t" \
  "v_cvt_pk_bf16_f32 v18, v48, v49\n\t" \
  "v_cvt_pk_bf16_f32 v19, v50, v51\n\t" \
  "v_smfmac_f32_16x16x64_bf16 v[64:67], v[248:251], v[0:7], v177 abid:1\n\t" \
  "s_min_u32 s30, s30, s25\n\t" \
  "buffer_load_dwordx4 v186, s[16:19], s34 offen offset:384 nt lds\n\t" \
  "ds_read_b128 v[48:51], v35 offset:192\n\t" \
  "v_cvt_pk_bf16_f32 v12, v52, v53\n\t" \
  "v_cvt_pk_bf16_f32 v13, v54, v55\n\t" \
  "v_smfmac_f32_16x16x64_bf16 v[68:71], v[252:255], v[0:7], v182\n\t" \
  "s_waitcnt lgkmcnt(3)\n\t" \
  "ds_read_b128 v[52:55], v35 offset:256\n\t" \
  "v_cvt_pk_bf16_f32 v14, v56, v57\n\t" \
  "v_cvt_pk_bf16_f32 v15, v58, v59\n\t" \
  "v_smfmac_f32_16x16x64_bf16 v[36:39], a[0:3], v[126:133], v142\n\t" \
  "s_waitcnt lgkmcnt(3)\n\t" \
  "ds_read_b128 v[56:59], v35 offset:320\n\t" \
  "v_cvt_pk_bf16_f32 v20, v60, v61\n\t" \
  "v_cvt_pk_bf16_f32 v21, v62, v63\n\t" \
  "v_smfmac_f32_16x16x64_bf16 v[40:43], a[4:7], v[126:133], v142 abid:1\n\t" \
  "s_waitcnt lgkmcnt(3)\n\t" \
  "ds_read_b128 v[60:63], v35 offset:384\n\t" \
  "v_cvt_pk_bf16_f32 v22, v64, v65\n\t" \
  "v_cvt_pk_bf16_f32 v23, v66, v67\n\t" \
  "v_smfmac_f32_16x16x64_bf16 v[44:47], a[8:11], v[126:133], v143\n\t" \
  "s_waitcnt lgkmcnt(3)\n\t" \
  "ds_read_b128 v[64:67], v35 offset:448\n\t" \
  "v_cvt_pk_bf16_f32 v26, v68, v69\n\t" \
  "v_cvt_pk_bf16_f32 v27, v70, v71\n\t" \
  "v_smfmac_f32_16x16x64_bf16 v[48:51], a[12:15], v[126:133], v143 abid:1\n\t" \
  "s_waitcnt lgkmcnt(3)\n\t" \
  "ds_read_b128 v[68:71], v35 offset:512\n\t" \
  "v_add_u32_sdwa v0, s29, v108 dst_sel:DWORD dst_unused:UNUSED_PAD src0_sel:DWORD src1_sel:WORD_0\n\t" \
  "v_smfmac_f32_16x16x64_bf16 v[52:55], a[16:19], v[126:133], v144\n\t" \
  "s_waitcnt lgkmcnt(3)\n\t" \
  "ds_write_b64 v24, v[8:9] offset:0\n\t" \
  "v_add_u32_sdwa v2, s29, v108 dst_sel:DWORD dst_unused:UNUSED_PAD src0_sel:DWORD src1_sel:WORD_1\n\t" \
  "v_smfmac_f32_16x16x64_bf16 v[56:59], a[20:23], v[126:133], v144 abid:1\n\t" \
  "s_waitcnt lgkmcnt(3)\n\t" \
  "ds_read_b64 v[0:1], v0 offset:0\n\t" \
  "v_add_u32_sdwa v4, s29, v109 dst_sel:DWORD dst_unused:UNUSED_PAD src0_sel:DWORD src1_sel:WORD_0\n\t" \
  "v_smfmac_f32_16x16x64_bf16 v[60:63], a[24:27], v[126:133], v145\n\t" \
  "s_waitcnt lgkmcnt(3)\n\t" \
  "ds_write_b64 v24, v[10:11] offset:8\n\t" \
  "v_add_u32_sdwa v6, s29, v109 dst_sel:DWORD dst_unused:UNUSED_PAD src0_sel:DWORD src1_sel:WORD_1\n\t" \
  "v_smfmac_f32_16x16x64_bf16 v[64:67], a[28:31], v[126:133], v145 abid:1\n\t" \
  "s_waitcnt lgkmcnt(3)\n\t" \
  "ds_read_b64 v[2:3], v2 offset:0\n\t" \
  "v_smfmac_f32_16x16x64_bf16 v[68:71], a[32:35], v[126:133], v178\n\t" \
  "s_sub_u32 s30, s30, s29\n\t" \
  "ds_write_b64 v24, v[12:13] offset:128\n\t" \
  "v_smfmac_f32_16x16x64_bf16 v[36:39], a[36:39], v[134:141], v146\n\t" \
  "s_mov_b32 s37, s29\n\t" \
  "ds_read_b128 v[126:129], v183 offset:4096\n\t" \
  "v_smfmac_f32_16x16x64_bf16 v[40:43], a[40:43], v[134:141], v146 abid:1\n\t" \
  "s_add_u32 s31, s31, 18432\n\t" \
  "ds_write_b64 v24, v[14:15] offset:136\n\t" \
  "v_xor_b32 v24, 64, v24\n\t" \
  "v_smfmac_f32_16x16x64_bf16 v[44:47], a[44:47], v[134:141], v147\n\t" \
  "s_sub_u32 s26, s31, 129024\n\t" \
  "ds_read_b128 v[130:133], v184 offset:4096\n\t" \
  "v_add_u32_sdwa v8, s29, v110 dst_sel:DWORD dst_unused:UNUSED_PAD src0_sel:DWORD src1_sel:WORD_0\n\t" \
  "v_smfmac_f32_16x16x64_bf16 v[48:51], a[48:51], v[134:141], v147 abid:1\n\t" \
  "s_min_u32 s31, s31, s26\n\t" \
  "ds_write_b64 v24, v[16:17] offset:0\n\t" \
  "v_add_u32_sdwa v10, s29, v110 dst_sel:DWORD dst_unused:UNUSED_PAD src0_sel:DWORD src1_sel:WORD_1\n\t" \
  "v_smfmac_f32_16x16x64_bf16 v[52:55], a[52:55], v[134:141], v148\n\t" \
  "s_add_u32 s33, s31, s32\n\t" \
  "ds_read_b64 v[4:5], v4 offset:0\n\t" \
  "v_add_u32_sdwa v12, s29, v111 dst_sel:DWORD dst_unused:UNUSED_PAD src0_sel:DWORD src1_sel:WORD_0\n\t" \
  "v_smfmac_f32_16x16x64_bf16 v[56:59], a[56:59], v[134:141], v148 abid:1\n\t" \
  "s_add_u32 s34, s34, 4718592\n\t" \
  "ds_write_b64 v24, v[18:19] offset:8\n\t" \
  "v_add_u32_sdwa v14, s29, v111 dst_sel:DWORD dst_unused:UNUSED_PAD src0_sel:DWORD src1_sel:WORD_1\n\t" \
  "v_smfmac_f32_16x16x64_bf16 v[60:63], a[60:63], v[134:141], v149\n\t" \
  "s_and_b32 s27, s64, 7\n\t" \
  "ds_read_b64 v[6:7], v6 offset:0\n\t" \
  "v_smfmac_f32_16x16x64_bf16 v[64:67], a[64:67], v[134:141], v149 abid:1\n\t" \
  "s_lshl2_add_u32 s66, s27, s68\n\t" \
  "ds_write_b64 v24, v[20:21] offset:128\n\t" \
  "v_smfmac_f32_16x16x64_bf16 v[68:71], a[68:71], v[134:141], v178 abid:1\n\t" \
  "s_waitcnt lgkmcnt(5)\n\t" \
  "ds_read_b128 v[134:137], v183 offset:6144\n\t" \
  "v_smfmac_f32_16x16x64_bf16 v[36:39], a[72:75], v[126:133], v150\n\t" \
  "s_and_b32 s27, s64, 8\n\t" \
  "ds_write_b64 v24, v[22:23] offset:136\n\t" \
  "v_add_u32_sdwa v16, s29, v112 dst_sel:DWORD dst_unused:UNUSED_PAD src0_sel:DWORD src1_sel:WORD_0\n\t" \
  "v_smfmac_f32_16x16x64_bf16 v[40:43], a[76:79], v[126:133], v150 abid:1\n\t" \
  "s_waitcnt lgkmcnt(3)\n\t" \
  "ds_read_b128 v[138:141], v184 offset:6144\n\t" \
  "v_add_u32_sdwa v18, s29, v112 dst_sel:DWORD dst_unused:UNUSED_PAD src0_sel:DWORD src1_sel:WORD_1\n\t" \
  "v_smfmac_f32_16x16x64_bf16 v[36:39], v[72:75], v[0:7], v182 abid:1\n\t" \
  "s_mul_i32 s27, s27, 176\n\t" \
  "ds_write_b64 v25, v[26:27] offset:16384\n\t" \
  "v_add_u32_sdwa v20, s29, v113 dst_sel:DWORD dst_unused:UNUSED_PAD src0_sel:DWORD src1_sel:WORD_0\n\t" \
  "v_smfmac_f32_16x16x64_bf16 v[44:47], a[80:83], v[126:133], v151\n\t" \
  "s_add_u32 s66, s66, s27\n\t" \
  "ds_read_b64 v[8:9], v8 offset:0\n\t" \
  "v_add_u32_sdwa v22, s29, v113 dst_sel:DWORD dst_unused:UNUSED_PAD src0_sel:DWORD src1_sel:WORD_1\n\t" \
  "v_smfmac_f32_16x16x64_bf16 v[48:51], a[84:87], v[126:133], v151 abid:1\n\t" \
  "s_lshr_b64 s[64:65], s[64:65], 4\n\t" \
  "ds_read_b64 v[10:11], v10 offset:0\n\t" \
  "v_add_u32_sdwa v0, s29, v114 dst_sel:DWORD dst_unused:UNUSED_PAD src0_sel:DWORD src1_sel:WORD_0\n\t" \
  "v_smfmac_f32_16x16x64_bf16 v[52:55], a[88:91], v[126:133], v152\n\t" \
  "s_sub_u32 s28, s72, s46\n\t" \
  "ds_read_b64 v[12:13], v12 offset:0\n\t" \
  "v_add_u32_sdwa v2, s29, v114 dst_sel:DWORD dst_unused:UNUSED_PAD src0_sel:DWORD src1_sel:WORD_1\n\t" \
  "v_smfmac_f32_16x16x64_bf16 v[56:59], a[92:95], v[126:133], v152 abid:1\n\t" \
  "s_add_u32 s46, s72, 185\n\t" \
  "ds_read_b64 v[14:15], v14 offset:0\n\t" \
  "v_add_u32_sdwa v4, s29, v115 dst_sel:DWORD dst_unused:UNUSED_PAD src0_sel:DWORD src1_sel:WORD_0\n\t" \
  "v_smfmac_f32_16x16x64_bf16 v[60:63], a[96:99], v[126:133], v153\n\t" \
  "s_max_i32 s28, s28, 0\n\t" \
  "ds_read_b64 v[16:17], v16 offset:0\n\t" \
  "v_add_u32_sdwa v6, s29, v115 dst_sel:DWORD dst_unused:UNUSED_PAD src0_sel:DWORD src1_sel:WORD_1\n\t" \
  "v_smfmac_f32_16x16x64_bf16 v[64:67], a[100:103], v[126:133], v153 abid:1\n\t" \
  "s_min_u32 s28, s28, 6\n\t" \
  "ds_read_b64 v[18:19], v18 offset:0\n\t" \
  "v_smfmac_f32_16x16x64_bf16 v[68:71], a[104:107], v[126:133], v179\n\t" \
  "s_waitcnt lgkmcnt(7)\n\t" \
  "ds_read_b128 v[126:129], v183 offset:8192\n\t" \
  "v_smfmac_f32_16x16x64_bf16 v[36:39], a[108:111], v[134:141], v154\n\t" \
  "s_waitcnt lgkmcnt(3)\n\t" \
  "ds_read_b128 v[130:133], v184 offset:8192\n\t" \
  "v_smfmac_f32_16x16x64_bf16 v[40:43], v[76:79], v[8:15], v182 abid:1\n\t" \
  "s_lshl_b32 s28, s28, 6\n\t" \
  "ds_read_b64 v[20:21], v20 offset:0\n\t" \
  "v_add_u32_sdwa v8, s29, v32 dst_sel:DWORD dst_unused:UNUSED_PAD src0_sel:DWORD src1_sel:WORD_0\n\t" \
  "v_smfmac_f32_16x16x64_bf16 v[40:43], a[112:115], v[134:141], v154 abid:1\n\t" \
  ".Ljn11%=:\n\t" \
  "s_add_u32 s28, s28, s53\n\t" \
  "ds_read_b64 v[22:23], v22 offset:0\n\t" \
  "v_add_u32_sdwa v10, s29, v32 dst_sel:DWORD dst_unused:UNUSED_PAD src0_sel:DWORD src1_sel:WORD_1\n\t" \
  "v_smfmac_f32_16x16x64_bf16 v[44:47], a[116:119], v[134:141], v155\n\t" \
  "s_max_u32 s51, s51, s28\n\t" \
  "ds_read_b64 v[0:1], v0 offset:0\n\t" \
  "v_add_u32 v28, s60, v187\n\t" \
  "v_smfmac_f32_16x16x64_bf16 v[48:51], a[120:123], v[134:141], v155 abid:1\n\t" \
  "s_add_u32 s54, s54, s51\n\t" \
  "ds_read_b64 v[2:3], v2 offset:0\n\t" \
  "v_smfmac_f32_16x16x64_bf16 v[52:55], a[124:127], v[134:141], v156\n\t" \
  "s_lshr_b32 s52, s54, 4\n\t" \
  "ds_read_b64 v[4:5], v4 offset:0\n\t" \
  "v_smfmac_f32_16x16x64_bf16 v[56:59], a[128:131], v[134:141], v156 abid:1\n\t" \
  "s_waitcnt lgkmcnt(3)\n\t" \
  "ds_read_b64 v[6:7], v6 offset:0\n\t" \
  "v_smfmac_f32_16x16x64_bf16 v[44:47], v[80:83], v[16:23], v182 abid:1\n\t" \
  "s_add_u32 s52, s52, s56\n\t" \
  "ds_read_b64 v[8:9], v8 offset:0\n\t" \
  "v_add_u32_sdwa v16, s29, v116 dst_sel:DWORD dst_unused:UNUSED_PAD src0_sel:DWORD src1_sel:WORD_0\n\t" \
  "v_smfmac_f32_16x16x64_bf16 v[60:63], a[132:135], v[134:141], v157\n\t" \
  "s_lshr_b32 s28, s51, 4\n\t" \
  "ds_read_b64 v[10:11], v10 offset:0\n\t" \
  "v_add_u32_sdwa v18, s29, v116 dst_sel:DWORD dst_unused:UNUSED_PAD src0_sel:DWORD src1_sel:WORD_1\n\t" \
  "v_smfmac_f32_16x16x64_bf16 v[64:67], a[136:139], v[134:141], v157 abid:1\n\t" \
  "s_add_u32 s28, s28, s72\n\t" \
  "ds_read_b128 v[28:31], v28 offset:0\n\t" \
  "v_add_u32 v24, s38, v187\n\t" \
  "v_smfmac_f32_16x16x64_bf16 v[68:71], a[140:143], v[134:141], v179 abid:1\n\t" \
  "s_waitcnt lgkmcnt(3)\n\t" \
  "ds_read_b128 v[24:27], v24 offset:4096\n\t" \
  "v_add_u32_sdwa v20, s29, v117 dst_sel:DWORD dst_unused:UNUSED_PAD src0_sel:DWORD src1_sel:WORD_0\n\t" \
  "v_smfmac_f32_16x16x64_bf16 v[48:51], v[84:87], v[0:7], v182 abid:1\n\t" \
  "s_sub_u32 s28, s28, s101\n\t" \
  "ds_read_b128 v[134:137], v183 offset:10240\n\t" \
  "v_add_u32_sdwa v22, s29, v117 dst_sel:DWORD dst_unused:UNUSED_PAD src0_sel:DWORD src1_sel:WORD_1\n\t" \
  "v_smfmac_f32_16x16x64_bf16 v[36:39], a[144:147], v[126:133], v158\n\t" \
  "s_max_u32 s52, s52, s28\n\t" \
  "ds_read_b128 v[138:141], v184 offset:10240\n\t" \
  "v_add_u32_sdwa v0, s29, v118 dst_sel:DWORD dst_unused:UNUSED_PAD src0_sel:DWORD src1_sel:WORD_0\n\t" \
  "v_smfmac_f32_16x16x64_bf16 v[40:43], a[148:151], v[126:133], v158 abid:1\n\t" \
  "s_add_u32 s36, s35, s39\n\t" \
  "s_waitcnt lgkmcnt(3)\n\t" \
  "ds_read_b64 v[16:17], v16 offset:0\n\t" \
  "v_add_u32_sdwa v2, s29, v118 dst_sel:DWORD dst_unused:UNUSED_PAD src0_sel:DWORD src1_sel:WORD_1\n\t" \
  "s_set_gpr_idx_on s6, gpr_idx(DST)\n\t" \
  "v_smfmac_f32_16x16x64_bf16 v[36:39], v[28:31], v[8:15], v182 abid:1\n\t" \
  "s_set_gpr_idx_off\n\t" \
  "s_waitcnt lgkmcnt(3)\n\t" \
  "buffer_store_dwordx4 v[24:27], v34, s[20:23], s36 offen sc1\n\t" \
  "v_add_u32_sdwa v4, s29, v119 dst_sel:DWORD dst_unused:UNUSED_PAD src0_sel:DWORD src1_sel:WORD_0\n\t" \
  "v_smfmac_f32_16x16x64_bf16 v[44:47], a[152:155], v[126:133], v159\n\t" \
  "s_sub_u32 s51, s51, 112\n\t" \
  "ds_read_b64 v[18:19], v18 offset:0\n\t" \
  "v_xad_u32 v24, v187, 64, s38\n\t" \
  "v_smfmac_f32_16x16x64_bf16 v[48:51], a[156:159], v[126:133], v159 abid:1\n\t" \
  "s_max_u32 s51, s51, s53\n\t" \
  "ds_read_b128 v[24:27], v24 offset:5120\n\t" \
  "v_add_u32_sdwa v6, s29, v119 dst_sel:DWORD dst_unused:UNUSED_PAD src0_sel:DWORD src1_sel:WORD_1\n\t" \
  "v_smfmac_f32_16x16x64_bf16 v[52:55], a[160:163], v[126:133], v160\n\t" \
  "ds_read_b64 v[20:21], v20 offset:0\n\t" \
  "v_add_u32_sdwa v8, s29, v120 dst_sel:DWORD dst_unused:UNUSED_PAD src0_sel:DWORD src1_sel:WORD_0\n\t" \
  "v_smfmac_f32_16x16x64_bf16 v[56:59], a[164:167], v[126:133], v160 abid:1\n\t" \
  "ds_read_b64 v[22:23], v22 offset:0\n\t" \
  "v_add_u32_sdwa v10, s29, v120 dst_sel:DWORD dst_unused:UNUSED_PAD src0_sel:DWORD src1_sel:WORD_1\n\t" \
  "v_smfmac_f32_16x16x64_bf16 v[60:63], a[168:171], v[126:133], v161\n\t" \
  "s_add_u32 s36, s36, 4608\n\t" \
  "ds_read_b64 v[0:1], v0 offset:0\n\t" \
  "v_add_u32_sdwa v12, s29, v121 dst_sel:DWORD dst_unused:UNUSED_PAD src0_sel:DWORD src1_sel:WORD_0\n\t" \
  "v_smfmac_f32_16x16x64_bf16 v[64:67], a[172:175], v[126:133], v161 abid:1\n\t" \
  "s_waitcnt lgkmcnt(3)\n\t" \
  "buffer_store_dwordx4 v[24:27], v34, s[20:23], s36 offen sc1\n\t" \
  "v_add_u32_sdwa v14, s29, v121 dst_sel:DWORD dst_unused:UNUSED_PAD src0_sel:DWORD src1_sel:WORD_1\n\t" \
  "v_smfmac_f32_16x16x64_bf16 v[68:71], a[176:179], v[126:133], v180\n\t" \
  "ds_read_b128 v[126:129], v183 offset:12288\n\t" \
  "v_add_u32 v24, s38, v187\n\t" \
  "v_smfmac_f32_16x16x64_bf16 v[36:39], a[180:183], v[134:141], v162\n\t" \
  "s_waitcnt lgkmcnt(2)\n\t" \
  "ds_read_b128 v[24:27], v24 offset:6144\n\t" \
  "v_smfmac_f32_16x16x64_bf16 v[52:55], v[88:91], v[16:23], v182 abid:1\n\t" \
  "ds_read_b128 v[130:133], v184 offset:12288\n\t" \
  "v_add_u32_sdwa v16, s29, v122 dst_sel:DWORD dst_unused:UNUSED_PAD src0_sel:DWORD src1_sel:WORD_0\n\t" \
  "v_smfmac_f32_16x16x64_bf16 v[40:43], a[184:187], v[134:141], v162 abid:1\n\t" \
  "ds_read_b64 v[2:3], v2 offset:0\n\t" \
  "v_add_u32_sdwa v18, s29, v122 dst_sel:DWORD dst_unused:UNUSED_PAD src0_sel:DWORD src1_sel:WORD_1\n\t" \
  "v_smfmac_f32_16x16x64_bf16 v[44:47], a[188:191], v[134:141], v163\n\t" \
  "s_add_u32 s36, s36, 4608\n\t" \
  "ds_read_b64 v[4:5], v4 offset:0\n\t" \
  "v_add_u32_sdwa v20, s29, v123 dst_sel:DWORD dst_unused:UNUSED_PAD src0_sel:DWORD src1_sel:WORD_0\n\t" \
  "v_smfmac_f32_16x16x64_bf16 v[48:51], a[192:195], v[134:141], v163 abid:1\n\t" \
  "s_waitcnt lgkmcnt(3)\n\t" \
  "buffer_store_dwordx4 v[24:27], v34, s[20:23], s36 offen sc1\n\t" \
  "v_add_u32_sdwa v22, s29, v123 dst_sel:DWORD dst_unused:UNUSED_PAD src0_sel:DWORD src1_sel:WORD_1\n\t" \
  "v_smfmac_f32_16x16x64_bf16 v[52:55], a[196:199], v[134:141], v164\n\t" \
  "ds_read_b64 v[6:7], v6 offset:0\n\t" \
  "v_xad_u32 v24, v187, 64, s38\n\t" \
  "v_smfmac_f32_16x16x64_bf16 v[56:59], a[200:203], v[134:141], v164 abid:1\n\t" \
  "s_add_u32 s38, s38, 18432\n\t" \
  "ds_read_b128 v[24:27], v24 offset:7168\n\t" \
  "v_smfmac_f32_16x16x64_bf16 v[60:63], a[204:207], v[134:141], v165\n\t" \
  "s_sub_u32 s44, s38, 129024\n\t" \
  "ds_read_b64 v[8:9], v8 offset:0\n\t" \
  "v_smfmac_f32_16x16x64_bf16 v[64:67], a[208:211], v[134:141], v165 abid:1\n\t" \
  "s_min_u32 s38, s38, s44\n\t" \
  "ds_read_b64 v[10:11], v10 offset:0\n\t" \
  "v_smfmac_f32_16x16x64_bf16 v[68:71], a[212:215], v[134:141], v180 abid:1\n\t" \
  "s_add_u32 s36, s36, 4608\n\t" \
  "s_waitcnt lgkmcnt(3)\n\t" \
  "ds_read_b128 v[134:137], v183 offset:14336\n\t" \
  "v_smfmac_f32_16x16x64_bf16 v[56:59], v[92:95], v[0:7], v182 abid:1\n\t" \
  "s_waitcnt lgkmcnt(3)\n\t" \
  "buffer_store_dwordx4 v[24:27], v34, s[20:23], s36 offen sc1\n\t" \
  "v_smfmac_f32_16x16x64_bf16 v[36:39], a[216:219], v[126:133], v166\n\t" \
  "s_add_u32 s35, s35, 4718592\n\t" \
  "ds_read_b128 v[138:141], v184 offset:14336\n\t" \
  "v_add_u32 v24, s43, v187\n\t" \
  "v_smfmac_f32_16x16x64_bf16 v[40:43], a[220:223], v[126:133], v166 abid:1\n\t" \
  "ds_read_b128 v[24:27], v24 offset:16384\n\t" \
  "v_smfmac_f32_16x16x64_bf16 v[44:47], a[224:227], v[126:133], v167\n\t" \
  "ds_read_b64 v[12:13], v12 offset:0\n\t" \
  "v_smfmac_f32_16x16x64_bf16 v[48:51], a[228:231], v[126:133], v167 abid:1\n\t" \
  "ds_read_b64 v[14:15], v14 offset:0\n\t" \
  "v_smfmac_f32_16x16x64_bf16 v[52:55], a[232:235], v[126:133], v168\n\t" \
  "ds_read_b64 v[16:17], v16 offset:0\n\t" \
  "v_smfmac_f32_16x16x64_bf16 v[56:59], a[236:239], v[126:133], v168 abid:1\n\t" \
  "s_waitcnt lgkmcnt(3)\n\t" \
  "buffer_store_dwordx4 v[24:27], v186, s[20:23], s40 offen offset:384 sc1\n\t" \
  "v_smfmac_f32_16x16x64_bf16 v[60:63], a[240:243], v[126:133], v169\n\t" \
  "s_add_u32 s43, s43, 18432\n\t" \
  "ds_read_b64 v[18:19], v18 offset:0\n\t" \
  "v_smfmac_f32_16x16x64_bf16 v[64:67], a[244:247], v[126:133], v169 abid:1\n\t" \
  "s_waitcnt lgkmcnt(2)\n\t" \
  "ds_read_b64 v[20:21], v20 offset:0\n\t" \
  "v_smfmac_f32_16x16x64_bf16 v[60:63], v[96:99], v[8:15], v182 abid:1\n\t" \
  "s_sub_u32 s42, s43, 129024\n\t" \
  "ds_read_b64 v[22:23], v22 offset:0\n\t" \
  "v_add_u32_sdwa v8, s29, v124 dst_sel:DWORD dst_unused:UNUSED_PAD src0_sel:DWORD src1_sel:WORD_0\n\t" \
  "v_smfmac_f32_16x16x64_bf16 v[68:71], a[248:251], v[126:133], v181\n\t" \
  "s_min_u32 s43, s43, s42\n\t" \
  "ds_read_b128 v[0:3], v183 offset:16384\n\t" \
  "v_add_u32_sdwa v10, s29, v124 dst_sel:DWORD dst_unused:UNUSED_PAD src0_sel:DWORD src1_sel:WORD_1\n\t" \
  "v_smfmac_f32_16x16x64_bf16 v[36:39], a[252:255], v[134:141], v170\n\t" \
  "s_add_u32 s40, s40, 4718592\n\t" \
  "ds_read_b128 v[4:7], v184 offset:16384\n\t" \
  "v_add_u32_sdwa v12, s29, v125 dst_sel:DWORD dst_unused:UNUSED_PAD src0_sel:DWORD src1_sel:WORD_0\n\t" \
  "v_smfmac_f32_16x16x64_bf16 v[40:43], v[188:191], v[134:141], v170 abid:1\n\t" \
  "ds_read_b64 v[8:9], v8 offset:0\n\t" \
  "v_add_u32_sdwa v14, s29, v125 dst_sel:DWORD dst_unused:UNUSED_PAD src0_sel:DWORD src1_sel:WORD_1\n\t" \
  "v_smfmac_f32_16x16x64_bf16 v[44:47], v[192:195], v[134:141], v171\n\t" \
  "s_waitcnt lgkmcnt(3)\n\t" \
  "ds_read_b64 v[10:11], v10 offset:0\n\t" \
  "v_add_u32 v183, s30, v183\n\t" \
  "v_smfmac_f32_16x16x64_bf16 v[64:67], v[100:103], v[16:23], v182 abid:1\n\t" \
  "s_add_u32 s29, s29, s30\n\t" \
  "ds_read_b64 v[12:13], v12 offset:0\n\t" \
  "v_add_u32 v184, s30, v184\n\t" \
  "v_smfmac_f32_16x16x64_bf16 v[48:51], v[196:199], v[134:141], v171 abid:1\n\t" \
  "ds_read_b64 v[14:15], v14 offset:0\n\t" \
  "v_smfmac_f32_16x16x64_bf16 v[52:55], v[200:203], v[134:141], v172\n\t" \
  "s_memrealtime s[72:73]\n\t" \
  "v_smfmac_f32_16x16x64_bf16 v[56:59], v[204:207], v[134:141], v172 abid:1\n\t" \
  "v_smfmac_f32_16x16x64_bf16 v[60:63], v[208:211], v[134:141], v173\n\t" \
  "v_smfmac_f32_16x16x64_bf16 v[64:67], v[212:215], v[134:141], v173 abid:1\n\t" \
  "s_waitcnt lgkmcnt(0)\n\t" \
  "v_smfmac_f32_16x16x64_bf16 v[68:71], v[104:107], v[8:15], v182 abid:1\n\t" \
  "v_smfmac_f32_16x16x64_bf16 v[68:71], v[216:219], v[134:141], v181 abid:1\n\t" \
  "s_setpc_b64 s[66:67]\n\t" \
  ".Lls11%=:\n\t" \
  "s_memrealtime s[72:73]\n\t" \
  "s_waitcnt lgkmcnt(0)\n\t" \
  "s_sub_i32 s28, s72, s52\n\t" \
  "s_cmp_lt_i32 s28, 0\n\t" \
  "s_cbranch_scc1 .Lls11%=\n\t" \
  "s_branch .Llg11%=\n\t" \
  ".fill (.Lvar%=+16384-.)/4, 4, 0xbf800000\n\t" \
  "s_branch .Lfin%=\n\t" \
  "s_branch .Lhe%=\n\t" \
  "s_waitcnt vmcnt(6)\n\t" \
  "s_waitcnt vmcnt(10)\n\t" \
  "s_waitcnt vmcnt(12)\n\t" \
  "s_waitcnt vmcnt(17)\n\t" \
  "s_waitcnt vmcnt(21) lgkmcnt(0)\n\t" \
  "s_cmp_lt_u32 s72, s52\n\t" \
  "s_cbranch_scc1 .Lns12%=\n\t" \
  ".Lng12%=:\n\t" \
  "s_barrier\n\t" \
  "v_smfmac_f32_16x16x64_bf16 v[36:39], v[220:223], v[0:7], v174\n\t" \
  "s_movk_i32 s53, 1952\n\t" \
  "ds_read_b128 v[126:129], v183 offset:0\n\t" \
  "v_add_u32 v24, s37, v185\n\t" \
  "v_smfmac_f32_16x16x64_bf16 v[40:43], v[224:227], v[0:7], v174 abid:1\n\t" \
  "ds_read_b128 v[130:133], v184 offset:0\n\t" \
  "v_xor_b32 v25, s41, v183\n\t" \
  "v_smfmac_f32_16x16x64_bf16 v[44:47], v[228:231], v[0:7], v175\n\t" \
  "s_mov_b32 s51, s53\n\t" \
  "ds_read_b128 v[134:137], v183 offset:2048\n\t" \
  "v_subrev_u32 v25, s30, v25\n\t" \
  "v_smfmac_f32_16x16x64_bf16 v[48:51], v[232:235], v[0:7], v175 abid:1\n\t" \
  "s_add_u32 s30, s29, 18432\n\t" \
  "ds_read_b128 v[138:141], v184 offset:2048\n\t" \
  "v_cvt_pk_bf16_f32 v8, v36, v37\n\t" \
  "v_cvt_pk_bf16_f32 v9, v38, v39\n\t" \
  "v_smfmac_f32_16x16x64_bf16 v[52:55], v[236:239], v[0:7], v176\n\t" \
  "ds_read_b128 v[36:39], v35 offset:0\n\t" \
  "v_cvt_pk_bf16_f32 v10, v40, v41\n\t" \
  "v_cvt_pk_bf16_f32 v11, v42, v43\n\t" \
  "v_smfmac_f32_16x16x64_bf16 v[56:59], v[240:243], v[0:7], v176 abid:1\n\t" \
  "s_sub_u32 s25, s30, 129024\n\t" \
  "ds_read_b128 v[40:43], v35 offset:64\n\t" \
  "v_cvt_pk_bf16_f32 v16, v44, v45\n\t" \
  "v_cvt_pk_bf16_f32 v17, v46, v47\n\t" \
  "v_smfmac_f32_16x16x64_bf16 v[60:63], v[244:247], v[0:7], v177\n\t" \
  "ds_read_b128 v[44:47], v35 offset:128\n\t" \
  "v_cvt_pk_bf16_f32 v18, v48, v49\n\t" \
  "v_cvt_pk_bf16_f32 v19, v50, v51\n\t" \
  "v_smfmac_f32_16x16x64_bf16 v[64:67], v[248:251], v[0:7], v177 abid:1\n\t" \
  "s_min_u32 s30, s30, s25\n\t" \
  "ds_read_b128 v[48:51], v35 offset:192\n\t" \
  "v_cvt_pk_bf16_f32 v12, v52, v53\n\t" \
  "v_cvt_pk_bf16_f32 v13, v54, v55\n\t" \
  "v_smfmac_f32_16x16x64_bf16 v[68:71], v[252:255], v[0:7], v182\n\t" \
  "s_waitcnt lgkmcnt(3)\n\t" \
  "ds_read_b128 v[52:55], v35 offset:256\n\t" \
  "v_cvt_pk_bf16_f32 v14, v56, v57\n\t" \
  "v_cvt_pk_bf16_f32 v15, v58, v59\n\t" \
  "v_smfmac_f32_16x16x64_bf16 v[36:39], a[0:3], v[126:133], v142\n\t" \
  "s_waitcnt lgkmcnt(3)\n\t" \
  "ds_read_b128 v[56:59], v35 offset:320\n\t" \
  "v_cvt_pk_bf16_f32 v20, v60, v61\n\t" \
  "v_cvt_pk_bf16_f32 v21, v62, v63\n\t" \
  "v_smfmac_f32_16x16x64_bf16 v[40:43], a[4:7], v[126:133], v142 abid:1\n\t" \
  "s_waitcnt lgkmcnt(3)\n\t" \
  "ds_read_b128 v[60:63], v35 offset:384\n\t" \
  "v_cvt_pk_bf16_f32 v22, v64, v65\n\t" \
  "v_cvt_pk_bf16_f32 v23, v66, v67\n\t" \
  "v_smfmac_f32_16x16x64_bf16 v[44:47], a[8:11], v[126:133], v143\n\t" \
  "s_waitcnt lgkmcnt(3)\n\t" \
  "ds_read_b128 v[64:67], v35 offset:448\n\t" \
  "v_cvt_pk_bf16_f32 v26, v68, v69\n\t" \
  "v_cvt_pk_bf16_f32 v27, v70, v71\n\t" \
  "v_smfmac_f32_16x16x64_bf16 v[48:51], a[12:15], v[126:133], v143 abid:1\n\t" \
  "s_waitcnt lgkmcnt(3)\n\t" \
  "ds_read_b128 v[68:71], v35 offset:512\n\t" \
  "v_add_u32_sdwa v0, s29, v108 dst_sel:DWORD dst_unused:UNUSED_PAD src0_sel:DWORD src1_sel:WORD_0\n\t" \
  "v_smfmac_f32_16x16x64_bf16 v[52:55], a[16:19], v[126:133], v144\n\t" \
  "s_waitcnt lgkmcnt(3)\n\t" \
  "ds_write_b64 v24, v[8:9] offset:0\n\t" \
  "v_add_u32_sdwa v2, s29, v108 dst_sel:DWORD dst_unused:UNUSED_PAD src0_sel:DWORD src1_sel:WORD_1\n\t" \
  "v_smfmac_f32_16x16x64_bf16 v[56:59], a[20:23], v[126:133], v144 abid:1\n\t" \
  "s_waitcnt lgkmcnt(3)\n\t" \
  "s_bfe_i32 s47, s48, 0x10000\n\t" \
  "ds_read_b64 v[0:1], v0 offset:0\n\t" \
  "v_add_u32_sdwa v4, s29, v109 dst_sel:DWORD dst_unused:UNUSED_PAD src0_sel:DWORD src1_sel:WORD_0\n\t" \
  "v_add_u32 v28, s38, v187\n\t" \
  "v_smfmac_f32_16x16x64_bf16 v[60:63], a[24:27], v[126:133], v145\n\t" \
  "s_waitcnt lgkmcnt(3)\n\t" \
  "s_lshr_b32 s48, s48, 1\n\t" \
  "ds_write_b64 v24, v[10:11] offset:8\n\t" \
  "ds_read_b128 v[28:31], v28 offset:4096\n\t" \
  "v_add_u32_sdwa v6, s29, v109 dst_sel:DWORD dst_unused:UNUSED_PAD src0_sel:DWORD src1_sel:WORD_1\n\t" \
  "v_smfmac_f32_16x16x64_bf16 v[64:67], a[28:31], v[126:133], v145 abid:1\n\t" \
  "s_waitcnt lgkmcnt(3)\n\t" \
  "s_add_u32 s36, s35, s39\n\t" \
  "ds_read_b64 v[2:3], v2 offset:0\n\t" \
  "v_smfmac_f32_16x16x64_bf16 v[68:71], a[32:35], v[126:133], v178\n\t" \
  "s_sub_u32 s30, s30, s29\n\t" \
  "s_andn2_b32 s44, 0x40000000, s47\n\t" \
  "ds_write_b64 v24, v[12:13] offset:128\n\t" \
  "v_smfmac_f32_16x16x64_bf16 v[36:39], a[36:39], v[134:141], v146\n\t" \
  "s_mov_b32 s37, s29\n\t" \
  "s_or_b32 s36, s36, s44\n\t" \
  "ds_read_b128 v[126:129], v183 offset:4096\n\t" \
  "v_smfmac_f32_16x16x64_bf16 v[40:43], a[40:43], v[134:141], v146 abid:1\n\t" \
  "ds_write_b64 v24, v[14:15] offset:136\n\t" \
  "s_waitcnt lgkmcnt(4)\n\t" \
  "buffer_store_dwordx4 v[28:31], v34, s[20:23], s36 offen sc1\n\t" \
  "v_xor_b32 v24, 64, v24\n\t" \
  "v_smfmac_f32_16x16x64_bf16 v[44:47], a[44:47], v[134:141], v147\n\t" \
  "s_and_b32 s44, s47, 4718592\n\t" \
  "ds_read_b128 v[130:133], v184 offset:4096\n\t" \
  "v_add_u32_sdwa v8, s29, v110 dst_sel:DWORD dst_unused:UNUSED_PAD src0_sel:DWORD src1_sel:WORD_0\n\t" \
  "v_xad_u32 v28, v187, 64, s38\n\t" \
  "v_smfmac_f32_16x16x64_bf16 v[48:51], a[48:51], v[134:141], v147 abid:1\n\t" \
  "s_add_u32 s35, s35, s44\n\t" \
  "ds_write_b64 v24, v[16:17] offset:0\n\t" \
  "ds_read_b128 v[28:31], v28 offset:5120\n\t" \
  "v_add_u32_sdwa v10, s29, v110 dst_sel:DWORD dst_unused:UNUSED_PAD src0_sel:DWORD src1_sel:WORD_1\n\t" \
  "v_smfmac_f32_16x16x64_bf16 v[52:55], a[52:55], v[134:141], v148\n\t" \
  "s_add_u32 s36, s36, 4608\n\t" \
  "ds_read_b64 v[4:5], v4 offset:0\n\t" \
  "v_add_u32_sdwa v12, s29, v111 dst_sel:DWORD dst_unused:UNUSED_PAD src0_sel:DWORD src1_sel:WORD_0\n\t" \
  "v_smfmac_f32_16x16x64_bf16 v[56:59], a[56:59], v[134:141], v148 abid:1\n\t" \
  "ds_write_b64 v24, v[18:19] offset:8\n\t" \
  "v_add_u32_sdwa v14, s29, v111 dst_sel:DWORD dst_unused:UNUSED_PAD src0_sel:DWORD src1_sel:WORD_1\n\t" \
  "v_smfmac_f32_16x16x64_bf16 v[60:63], a[60:63], v[134:141], v149\n\t" \
  "s_and_b32 s27, s64, 7\n\t" \
  "ds_read_b64 v[6:7], v6 offset:0\n\t" \
  "v_smfmac_f32_16x16x64_bf16 v[64:67], a[64:67], v[134:141], v149 abid:1\n\t" \
  "s_lshl2_add_u32 s66, s27, s68\n\t" \
  "ds_write_b64 v24, v[20:21] offset:128\n\t" \
  "s_waitcnt lgkmcnt(4)\n\t" \
  "buffer_store_dwordx4 v[28:31], v34, s[20:23], s36 offen sc1\n\t" \
  "v_smfmac_f32_16x16x64_bf16 v[68:71], a[68:71], v[134:141], v178 abid:1\n\t" \
  "s_waitcnt lgkmcnt(5)\n\t" \
  "s_and_b32 s44, s47, 18432\n\t" \
  "ds_read_b128 v[134:137], v183 offset:6144\n\t" \
  "v_add_u32 v28, s38, v187\n\t" \
  "v_smfmac_f32_16x16x64_bf16 v[36:39], a[72:75], v[126:133], v150\n\t" \
  "s_and_b32 s27, s64, 8\n\t" \
  "ds_write_b64 v24, v[22:23] offset:136\n\t" \
  "ds_read_b128 v[28:31], v28 offset:6144\n\t" \
  "v_add_u32_sdwa v16, s29, v112 dst_sel:DWORD dst_unused:UNUSED_PAD src0_sel:DWORD src1_sel:WORD_0\n\t" \
  "v_smfmac_f32_16x16x64_bf16 v[40:43], a[76:79], v[126:133], v150 abid:1\n\t" \
  "s_waitcnt lgkmcnt(3)\n\t" \
  "s_add_u32 s36, s36, 4608\n\t" \
  "ds_read_b128 v[138:141], v184 offset:6144\n\t" \
  "v_add_u32_sdwa v18, s29, v112 dst_sel:DWORD dst_unused:UNUSED_PAD src0_sel:DWORD src1_sel:WORD_1\n\t" \
  "v_smfmac_f32_16x16x64_bf16 v[36:39], v[72:75], v[0:7], v182 abid:1\n\t" \
  "s_mul_i32 s27, s27, 176\n\t" \
  "ds_write_b64 v25, v[26:27] offset:16384\n\t" \
  "v_add_u32_sdwa v20, s29, v113 dst_sel:DWORD dst_unused:UNUSED_PAD src0_sel:DWORD src1_sel:WORD_0\n\t" \
  "v_smfmac_f32_16x16x64_bf16 v[44:47], a[80:83], v[126:133], v151\n\t" \
  "s_add_u32 s66, s66, s27\n\t" \
  "ds_read_b64 v[8:9], v8 offset:0\n\t" \
  "ds_read_b64 v[10:11], v10 offset:0\n\t" \
  "v_add_u32_sdwa v22, s29, v113 dst_sel:DWORD dst_unused:UNUSED_PAD src0_sel:DWORD src1_sel:WORD_1\n\t" \
  "v_smfmac_f32_16x16x64_bf16 v[48:51], a[84:87], v[126:133], v151 abid:1\n\t" \
  "s_lshr_b64 s[64:65], s[64:65], 4\n\t" \
  "ds_read_b64 v[12:13], v12 offset:0\n\t" \
  "ds_read_b64 v[14:15], v14 offset:0\n\t" \
  "s_waitcnt lgkmcnt(6)\n\t" \
  "buffer_store_dwordx4 v[28:31], v34, s[20:23], s36 offen sc1\n\t" \
  "v_add_u32_sdwa v0, s29, v114 dst_sel:DWORD dst_unused:UNUSED_PAD src0_sel:DWORD src1_sel:WORD_0\n\t" \
  "v_smfmac_f32_16x16x64_bf16 v[52:55], a[88:91], v[126:133], v152\n\t" \
  "s_sub_u32 s28, s72, s46\n\t" \
  "ds_read_b64 v[16:17], v16 offset:0\n\t" \
  "v_add_u32_sdwa v2, s29, v114 dst_sel:DWORD dst_unused:UNUSED_PAD src0_sel:DWORD src1_sel:WORD_1\n\t" \
  "v_xad_u32 v28, v187, 64, s38\n\t" \
  "v_smfmac_f32_16x16x64_bf16 v[56:59], a[92:95], v[126:133], v152 abid:1\n\t" \
  "s_add_u32 s46, s72, 185\n\t" \
  "s_add_u32 s38, s38, s44\n\t" \
  "ds_read_b64 v[18:19], v18 offset:0\n\t" \
  "ds_read_b128 v[28:31], v28 offset:7168\n\t" \
  "v_add_u32_sdwa v4, s29, v115 dst_sel:DWORD dst_unused:UNUSED_PAD src0_sel:DWORD src1_sel:WORD_0\n\t" \
  "v_smfmac_f32_16x16x64_bf16 v[60:63], a[96:99], v[126:133], v153\n\t" \
  "s_max_i32 s28, s28, 0\n\t" \
  "s_add_u32 s36, s36, 4608\n\t" \
  "ds_read_b64 v[20:21], v20 offset:0\n\t" \
  "v_add_u32_sdwa v6, s29, v115 dst_sel:DWORD dst_unused:UNUSED_PAD src0_sel:DWORD src1_sel:WORD_1\n\t" \
  "v_smfmac_f32_16x16x64_bf16 v[64:67], a[100:103], v[126:133], v153 abid:1\n\t" \
  "s_waitcnt lgkmcnt(3)\n\t" \
  "s_sub_u32 s44, s38, 129024\n\t" \
  "ds_read_b64 v[22:23], v22 offset:0\n\t" \
  "v_smfmac_f32_16x16x64_bf16 v[40:43], v[76:79], v[8:15], v182 abid:1\n\t" \
  "s_min_u32 s28, s28, 6\n\t" \
  "s_min_u32 s38, s38, s44\n\t" \
  "ds_read_b64 v[0:1], v0 offset:0\n\t" \
  "v_smfmac_f32_16x16x64_bf16 v[68:71], a[104:107], v[126:133], v179\n\t" \
  "s_lshl_b32 s28, s28, 6\n\t" \
  "ds_read_b128 v[126:129], v183 offset:8192\n\t" \
  "s_waitcnt lgkmcnt(4)\n\t" \
  "buffer_store_dwordx4 v[28:31], v34, s[20:23], s36 offen sc1\n\t" \
  "v_smfmac_f32_16x16x64_bf16 v[36:39], a[108:111], v[134:141], v154\n\t" \
  "s_add_u32 s28, s28, s53\n\t" \
  "ds_read_b128 v[130:133], v184 offset:8192\n\t" \
  "v_add_u32_sdwa v8, s29, v32 dst_sel:DWORD dst_unused:UNUSED_PAD src0_sel:DWORD src1_sel:WORD_0\n\t" \
  "v_smfmac_f32_16x16x64_bf16 v[40:43], a[112:115], v[134:141], v154 abid:1\n\t" \
  "s_branch .Ljn12%=\n\t" \
  ".Lns12%=:\n\t" \
  "s_memrealtime s[72:73]\n\t" \
  "s_waitcnt lgkmcnt(0)\n\t" \
  "s_sub_i32 s28, s72, s52\n\t" \
  "s_cmp_lt_i32 s28, 0\n\t" \
  "s_cbranch_scc1 .Lns12%=\n\t" \
  "s_branch .Lng12%=\n\t" \
  ".fill (.Lvar%=+17788-.)/4, 4, 0xbf800000\n\t" \
  "s_nop 0\n\t" \
  "s_branch .Lfin%=\n\t" \
  "s_branch .Lhe%=\n\t" \
  "s_waitcnt vmcnt(6)\n\t" \
  "s_waitcnt vmcnt(10)\n\t" \
  "s_waitcnt vmcnt(12)\n\t" \
  "s_waitcnt vmcnt(17)\n\t" \
  "s_waitcnt vmcnt(21) lgkmcnt(0)\n\t" \
  "s_cmp_lt_u32 s72, s52\n\t" \
  "s_cbranch_scc1 .Lls12%=\n\t" \
  ".Llg12%=:\n\t" \
  "s_barrier\n\t" \
  "v_smfmac_f32_16x16x64_bf16 v[36:39], v[220:223], v[0:7], v174\n\t" \
  "s_add_u32 m0, s33, 0\n\t" \
  "ds_read_b128 v[126:129], v183 offset:0\n\t" \
  "v_add_u32 v24, s37, v185\n\t" \
  "v_smfmac_f32_16x16x64_bf16 v[40:43], v[224:227], v[0:7], v174 abid:1\n\t" \
  "buffer_load_dwordx4 v186, s[16:19], s34 offen offset:0 nt lds\n\t" \
  "ds_read_b128 v[130:133], v184 offset:0\n\t" \
  "v_xor_b32 v25, s41, v183\n\t" \
  "v_smfmac_f32_16x16x64_bf16 v[44:47], v[228:231], v[0:7], v175\n\t" \
  "s_add_u32 m0, s33, 1920\n\t" \
  "ds_read_b128 v[134:137], v183 offset:2048\n\t" \
  "v_subrev_u32 v25, s30, v25\n\t" \
  "v_smfmac_f32_16x16x64_bf16 v[48:51], v[232:235], v[0:7], v175 abid:1\n\t" \
  "s_add_u32 s30, s29, 18432\n\t" \
  "buffer_load_dwordx4 v186, s[16:19], s34 offen offset:128 nt lds\n\t" \
  "ds_read_b128 v[138:141], v184 offset:2048\n\t" \
  "v_cvt_pk_bf16_f32 v8, v36, v37\n\t" \
  "v_cvt_pk_bf16_f32 v9, v38, v39\n\t" \
  "v_smfmac_f32_16x16x64_bf16 v[52:55], v[236:239], v[0:7], v176\n\t" \
  "s_add_u32 m0, s33, 3840\n\t" \
  "ds_read_b128 v[36:39], v35 offset:0\n\t" \
  "v_cvt_pk_bf16_f32 v10, v40, v41\n\t" \
  "v_cvt_pk_bf16_f32 v11, v42, v43\n\t" \
  "v_smfmac_f32_16x16x64_bf16 v[56:59], v[240:243], v[0:7], v176 abid:1\n\t" \
  "s_sub_u32 s25, s30, 129024\n\t" \
  "buffer_load_dwordx4 v186, s[16:19], s34 offen offset:256 nt lds\n\t" \
  "ds_read_b128 v[40:43], v35 offset:64\n\t" \
  "v_cvt_pk_bf16_f32 v16, v44, v45\n\t" \
  "v_cvt_pk_bf16_f32 v17, v46, v47\n\t" \
  "v_smfmac_f32_16x16x64_bf16 v[60:63], v[244:247], v[0:7], v177\n\t" \
  "s_add_u32 m0, s33, 5760\n\t" \
  "ds_read_b128 v[44:47], v35 offset:128\n\t" \
  "v_cvt_pk_bf16_f32 v18, v48, v49\n\t" \
  "v_cvt_pk_bf16_f32 v19, v50, v51\n\t" \
  "v_smfmac_f32_16x16x64_bf16 v[64:67], v[248:251], v[0:7], v177 abid:1\n\t" \
  "s_min_u32 s30, s30, s25\n\t" \
  "buffer_load_dwordx4 v186, s[16:19], s34 offen offset:384 nt lds\n\t" \
  "ds_read_b128 v[48:51], v35 offset:192\n\t" \
  "v_cvt_pk_bf16_f32 v12, v52, v53\n\t" \
  "v_cvt_pk_bf16_f32 v13, v54, v55\n\t" \
  "v_smfmac_f32_16x16x64_bf16 v[68:71], v[252:255], v[0:7], v182\n\t" \
  "s_waitcnt lgkmcnt(3)\n\t" \
  "ds_read_b128 v[52:55], v35 offset:256\n\t" \
  "v_cvt_pk_bf16_f32 v14, v56, v57\n\t" \
  "v_cvt_pk_bf16_f32 v15, v58, v59\n\t" \
  "v_smfmac_f32_16x16x64_bf16 v[36:39], a[0:3], v[126:133], v142\n\t" \
  "s_waitcnt lgkmcnt(3)\n\t" \
  "ds_read_b128 v[56:59], v35 offset:320\n\t" \
  "v_cvt_pk_bf16_f32 v20, v60, v61\n\t" \
  "v_cvt_pk_bf16_f32 v21, v62, v63\n\t" \
  "v_smfmac_f32_16x16x64_bf16 v[40:43], a[4:7], v[126:133], v142 abid:1\n\t" \
  "s_waitcnt lgkmcnt(3)\n\t" \
  "ds_read_b128 v[60:63], v35 offset:384\n\t" \
  "v_cvt_pk_bf16_f32 v22, v64, v65\n\t" \
  "v_cvt_pk_bf16_f32 v23, v66, v67\n\t" \
  "v_smfmac_f32_16x16x64_bf16 v[44:47], a[8:11], v[126:133], v143\n\t" \
  "s_waitcnt lgkmcnt(3)\n\t" \
  "ds_read_b128 v[64:67], v35 offset:448\n\t" \
  "v_cvt_pk_bf16_f32 v26, v68, v69\n\t" \
  "v_cvt_pk_bf16_f32 v27, v70, v71\n\t" \
  "v_smfmac_f32_16x16x64_bf16 v[48:51], a[12:15], v[126:133], v143 abid:1\n\t" \
  "s_waitcnt lgkmcnt(3)\n\t" \
  "ds_read_b128 v[68:71], v35 offset:512\n\t" \
  "v_add_u32_sdwa v0, s29, v108 dst_sel:DWORD dst_unused:UNUSED_PAD src0_sel:DWORD src1_sel:WORD_0\n\t" \
  "v_smfmac_f32_16x16x64_bf16 v[52:55], a[16:19], v[126:133], v144\n\t" \
  "s_waitcnt lgkmcnt(3)\n\t" \
  "ds_write_b64 v24, v[8:9] offset:0\n\t" \
  "v_add_u32_sdwa v2, s29, v108 dst_sel:DWORD dst_unused:UNUSED_PAD src0_sel:DWORD src1_sel:WORD_1\n\t" \
  "v_smfmac_f32_16x16x64_bf16 v[56:59], a[20:23], v[126:133], v144 abid:1\n\t" \
  "s_waitcnt lgkmcnt(3)\n\t" \
  "ds_read_b64 v[0:1], v0 offset:0\n\t" \
  "v_add_u32_sdwa v4, s29, v109 dst_sel:DWORD dst_unused:UNUSED_PAD src0_sel:DWORD src1_sel:WORD_0\n\t" \
  "v_smfmac_f32_16x16x64_bf16 v[60:63], a[24:27], v[126:133], v145\n\t" \
  "s_waitcnt lgkmcnt(3)\n\t" \
  "ds_write_b64 v24, v[10:11] offset:8\n\t" \
  "v_add_u32_sdwa v6, s29, v109 dst_sel:DWORD dst_unused:UNUSED_PAD src0_sel:DWORD src1_sel:WORD_1\n\t" \
  "v_smfmac_f32_16x16x64_bf16 v[64:67], a[28:31], v[126:133], v145 abid:1\n\t" \
  "s_waitcnt lgkmcnt(3)\n\t" \
  "ds_read_b64 v[2:3], v2 offset:0\n\t" \
  "v_smfmac_f32_16x16x64_bf16 v[68:71], a[32:35], v[126:133], v178\n\t" \
  "s_sub_u32 s30, s30, s29\n\t" \
  "ds_write_b64 v24, v[12:13] offset:128\n\t" \
  "v_smfmac_f32_16x16x64_bf16 v[36:39], a[36:39], v[134:141], v146\n\t" \
  "s_mov_b32 s37, s29\n\t" \
  "ds_read_b128 v[126:129], v183 offset:4096\n\t" \
  "v_smfmac_f32_16x16x64_bf16 v[40:43], a[40:43], v[134:141], v146 abid:1\n\t" \
  "s_add_u32 s31, s31, 18432\n\t" \
  "ds_write_b64 v24, v[14:15] offset:136\n\t" \
  "v_xor_b32 v24, 64, v24\n\t" \
  "v_smfmac_f32_16x16x64_bf16 v[44:47], a[44:47], v[134:141], v147\n\t" \
  "s_sub_u32 s26, s31, 129024\n\t" \
  "ds_read_b128 v[130:133], v184 offset:4096\n\t" \
  "v_add_u32_sdwa v8, s29, v110 dst_sel:DWORD dst_unused:UNUSED_PAD src0_sel:DWORD src1_sel:WORD_0\n\t" \
  "v_smfmac_f32_16x16x64_bf16 v[48:51], a[48:51], v[134:141], v147 abid:1\n\t" \
  "s_min_u32 s31, s31, s26\n\t" \
  "ds_write_b64 v24, v[16:17] offset:0\n\t" \
  "v_add_u32_sdwa v10, s29, v110 dst_sel:DWORD dst_unused:UNUSED_PAD src0_sel:DWORD src1_sel:WORD_1\n\t" \
  "v_smfmac_f32_16x16x64_bf16 v[52:55], a[52:55], v[134:141], v148\n\t" \
  "s_add_u32 s33, s31, s32\n\t" \
  "ds_read_b64 v[4:5], v4 offset:0\n\t" \
  "v_add_u32_sdwa v12, s29, v111 dst_sel:DWORD dst_unused:UNUSED_PAD src0_sel:DWORD src1_sel:WORD_0\n\t" \
  "v_smfmac_f32_16x16x64_bf16 v[56:59], a[56:59], v[134:141], v148 abid:1\n\t" \
  "s_add_u32 s34, s34, 4718592\n\t" \
  "ds_write_b64 v24, v[18:19] offset:8\n\t" \
  "v_add_u32_sdwa v14, s29, v111 dst_sel:DWORD dst_unused:UNUSED_PAD src0_sel:DWORD src1_sel:WORD_1\n\t" \
  "v_smfmac_f32_16x16x64_bf16 v[60:63], a[60:63], v[134:141], v149\n\t" \
  "s_and_b32 s27, s64, 7\n\t" \
  "ds_read_b64 v[6:7], v6 offset:0\n\t" \
  "v_smfmac_f32_16x16x64_bf16 v[64:67], a[64:67], v[134:141], v149 abid:1\n\t" \
  "s_lshl2_add_u32 s66, s27, s68\n\t" \
  "ds_write_b64 v24, v[20:21] offset:128\n\t" \
  "v_smfmac_f32_16x16x64_bf16 v[68:71], a[68:71], v[134:141], v178 abid:1\n\t" \
  "s_waitcnt lgkmcnt(5)\n\t" \
  "ds_read_b128 v[134:137], v183 offset:6144\n\t" \
  "v_smfmac_f32_16x16x64_bf16 v[36:39], a[72:75], v[126:133], v150\n\t" \
  "s_and_b32 s27, s64, 8\n\t" \
  "ds_write_b64 v24, v[22:23] offset:136\n\t" \
  "v_add_u32_sdwa v16, s29, v112 dst_sel:DWORD dst_unused:UNUSED_PAD src0_sel:DWORD src1_sel:WORD_0\n\t" \
  "v_smfmac_f32_16x16x64_bf16 v[40:43], a[76:79], v[126:133], v150 abid:1\n\t" \
  "s_waitcnt lgkmcnt(3)\n\t" \
  "ds_read_b128 v[138:141], v184 offset:6144\n\t" \
  "v_add_u32_sdwa v18, s29, v112 dst_sel:DWORD dst_unused:UNUSED_PAD src0_sel:DWORD src1_sel:WORD_1\n\t" \
  "v_smfmac_f32_16x16x64_bf16 v[36:39], v[72:75], v[0:7], v182 abid:1\n\t" \
  "s_mul_i32 s27, s27, 176\n\t" \
  "ds_write_b64 v25, v[26:27] offset:16384\n\t" \
  "v_add_u32_sdwa v20, s29, v113 dst_sel:DWORD dst_unused:UNUSED_PAD src0_sel:DWORD src1_sel:WORD_0\n\t" \
  "v_smfmac_f32_16x16x64_bf16 v[44:47], a[80:83], v[126:133], v151\n\t" \
  "s_add_u32 s66, s66, s27\n\t" \
  "ds_read_b64 v[8:9], v8 offset:0\n\t" \
  "ds_read_b64 v[10:11], v10 offset:0\n\t" \
  "v_add_u32_sdwa v22, s29, v113 dst_sel:DWORD dst_unused:UNUSED_PAD src0_sel:DWORD src1_sel:WORD_1\n\t" \
  "v_smfmac_f32_16x16x64_bf16 v[48:51], a[84:87], v[126:133], v151 abid:1\n\t" \
  "s_lshr_b64 s[64:65], s[64:65], 4\n\t" \
  "ds_read_b64 v[12:13], v12 offset:0\n\t" \
  "ds_read_b64 v[14:15], v14 offset:0\n\t" \
  "v_add_u32_sdwa v0, s29, v114 dst_sel:DWORD dst_unused:UNUSED_PAD src0_sel:DWORD src1_sel:WORD_0\n\t" \
  "v_smfmac_f32_16x16x64_bf16 v[52:55], a[88:91], v[126:133], v152\n\t" \
  "s_sub_u32 s28, s72, s46\n\t" \
  "ds_read_b64 v[16:17], v16 offset:0\n\t" \
  "v_add_u32_sdwa v2, s29, v114 dst_sel:DWORD dst_unused:UNUSED_PAD src0_sel:DWORD src1_sel:WORD_1\n\t" \
  "v_smfmac_f32_16x16x64_bf16 v[56:59], a[92:95], v[126:133], v152 abid:1\n\t" \
  "s_add_u32 s46, s72, 185\n\t" \
  "ds_read_b64 v[18:19], v18 offset:0\n\t" \
  "v_add_u32_sdwa v4, s29, v115 dst_sel:DWORD dst_unused:UNUSED_PAD src0_sel:DWORD src1_sel:WORD_0\n\t" \
  "v_smfmac_f32_16x16x64_bf16 v[60:63], a[96:99], v[126:133], v153\n\t" \
  "s_max_i32 s28, s28, 0\n\t" \
  "ds_read_b64 v[20:21], v20 offset:0\n\t" \
  "v_add_u32_sdwa v6, s29, v115 dst_sel:DWORD dst_unused:UNUSED_PAD src0_sel:DWORD src1_sel:WORD_1\n\t" \
  "v_smfmac_f32_16x16x64_bf16 v[64:67], a[100:103], v[126:133], v153 abid:1\n\t" \
  "s_waitcnt lgkmcnt(3)\n\t" \
  "ds_read_b64 v[22:23], v22 offset:0\n\t" \
  "v_smfmac_f32_16x16x64_bf16 v[40:43], v[76:79], v[8:15], v182 abid:1\n\t" \
  "s_min_u32 s28, s28, 6\n\t" \
  "ds_read_b64 v[0:1], v0 offset:0\n\t" \
  "v_smfmac_f32_16x16x64_bf16 v[68:71], a[104:107], v[126:133], v179\n\t" \
  "s_lshl_b32 s28, s28, 6\n\t" \
  "ds_read_b128 v[126:129], v183 offset:8192\n\t" \
  "v_smfmac_f32_16x16x64_bf16 v[36:39], a[108:111], v[134:141], v154\n\t" \
  "s_add_u32 s28, s28, s53\n\t" \
  "ds_read_b128 v[130:133], v184 offset:8192\n\t" \
  "v_add_u32_sdwa v8, s29, v32 dst_sel:DWORD dst_unused:UNUSED_PAD src0_sel:DWORD src1_sel:WORD_0\n\t" \
  "v_smfmac_f32_16x16x64_bf16 v[40:43], a[112:115], v[134:141], v154 abid:1\n\t" \
  ".Ljn12%=:\n\t" \
  "s_waitcnt lgkmcnt(3)\n\t" \
  "ds_read_b64 v[2:3], v2 offset:0\n\t" \
  "v_add_u32_sdwa v10, s29, v32 dst_sel:DWORD dst_unused:UNUSED_PAD src0_sel:DWORD src1_sel:WORD_1\n\t" \
  "v_smfmac_f32_16x16x64_bf16 v[44:47], v[80:83], v[16:23], v182 abid:1\n\t" \
  "s_max_u32 s51, s51, s28\n\t" \
  "ds_read_b64 v[4:5], v4 offset:0\n\t" \
  "v_add_u32 v28, s60, v187\n\t" \
  "v_smfmac_f32_16x16x64_bf16 v[44:47], a[116:119], v[134:141], v155\n\t" \
  "s_add_u32 s54, s54, s51\n\t" \
  "ds_read_b64 v[6:7], v6 offset:0\n\t" \
  "v_add_u32_sdwa v16, s29, v116 dst_sel:DWORD dst_unused:UNUSED_PAD src0_sel:DWORD src1_sel:WORD_0\n\t" \
  "v_smfmac_f32_16x16x64_bf16 v[48:51], a[120:123], v[134:141], v155 abid:1\n\t" \
  "s_lshr_b32 s52, s54, 4\n\t" \
  "ds_read_b64 v[8:9], v8 offset:0\n\t" \
  "v_add_u32_sdwa v18, s29, v116 dst_sel:DWORD dst_unused:UNUSED_PAD src0_sel:DWORD src1_sel:WORD_1\n\t" \
  "v_smfmac_f32_16x16x64_bf16 v[52:55], a[124:127], v[134:141], v156\n\t" \
  "s_add_u32 s52, s52, s56\n\t" \
  "ds_read_b64 v[10:11], v10 offset:0\n\t" \
  "v_add_u32_sdwa v20, s29, v117 dst_sel:DWORD dst_unused:UNUSED_PAD src0_sel:DWORD src1_sel:WORD_0\n\t" \
  "v_smfmac_f32_16x16x64_bf16 v[56:59], a[128:131], v[134:141], v156 abid:1\n\t" \
  "s_lshr_b32 s28, s51, 4\n\t" \
  "ds_read_b128 v[28:31], v28 offset:0\n\t" \
  "v_add_u32_sdwa v22, s29, v117 dst_sel:DWORD dst_unused:UNUSED_PAD src0_sel:DWORD src1_sel:WORD_1\n\t" \
  "v_smfmac_f32_16x16x64_bf16 v[60:63], a[132:135], v[134:141], v157\n\t" \
  "s_waitcnt lgkmcnt(3)\n\t" \
  "ds_read_b64 v[16:17], v16 offset:0\n\t" \
  "v_smfmac_f32_16x16x64_bf16 v[48:51], v[84:87], v[0:7], v182 abid:1\n\t" \
  "s_add_u32 s28, s28, s72\n\t" \
  "ds_read_b64 v[18:19], v18 offset:0\n\t" \
  "v_add_u32 v24, s38, v187\n\t" \
  "v_smfmac_f32_16x16x64_bf16 v[64:67], a[136:139], v[134:141], v157 abid:1\n\t" \
  "s_sub_u32 s28, s28, s101\n\t" \
  "ds_read_b128 v[24:27], v24 offset:4096\n\t" \
  "v_add_u32_sdwa v0, s29, v118 dst_sel:DWORD dst_unused:UNUSED_PAD src0_sel:DWORD src1_sel:WORD_0\n\t" \
  "v_smfmac_f32_16x16x64_bf16 v[68:71], a[140:143], v[134:141], v179 abid:1\n\t" \
  "s_waitcnt lgkmcnt(3)\n\t" \
  "ds_read_b128 v[134:137], v183 offset:10240\n\t" \
  "v_add_u32_sdwa v2, s29, v118 dst_sel:DWORD dst_unused:UNUSED_PAD src0_sel:DWORD src1_sel:WORD_1\n\t" \
  "s_set_gpr_idx_on s6, gpr_idx(DST)\n\t" \
  "v_smfmac_f32_16x16x64_bf16 v[36:39], v[28:31], v[8:15], v182 abid:1\n\t" \
  "s_set_gpr_idx_off\n\t" \
  "ds_read_b128 v[138:141], v184 offset:10240\n\t" \
  "v_add_u32_sdwa v4, s29, v119 dst_sel:DWORD dst_unused:UNUSED_PAD src0_sel:DWORD src1_sel:WORD_0\n\t" \
  "v_smfmac_f32_16x16x64_bf16 v[36:39], a[144:147], v[126:133], v158\n\t" \
  "s_add_u32 s36, s35, s39\n\t" \
  "ds_read_b64 v[20:21], v20 offset:0\n\t" \
  "v_add_u32_sdwa v6, s29, v119 dst_sel:DWORD dst_unused:UNUSED_PAD src0_sel:DWORD src1_sel:WORD_1\n\t" \
  "v_smfmac_f32_16x16x64_bf16 v[40:43], a[148:151], v[126:133], v158 abid:1\n\t" \
  "s_waitcnt lgkmcnt(3)\n\t" \
  "buffer_store_dwordx4 v[24:27], v34, s[20:23], s36 offen sc1\n\t" \
  "v_add_u32_sdwa v8, s29, v33 dst_sel:DWORD dst_unused:UNUSED_PAD src0_sel:DWORD src1_sel:WORD_0\n\t" \
  "v_smfmac_f32_16x16x64_bf16 v[44:47], a[152:155], v[126:133], v159\n\t" \
  "s_max_u32 s52, s52, s28\n\t" \
  "ds_read_b64 v[22:23], v22 offset:0\n\t" \
  "v_xad_u32 v24, v187, 64, s38\n\t" \
  "v_smfmac_f32_16x16x64_bf16 v[48:51], a[156:159], v[126:133], v159 abid:1\n\t" \
  "s_sub_u32 s51, s51, 112\n\t" \
  "ds_read_b128 v[24:27], v24 offset:5120\n\t" \
  "v_add_u32_sdwa v10, s29, v33 dst_sel:DWORD dst_unused:UNUSED_PAD src0_sel:DWORD src1_sel:WORD_1\n\t" \
  "v_smfmac_f32_16x16x64_bf16 v[52:55], a[160:163], v[126:133], v160\n\t" \
  "s_max_u32 s51, s51, s53\n\t" \
  "ds_read_b64 v[0:1], v0 offset:0\n\t" \
  "v_add_u32 v28, s60, v187\n\t" \
  "v_smfmac_f32_16x16x64_bf16 v[56:59], a[164:167], v[126:133], v160 abid:1\n\t" \
  "ds_read_b64 v[2:3], v2 offset:0\n\t" \
  "v_smfmac_f32_16x16x64_bf16 v[60:63], a[168:171], v[126:133], v161\n\t" \
  "s_add_u32 s36, s36, 4608\n\t" \
  "s_waitcnt lgkmcnt(3)\n\t" \
  "ds_read_b64 v[4:5], v4 offset:0\n\t" \
  "v_smfmac_f32_16x16x64_bf16 v[52:55], v[88:91], v[16:23], v182 abid:1\n\t" \
  "s_waitcnt lgkmcnt(3)\n\t" \
  "buffer_store_dwordx4 v[24:27], v34, s[20:23], s36 offen sc1\n\t" \
  "v_add_u32_sdwa v16, s29, v120 dst_sel:DWORD dst_unused:UNUSED_PAD src0_sel:DWORD src1_sel:WORD_0\n\t" \
  "v_smfmac_f32_16x16x64_bf16 v[64:67], a[172:175], v[126:133], v161 abid:1\n\t" \
  "ds_read_b64 v[6:7], v6 offset:0\n\t" \
  "v_add_u32 v24, s38, v187\n\t" \
  "v_smfmac_f32_16x16x64_bf16 v[68:71], a[176:179], v[126:133], v180\n\t" \
  "ds_read_b128 v[24:27], v24 offset:6144\n\t" \
  "v_add_u32_sdwa v18, s29, v120 dst_sel:DWORD dst_unused:UNUSED_PAD src0_sel:DWORD src1_sel:WORD_1\n\t" \
  "v_smfmac_f32_16x16x64_bf16 v[36:39], a[180:183], v[134:141], v162\n\t" \
  "ds_read_b128 v[126:129], v183 offset:12288\n\t" \
  "v_add_u32_sdwa v20, s29, v121 dst_sel:DWORD dst_unused:UNUSED_PAD src0_sel:DWORD src1_sel:WORD_0\n\t" \
  "v_smfmac_f32_16x16x64_bf16 v[40:43], a[184:187], v[134:141], v162 abid:1\n\t" \
  "ds_read_b128 v[130:133], v184 offset:12288\n\t" \
  "v_add_u32_sdwa v22, s29, v121 dst_sel:DWORD dst_unused:UNUSED_PAD src0_sel:DWORD src1_sel:WORD_1\n\t" \
  "v_smfmac_f32_16x16x64_bf16 v[44:47], a[188:191], v[134:141], v163\n\t" \
  "s_add_u32 s36, s36, 4608\n\t" \
  "s_waitcnt lgkmcnt(3)\n\t" \
  "ds_read_b64 v[8:9], v8 offset:0\n\t" \
  "v_smfmac_f32_16x16x64_bf16 v[56:59], v[92:95], v[0:7], v182 abid:1\n\t" \
  "s_waitcnt lgkmcnt(3)\n\t" \
  "buffer_store_dwordx4 v[24:27], v34, s[20:23], s36 offen sc1\n\t" \
  "v_add_u32_sdwa v0, s29, v122 dst_sel:DWORD dst_unused:UNUSED_PAD src0_sel:DWORD src1_sel:WORD_0\n\t" \
  "v_smfmac_f32_16x16x64_bf16 v[48:51], a[192:195], v[134:141], v163 abid:1\n\t" \
  "ds_read_b64 v[10:11], v10 offset:0\n\t" \
  "v_xad_u32 v24, v187, 64, s38\n\t" \
  "v_smfmac_f32_16x16x64_bf16 v[52:55], a[196:199], v[134:141], v164\n\t" \
  "s_add_u32 s38, s38, 18432\n\t" \
  "ds_read_b128 v[24:27], v24 offset:7168\n\t" \
  "v_add_u32_sdwa v2, s29, v122 dst_sel:DWORD dst_unused:UNUSED_PAD src0_sel:DWORD src1_sel:WORD_1\n\t" \
  "v_smfmac_f32_16x16x64_bf16 v[56:59], a[200:203], v[134:141], v164 abid:1\n\t" \
  "s_sub_u32 s44, s38, 129024\n\t" \
  "ds_read_b128 v[28:31], v28 offset:1280\n\t" \
  "v_add_u32_sdwa v4, s29, v123 dst_sel:DWORD dst_unused:UNUSED_PAD src0_sel:DWORD src1_sel:WORD_0\n\t" \
  "v_smfmac_f32_16x16x64_bf16 v[60:63], a[204:207], v[134:141], v165\n\t" \
  "s_min_u32 s38, s38, s44\n\t" \
  "ds_read_b64 v[16:17], v16 offset:0\n\t" \
  "v_add_u32_sdwa v6, s29, v123 dst_sel:DWORD dst_unused:UNUSED_PAD src0_sel:DWORD src1_sel:WORD_1\n\t" \
  "v_smfmac_f32_16x16x64_bf16 v[64:67], a[208:211], v[134:141], v165 abid:1\n\t" \
  "s_add_u32 s36, s36, 4608\n\t" \
  "ds_read_b64 v[18:19], v18 offset:0\n\t" \
  "v_smfmac_f32_16x16x64_bf16 v[68:71], a[212:215], v[134:141], v180 abid:1\n\t" \
  "s_waitcnt lgkmcnt(3)\n\t" \
  "buffer_store_dwordx4 v[24:27], v34, s[20:23], s36 offen sc1\n\t" \
  "v_smfmac_f32_16x16x64_bf16 v[36:39], a[216:219], v[126:133], v166\n\t" \
  "s_waitcnt lgkmcnt(2)\n\t" \
  "ds_read_b128 v[134:137], v183 offset:14336\n\t" \
  "v_add_u32 v24, s43, v187\n\t" \
  "s_set_gpr_idx_on s7, gpr_idx(DST)\n\t" \
  "v_smfmac_f32_16x16x64_bf16 v[36:39], v[28:31], v[8:15], v182 abid:1\n\t" \
  "s_set_gpr_idx_off\n\t" \
  "ds_read_b128 v[24:27], v24 offset:16384\n\t" \
  "v_add_u32_sdwa v8, s29, v124 dst_sel:DWORD dst_unused:UNUSED_PAD src0_sel:DWORD src1_sel:WORD_0\n\t" \
  "v_smfmac_f32_16x16x64_bf16 v[40:43], a[220:223], v[126:133], v166 abid:1\n\t" \
  "s_add_u32 s35, s35, 4718592\n\t" \
  "ds_read_b128 v[138:141], v184 offset:14336\n\t" \
  "v_add_u32_sdwa v10, s29, v124 dst_sel:DWORD dst_unused:UNUSED_PAD src0_sel:DWORD src1_sel:WORD_1\n\t" \
  "v_smfmac_f32_16x16x64_bf16 v[44:47], a[224:227], v[126:133], v167\n\t" \
  "ds_read_b64 v[20:21], v20 offset:0\n\t" \
  "v_add_u32_sdwa v12, s29, v125 dst_sel:DWORD dst_unused:UNUSED_PAD src0_sel:DWORD src1_sel:WORD_0\n\t" \
  "v_smfmac_f32_16x16x64_bf16 v[48:51], a[228:231], v[126:133], v167 abid:1\n\t" \
  "ds_read_b64 v[22:23], v22 offset:0\n\t" \
  "v_add_u32_sdwa v14, s29, v125 dst_sel:DWORD dst_unused:UNUSED_PAD src0_sel:DWORD src1_sel:WORD_1\n\t" \
  "v_smfmac_f32_16x16x64_bf16 v[52:55], a[232:235], v[126:133], v168\n\t" \
  "s_waitcnt lgkmcnt(3)\n\t" \
  "buffer_store_dwordx4 v[24:27], v186, s[20:23], s40 offen offset:384 sc1\n\t" \
  "v_smfmac_f32_16x16x64_bf16 v[56:59], a[236:239], v[126:133], v168 abid:1\n\t" \
  "s_add_u32 s29, s29, s30\n\t" \
  "ds_read_b64 v[0:1], v0 offset:0\n\t" \
  "v_smfmac_f32_16x16x64_bf16 v[60:63], a[240:243], v[126:133], v169\n\t" \
  "s_add_u32 s43, s43, 18432\n\t" \
  "ds_read_b64 v[2:3], v2 offset:0\n\t" \
  "v_smfmac_f32_16x16x64_bf16 v[64:67], a[244:247], v[126:133], v169 abid:1\n\t" \
  "s_waitcnt lgkmcnt(2)\n\t" \
  "ds_read_b64 v[4:5], v4 offset:0\n\t" \
  "v_smfmac_f32_16x16x64_bf16 v[60:63], v[96:99], v[16:23], v182 abid:1\n\t" \
  "s_sub_u32 s42, s43, 129024\n\t" \
  "ds_read_b64 v[6:7], v6 offset:0\n\t" \
  "v_smfmac_f32_16x16x64_bf16 v[68:71], a[248:251], v[126:133], v181\n\t" \
  "s_min_u32 s43, s43, s42\n\t" \
  "ds_read_b64 v[8:9], v8 offset:0\n\t" \
  "v_smfmac_f32_16x16x64_bf16 v[36:39], a[252:255], v[134:141], v170\n\t" \
  "s_add_u32 s40, s40, 4718592\n\t" \
  "ds_read_b64 v[10:11], v10 offset:0\n\t" \
  "v_smfmac_f32_16x16x64_bf16 v[40:43], v[188:191], v[134:141], v170 abid:1\n\t" \
  "ds_read_b64 v[12:13], v12 offset:0\n\t" \
  "v_smfmac_f32_16x16x64_bf16 v[44:47], v[192:195], v[134:141], v171\n\t" \
  "s_waitcnt lgkmcnt(3)\n\t" \
  "ds_read_b64 v[14:15], v14 offset:0\n\t" \
  "v_smfmac_f32_16x16x64_bf16 v[64:67], v[100:103], v[0:7], v182 abid:1\n\t" \
  "ds_read_b128 v[0:3], v183 offset:16384\n\t" \
  "v_smfmac_f32_16x16x64_bf16 v[48:51], v[196:199], v[134:141], v171 abid:1\n\t" \
  "ds_read_b128 v[4:7], v184 offset:16384\n\t" \
  "v_add_u32 v183, s30, v183\n\t" \
  "v_smfmac_f32_16x16x64_bf16 v[52:55], v[200:203], v[134:141], v172\n\t" \
  "s_memrealtime s[72:73]\n\t" \
  "v_add_u32 v184, s30, v184\n\t" \
  "v_smfmac_f32_16x16x64_bf16 v[56:59], v[204:207], v[134:141], v172 abid:1\n\t" \
  "s_waitcnt lgkmcnt(2)\n\t" \
  "v_smfmac_f32_16x16x64_bf16 v[68:71], v[104:107], v[8:15], v182 abid:1\n\t" \
  "v_smfmac_f32_16x16x64_bf16 v[60:63], v[208:211], v[134:141], v173\n\t" \
  "v_smfmac_f32_16x16x64_bf16 v[64:67], v[212:215], v[134:141], v173 abid:1\n\t" \
  "v_smfmac_f32_16x16x64_bf16 v[68:71], v[216:219], v[134:141], v181 abid:1\n\t" \
  "s_setpc_b64 s[66:67]\n\t" \
  ".Lls12%=:\n\t" \
  "s_memrealtime s[72:73]\n\t" \
  "s_waitcnt lgkmcnt(0)\n\t" \
  "s_sub_i32 s28, s72, s52\n\t" \
  "s_cmp_lt_i32 s28, 0\n\t" \
  "s_cbranch_scc1 .Lls12%=\n\t" \
  "s_branch .Llg12%=\n\t" \
  ".fill (.Lvar%=+20480-.)/4, 4, 0xbf800000\n\t" \
  "s_branch .Lfin%=\n\t" \
  "s_branch .Lhe%=\n\t" \
  "s_waitcnt vmcnt(6)\n\t" \
  "s_waitcnt vmcnt(10)\n\t" \
  "s_waitcnt vmcnt(12)\n\t" \
  "s_waitcnt vmcnt(17)\n\t" \
  "s_waitcnt vmcnt(21) lgkmcnt(0)\n\t" \
  "s_cmp_lt_u32 s72, s52\n\t" \
  "s_cbranch_scc1 .Lns13%=\n\t" \
  ".Lng13%=:\n\t" \
  "s_barrier\n\t" \
  "v_smfmac_f32_16x16x64_bf16 v[36:39], v[220:223], v[0:7], v174\n\t" \
  "s_movk_i32 s53, 1952\n\t" \
  "ds_read_b128 v[126:129], v183 offset:0\n\t" \
  "v_add_u32 v24, s37, v185\n\t" \
  "v_lshrrev_b32 v28, 4, v187\n\t" \
  "v_smfmac_f32_16x16x64_bf16 v[40:43], v[224:227], v[0:7], v174 abid:1\n\t" \
  "ds_read_b128 v[130:133], v184 offset:0\n\t" \
  "v_xor_b32 v25, s41, v183\n\t" \
  "v_and_or_b32 v28, v28, 48, s45\n\t" \
  "v_smfmac_f32_16x16x64_bf16 v[44:47], v[228:231], v[0:7], v175\n\t" \
  "s_mov_b32 s51, s53\n\t" \
  "ds_read_b128 v[134:137], v183 offset:2048\n\t" \
  "v_subrev_u32 v25, s30, v25\n\t" \
  "v_smfmac_f32_16x16x64_bf16 v[48:51], v[232:235], v[0:7], v175 abid:1\n\t" \
  "s_add_u32 s30, s29, 18432\n\t" \
  "ds_read_b128 v[138:141], v184 offset:2048\n\t" \
  "v_cvt_pk_bf16_f32 v8, v36, v37\n\t" \
  "v_cvt_pk_bf16_f32 v9, v38, v39\n\t" \
  "v_smfmac_f32_16x16x64_bf16 v[52:55], v[236:239], v[0:7], v176\n\t" \
  "ds_read_b128 v[36:39], v28 offset:0\n\t" \
  "v_cvt_pk_bf16_f32 v10, v40, v41\n\t" \
  "v_cvt_pk_bf16_f32 v11, v42, v43\n\t" \
  "v_smfmac_f32_16x16x64_bf16 v[56:59], v[240:243], v[0:7], v176 abid:1\n\t" \
  "s_sub_u32 s25, s30, 129024\n\t" \
  "ds_read_b128 v[40:43], v28 offset:64\n\t" \
  "v_cvt_pk_bf16_f32 v16, v44, v45\n\t" \
  "v_cvt_pk_bf16_f32 v17, v46, v47\n\t" \
  "v_smfmac_f32_16x16x64_bf16 v[60:63], v[244:247], v[0:7], v177\n\t" \
  "ds_read_b128 v[44:47], v28 offset:128\n\t" \
  "v_cvt_pk_bf16_f32 v18, v48, v49\n\t" \
  "v_cvt_pk_bf16_f32 v19, v50, v51\n\t" \
  "v_smfmac_f32_16x16x64_bf16 v[64:67], v[248:251], v[0:7], v177 abid:1\n\t" \
  "s_min_u32 s30, s30, s25\n\t" \
  "ds_read_b128 v[48:51], v28 offset:192\n\t" \
  "v_cvt_pk_bf16_f32 v12, v52, v53\n\t" \
  "v_cvt_pk_bf16_f32 v13, v54, v55\n\t" \
  "v_smfmac_f32_16x16x64_bf16 v[68:71], v[252:255], v[0:7], v182\n\t" \
  "s_waitcnt lgkmcnt(3)\n\t" \
  "ds_read_b128 v[52:55], v28 offset:256\n\t" \
  "v_cvt_pk_bf16_f32 v14, v56, v57\n\t" \
  "v_cvt_pk_bf16_f32 v15, v58, v59\n\t" \
  "v_smfmac_f32_16x16x64_bf16 v[36:39], a[0:3], v[126:133], v142\n\t" \
  "s_waitcnt lgkmcnt(3)\n\t" \
  "ds_read_b128 v[56:59], v28 offset:320\n\t" \
  "v_cvt_pk_bf16_f32 v20, v60, v61\n\t" \
  "v_cvt_pk_bf16_f32 v21, v62, v63\n\t" \
  "v_smfmac_f32_16x16x64_bf16 v[40:43], a[4:7], v[126:133], v142 abid:1\n\t" \
  "s_waitcnt lgkmcnt(3)\n\t" \
  "ds_read_b128 v[60:63], v28 offset:384\n\t" \
  "v_cvt_pk_bf16_f32 v22, v64, v65\n\t" \
  "v_cvt_pk_bf16_f32 v23, v66, v67\n\t" \
  "v_smfmac_f32_16x16x64_bf16 v[44:47], a[8:11], v[126:133], v143\n\t" \
  "s_waitcnt lgkmcnt(3)\n\t" \
  "ds_read_b128 v[64:67], v28 offset:448\n\t" \
  "v_cvt_pk_bf16_f32 v26, v68, v69\n\t" \
  "v_cvt_pk_bf16_f32 v27, v70, v71\n\t" \
  "v_smfmac_f32_16x16x64_bf16 v[48:51], a[12:15], v[126:133], v143 abid:1\n\t" \
  "s_waitcnt lgkmcnt(3)\n\t" \
  "ds_read_b128 v[68:71], v28 offset:512\n\t" \
  "v_add_u32_sdwa v0, s29, v108 dst_sel:DWORD dst_unused:UNUSED_PAD src0_sel:DWORD src1_sel:WORD_0\n\t" \
  "v_smfmac_f32_16x16x64_bf16 v[52:55], a[16:19], v[126:133], v144\n\t" \
  "s_waitcnt lgkmcnt(3)\n\t" \
  "ds_write_b64 v24, v[8:9] offset:0\n\t" \
  "v_add_u32_sdwa v2, s29, v108 dst_sel:DWORD dst_unused:UNUSED_PAD src0_sel:DWORD src1_sel:WORD_1\n\t" \
  "v_smfmac_f32_16x16x64_bf16 v[56:59], a[20:23], v[126:133], v144 abid:1\n\t" \
  "s_waitcnt lgkmcnt(3)\n\t" \
  "s_bfe_i32 s47, s48, 0x10000\n\t" \
  "ds_read_b64 v[0:1], v0 offset:0\n\t" \
  "v_add_u32_sdwa v4, s29, v109 dst_sel:DWORD dst_unused:UNUSED_PAD src0_sel:DWORD src1_sel:WORD_0\n\t" \
  "v_add_u32 v28, s38, v187\n\t" \
  "v_smfmac_f32_16x16x64_bf16 v[60:63], a[24:27], v[126:133], v145\n\t" \
  "s_waitcnt lgkmcnt(3)\n\t" \
  "s_lshr_b32 s48, s48, 1\n\t" \
  "ds_write_b64 v24, v[10:11] offset:8\n\t" \
  "ds_read_b128 v[28:31], v28 offset:4096\n\t" \
  "v_add_u32_sdwa v6, s29, v109 dst_sel:DWORD dst_unused:UNUSED_PAD src0_sel:DWORD src1_sel:WORD_1\n\t" \
  "v_smfmac_f32_16x16x64_bf16 v[64:67], a[28:31], v[126:133], v145 abid:1\n\t" \
  "s_waitcnt lgkmcnt(3)\n\t" \
  "s_add_u32 s36, s35, s39\n\t" \
  "ds_read_b64 v[2:3], v2 offset:0\n\t" \
  "v_smfmac_f32_16x16x64_bf16 v[68:71], a[32:35], v[126:133], v178\n\t" \
  "s_sub_u32 s30, s30, s29\n\t" \
  "s_andn2_b32 s44, 0x40000000, s47\n\t" \
  "ds_write_b64 v24, v[12:13] offset:128\n\t" \
  "v_smfmac_f32_16x16x64_bf16 v[36:39], a[36:39], v[134:141], v146\n\t" \
  "s_mov_b32 s37, s29\n\t" \
  "s_or_b32 s36, s36, s44\n\t" \
  "ds_read_b128 v[126:129], v183 offset:4096\n\t" \
  "v_smfmac_f32_16x16x64_bf16 v[40:43], a[40:43], v[134:141], v146 abid:1\n\t" \
  "ds_write_b64 v24, v[14:15] offset:136\n\t" \
  "s_waitcnt lgkmcnt(4)\n\t" \
  "buffer_store_dwordx4 v[28:31], v34, s[20:23], s36 offen sc1\n\t" \
  "v_xor_b32 v24, 64, v24\n\t" \
  "v_smfmac_f32_16x16x64_bf16 v[44:47], a[44:47], v[134:141], v147\n\t" \
  "s_and_b32 s44, s47, 4718592\n\t" \
  "ds_read_b128 v[130:133], v184 offset:4096\n\t" \
  "v_add_u32_sdwa v8, s29, v110 dst_sel:DWORD dst_unused:UNUSED_PAD src0_sel:DWORD src1_sel:WORD_0\n\t" \
  "v_xad_u32 v28, v187, 64, s38\n\t" \
  "v_smfmac_f32_16x16x64_bf16 v[48:51], a[48:51], v[134:141], v147 abid:1\n\t" \
  "s_add_u32 s35, s35, s44\n\t" \
  "ds_write_b64 v24, v[16:17] offset:0\n\t" \
  "ds_read_b128 v[28:31], v28 offset:5120\n\t" \
  "v_add_u32_sdwa v10, s29, v110 dst_sel:DWORD dst_unused:UNUSED_PAD src0_sel:DWORD src1_sel:WORD_1\n\t" \
  "v_smfmac_f32_16x16x64_bf16 v[52:55], a[52:55], v[134:141], v148\n\t" \
  "s_add_u32 s36, s36, 4608\n\t" \
  "ds_read_b64 v[4:5], v4 offset:0\n\t" \
  "v_add_u32_sdwa v12, s29, v111 dst_sel:DWORD dst_unused:UNUSED_PAD src0_sel:DWORD src1_sel:WORD_0\n\t" \
  "v_smfmac_f32_16x16x64_bf16 v[56:59], a[56:59], v[134:141], v148 abid:1\n\t" \
  "ds_write_b64 v24, v[18:19] offset:8\n\t" \
  "v_add_u32_sdwa v14, s29, v111 dst_sel:DWORD dst_unused:UNUSED_PAD src0_sel:DWORD src1_sel:WORD_1\n\t" \
  "v_smfmac_f32_16x16x64_bf16 v[60:63], a[60:63], v[134:141], v149\n\t" \
  "s_and_b32 s27, s64, 7\n\t" \
  "ds_read_b64 v[6:7], v6 offset:0\n\t" \
  "v_smfmac_f32_16x16x64_bf16 v[64:67], a[64:67], v[134:141], v149 abid:1\n\t" \
  "s_lshl2_add_u32 s66, s27, s68\n\t" \
  "ds_write_b64 v24, v[20:21] offset:128\n\t" \
  "s_waitcnt lgkmcnt(4)\n\t" \
  "buffer_store_dwordx4 v[28:31], v34, s[20:23], s36 offen sc1\n\t" \
  "v_smfmac_f32_16x16x64_bf16 v[68:71], a[68:71], v[134:141], v178 abid:1\n\t" \
  "s_waitcnt lgkmcnt(5)\n\t" \
  "s_and_b32 s44, s47, 18432\n\t" \
  "ds_read_b128 v[134:137], v183 offset:6144\n\t" \
  "v_add_u32 v28, s38, v187\n\t" \
  "v_smfmac_f32_16x16x64_bf16 v[36:39], a[72:75], v[126:133], v150\n\t" \
  "s_and_b32 s27, s64, 8\n\t" \
  "ds_write_b64 v24, v[22:23] offset:136\n\t" \
  "ds_read_b128 v[28:31], v28 offset:6144\n\t" \
  "v_add_u32_sdwa v16, s29, v112 dst_sel:DWORD dst_unused:UNUSED_PAD src0_sel:DWORD src1_sel:WORD_0\n\t" \
  "v_smfmac_f32_16x16x64_bf16 v[40:43], a[76:79], v[126:133], v150 abid:1\n\t" \
  "s_waitcnt lgkmcnt(3)\n\t" \
  "s_add_u32 s36, s36, 4608\n\t" \
  "ds_read_b128 v[138:141], v184 offset:6144\n\t" \
  "v_add_u32_sdwa v18, s29, v112 dst_sel:DWORD dst_unused:UNUSED_PAD src0_sel:DWORD src1_sel:WORD_1\n\t" \
  "v_smfmac_f32_16x16x64_bf16 v[36:39], v[72:75], v[0:7], v182 abid:1\n\t" \
  "s_mul_i32 s27, s27, 176\n\t" \
  "ds_write_b64 v25, v[26:27] offset:16384\n\t" \
  "v_add_u32_sdwa v20, s29, v113 dst_sel:DWORD dst_unused:UNUSED_PAD src0_sel:DWORD src1_sel:WORD_0\n\t" \
  "v_smfmac_f32_16x16x64_bf16 v[44:47], a[80:83], v[126:133], v151\n\t" \
  "s_add_u32 s66, s66, s27\n\t" \
  "ds_read_b64 v[8:9], v8 offset:0\n\t" \
  "ds_read_b64 v[10:11], v10 offset:0\n\t" \
  "v_add_u32_sdwa v22, s29, v113 dst_sel:DWORD dst_unused:UNUSED_PAD src0_sel:DWORD src1_sel:WORD_1\n\t" \
  "v_smfmac_f32_16x16x64_bf16 v[48:51], a[84:87], v[126:133], v151 abid:1\n\t" \
  "s_lshr_b64 s[64:65], s[64:65], 4\n\t" \
  "ds_read_b64 v[12:13], v12 offset:0\n\t" \
  "ds_read_b64 v[14:15], v14 offset:0\n\t" \
  "s_waitcnt lgkmcnt(6)\n\t" \
  "buffer_store_dwordx4 v[28:31], v34, s[20:23], s36 offen sc1\n\t" \
  "v_add_u32_sdwa v0, s29, v114 dst_sel:DWORD dst_unused:UNUSED_PAD src0_sel:DWORD src1_sel:WORD_0\n\t" \
  "v_smfmac_f32_16x16x64_bf16 v[52:55], a[88:91], v[126:133], v152\n\t" \
  "s_sub_u32 s28, s72, s46\n\t" \
  "ds_read_b64 v[16:17], v16 offset:0\n\t" \
  "ds_read_b64 v[18:19], v18 offset:0\n\t" \
  "v_add_u32_sdwa v2, s29, v114 dst_sel:DWORD dst_unused:UNUSED_PAD src0_sel:DWORD src1_sel:WORD_1\n\t" \
  "v_xad_u32 v28, v187, 64, s38\n\t" \
  "v_smfmac_f32_16x16x64_bf16 v[56:59], a[92:95], v[126:133], v152 abid:1\n\t" \
  "s_add_u32 s46, s72, 185\n\t" \
  "s_add_u32 s38, s38, s44\n\t" \
  "ds_read_b64 v[20:21], v20 offset:0\n\t" \
  "ds_read_b64 v[22:23], v22 offset:0\n\t" \
  "ds_read_b128 v[28:31], v28 offset:7168\n\t" \
  "v_add_u32_sdwa v4, s29, v115 dst_sel:DWORD dst_unused:UNUSED_PAD src0_sel:DWORD src1_sel:WORD_0\n\t" \
  "v_smfmac_f32_16x16x64_bf16 v[60:63], a[96:99], v[126:133], v153\n\t" \
  "s_max_i32 s28, s28, 0\n\t" \
  "s_add_u32 s36, s36, 4608\n\t" \
  "ds_read_b64 v[0:1], v0 offset:0\n\t" \
  "v_add_u32_sdwa v6, s29, v115 dst_sel:DWORD dst_unused:UNUSED_PAD src0_sel:DWORD src1_sel:WORD_1\n\t" \
  "v_smfmac_f32_16x16x64_bf16 v[64:67], a[100:103], v[126:133], v153 abid:1\n\t" \
  "s_waitcnt lgkmcnt(5)\n\t" \
  "s_sub_u32 s44, s38, 129024\n\t" \
  "ds_read_b64 v[2:3], v2 offset:0\n\t" \
  "v_smfmac_f32_16x16x64_bf16 v[40:43], v[76:79], v[8:15], v182 abid:1\n\t" \
  "s_min_u32 s28, s28, 6\n\t" \
  "s_min_u32 s38, s38, s44\n\t" \
  "ds_read_b64 v[4:5], v4 offset:0\n\t" \
  "v_smfmac_f32_16x16x64_bf16 v[68:71], a[104:107], v[126:133], v179\n\t" \
  "s_waitcnt lgkmcnt(3)\n\t" \
  "ds_read_b128 v[126:129], v183 offset:8192\n\t" \
  "s_waitcnt lgkmcnt(4)\n\t" \
  "buffer_store_dwordx4 v[28:31], v34, s[20:23], s36 offen sc1\n\t" \
  "v_smfmac_f32_16x16x64_bf16 v[44:47], v[80:83], v[16:23], v182 abid:1\n\t" \
  "s_lshl_b32 s28, s28, 6\n\t" \
  "ds_read_b128 v[130:133], v184 offset:8192\n\t" \
  "v_add_u32_sdwa v8, s29, v32 dst_sel:DWORD dst_unused:UNUSED_PAD src0_sel:DWORD src1_sel:WORD_0\n\t" \
  "v_smfmac_f32_16x16x64_bf16 v[36:39], a[108:111], v[134:141], v154\n\t" \
  "s_branch .Ljn13%=\n\t" \
  ".Lns13%=:\n\t" \
  "s_memrealtime s[72:73]\n\t" \
  "s_waitcnt lgkmcnt(0)\n\t" \
  "s_sub_i32 s28, s72, s52\n\t" \
  "s_cmp_lt_i32 s28, 0\n\t" \
  "s_cbranch_scc1 .Lns13%=\n\t" \
  "s_branch .Lng13%=\n\t" \
  ".fill (.Lvar%=+21884-.)/4, 4, 0xbf800000\n\t" \
  "s_nop 0\n\t" \
  "s_branch .Lfin%=\n\t" \
  "s_branch .Lhe%=\n\t" \
  "s_waitcnt vmcnt(6)\n\t" \
  "s_waitcnt vmcnt(10)\n\t" \
  "s_waitcnt vmcnt(12)\n\t" \
  "s_waitcnt vmcnt(17)\n\t" \
  "s_waitcnt vmcnt(21) lgkmcnt(0)\n\t" \
  "s_cmp_lt_u32 s72, s52\n\t" \
  "s_cbranch_scc1 .Lls13%=\n\t" \
  ".Llg13%=:\n\t" \
  "s_barrier\n\t" \
  "v_smfmac_f32_16x16x64_bf16 v[36:39], v[220:223], v[0:7], v174\n\t" \
  "s_add_u32 m0, s33, 0\n\t" \
  "ds_read_b128 v[126:129], v183 offset:0\n\t" \
  "v_add_u32 v24, s37, v185\n\t" \
  "v_lshrrev_b32 v28, 4, v187\n\t" \
  "v_smfmac_f32_16x16x64_bf16 v[40:43], v[224:227], v[0:7], v174 abid:1\n\t" \
  "buffer_load_dwordx4 v186, s[16:19], s34 offen offset:0 nt lds\n\t" \
  "ds_read_b128 v[130:133], v184 offset:0\n\t" \
  "v_xor_b32 v25, s41, v183\n\t" \
  "v_and_or_b32 v28, v28, 48, s45\n\t" \
  "v_smfmac_f32_16x16x64_bf16 v[44:47], v[228:231], v[0:7], v175\n\t" \
  "s_add_u32 m0, s33, 1920\n\t" \
  "ds_read_b128 v[134:137], v183 offset:2048\n\t" \
  "v_subrev_u32 v25, s30, v25\n\t" \
  "v_smfmac_f32_16x16x64_bf16 v[48:51], v[232:235], v[0:7], v175 abid:1\n\t" \
  "s_add_u32 s30, s29, 18432\n\t" \
  "buffer_load_dwordx4 v186, s[16:19], s34 offen offset:128 nt lds\n\t" \
  "ds_read_b128 v[138:141], v184 offset:2048\n\t" \
  "v_cvt_pk_bf16_f32 v8, v36, v37\n\t" \
  "v_cvt_pk_bf16_f32 v9, v38, v39\n\t" \
  "v_smfmac_f32_16x16x64_bf16 v[52:55], v[236:239], v[0:7], v176\n\t" \
  "s_add_u32 m0, s33, 3840\n\t" \
  "ds_read_b128 v[36:39], v28 offset:0\n\t" \
  "v_cvt_pk_bf16_f32 v10, v40, v41\n\t" \
  "v_cvt_pk_bf16_f32 v11, v42, v43\n\t" \
  "v_smfmac_f32_16x16x64_bf16 v[56:59], v[240:243], v[0:7], v176 abid:1\n\t" \
  "s_sub_u32 s25, s30, 129024\n\t" \
  "buffer_load_dwordx4 v186, s[16:19], s34 offen offset:256 nt lds\n\t" \
  "ds_read_b128 v[40:43], v28 offset:64\n\t" \
  "v_cvt_pk_bf16_f32 v16, v44, v45\n\t" \
  "v_cvt_pk_bf16_f32 v17, v46, v47\n\t" \
  "v_smfmac_f32_16x16x64_bf16 v[60:63], v[244:247], v[0:7], v177\n\t" \
  "s_add_u32 m0, s33, 5760\n\t" \
  "ds_read_b128 v[44:47], v28 offset:128\n\t" \
  "v_cvt_pk_bf16_f32 v18, v48, v49\n\t" \
  "v_cvt_pk_bf16_f32 v19, v50, v51\n\t" \
  "v_smfmac_f32_16x16x64_bf16 v[64:67], v[248:251], v[0:7], v177 abid:1\n\t" \
  "s_min_u32 s30, s30, s25\n\t" \
  "buffer_load_dwordx4 v186, s[16:19], s34 offen offset:384 nt lds\n\t" \
  "ds_read_b128 v[48:51], v28 offset:192\n\t" \
  "v_cvt_pk_bf16_f32 v12, v52, v53\n\t" \
  "v_cvt_pk_bf16_f32 v13, v54, v55\n\t" \
  "v_smfmac_f32_16x16x64_bf16 v[68:71], v[252:255], v[0:7], v182\n\t" \
  "s_waitcnt lgkmcnt(3)\n\t" \
  "ds_read_b128 v[52:55], v28 offset:256\n\t" \
  "v_cvt_pk_bf16_f32 v14, v56, v57\n\t" \
  "v_cvt_pk_bf16_f32 v15, v58, v59\n\t" \
  "v_smfmac_f32_16x16x64_bf16 v[36:39], a[0:3], v[126:133], v142\n\t" \
  "s_waitcnt lgkmcnt(3)\n\t" \
  "ds_read_b128 v[56:59], v28 offset:320\n\t" \
  "v_cvt_pk_bf16_f32 v20, v60, v61\n\t" \
  "v_cvt_pk_bf16_f32 v21, v62, v63\n\t" \
  "v_smfmac_f32_16x16x64_bf16 v[40:43], a[4:7], v[126:133], v142 abid:1\n\t" \
  "s_waitcnt lgkmcnt(3)\n\t" \
  "ds_read_b128 v[60:63], v28 offset:384\n\t" \
  "v_cvt_pk_bf16_f32 v22, v64, v65\n\t" \
  "v_cvt_pk_bf16_f32 v23, v66, v67\n\t" \
  "v_smfmac_f32_16x16x64_bf16 v[44:47], a[8:11], v[126:133], v143\n\t" \
  "s_waitcnt lgkmcnt(3)\n\t" \
  "ds_read_b128 v[64:67], v28 offset:448\n\t" \
  "v_cvt_pk_bf16_f32 v26, v68, v69\n\t" \
  "v_cvt_pk_bf16_f32 v27, v70, v71\n\t" \
  "v_smfmac_f32_16x16x64_bf16 v[48:51], a[12:15], v[126:133], v143 abid:1\n\t" \
  "s_waitcnt lgkmcnt(3)\n\t" \
  "ds_read_b128 v[68:71], v28 offset:512\n\t" \
  "v_add_u32_sdwa v0, s29, v108 dst_sel:DWORD dst_unused:UNUSED_PAD src0_sel:DWORD src1_sel:WORD_0\n\t" \
  "v_smfmac_f32_16x16x64_bf16 v[52:55], a[16:19], v[126:133], v144\n\t" \
  "s_waitcnt lgkmcnt(3)\n\t" \
  "ds_write_b64 v24, v[8:9] offset:0\n\t" \
  "v_add_u32_sdwa v2, s29, v108 dst_sel:DWORD dst_unused:UNUSED_PAD src0_sel:DWORD src1_sel:WORD_1\n\t" \
  "v_smfmac_f32_16x16x64_bf16 v[56:59], a[20:23], v[126:133], v144 abid:1\n\t" \
  "s_waitcnt lgkmcnt(3)\n\t" \
  "ds_read_b64 v[0:1], v0 offset:0\n\t" \
  "v_add_u32_sdwa v4, s29, v109 dst_sel:DWORD dst_unused:UNUSED_PAD src0_sel:DWORD src1_sel:WORD_0\n\t" \
  "v_smfmac_f32_16x16x64_bf16 v[60:63], a[24:27], v[126:133], v145\n\t" \
  "s_waitcnt lgkmcnt(3)\n\t" \
  "ds_write_b64 v24, v[10:11] offset:8\n\t" \
  "v_add_u32_sdwa v6, s29, v109 dst_sel:DWORD dst_unused:UNUSED_PAD src0_sel:DWORD src1_sel:WORD_1\n\t" \
  "v_smfmac_f32_16x16x64_bf16 v[64:67], a[28:31], v[126:133], v145 abid:1\n\t" \
  "s_waitcnt lgkmcnt(3)\n\t" \
  "ds_read_b64 v[2:3], v2 offset:0\n\t" \
  "v_smfmac_f32_16x16x64_bf16 v[68:71], a[32:35], v[126:133], v178\n\t" \
  "s_sub_u32 s30, s30, s29\n\t" \
  "ds_write_b64 v24, v[12:13] offset:128\n\t" \
  "v_smfmac_f32_16x16x64_bf16 v[36:39], a[36:39], v[134:141], v146\n\t" \
  "s_mov_b32 s37, s29\n\t" \
  "ds_read_b128 v[126:129], v183 offset:4096\n\t" \
  "v_smfmac_f32_16x16x64_bf16 v[40:43], a[40:43], v[134:141], v146 abid:1\n\t" \
  "s_add_u32 s31, s31, 18432\n\t" \
  "ds_write_b64 v24, v[14:15] offset:136\n\t" \
  "v_xor_b32 v24, 64, v24\n\t" \
  "v_smfmac_f32_16x16x64_bf16 v[44:47], a[44:47], v[134:141], v147\n\t" \
  "s_sub_u32 s26, s31, 129024\n\t" \
  "ds_read_b128 v[130:133], v184 offset:4096\n\t" \
  "v_add_u32_sdwa v8, s29, v110 dst_sel:DWORD dst_unused:UNUSED_PAD src0_sel:DWORD src1_sel:WORD_0\n\t" \
  "v_smfmac_f32_16x16x64_bf16 v[48:51], a[48:51], v[134:141], v147 abid:1\n\t" \
  "s_min_u32 s31, s31, s26\n\t" \
  "ds_write_b64 v24, v[16:17] offset:0\n\t" \
  "v_add_u32_sdwa v10, s29, v110 dst_sel:DWORD dst_unused:UNUSED_PAD src0_sel:DWORD src1_sel:WORD_1\n\t" \
  "v_smfmac_f32_16x16x64_bf16 v[52:55], a[52:55], v[134:141], v148\n\t" \
  "s_add_u32 s33, s31, s32\n\t" \
  "ds_read_b64 v[4:5], v4 offset:0\n\t" \
  "v_add_u32_sdwa v12, s29, v111 dst_sel:DWORD dst_unused:UNUSED_PAD src0_sel:DWORD src1_sel:WORD_0\n\t" \
  "v_smfmac_f32_16x16x64_bf16 v[56:59], a[56:59], v[134:141], v148 abid:1\n\t" \
  "s_add_u32 s34, s34, 4718592\n\t" \
  "ds_write_b64 v24, v[18:19] offset:8\n\t" \
  "v_add_u32_sdwa v14, s29, v111 dst_sel:DWORD dst_unused:UNUSED_PAD src0_sel:DWORD src1_sel:WORD_1\n\t" \
  "v_smfmac_f32_16x16x64_bf16 v[60:63], a[60:63], v[134:141], v149\n\t" \
  "s_and_b32 s27, s64, 7\n\t" \
  "ds_read_b64 v[6:7], v6 offset:0\n\t" \
  "v_smfmac_f32_16x16x64_bf16 v[64:67], a[64:67], v[134:141], v149 abid:1\n\t" \
  "s_lshl2_add_u32 s66, s27, s68\n\t" \
  "ds_write_b64 v24, v[20:21] offset:128\n\t" \
  "v_smfmac_f32_16x16x64_bf16 v[68:71], a[68:71], v[134:141], v178 abid:1\n\t" \
  "s_waitcnt lgkmcnt(5)\n\t" \
  "ds_read_b128 v[134:137], v183 offset:6144\n\t" \
  "v_smfmac_f32_16x16x64_bf16 v[36:39], a[72:75], v[126:133], v150\n\t" \
  "s_and_b32 s27, s64, 8\n\t" \
  "ds_write_b64 v24, v[22:23] offset:136\n\t" \
  "v_add_u32_sdwa v16, s29, v112 dst_sel:DWORD dst_unused:UNUSED_PAD src0_sel:DWORD src1_sel:WORD_0\n\t" \
  "v_smfmac_f32_16x16x64_bf16 v[40:43], a[76:79], v[126:133], v150 abid:1\n\t" \
  "s_waitcnt lgkmcnt(3)\n\t" \
  "ds_read_b128 v[138:141], v184 offset:6144\n\t" \
  "v_add_u32_sdwa v18, s29, v112 dst_sel:DWORD dst_unused:UNUSED_PAD src0_sel:DWORD src1_sel:WORD_1\n\t" \
  "v_smfmac_f32_16x16x64_bf16 v[36:39], v[72:75], v[0:7], v182 abid:1\n\t" \
  "s_mul_i32 s27, s27, 176\n\t" \
  "ds_write_b64 v25, v[26:27] offset:16384\n\t" \
  "v_add_u32_sdwa v20, s29, v113 dst_sel:DWORD dst_unused:UNUSED_PAD src0_sel:DWORD src1_sel:WORD_0\n\t" \
  "v_smfmac_f32_16x16x64_bf16 v[44:47], a[80:83], v[126:133], v151\n\t" \
  "s_add_u32 s66, s66, s27\n\t" \
  "ds_read_b64 v[8:9], v8 offset:0\n\t" \
  "ds_read_b64 v[10:11], v10 offset:0\n\t" \
  "v_add_u32_sdwa v22, s29, v113 dst_sel:DWORD dst_unused:UNUSED_PAD src0_sel:DWORD src1_sel:WORD_1\n\t" \
  "v_smfmac_f32_16x16x64_bf16 v[48:51], a[84:87], v[126:133], v151 abid:1\n\t" \
  "s_lshr_b64 s[64:65], s[64:65], 4\n\t" \
  "ds_read_b64 v[12:13], v12 offset:0\n\t" \
  "ds_read_b64 v[14:15], v14 offset:0\n\t" \
  "v_add_u32_sdwa v0, s29, v114 dst_sel:DWORD dst_unused:UNUSED_PAD src0_sel:DWORD src1_sel:WORD_0\n\t" \
  "v_smfmac_f32_16x16x64_bf16 v[52:55], a[88:91], v[126:133], v152\n\t" \
  "s_sub_u32 s28, s72, s46\n\t" \
  "ds_read_b64 v[16:17], v16 offset:0\n\t" \
  "ds_read_b64 v[18:19], v18 offset:0\n\t" \
  "v_add_u32_sdwa v2, s29, v114 dst_sel:DWORD dst_unused:UNUSED_PAD src0_sel:DWORD src1_sel:WORD_1\n\t" \
  "v_smfmac_f32_16x16x64_bf16 v[56:59], a[92:95], v[126:133], v152 abid:1\n\t" \
  "s_add_u32 s46, s72, 185\n\t" \
  "ds_read_b64 v[20:21], v20 offset:0\n\t" \
  "ds_read_b64 v[22:23], v22 offset:0\n\t" \
  "v_add_u32_sdwa v4, s29, v115 dst_sel:DWORD dst_unused:UNUSED_PAD src0_sel:DWORD src1_sel:WORD_0\n\t" \
  "v_smfmac_f32_16x16x64_bf16 v[60:63], a[96:99], v[126:133], v153\n\t" \
  "s_max_i32 s28, s28, 0\n\t" \
  "ds_read_b64 v[0:1], v0 offset:0\n\t" \
  "v_add_u32_sdwa v6, s29, v115 dst_sel:DWORD dst_unused:UNUSED_PAD src0_sel:DWORD src1_sel:WORD_1\n\t" \
  "v_smfmac_f32_16x16x64_bf16 v[64:67], a[100:103], v[126:133], v153 abid:1\n\t" \
  "s_waitcnt lgkmcnt(5)\n\t" \
  "ds_read_b64 v[2:3], v2 offset:0\n\t" \
  "v_smfmac_f32_16x16x64_bf16 v[40:43], v[76:79], v[8:15], v182 abid:1\n\t" \
  "s_min_u32 s28, s28, 6\n\t" \
  "ds_read_b64 v[4:5], v4 offset:0\n\t" \
  "v_smfmac_f32_16x16x64_bf16 v[68:71], a[104:107], v[126:133], v179\n\t" \
  "s_waitcnt lgkmcnt(3)\n\t" \
  "ds_read_b128 v[126:129], v183 offset:8192\n\t" \
  "v_smfmac_f32_16x16x64_bf16 v[44:47], v[80:83], v[16:23], v182 abid:1\n\t" \
  "s_lshl_b32 s28, s28, 6\n\t" \
  "ds_read_b128 v[130:133], v184 offset:8192\n\t" \
  "v_add_u32_sdwa v8, s29, v32 dst_sel:DWORD dst_unused:UNUSED_PAD src0_sel:DWORD src1_sel:WORD_0\n\t" \
  "v_smfmac_f32_16x16x64_bf16 v[36:39], a[108:111], v[134:141], v154\n\t" \
  ".Ljn13%=:\n\t" \
  "s_add_u32 s28, s28, s53\n\t" \
  "ds_read_b64 v[6:7], v6 offset:0\n\t" \
  "v_add_u32_sdwa v10, s29, v32 dst_sel:DWORD dst_unused:UNUSED_PAD src0_sel:DWORD src1_sel:WORD_1\n\t" \
  "v_smfmac_f32_16x16x64_bf16 v[40:43], a[112:115], v[134:141], v154 abid:1\n\t" \
  "s_max_u32 s51, s51, s28\n\t" \
  "ds_read_b64 v[8:9], v8 offset:0\n\t" \
  "v_add_u32 v28, s60, v187\n\t" \
  "v_smfmac_f32_16x16x64_bf16 v[44:47], a[116:119], v[134:141], v155\n\t" \
  "s_add_u32 s54, s54, s51\n\t" \
  "ds_read_b64 v[10:11], v10 offset:0\n\t" \
  "v_add_u32_sdwa v16, s29, v116 dst_sel:DWORD dst_unused:UNUSED_PAD src0_sel:DWORD src1_sel:WORD_0\n\t" \
  "v_smfmac_f32_16x16x64_bf16 v[48:51], a[120:123], v[134:141], v155 abid:1\n\t" \
  "s_lshr_b32 s52, s54, 4\n\t" \
  "ds_read_b128 v[28:31], v28 offset:0\n\t" \
  "v_add_u32_sdwa v18, s29, v116 dst_sel:DWORD dst_unused:UNUSED_PAD src0_sel:DWORD src1_sel:WORD_1\n\t" \
  "v_smfmac_f32_16x16x64_bf16 v[52:55], a[124:127], v[134:141], v156\n\t" \
  "s_waitcnt lgkmcnt(3)\n\t" \
  "ds_read_b64 v[16:17], v16 offset:0\n\t" \
  "v_add_u32_sdwa v20, s29, v117 dst_sel:DWORD dst_unused:UNUSED_PAD src0_sel:DWORD src1_sel:WORD_0\n\t" \
  "v_smfmac_f32_16x16x64_bf16 v[48:51], v[84:87], v[0:7], v182 abid:1\n\t" \
  "s_add_u32 s52, s52, s56\n\t" \
  "ds_read_b64 v[18:19], v18 offset:0\n\t" \
  "v_add_u32_sdwa v22, s29, v117 dst_sel:DWORD dst_unused:UNUSED_PAD src0_sel:DWORD src1_sel:WORD_1\n\t" \
  "v_smfmac_f32_16x16x64_bf16 v[56:59], a[128:131], v[134:141], v156 abid:1\n\t" \
  "s_lshr_b32 s28, s51, 4\n\t" \
  "ds_read_b64 v[20:21], v20 offset:0\n\t" \
  "v_add_u32_sdwa v0, s29, v118 dst_sel:DWORD dst_unused:UNUSED_PAD src0_sel:DWORD src1_sel:WORD_0\n\t" \
  "v_smfmac_f32_16x16x64_bf16 v[60:63], a[132:135], v[134:141], v157\n\t" \
  "s_waitcnt lgkmcnt(3)\n\t" \
  "ds_read_b64 v[22:23], v22 offset:0\n\t" \
  "v_add_u32 v24, s38, v187\n\t" \
  "s_set_gpr_idx_on s6, gpr_idx(DST)\n\t" \
  "v_smfmac_f32_16x16x64_bf16 v[36:39], v[28:31], v[8:15], v182 abid:1\n\t" \
  "s_set_gpr_idx_off\n\t" \
  "ds_read_b128 v[24:27], v24 offset:4096\n\t" \
  "v_add_u32_sdwa v2, s29, v118 dst_sel:DWORD dst_unused:UNUSED_PAD src0_sel:DWORD src1_sel:WORD_1\n\t" \
  "v_smfmac_f32_16x16x64_bf16 v[64:67], a[136:139], v[134:141], v157 abid:1\n\t" \
  "s_add_u32 s28, s28, s72\n\t" \
  "ds_read_b64 v[0:1], v0 offset:0\n\t" \
  "v_add_u32_sdwa v4, s29, v119 dst_sel:DWORD dst_unused:UNUSED_PAD src0_sel:DWORD src1_sel:WORD_0\n\t" \
  "v_smfmac_f32_16x16x64_bf16 v[68:71], a[140:143], v[134:141], v179 abid:1\n\t" \
  "s_sub_u32 s28, s28, s101\n\t" \
  "ds_read_b128 v[134:137], v183 offset:10240\n\t" \
  "v_add_u32_sdwa v6, s29, v119 dst_sel:DWORD dst_unused:UNUSED_PAD src0_sel:DWORD src1_sel:WORD_1\n\t" \
  "v_smfmac_f32_16x16x64_bf16 v[36:39], a[144:147], v[126:133], v158\n\t" \
  "s_add_u32 s36, s35, s39\n\t" \
  "s_waitcnt lgkmcnt(3)\n\t" \
  "ds_read_b128 v[138:141], v184 offset:10240\n\t" \
  "v_add_u32_sdwa v8, s29, v33 dst_sel:DWORD dst_unused:UNUSED_PAD src0_sel:DWORD src1_sel:WORD_0\n\t" \
  "v_smfmac_f32_16x16x64_bf16 v[52:55], v[88:91], v[16:23], v182 abid:1\n\t" \
  "s_waitcnt lgkmcnt(3)\n\t" \
  "buffer_store_dwordx4 v[24:27], v34, s[20:23], s36 offen sc1\n\t" \
  "v_add_u32_sdwa v10, s29, v33 dst_sel:DWORD dst_unused:UNUSED_PAD src0_sel:DWORD src1_sel:WORD_1\n\t" \
  "v_smfmac_f32_16x16x64_bf16 v[40:43], a[148:151], v[126:133], v158 abid:1\n\t" \
  "s_max_u32 s52, s52, s28\n\t" \
  "ds_read_b64 v[2:3], v2 offset:0\n\t" \
  "v_xad_u32 v24, v187, 64, s38\n\t" \
  "v_smfmac_f32_16x16x64_bf16 v[44:47], a[152:155], v[126:133], v159\n\t" \
  "s_sub_u32 s51, s51, 112\n\t" \
  "ds_read_b128 v[24:27], v24 offset:5120\n\t" \
  "v_add_u32 v28, s60, v187\n\t" \
  "v_smfmac_f32_16x16x64_bf16 v[48:51], a[156:159], v[126:133], v159 abid:1\n\t" \
  "s_max_u32 s51, s51, s53\n\t" \
  "ds_read_b64 v[4:5], v4 offset:0\n\t" \
  "v_add_u32_sdwa v16, s29, v120 dst_sel:DWORD dst_unused:UNUSED_PAD src0_sel:DWORD src1_sel:WORD_0\n\t" \
  "v_smfmac_f32_16x16x64_bf16 v[52:55], a[160:163], v[126:133], v160\n\t" \
  "ds_read_b64 v[6:7], v6 offset:0\n\t" \
  "v_add_u32_sdwa v18, s29, v120 dst_sel:DWORD dst_unused:UNUSED_PAD src0_sel:DWORD src1_sel:WORD_1\n\t" \
  "v_smfmac_f32_16x16x64_bf16 v[56:59], a[164:167], v[126:133], v160 abid:1\n\t" \
  "s_add_u32 s36, s36, 4608\n\t" \
  "ds_read_b64 v[8:9], v8 offset:0\n\t" \
  "v_add_u32_sdwa v20, s29, v121 dst_sel:DWORD dst_unused:UNUSED_PAD src0_sel:DWORD src1_sel:WORD_0\n\t" \
  "v_smfmac_f32_16x16x64_bf16 v[60:63], a[168:171], v[126:133], v161\n\t" \
  "s_waitcnt lgkmcnt(3)\n\t" \
  "buffer_store_dwordx4 v[24:27], v34, s[20:23], s36 offen sc1\n\t" \
  "v_add_u32_sdwa v22, s29, v121 dst_sel:DWORD dst_unused:UNUSED_PAD src0_sel:DWORD src1_sel:WORD_1\n\t" \
  "v_smfmac_f32_16x16x64_bf16 v[64:67], a[172:175], v[126:133], v161 abid:1\n\t" \
  "ds_read_b64 v[10:11], v10 offset:0\n\t" \
  "v_add_u32 v24, s38, v187\n\t" \
  "v_smfmac_f32_16x16x64_bf16 v[68:71], a[176:179], v[126:133], v180\n\t" \
  "s_waitcnt lgkmcnt(2)\n\t" \
  "ds_read_b128 v[24:27], v24 offset:6144\n\t" \
  "v_smfmac_f32_16x16x64_bf16 v[56:59], v[92:95], v[0:7], v182 abid:1\n\t" \
  "ds_read_b128 v[126:129], v183 offset:12288\n\t" \
  "v_add_u32_sdwa v0, s29, v122 dst_sel:DWORD dst_unused:UNUSED_PAD src0_sel:DWORD src1_sel:WORD_0\n\t" \
  "v_smfmac_f32_16x16x64_bf16 v[36:39], a[180:183], v[134:141], v162\n\t" \
  "ds_read_b128 v[130:133], v184 offset:12288\n\t" \
  "v_add_u32_sdwa v2, s29, v122 dst_sel:DWORD dst_unused:UNUSED_PAD src0_sel:DWORD src1_sel:WORD_1\n\t" \
  "v_smfmac_f32_16x16x64_bf16 v[40:43], a[184:187], v[134:141], v162 abid:1\n\t" \
  "s_add_u32 s36, s36, 4608\n\t" \
  "ds_read_b128 v[28:31], v28 offset:1280\n\t" \
  "v_add_u32_sdwa v4, s29, v123 dst_sel:DWORD dst_unused:UNUSED_PAD src0_sel:DWORD src1_sel:WORD_0\n\t" \
  "v_smfmac_f32_16x16x64_bf16 v[44:47], a[188:191], v[134:141], v163\n\t" \
  "s_waitcnt lgkmcnt(3)\n\t" \
  "buffer_store_dwordx4 v[24:27], v34, s[20:23], s36 offen sc1\n\t" \
  "v_add_u32_sdwa v6, s29, v123 dst_sel:DWORD dst_unused:UNUSED_PAD src0_sel:DWORD src1_sel:WORD_1\n\t" \
  "v_smfmac_f32_16x16x64_bf16 v[48:51], a[192:195], v[134:141], v163 abid:1\n\t" \
  "ds_read_b64 v[16:17], v16 offset:0\n\t" \
  "v_xad_u32 v24, v187, 64, s38\n\t" \
  "v_smfmac_f32_16x16x64_bf16 v[52:55], a[196:199], v[134:141], v164\n\t" \
  "s_add_u32 s38, s38, 18432\n\t" \
  "ds_read_b128 v[24:27], v24 offset:7168\n\t" \
  "v_smfmac_f32_16x16x64_bf16 v[56:59], a[200:203], v[134:141], v164 abid:1\n\t" \
  "s_waitcnt lgkmcnt(2)\n\t" \
  "ds_read_b64 v[18:19], v18 offset:0\n\t" \
  "s_set_gpr_idx_on s7, gpr_idx(DST)\n\t" \
  "v_smfmac_f32_16x16x64_bf16 v[36:39], v[28:31], v[8:15], v182 abid:1\n\t" \
  "s_set_gpr_idx_off\n\t" \
  "ds_read_b64 v[20:21], v20 offset:0\n\t" \
  "v_add_u32_sdwa v8, s29, v35 dst_sel:DWORD dst_unused:UNUSED_PAD src0_sel:DWORD src1_sel:WORD_0\n\t" \
  "v_smfmac_f32_16x16x64_bf16 v[60:63], a[204:207], v[134:141], v165\n\t" \
  "s_add_u32 s36, s36, 4608\n\t" \
  "ds_read_b64 v[22:23], v22 offset:0\n\t" \
  "v_add_u32_sdwa v10, s29, v35 dst_sel:DWORD dst_unused:UNUSED_PAD src0_sel:DWORD src1_sel:WORD_1\n\t" \
  "v_smfmac_f32_16x16x64_bf16 v[64:67], a[208:211], v[134:141], v165 abid:1\n\t" \
  "s_waitcnt lgkmcnt(3)\n\t" \
  "buffer_store_dwordx4 v[24:27], v34, s[20:23], s36 offen sc1\n\t" \
  "v_add_u32 v28, s60, v187\n\t" \
  "v_smfmac_f32_16x16x64_bf16 v[68:71], a[212:215], v[134:141], v180 abid:1\n\t" \
  "s_sub_u32 s44, s38, 129024\n\t" \
  "ds_read_b128 v[134:137], v183 offset:14336\n\t" \
  "v_add_u32 v24, s43, v187\n\t" \
  "v_smfmac_f32_16x16x64_bf16 v[36:39], a[216:219], v[126:133], v166\n\t" \
  "s_min_u32 s38, s38, s44\n\t" \
  "ds_read_b128 v[24:27], v24 offset:16384\n\t" \
  "v_smfmac_f32_16x16x64_bf16 v[40:43], a[220:223], v[126:133], v166 abid:1\n\t" \
  "s_waitcnt lgkmcnt(2)\n\t" \
  "ds_read_b128 v[138:141], v184 offset:14336\n\t" \
  "v_smfmac_f32_16x16x64_bf16 v[60:63], v[96:99], v[16:23], v182 abid:1\n\t" \
  "s_add_u32 s35, s35, 4718592\n\t" \
  "ds_read_b64 v[0:1], v0 offset:0\n\t" \
  "v_add_u32_sdwa v16, s29, v124 dst_sel:DWORD dst_unused:UNUSED_PAD src0_sel:DWORD src1_sel:WORD_0\n\t" \
  "v_smfmac_f32_16x16x64_bf16 v[44:47], a[224:227], v[126:133], v167\n\t" \
  "ds_read_b64 v[2:3], v2 offset:0\n\t" \
  "v_add_u32_sdwa v18, s29, v124 dst_sel:DWORD dst_unused:UNUSED_PAD src0_sel:DWORD src1_sel:WORD_1\n\t" \
  "v_smfmac_f32_16x16x64_bf16 v[48:51], a[228:231], v[126:133], v167 abid:1\n\t" \
  "s_waitcnt lgkmcnt(3)\n\t" \
  "buffer_store_dwordx4 v[24:27], v186, s[20:23], s40 offen offset:384 sc1\n\t" \
  "v_add_u32_sdwa v20, s29, v125 dst_sel:DWORD dst_unused:UNUSED_PAD src0_sel:DWORD src1_sel:WORD_0\n\t" \
  "v_smfmac_f32_16x16x64_bf16 v[52:55], a[232:235], v[126:133], v168\n\t" \
  "s_add_u32 s43, s43, 18432\n\t" \
  "ds_read_b64 v[4:5], v4 offset:0\n\t" \
  "v_add_u32_sdwa v22, s29, v125 dst_sel:DWORD dst_unused:UNUSED_PAD src0_sel:DWORD src1_sel:WORD_1\n\t" \
  "v_smfmac_f32_16x16x64_bf16 v[56:59], a[236:239], v[126:133], v168 abid:1\n\t" \
  "s_add_u32 s29, s29, s30\n\t" \
  "ds_read_b64 v[6:7], v6 offset:0\n\t" \
  "v_smfmac_f32_16x16x64_bf16 v[60:63], a[240:243], v[126:133], v169\n\t" \
  "s_sub_u32 s42, s43, 129024\n\t" \
  "ds_read_b64 v[8:9], v8 offset:0\n\t" \
  "v_smfmac_f32_16x16x64_bf16 v[64:67], a[244:247], v[126:133], v169 abid:1\n\t" \
  "s_min_u32 s43, s43, s42\n\t" \
  "ds_read_b64 v[10:11], v10 offset:0\n\t" \
  "v_smfmac_f32_16x16x64_bf16 v[68:71], a[248:251], v[126:133], v181\n\t" \
  "s_waitcnt lgkmcnt(6)\n\t" \
  "ds_read_b128 v[28:31], v28 offset:2560\n\t" \
  "v_smfmac_f32_16x16x64_bf16 v[36:39], a[252:255], v[134:141], v170\n\t" \
  "s_waitcnt lgkmcnt(3)\n\t" \
  "ds_read_b64 v[16:17], v16 offset:0\n\t" \
  "v_smfmac_f32_16x16x64_bf16 v[64:67], v[100:103], v[0:7], v182 abid:1\n\t" \
  "s_add_u32 s40, s40, 4718592\n\t" \
  "ds_read_b128 v[0:3], v183 offset:16384\n\t" \
  "v_smfmac_f32_16x16x64_bf16 v[40:43], v[188:191], v[134:141], v170 abid:1\n\t" \
  "ds_read_b128 v[4:7], v184 offset:16384\n\t" \
  "v_add_u32 v183, s30, v183\n\t" \
  "v_smfmac_f32_16x16x64_bf16 v[44:47], v[192:195], v[134:141], v171\n\t" \
  "s_waitcnt lgkmcnt(3)\n\t" \
  "ds_read_b64 v[18:19], v18 offset:0\n\t" \
  "v_add_u32 v184, s30, v184\n\t" \
  "s_set_gpr_idx_on s8, gpr_idx(DST)\n\t" \
  "v_smfmac_f32_16x16x64_bf16 v[36:39], v[28:31], v[8:15], v182 abid:1\n\t" \
  "s_set_gpr_idx_off\n\t" \
  "ds_read_b64 v[20:21], v20 offset:0\n\t" \
  "v_smfmac_f32_16x16x64_bf16 v[48:51], v[196:199], v[134:141], v171 abid:1\n\t" \
  "ds_read_b64 v[22:23], v22 offset:0\n\t" \
  "v_smfmac_f32_16x16x64_bf16 v[52:55], v[200:203], v[134:141], v172\n\t" \
  "s_memrealtime s[72:73]\n\t" \
  "v_smfmac_f32_16x16x64_bf16 v[56:59], v[204:207], v[134:141], v172 abid:1\n\t" \
  "v_smfmac_f32_16x16x64_bf16 v[60:63], v[208:211], v[134:141], v173\n\t" \
  "v_smfmac_f32_16x16x64_bf16 v[64:67], v[212:215], v[134:141], v173 abid:1\n\t" \
  "s_waitcnt lgkmcnt(0)\n\t" \
  "v_smfmac_f32_16x16x64_bf16 v[68:71], v[104:107], v[16:23], v182 abid:1\n\t" \
  "v_smfmac_f32_16x16x64_bf16 v[68:71], v[216:219], v[134:141], v181 abid:1\n\t" \
  "s_setpc_b64 s[66:67]\n\t" \
  ".Lls13%=:\n\t" \
  "s_memrealtime s[72:73]\n\t" \
  "s_waitcnt lgkmcnt(0)\n\t" \
  "s_sub_i32 s28, s72, s52\n\t" \
  "s_cmp_lt_i32 s28, 0\n\t" \
  "s_cbranch_scc1 .Lls13%=\n\t" \
  "s_branch .Llg13%=\n\t" \
  ".fill (.Lvar%=+24576-.)/4, 4, 0xbf800000\n\t" \
  "s_branch .Lfin%=\n\t" \
  "s_branch .Lhe%=\n\t" \
  "s_waitcnt vmcnt(6)\n\t" \
  "s_waitcnt vmcnt(10)\n\t" \
  "s_waitcnt vmcnt(12)\n\t" \
  "s_waitcnt vmcnt(17)\n\t" \
  "s_waitcnt vmcnt(21) lgkmcnt(0)\n\t" \
  "s_barrier\n\t" \
  "v_smfmac_f32_16x16x64_bf16 v[36:39], v[220:223], v[0:7], v174\n\t" \
  "ds_read_b128 v[126:129], v183 offset:0\n\t" \
  "v_add_u32 v24, s37, v185\n\t" \
  "v_smfmac_f32_16x16x64_bf16 v[40:43], v[224:227], v[0:7], v174 abid:1\n\t" \
  "ds_read_b128 v[130:133], v184 offset:0\n\t" \
  "v_xor_b32 v25, s41, v183\n\t" \
  "v_smfmac_f32_16x16x64_bf16 v[44:47], v[228:231], v[0:7], v175\n\t" \
  "ds_read_b128 v[134:137], v183 offset:2048\n\t" \
  "v_subrev_u32 v25, s30, v25\n\t" \
  "v_smfmac_f32_16x16x64_bf16 v[48:51], v[232:235], v[0:7], v175 abid:1\n\t" \
  "s_add_u32 s30, s29, 18432\n\t" \
  "ds_read_b128 v[138:141], v184 offset:2048\n\t" \
  "v_cvt_pk_bf16_f32 v8, v36, v37\n\t" \
  "v_cvt_pk_bf16_f32 v9, v38, v39\n\t" \
  "v_smfmac_f32_16x16x64_bf16 v[52:55], v[236:239], v[0:7], v176\n\t" \
  "ds_read_b128 v[36:39], v35 offset:0\n\t" \
  "v_cvt_pk_bf16_f32 v10, v40, v41\n\t" \
  "v_cvt_pk_bf16_f32 v11, v42, v43\n\t" \
  "v_smfmac_f32_16x16x64_bf16 v[56:59], v[240:243], v[0:7], v176 abid:1\n\t" \
  "s_sub_u32 s25, s30, 129024\n\t" \
  "ds_read_b128 v[40:43], v35 offset:64\n\t" \
  "v_cvt_pk_bf16_f32 v16, v44, v45\n\t" \
  "v_cvt_pk_bf16_f32 v17, v46, v47\n\t" \
  "v_smfmac_f32_16x16x64_bf16 v[60:63], v[244:247], v[0:7], v177\n\t" \
  "ds_read_b128 v[44:47], v35 offset:128\n\t" \
  "v_cvt_pk_bf16_f32 v18, v48, v49\n\t" \
  "v_cvt_pk_bf16_f32 v19, v50, v51\n\t" \
  "v_smfmac_f32_16x16x64_bf16 v[64:67], v[248:251], v[0:7], v177 abid:1\n\t" \
  "s_min_u32 s30, s30, s25\n\t" \
  "ds_read_b128 v[48:51], v35 offset:192\n\t" \
  "v_cvt_pk_bf16_f32 v12, v52, v53\n\t" \
  "v_cvt_pk_bf16_f32 v13, v54, v55\n\t" \
  "v_smfmac_f32_16x16x64_bf16 v[68:71], v[252:255], v[0:7], v182\n\t" \
  "s_waitcnt lgkmcnt(3)\n\t" \
  "ds_read_b128 v[52:55], v35 offset:256\n\t" \
  "v_cvt_pk_bf16_f32 v14, v56, v57\n\t" \
  "v_cvt_pk_bf16_f32 v15, v58, v59\n\t" \
  "v_smfmac_f32_16x16x64_bf16 v[36:39], a[0:3], v[126:133], v142\n\t" \
  "s_waitcnt lgkmcnt(3)\n\t" \
  "ds_read_b128 v[56:59], v35 offset:320\n\t" \
  "v_cvt_pk_bf16_f32 v20, v60, v61\n\t" \
  "v_cvt_pk_bf16_f32 v21, v62, v63\n\t" \
  "v_smfmac_f32_16x16x64_bf16 v[40:43], a[4:7], v[126:133], v142 abid:1\n\t" \
  "s_waitcnt lgkmcnt(3)\n\t" \
  "ds_read_b128 v[60:63], v35 offset:384\n\t" \
  "v_cvt_pk_bf16_f32 v22, v64, v65\n\t" \
  "v_cvt_pk_bf16_f32 v23, v66, v67\n\t" \
  "v_smfmac_f32_16x16x64_bf16 v[44:47], a[8:11], v[126:133], v143\n\t" \
  "s_waitcnt lgkmcnt(3)\n\t" \
  "ds_read_b128 v[64:67], v35 offset:448\n\t" \
  "v_cvt_pk_bf16_f32 v26, v68, v69\n\t" \
  "v_cvt_pk_bf16_f32 v27, v70, v71\n\t" \
  "v_smfmac_f32_16x16x64_bf16 v[48:51], a[12:15], v[126:133], v143 abid:1\n\t" \
  "s_waitcnt lgkmcnt(3)\n\t" \
  "ds_read_b128 v[68:71], v35 offset:512\n\t" \
  "v_add_u32_sdwa v0, s29, v108 dst_sel:DWORD dst_unused:UNUSED_PAD src0_sel:DWORD src1_sel:WORD_0\n\t" \
  "v_smfmac_f32_16x16x64_bf16 v[52:55], a[16:19], v[126:133], v144\n\t" \
  "s_waitcnt lgkmcnt(3)\n\t" \
  "ds_read_b64 v[0:1], v0 offset:0\n\t" \
  "v_add_u32_sdwa v2, s29, v108 dst_sel:DWORD dst_unused:UNUSED_PAD src0_sel:DWORD src1_sel:WORD_1\n\t" \
  "v_smfmac_f32_16x16x64_bf16 v[56:59], a[20:23], v[126:133], v144 abid:1\n\t" \
  "s_waitcnt lgkmcnt(3)\n\t" \
  "ds_write_b64 v24, v[8:9] offset:0\n\t" \
  "v_add_u32_sdwa v4, s29, v109 dst_sel:DWORD dst_unused:UNUSED_PAD src0_sel:DWORD src1_sel:WORD_0\n\t" \
  "v_smfmac_f32_16x16x64_bf16 v[60:63], a[24:27], v[126:133], v145\n\t" \
  "s_waitcnt lgkmcnt(3)\n\t" \
  "ds_read_b64 v[2:3], v2 offset:0\n\t" \
  "v_add_u32_sdwa v6, s29, v109 dst_sel:DWORD dst_unused:UNUSED_PAD src0_sel:DWORD src1_sel:WORD_1\n\t" \
  "v_smfmac_f32_16x16x64_bf16 v[64:67], a[28:31], v[126:133], v145 abid:1\n\t" \
  "s_waitcnt lgkmcnt(3)\n\t" \
  "s_bfe_i32 s47, s48, 0x10000\n\t" \
  "ds_write_b64 v24, v[10:11] offset:8\n\t" \
  "v_add_u32 v28, s38, v187\n\t" \
  "v_smfmac_f32_16x16x64_bf16 v[68:71], a[32:35], v[126:133], v178\n\t" \
  "s_sub_u32 s30, s30, s29\n\t" \
  "s_lshr_b32 s48, s48, 1\n\t" \
  "ds_read_b128 v[126:129], v183 offset:4096\n\t" \
  "ds_read_b128 v[28:31], v28 offset:8192\n\t" \
  "v_smfmac_f32_16x16x64_bf16 v[36:39], a[36:39], v[134:141], v146\n\t" \
  "s_mov_b32 s37, s29\n\t" \
  "s_add_u32 s36, s35, s39\n\t" \
  "ds_write_b64 v24, v[12:13] offset:128\n\t" \
  "v_smfmac_f32_16x16x64_bf16 v[40:43], a[40:43], v[134:141], v146 abid:1\n\t" \
  "s_andn2_b32 s44, 0x40000000, s47\n\t" \
  "ds_read_b128 v[130:133], v184 offset:4096\n\t" \
  "v_smfmac_f32_16x16x64_bf16 v[44:47], a[44:47], v[134:141], v147\n\t" \
  "s_or_b32 s36, s36, s44\n\t" \
  "ds_write_b64 v24, v[14:15] offset:136\n\t" \
  "v_xor_b32 v24, 64, v24\n\t" \
  "v_smfmac_f32_16x16x64_bf16 v[48:51], a[48:51], v[134:141], v147 abid:1\n\t" \
  "ds_read_b64 v[4:5], v4 offset:0\n\t" \
  "s_waitcnt lgkmcnt(4)\n\t" \
  "buffer_store_dwordx4 v[28:31], v34, s[20:23], s36 offen sc1\n\t" \
  "v_add_u32_sdwa v8, s29, v110 dst_sel:DWORD dst_unused:UNUSED_PAD src0_sel:DWORD src1_sel:WORD_0\n\t" \
  "v_smfmac_f32_16x16x64_bf16 v[52:55], a[52:55], v[134:141], v148\n\t" \
  "s_and_b32 s44, s47, 4718592\n\t" \
  "ds_write_b64 v24, v[16:17] offset:0\n\t" \
  "v_add_u32_sdwa v10, s29, v110 dst_sel:DWORD dst_unused:UNUSED_PAD src0_sel:DWORD src1_sel:WORD_1\n\t" \
  "v_xad_u32 v28, v187, 64, s38\n\t" \
  "v_smfmac_f32_16x16x64_bf16 v[56:59], a[56:59], v[134:141], v148 abid:1\n\t" \
  "s_add_u32 s35, s35, s44\n\t" \
  "ds_read_b64 v[6:7], v6 offset:0\n\t" \
  "ds_read_b128 v[28:31], v28 offset:9216\n\t" \
  "v_add_u32_sdwa v12, s29, v111 dst_sel:DWORD dst_unused:UNUSED_PAD src0_sel:DWORD src1_sel:WORD_0\n\t" \
  "v_smfmac_f32_16x16x64_bf16 v[60:63], a[60:63], v[134:141], v149\n\t" \
  "s_and_b32 s27, s64, 7\n\t" \
  "s_add_u32 s36, s36, 4608\n\t" \
  "ds_write_b64 v24, v[18:19] offset:8\n\t" \
  "v_add_u32_sdwa v14, s29, v111 dst_sel:DWORD dst_unused:UNUSED_PAD src0_sel:DWORD src1_sel:WORD_1\n\t" \
  "v_smfmac_f32_16x16x64_bf16 v[64:67], a[64:67], v[134:141], v149 abid:1\n\t" \
  "s_lshl2_add_u32 s66, s27, s68\n\t" \
  "ds_read_b64 v[8:9], v8 offset:0\n\t" \
  "v_smfmac_f32_16x16x64_bf16 v[68:71], a[68:71], v[134:141], v178 abid:1\n\t" \
  "s_waitcnt lgkmcnt(6)\n\t" \
  "ds_write_b64 v24, v[20:21] offset:128\n\t" \
  "v_smfmac_f32_16x16x64_bf16 v[36:39], a[72:75], v[126:133], v150\n\t" \
  "s_waitcnt lgkmcnt(3)\n\t" \
  "ds_read_b128 v[134:137], v183 offset:6144\n\t" \
  "s_waitcnt lgkmcnt(4)\n\t" \
  "buffer_store_dwordx4 v[28:31], v34, s[20:23], s36 offen sc1\n\t" \
  "v_smfmac_f32_16x16x64_bf16 v[36:39], v[72:75], v[0:7], v182 abid:1\n\t" \
  "s_and_b32 s27, s64, 8\n\t" \
  "s_and_b32 s44, s47, 18432\n\t" \
  "ds_write_b64 v24, v[22:23] offset:136\n\t" \
  "v_add_u32_sdwa v0, s29, v112 dst_sel:DWORD dst_unused:UNUSED_PAD src0_sel:DWORD src1_sel:WORD_0\n\t" \
  "v_add_u32 v28, s38, v187\n\t" \
  "v_smfmac_f32_16x16x64_bf16 v[40:43], a[76:79], v[126:133], v150 abid:1\n\t" \
  "s_mul_i32 s27, s27, 176\n\t" \
  "ds_read_b128 v[138:141], v184 offset:6144\n\t" \
  "ds_read_b128 v[28:31], v28 offset:10240\n\t" \
  "v_add_u32_sdwa v2, s29, v112 dst_sel:DWORD dst_unused:UNUSED_PAD src0_sel:DWORD src1_sel:WORD_1\n\t" \
  "v_smfmac_f32_16x16x64_bf16 v[44:47], a[80:83], v[126:133], v151\n\t" \
  "s_add_u32 s66, s66, s27\n\t" \
  "s_add_u32 s36, s36, 4608\n\t" \
  "ds_write_b64 v25, v[26:27] offset:16384\n\t" \
  "v_add_u32_sdwa v4, s29, v113 dst_sel:DWORD dst_unused:UNUSED_PAD src0_sel:DWORD src1_sel:WORD_0\n\t" \
  "v_smfmac_f32_16x16x64_bf16 v[48:51], a[84:87], v[126:133], v151 abid:1\n\t" \
  "s_lshr_b64 s[64:65], s[64:65], 4\n\t" \
  "ds_read_b64 v[10:11], v10 offset:0\n\t" \
  "v_add_u32_sdwa v6, s29, v113 dst_sel:DWORD dst_unused:UNUSED_PAD src0_sel:DWORD src1_sel:WORD_1\n\t" \
  "v_smfmac_f32_16x16x64_bf16 v[52:55], a[88:91], v[126:133], v152\n\t" \
  "ds_read_b64 v[12:13], v12 offset:0\n\t" \
  "v_add_u32_sdwa v16, s29, v114 dst_sel:DWORD dst_unused:UNUSED_PAD src0_sel:DWORD src1_sel:WORD_0\n\t" \
  "v_smfmac_f32_16x16x64_bf16 v[56:59], a[92:95], v[126:133], v152 abid:1\n\t" \
  "ds_read_b64 v[14:15], v14 offset:0\n\t" \
  "s_waitcnt lgkmcnt(4)\n\t" \
  "buffer_store_dwordx4 v[28:31], v34, s[20:23], s36 offen sc1\n\t" \
  "v_add_u32_sdwa v18, s29, v114 dst_sel:DWORD dst_unused:UNUSED_PAD src0_sel:DWORD src1_sel:WORD_1\n\t" \
  "v_smfmac_f32_16x16x64_bf16 v[60:63], a[96:99], v[126:133], v153\n\t" \
  "ds_read_b64 v[0:1], v0 offset:0\n\t" \
  "v_add_u32_sdwa v20, s29, v115 dst_sel:DWORD dst_unused:UNUSED_PAD src0_sel:DWORD src1_sel:WORD_0\n\t" \
  "v_xad_u32 v28, v187, 64, s38\n\t" \
  "v_smfmac_f32_16x16x64_bf16 v[64:67], a[100:103], v[126:133], v153 abid:1\n\t" \
  "s_add_u32 s38, s38, s44\n\t" \
  "ds_read_b64 v[2:3], v2 offset:0\n\t" \
  "ds_read_b128 v[28:31], v28 offset:11264\n\t" \
  "v_add_u32_sdwa v22, s29, v115 dst_sel:DWORD dst_unused:UNUSED_PAD src0_sel:DWORD src1_sel:WORD_1\n\t" \
  "v_smfmac_f32_16x16x64_bf16 v[68:71], a[104:107], v[126:133], v179\n\t" \
  "s_waitcnt lgkmcnt(6)\n\t" \
  "s_add_u32 s36, s36, 4608\n\t" \
  "ds_read_b128 v[126:129], v183 offset:8192\n\t" \
  "v_smfmac_f32_16x16x64_bf16 v[36:39], a[108:111], v[134:141], v154\n\t" \
  "s_waitcnt lgkmcnt(3)\n\t" \
  "s_sub_u32 s44, s38, 129024\n\t" \
  "ds_read_b128 v[130:133], v184 offset:8192\n\t" \
  "v_smfmac_f32_16x16x64_bf16 v[40:43], v[76:79], v[8:15], v182 abid:1\n\t" \
  "s_min_u32 s38, s38, s44\n\t" \
  "ds_read_b64 v[4:5], v4 offset:0\n\t" \
  "v_smfmac_f32_16x16x64_bf16 v[40:43], a[112:115], v[134:141], v154 abid:1\n\t" \
  "ds_read_b64 v[6:7], v6 offset:0\n\t" \
  "s_waitcnt lgkmcnt(4)\n\t" \
  "buffer_store_dwordx4 v[28:31], v34, s[20:23], s36 offen sc1\n\t" \
  "v_smfmac_f32_16x16x64_bf16 v[44:47], a[116:119], v[134:141], v155\n\t" \
  "ds_read_b64 v[16:17], v16 offset:0\n\t" \
  "v_add_u32_sdwa v8, s29, v32 dst_sel:DWORD dst_unused:UNUSED_PAD src0_sel:DWORD src1_sel:WORD_0\n\t" \
  "v_smfmac_f32_16x16x64_bf16 v[48:51], a[120:123], v[134:141], v155 abid:1\n\t" \
  "s_branch .Ljn21%=\n\t" \
  ".fill (.Lvar%=+25980-.)/4, 4, 0xbf800000\n\t" \
  "s_nop 0\n\t" \
  "s_branch .Lfin%=\n\t" \
  "s_branch .Lhe%=\n\t" \
  "s_waitcnt vmcnt(6)\n\t" \
  "s_waitcnt vmcnt(10)\n\t" \
  "s_waitcnt vmcnt(12)\n\t" \
  "s_waitcnt vmcnt(17)\n\t" \
  "s_waitcnt vmcnt(21) lgkmcnt(0)\n\t" \
  "s_barrier\n\t" \
  "v_smfmac_f32_16x16x64_bf16 v[36:39], v[220:223], v[0:7], v174\n\t" \
  "s_add_u32 m0, s33, 0\n\t" \
  "ds_read_b128 v[126:129], v183 offset:0\n\t" \
  "v_add_u32 v24, s37, v185\n\t" \
  "v_smfmac_f32_16x16x64_bf16 v[40:43], v[224:227], v[0:7], v174 abid:1\n\t" \
  "buffer_load_dwordx4 v186, s[16:19], s34 offen offset:0 nt lds\n\t" \
  "ds_read_b128 v[130:133], v184 offset:0\n\t" \
  "v_xor_b32 v25, s41, v183\n\t" \
  "v_smfmac_f32_16x16x64_bf16 v[44:47], v[228:231], v[0:7], v175\n\t" \
  "s_add_u32 m0, s33, 1920\n\t" \
  "ds_read_b128 v[134:137], v183 offset:2048\n\t" \
  "v_subrev_u32 v25, s30, v25\n\t" \
  "v_smfmac_f32_16x16x64_bf16 v[48:51], v[232:235], v[0:7], v175 abid:1\n\t" \
  "s_add_u32 s30, s29, 18432\n\t" \
  "buffer_load_dwordx4 v186, s[16:19], s34 offen offset:128 nt lds\n\t" \
  "ds_read_b128 v[138:141], v184 offset:2048\n\t" \
  "v_cvt_pk_bf16_f32 v8, v36, v37\n\t" \
  "v_cvt_pk_bf16_f32 v9, v38, v39\n\t" \
  "v_smfmac_f32_16x16x64_bf16 v[52:55], v[236:239], v[0:7], v176\n\t" \
  "s_add_u32 m0, s33, 3840\n\t" \
  "ds_read_b128 v[36:39], v35 offset:0\n\t" \
  "v_cvt_pk_bf16_f32 v10, v40, v41\n\t" \
  "v_cvt_pk_bf16_f32 v11, v42, v43\n\t" \
  "v_smfmac_f32_16x16x64_bf16 v[56:59], v[240:243], v[0:7], v176 abid:1\n\t" \
  "s_sub_u32 s25, s30, 129024\n\t" \
  "buffer_load_dwordx4 v186, s[16:19], s34 offen offset:256 nt lds\n\t" \
  "ds_read_b128 v[40:43], v35 offset:64\n\t" \
  "v_cvt_pk_bf16_f32 v16, v44, v45\n\t" \
  "v_cvt_pk_bf16_f32 v17, v46, v47\n\t" \
  "v_smfmac_f32_16x16x64_bf16 v[60:63], v[244:247], v[0:7], v177\n\t" \
  "s_add_u32 m0, s33, 5760\n\t" \
  "ds_read_b128 v[44:47], v35 offset:128\n\t" \
  "v_cvt_pk_bf16_f32 v18, v48, v49\n\t" \
  "v_cvt_pk_bf16_f32 v19, v50, v51\n\t" \
  "v_smfmac_f32_16x16x64_bf16 v[64:67], v[248:251], v[0:7], v177 abid:1\n\t" \
  "s_min_u32 s30, s30, s25\n\t" \
  "buffer_load_dwordx4 v186, s[16:19], s34 offen offset:384 nt lds\n\t" \
  "ds_read_b128 v[48:51], v35 offset:192\n\t" \
  "v_cvt_pk_bf16_f32 v12, v52, v53\n\t" \
  "v_cvt_pk_bf16_f32 v13, v54, v55\n\t" \
  "v_smfmac_f32_16x16x64_bf16 v[68:71], v[252:255], v[0:7], v182\n\t" \
  "s_add_u32 m0, s33, 7680\n\t" \
  "s_waitcnt lgkmcnt(3)\n\t" \
  "ds_read_b128 v[52:55], v35 offset:256\n\t" \
  "v_cvt_pk_bf16_f32 v14, v56, v57\n\t" \
  "v_cvt_pk_bf16_f32 v15, v58, v59\n\t" \
  "v_smfmac_f32_16x16x64_bf16 v[36:39], a[0:3], v[126:133], v142\n\t" \
  "s_waitcnt lgkmcnt(3)\n\t" \
  "buffer_load_dwordx4 v186, s[16:19], s34 offen offset:512 nt lds\n\t" \
  "ds_read_b128 v[56:59], v35 offset:320\n\t" \
  "v_cvt_pk_bf16_f32 v20, v60, v61\n\t" \
  "v_cvt_pk_bf16_f32 v21, v62, v63\n\t" \
  "v_smfmac_f32_16x16x64_bf16 v[40:43], a[4:7], v[126:133], v142 abid:1\n\t" \
  "s_waitcnt lgkmcnt(3)\n\t" \
  "ds_read_b128 v[60:63], v35 offset:384\n\t" \
  "v_cvt_pk_bf16_f32 v22, v64, v65\n\t" \
  "v_cvt_pk_bf16_f32 v23, v66, v67\n\t" \
  "v_smfmac_f32_16x16x64_bf16 v[44:47], a[8:11], v[126:133], v143\n\t" \
  "s_waitcnt lgkmcnt(3)\n\t" \
  "ds_read_b128 v[64:67], v35 offset:448\n\t" \
  "v_cvt_pk_bf16_f32 v26, v68, v69\n\t" \
  "v_cvt_pk_bf16_f32 v27, v70, v71\n\t" \
  "v_smfmac_f32_16x16x64_bf16 v[48:51], a[12:15], v[126:133], v143 abid:1\n\t" \
  "s_waitcnt lgkmcnt(3)\n\t" \
  "ds_read_b128 v[68:71], v35 offset:512\n\t" \
  "v_add_u32_sdwa v0, s29, v108 dst_sel:DWORD dst_unused:UNUSED_PAD src0_sel:DWORD src1_sel:WORD_0\n\t" \
  "v_smfmac_f32_16x16x64_bf16 v[52:55], a[16:19], v[126:133], v144\n\t" \
  "s_waitcnt lgkmcnt(3)\n\t" \
  "ds_read_b64 v[0:1], v0 offset:0\n\t" \
  "v_add_u32_sdwa v2, s29, v108 dst_sel:DWORD dst_unused:UNUSED_PAD src0_sel:DWORD src1_sel:WORD_1\n\t" \
  "v_smfmac_f32_16x16x64_bf16 v[56:59], a[20:23], v[126:133], v144 abid:1\n\t" \
  "s_waitcnt lgkmcnt(3)\n\t" \
  "ds_write_b64 v24, v[8:9] offset:0\n\t" \
  "v_add_u32_sdwa v4, s29, v109 dst_sel:DWORD dst_unused:UNUSED_PAD src0_sel:DWORD src1_sel:WORD_0\n\t" \
  "v_smfmac_f32_16x16x64_bf16 v[60:63], a[24:27], v[126:133], v145\n\t" \
  "s_waitcnt lgkmcnt(3)\n\t" \
  "ds_read_b64 v[2:3], v2 offset:0\n\t" \
  "v_add_u32_sdwa v6, s29, v109 dst_sel:DWORD dst_unused:UNUSED_PAD src0_sel:DWORD src1_sel:WORD_1\n\t" \
  "v_smfmac_f32_16x16x64_bf16 v[64:67], a[28:31], v[126:133], v145 abid:1\n\t" \
  "s_waitcnt lgkmcnt(3)\n\t" \
  "ds_write_b64 v24, v[10:11] offset:8\n\t" \
  "v_smfmac_f32_16x16x64_bf16 v[68:71], a[32:35], v[126:133], v178\n\t" \
  "s_sub_u32 s30, s30, s29\n\t" \
  "ds_read_b128 v[126:129], v183 offset:4096\n\t" \
  "v_smfmac_f32_16x16x64_bf16 v[36:39], a[36:39], v[134:141], v146\n\t" \
  "s_mov_b32 s37, s29\n\t" \
  "ds_write_b64 v24, v[12:13] offset:128\n\t" \
  "v_smfmac_f32_16x16x64_bf16 v[40:43], a[40:43], v[134:141], v146 abid:1\n\t" \
  "s_add_u32 s31, s31, 18432\n\t" \
  "ds_read_b128 v[130:133], v184 offset:4096\n\t" \
  "v_smfmac_f32_16x16x64_bf16 v[44:47], a[44:47], v[134:141], v147\n\t" \
  "s_sub_u32 s26, s31, 129024\n\t" \
  "ds_write_b64 v24, v[14:15] offset:136\n\t" \
  "v_xor_b32 v24, 64, v24\n\t" \
  "v_smfmac_f32_16x16x64_bf16 v[48:51], a[48:51], v[134:141], v147 abid:1\n\t" \
  "s_min_u32 s31, s31, s26\n\t" \
  "ds_read_b64 v[4:5], v4 offset:0\n\t" \
  "v_add_u32_sdwa v8, s29, v110 dst_sel:DWORD dst_unused:UNUSED_PAD src0_sel:DWORD src1_sel:WORD_0\n\t" \
  "v_smfmac_f32_16x16x64_bf16 v[52:55], a[52:55], v[134:141], v148\n\t" \
  "s_add_u32 s33, s31, s32\n\t" \
  "ds_write_b64 v24, v[16:17] offset:0\n\t" \
  "v_add_u32_sdwa v10, s29, v110 dst_sel:DWORD dst_unused:UNUSED_PAD src0_sel:DWORD src1_sel:WORD_1\n\t" \
  "v_smfmac_f32_16x16x64_bf16 v[56:59], a[56:59], v[134:141], v148 abid:1\n\t" \
  "s_add_u32 s34, s34, 4718592\n\t" \
  "ds_read_b64 v[6:7], v6 offset:0\n\t" \
  "v_add_u32_sdwa v12, s29, v111 dst_sel:DWORD dst_unused:UNUSED_PAD src0_sel:DWORD src1_sel:WORD_0\n\t" \
  "v_smfmac_f32_16x16x64_bf16 v[60:63], a[60:63], v[134:141], v149\n\t" \
  "s_and_b32 s27, s64, 7\n\t" \
  "ds_write_b64 v24, v[18:19] offset:8\n\t" \
  "v_add_u32_sdwa v14, s29, v111 dst_sel:DWORD dst_unused:UNUSED_PAD src0_sel:DWORD src1_sel:WORD_1\n\t" \
  "v_smfmac_f32_16x16x64_bf16 v[64:67], a[64:67], v[134:141], v149 abid:1\n\t" \
  "s_lshl2_add_u32 s66, s27, s68\n\t" \
  "ds_read_b64 v[8:9], v8 offset:0\n\t" \
  "v_smfmac_f32_16x16x64_bf16 v[68:71], a[68:71], v[134:141], v178 abid:1\n\t" \
  "s_waitcnt lgkmcnt(6)\n\t" \
  "ds_write_b64 v24, v[20:21] offset:128\n\t" \
  "v_smfmac_f32_16x16x64_bf16 v[36:39], a[72:75], v[126:133], v150\n\t" \
  "s_waitcnt lgkmcnt(3)\n\t" \
  "ds_read_b128 v[134:137], v183 offset:6144\n\t" \
  "v_smfmac_f32_16x16x64_bf16 v[36:39], v[72:75], v[0:7], v182 abid:1\n\t" \
  "s_and_b32 s27, s64, 8\n\t" \
  "ds_write_b64 v24, v[22:23] offset:136\n\t" \
  "v_add_u32_sdwa v0, s29, v112 dst_sel:DWORD dst_unused:UNUSED_PAD src0_sel:DWORD src1_sel:WORD_0\n\t" \
  "v_smfmac_f32_16x16x64_bf16 v[40:43], a[76:79], v[126:133], v150 abid:1\n\t" \
  "s_mul_i32 s27, s27, 176\n\t" \
  "ds_read_b128 v[138:141], v184 offset:6144\n\t" \
  "v_add_u32_sdwa v2, s29, v112 dst_sel:DWORD dst_unused:UNUSED_PAD src0_sel:DWORD src1_sel:WORD_1\n\t" \
  "v_smfmac_f32_16x16x64_bf16 v[44:47], a[80:83], v[126:133], v151\n\t" \
  "s_add_u32 s66, s66, s27\n\t" \
  "ds_write_b64 v25, v[26:27] offset:16384\n\t" \
  "v_add_u32_sdwa v4, s29, v113 dst_sel:DWORD dst_unused:UNUSED_PAD src0_sel:DWORD src1_sel:WORD_0\n\t" \
  "v_smfmac_f32_16x16x64_bf16 v[48:51], a[84:87], v[126:133], v151 abid:1\n\t" \
  "s_lshr_b64 s[64:65], s[64:65], 4\n\t" \
  "ds_read_b64 v[10:11], v10 offset:0\n\t" \
  "v_add_u32_sdwa v6, s29, v113 dst_sel:DWORD dst_unused:UNUSED_PAD src0_sel:DWORD src1_sel:WORD_1\n\t" \
  "v_smfmac_f32_16x16x64_bf16 v[52:55], a[88:91], v[126:133], v152\n\t" \
  "ds_read_b64 v[12:13], v12 offset:0\n\t" \
  "v_add_u32_sdwa v16, s29, v114 dst_sel:DWORD dst_unused:UNUSED_PAD src0_sel:DWORD src1_sel:WORD_0\n\t" \
  "v_smfmac_f32_16x16x64_bf16 v[56:59], a[92:95], v[126:133], v152 abid:1\n\t" \
  "ds_read_b64 v[14:15], v14 offset:0\n\t" \
  "v_add_u32_sdwa v18, s29, v114 dst_sel:DWORD dst_unused:UNUSED_PAD src0_sel:DWORD src1_sel:WORD_1\n\t" \
  "v_smfmac_f32_16x16x64_bf16 v[60:63], a[96:99], v[126:133], v153\n\t" \
  "ds_read_b64 v[0:1], v0 offset:0\n\t" \
  "v_add_u32_sdwa v20, s29, v115 dst_sel:DWORD dst_unused:UNUSED_PAD src0_sel:DWORD src1_sel:WORD_0\n\t" \
  "v_smfmac_f32_16x16x64_bf16 v[64:67], a[100:103], v[126:133], v153 abid:1\n\t" \
  "ds_read_b64 v[2:3], v2 offset:0\n\t" \
  "v_add_u32_sdwa v22, s29, v115 dst_sel:DWORD dst_unused:UNUSED_PAD src0_sel:DWORD src1_sel:WORD_1\n\t" \
  "v_smfmac_f32_16x16x64_bf16 v[68:71], a[104:107], v[126:133], v179\n\t" \
  "s_waitcnt lgkmcnt(6)\n\t" \
  "ds_read_b128 v[126:129], v183 offset:8192\n\t" \
  "v_smfmac_f32_16x16x64_bf16 v[36:39], a[108:111], v[134:141], v154\n\t" \
  "s_waitcnt lgkmcnt(3)\n\t" \
  "ds_read_b128 v[130:133], v184 offset:8192\n\t" \
  "v_smfmac_f32_16x16x64_bf16 v[40:43], v[76:79], v[8:15], v182 abid:1\n\t" \
  "ds_read_b64 v[4:5], v4 offset:0\n\t" \
  "v_smfmac_f32_16x16x64_bf16 v[40:43], a[112:115], v[134:141], v154 abid:1\n\t" \
  "ds_read_b64 v[6:7], v6 offset:0\n\t" \
  "v_smfmac_f32_16x16x64_bf16 v[44:47], a[116:119], v[134:141], v155\n\t" \
  "ds_read_b64 v[16:17], v16 offset:0\n\t" \
  "v_add_u32_sdwa v8, s29, v32 dst_sel:DWORD dst_unused:UNUSED_PAD src0_sel:DWORD src1_sel:WORD_0\n\t" \
  "v_smfmac_f32_16x16x64_bf16 v[48:51], a[120:123], v[134:141], v155 abid:1\n\t" \
  ".Ljn21%=:\n\t" \
  "ds_read_b64 v[18:19], v18 offset:0\n\t" \
  "v_add_u32_sdwa v10, s29, v32 dst_sel:DWORD dst_unused:UNUSED_PAD src0_sel:DWORD src1_sel:WORD_1\n\t" \
  "v_smfmac_f32_16x16x64_bf16 v[52:55], a[124:127], v[134:141], v156\n\t" \
  "ds_read_b64 v[20:21], v20 offset:0\n\t" \
  "v_add_u32 v28, s60, v187\n\t" \
  "v_smfmac_f32_16x16x64_bf16 v[56:59], a[128:131], v[134:141], v156 abid:1\n\t" \
  "s_waitcnt lgkmcnt(3)\n\t" \
  "ds_read_b64 v[22:23], v22 offset:0\n\t" \
  "v_smfmac_f32_16x16x64_bf16 v[44:47], v[80:83], v[0:7], v182 abid:1\n\t" \
  "ds_read_b64 v[8:9], v8 offset:0\n\t" \
  "v_add_u32_sdwa v0, s29, v116 dst_sel:DWORD dst_unused:UNUSED_PAD src0_sel:DWORD src1_sel:WORD_0\n\t" \
  "v_smfmac_f32_16x16x64_bf16 v[60:63], a[132:135], v[134:141], v157\n\t" \
  "ds_read_b64 v[10:11], v10 offset:0\n\t" \
  "v_add_u32_sdwa v2, s29, v116 dst_sel:DWORD dst_unused:UNUSED_PAD src0_sel:DWORD src1_sel:WORD_1\n\t" \
  "v_smfmac_f32_16x16x64_bf16 v[64:67], a[136:139], v[134:141], v157 abid:1\n\t" \
  "ds_read_b128 v[28:31], v28 offset:0\n\t" \
  "v_add_u32_sdwa v4, s29, v117 dst_sel:DWORD dst_unused:UNUSED_PAD src0_sel:DWORD src1_sel:WORD_0\n\t" \
  "v_smfmac_f32_16x16x64_bf16 v[68:71], a[140:143], v[134:141], v179 abid:1\n\t" \
  "s_waitcnt lgkmcnt(3)\n\t" \
  "ds_read_b128 v[134:137], v183 offset:10240\n\t" \
  "v_add_u32_sdwa v6, s29, v117 dst_sel:DWORD dst_unused:UNUSED_PAD src0_sel:DWORD src1_sel:WORD_1\n\t" \
  "v_smfmac_f32_16x16x64_bf16 v[48:51], v[84:87], v[16:23], v182 abid:1\n\t" \
  "ds_read_b128 v[138:141], v184 offset:10240\n\t" \
  "v_add_u32 v24, s38, v187\n\t" \
  "v_smfmac_f32_16x16x64_bf16 v[36:39], a[144:147], v[126:133], v158\n\t" \
  "ds_read_b128 v[24:27], v24 offset:8192\n\t" \
  "v_add_u32_sdwa v16, s29, v118 dst_sel:DWORD dst_unused:UNUSED_PAD src0_sel:DWORD src1_sel:WORD_0\n\t" \
  "v_smfmac_f32_16x16x64_bf16 v[40:43], a[148:151], v[126:133], v158 abid:1\n\t" \
  "s_waitcnt lgkmcnt(3)\n\t" \
  "ds_read_b64 v[0:1], v0 offset:0\n\t" \
  "v_add_u32_sdwa v18, s29, v118 dst_sel:DWORD dst_unused:UNUSED_PAD src0_sel:DWORD src1_sel:WORD_1\n\t" \
  "s_set_gpr_idx_on s6, gpr_idx(DST)\n\t" \
  "v_smfmac_f32_16x16x64_bf16 v[36:39], v[28:31], v[8:15], v182 abid:1\n\t" \
  "s_set_gpr_idx_off\n\t" \
  "ds_read_b64 v[2:3], v2 offset:0\n\t" \
  "v_add_u32_sdwa v20, s29, v119 dst_sel:DWORD dst_unused:UNUSED_PAD src0_sel:DWORD src1_sel:WORD_0\n\t" \
  "v_smfmac_f32_16x16x64_bf16 v[44:47], a[152:155], v[126:133], v159\n\t" \
  "s_add_u32 s36, s35, s39\n\t" \
  "ds_read_b64 v[4:5], v4 offset:0\n\t" \
  "v_add_u32_sdwa v22, s29, v119 dst_sel:DWORD dst_unused:UNUSED_PAD src0_sel:DWORD src1_sel:WORD_1\n\t" \
  "v_smfmac_f32_16x16x64_bf16 v[48:51], a[156:159], v[126:133], v159 abid:1\n\t" \
  "s_waitcnt lgkmcnt(3)\n\t" \
  "buffer_store_dwordx4 v[24:27], v34, s[20:23], s36 offen sc1\n\t" \
  "v_add_u32_sdwa v8, s29, v120 dst_sel:DWORD dst_unused:UNUSED_PAD src0_sel:DWORD src1_sel:WORD_0\n\t" \
  "v_smfmac_f32_16x16x64_bf16 v[52:55], a[160:163], v[126:133], v160\n\t" \
  "ds_read_b64 v[6:7], v6 offset:0\n\t" \
  "v_xad_u32 v24, v187, 64, s38\n\t" \
  "v_smfmac_f32_16x16x64_bf16 v[56:59], a[164:167], v[126:133], v160 abid:1\n\t" \
  "ds_read_b128 v[24:27], v24 offset:9216\n\t" \
  "v_add_u32_sdwa v10, s29, v120 dst_sel:DWORD dst_unused:UNUSED_PAD src0_sel:DWORD src1_sel:WORD_1\n\t" \
  "v_smfmac_f32_16x16x64_bf16 v[60:63], a[168:171], v[126:133], v161\n\t" \
  "ds_read_b64 v[16:17], v16 offset:0\n\t" \
  "v_add_u32_sdwa v12, s29, v121 dst_sel:DWORD dst_unused:UNUSED_PAD src0_sel:DWORD src1_sel:WORD_0\n\t" \
  "v_smfmac_f32_16x16x64_bf16 v[64:67], a[172:175], v[126:133], v161 abid:1\n\t" \
  "ds_read_b64 v[18:19], v18 offset:0\n\t" \
  "v_add_u32_sdwa v14, s29, v121 dst_sel:DWORD dst_unused:UNUSED_PAD src0_sel:DWORD src1_sel:WORD_1\n\t" \
  "v_smfmac_f32_16x16x64_bf16 v[68:71], a[176:179], v[126:133], v180\n\t" \
  "s_add_u32 s36, s36, 4608\n\t" \
  "s_waitcnt lgkmcnt(3)\n\t" \
  "ds_read_b128 v[126:129], v183 offset:12288\n\t" \
  "v_smfmac_f32_16x16x64_bf16 v[52:55], v[88:91], v[0:7], v182 abid:1\n\t" \
  "s_waitcnt lgkmcnt(3)\n\t" \
  "buffer_store_dwordx4 v[24:27], v34, s[20:23], s36 offen sc1\n\t" \
  "v_add_u32_sdwa v0, s29, v122 dst_sel:DWORD dst_unused:UNUSED_PAD src0_sel:DWORD src1_sel:WORD_0\n\t" \
  "v_smfmac_f32_16x16x64_bf16 v[36:39], a[180:183], v[134:141], v162\n\t" \
  "ds_read_b128 v[130:133], v184 offset:12288\n\t" \
  "v_add_u32 v24, s38, v187\n\t" \
  "v_smfmac_f32_16x16x64_bf16 v[40:43], a[184:187], v[134:141], v162 abid:1\n\t" \
  "ds_read_b128 v[24:27], v24 offset:10240\n\t" \
  "v_add_u32_sdwa v2, s29, v122 dst_sel:DWORD dst_unused:UNUSED_PAD src0_sel:DWORD src1_sel:WORD_1\n\t" \
  "v_smfmac_f32_16x16x64_bf16 v[44:47], a[188:191], v[134:141], v163\n\t" \
  "ds_read_b64 v[20:21], v20 offset:0\n\t" \
  "v_add_u32_sdwa v4, s29, v123 dst_sel:DWORD dst_unused:UNUSED_PAD src0_sel:DWORD src1_sel:WORD_0\n\t" \
  "v_smfmac_f32_16x16x64_bf16 v[48:51], a[192:195], v[134:141], v163 abid:1\n\t" \
  "ds_read_b64 v[22:23], v22 offset:0\n\t" \
  "v_add_u32_sdwa v6, s29, v123 dst_sel:DWORD dst_unused:UNUSED_PAD src0_sel:DWORD src1_sel:WORD_1\n\t" \
  "v_smfmac_f32_16x16x64_bf16 v[52:55], a[196:199], v[134:141], v164\n\t" \
  "s_add_u32 s36, s36, 4608\n\t" \
  "ds_read_b64 v[8:9], v8 offset:0\n\t" \
  "v_smfmac_f32_16x16x64_bf16 v[56:59], a[200:203], v[134:141], v164 abid:1\n\t" \
  "s_waitcnt lgkmcnt(3)\n\t" \
  "buffer_store_dwordx4 v[24:27], v34, s[20:23], s36 offen sc1\n\t" \
  "v_smfmac_f32_16x16x64_bf16 v[60:63], a[204:207], v[134:141], v165\n\t" \
  "ds_read_b64 v[10:11], v10 offset:0\n\t" \
  "v_xad_u32 v24, v187, 64, s38\n\t" \
  "v_smfmac_f32_16x16x64_bf16 v[64:67], a[208:211], v[134:141], v165 abid:1\n\t" \
  "s_waitcnt lgkmcnt(2)\n\t" \
  "ds_read_b128 v[24:27], v24 offset:11264\n\t" \
  "v_smfmac_f32_16x16x64_bf16 v[56:59], v[92:95], v[16:23], v182 abid:1\n\t" \
  "s_add_u32 s38, s38, 18432\n\t" \
  "ds_read_b64 v[12:13], v12 offset:0\n\t" \
  "v_add_u32_sdwa v16, s29, v124 dst_sel:DWORD dst_unused:UNUSED_PAD src0_sel:DWORD src1_sel:WORD_0\n\t" \
  "v_smfmac_f32_16x16x64_bf16 v[68:71], a[212:215], v[134:141], v180 abid:1\n\t" \
  "s_sub_u32 s44, s38, 129024\n\t" \
  "ds_read_b128 v[134:137], v183 offset:14336\n\t" \
  "v_add_u32_sdwa v18, s29, v124 dst_sel:DWORD dst_unused:UNUSED_PAD src0_sel:DWORD src1_sel:WORD_1\n\t" \
  "v_smfmac_f32_16x16x64_bf16 v[36:39], a[216:219], v[126:133], v166\n\t" \
  "s_add_u32 s36, s36, 4608\n\t" \
  "ds_read_b128 v[138:141], v184 offset:14336\n\t" \
  "v_add_u32_sdwa v20, s29, v125 dst_sel:DWORD dst_unused:UNUSED_PAD src0_sel:DWORD src1_sel:WORD_0\n\t" \
  "v_smfmac_f32_16x16x64_bf16 v[40:43], a[220:223], v[126:133], v166 abid:1\n\t" \
  "s_waitcnt lgkmcnt(3)\n\t" \
  "buffer_store_dwordx4 v[24:27], v34, s[20:23], s36 offen sc1\n\t" \
  "v_add_u32_sdwa v22, s29, v125 dst_sel:DWORD dst_unused:UNUSED_PAD src0_sel:DWORD src1_sel:WORD_1\n\t" \
  "v_smfmac_f32_16x16x64_bf16 v[44:47], a[224:227], v[126:133], v167\n\t" \
  "s_add_u32 s29, s29, s30\n\t" \
  "ds_read_b64 v[14:15], v14 offset:0\n\t" \
  "v_smfmac_f32_16x16x64_bf16 v[48:51], a[228:231], v[126:133], v167 abid:1\n\t" \
  "s_min_u32 s38, s38, s44\n\t" \
  "ds_read_b64 v[0:1], v0 offset:0\n\t" \
  "v_smfmac_f32_16x16x64_bf16 v[52:55], a[232:235], v[126:133], v168\n\t" \
  "s_add_u32 s35, s35, 4718592\n\t" \
  "ds_read_b64 v[2:3], v2 offset:0\n\t" \
  "v_smfmac_f32_16x16x64_bf16 v[56:59], a[236:239], v[126:133], v168 abid:1\n\t" \
  "ds_read_b64 v[4:5], v4 offset:0\n\t" \
  "v_smfmac_f32_16x16x64_bf16 v[60:63], a[240:243], v[126:133], v169\n\t" \
  "s_waitcnt lgkmcnt(3)\n\t" \
  "ds_read_b64 v[6:7], v6 offset:0\n\t" \
  "v_smfmac_f32_16x16x64_bf16 v[60:63], v[96:99], v[8:15], v182 abid:1\n\t" \
  "ds_read_b64 v[16:17], v16 offset:0\n\t" \
  "v_smfmac_f32_16x16x64_bf16 v[64:67], a[244:247], v[126:133], v169 abid:1\n\t" \
  "ds_read_b64 v[18:19], v18 offset:0\n\t" \
  "v_smfmac_f32_16x16x64_bf16 v[68:71], a[248:251], v[126:133], v181\n\t" \
  "ds_read_b64 v[20:21], v20 offset:0\n\t" \
  "v_smfmac_f32_16x16x64_bf16 v[36:39], a[252:255], v[134:141], v170\n\t" \
  "s_waitcnt lgkmcnt(3)\n\t" \
  "ds_read_b64 v[22:23], v22 offset:0\n\t" \
  "v_smfmac_f32_16x16x64_bf16 v[64:67], v[100:103], v[0:7], v182 abid:1\n\t" \
  "ds_read_b128 v[0:3], v183 offset:16384\n\t" \
  "v_smfmac_f32_16x16x64_bf16 v[40:43], v[188:191], v[134:141], v170 abid:1\n\t" \
  "ds_read_b128 v[4:7], v184 offset:16384\n\t" \
  "v_add_u32 v183, s30, v183\n\t" \
  "v_smfmac_f32_16x16x64_bf16 v[44:47], v[192:195], v[134:141], v171\n\t" \
  "v_add_u32 v184, s30, v184\n\t" \
  "v_smfmac_f32_16x16x64_bf16 v[48:51], v[196:199], v[134:141], v171 abid:1\n\t" \
  "s_waitcnt lgkmcnt(2)\n\t" \
  "v_smfmac_f32_16x16x64_bf16 v[68:71], v[104:107], v[16:23], v182 abid:1\n\t" \
  "v_smfmac_f32_16x16x64_bf16 v[52:55], v[200:203], v[134:141], v172\n\t" \
  "v_smfmac_f32_16x16x64_bf16 v[56:59], v[204:207], v[134:141], v172 abid:1\n\t" \
  "v_smfmac_f32_16x16x64_bf16 v[60:63], v[208:211], v[134:141], v173\n\t" \
  "v_smfmac_f32_16x16x64_bf16 v[64:67], v[212:215], v[134:141], v173 abid:1\n\t" \
  "v_smfmac_f32_16x16x64_bf16 v[68:71], v[216:219], v[134:141], v181 abid:1\n\t" \
  "s_setpc_b64 s[66:67]\n\t" \
  ".fill (.Lvar%=+28672-.)/4, 4, 0xbf800000\n\t" \
  "s_branch .Lfin%=\n\t" \
  "s_branch .Lhe%=\n\t" \
  "s_waitcnt vmcnt(6)\n\t" \
  "s_waitcnt vmcnt(10)\n\t" \
  "s_waitcnt vmcnt(12)\n\t" \
  "s_waitcnt vmcnt(17)\n\t" \
  "s_waitcnt vmcnt(21) lgkmcnt(0)\n\t" \
  "s_barrier\n\t" \
  "v_smfmac_f32_16x16x64_bf16 v[36:39], v[220:223], v[0:7], v174\n\t" \
  "ds_read_b128 v[126:129], v183 offset:0\n\t" \
  "v_add_u32 v24, s37, v185\n\t" \
  "v_smfmac_f32_16x16x64_bf16 v[40:43], v[224:227], v[0:7], v174 abid:1\n\t" \
  "ds_read_b128 v[130:133], v184 offset:0\n\t" \
  "v_xor_b32 v25, s41, v183\n\t" \
  "v_smfmac_f32_16x16x64_bf16 v[44:47], v[228:231], v[0:7], v175\n\t" \
  "ds_read_b128 v[134:137], v183 offset:2048\n\t" \
  "v_subrev_u32 v25, s30, v25\n\t" \
  "v_smfmac_f32_16x16x64_bf16 v[48:51], v[232:235], v[0:7], v175 abid:1\n\t" \
  "s_add_u32 s30, s29, 18432\n\t" \
  "ds_read_b128 v[138:141], v184 offset:2048\n\t" \
  "v_cvt_pk_bf16_f32 v8, v36, v37\n\t" \
  "v_cvt_pk_bf16_f32 v9, v38, v39\n\t" \
  "v_smfmac_f32_16x16x64_bf16 v[52:55], v[236:239], v[0:7], v176\n\t" \
  "ds_read_b128 v[36:39], v35 offset:0\n\t" \
  "v_cvt_pk_bf16_f32 v10, v40, v41\n\t" \
  "v_cvt_pk_bf16_f32 v11, v42, v43\n\t" \
  "v_smfmac_f32_16x16x64_bf16 v[56:59], v[240:243], v[0:7], v176 abid:1\n\t" \
  "s_sub_u32 s25, s30, 129024\n\t" \
  "ds_read_b128 v[40:43], v35 offset:64\n\t" \
  "v_cvt_pk_bf16_f32 v16, v44, v45\n\t" \
  "v_cvt_pk_bf16_f32 v17, v46, v47\n\t" \
  "v_smfmac_f32_16x16x64_bf16 v[60:63], v[244:247], v[0:7], v177\n\t" \
  "ds_read_b128 v[44:47], v35 offset:128\n\t" \
  "v_cvt_pk_bf16_f32 v18, v48, v49\n\t" \
  "v_cvt_pk_bf16_f32 v19, v50, v51\n\t" \
  "v_smfmac_f32_16x16x64_bf16 v[64:67], v[248:251], v[0:7], v177 abid:1\n\t" \
  "s_min_u32 s30, s30, s25\n\t" \
  "ds_read_b128 v[48:51], v35 offset:192\n\t" \
  "v_cvt_pk_bf16_f32 v12, v52, v53\n\t" \
  "v_cvt_pk_bf16_f32 v13, v54, v55\n\t" \
  "v_smfmac_f32_16x16x64_bf16 v[68:71], v[252:255], v[0:7], v182\n\t" \
  "s_waitcnt lgkmcnt(3)\n\t" \
  "ds_read_b128 v[52:55], v35 offset:256\n\t" \
  "v_cvt_pk_bf16_f32 v14, v56, v57\n\t" \
  "v_cvt_pk_bf16_f32 v15, v58, v59\n\t" \
  "v_smfmac_f32_16x16x64_bf16 v[36:39], a[0:3], v[126:133], v142\n\t" \
  "s_waitcnt lgkmcnt(3)\n\t" \
  "ds_read_b128 v[56:59], v35 offset:320\n\t" \
  "v_cvt_pk_bf16_f32 v20, v60, v61\n\t" \
  "v_cvt_pk_bf16_f32 v21, v62, v63\n\t" \
  "v_smfmac_f32_16x16x64_bf16 v[40:43], a[4:7], v[126:133], v142 abid:1\n\t" \
  "s_waitcnt lgkmcnt(3)\n\t" \
  "ds_read_b128 v[60:63], v35 offset:384\n\t" \
  "v_cvt_pk_bf16_f32 v22, v64, v65\n\t" \
  "v_cvt_pk_bf16_f32 v23, v66, v67\n\t" \
  "v_smfmac_f32_16x16x64_bf16 v[44:47], a[8:11], v[126:133], v143\n\t" \
  "s_waitcnt lgkmcnt(3)\n\t" \
  "ds_read_b128 v[64:67], v35 offset:448\n\t" \
  "v_cvt_pk_bf16_f32 v26, v68, v69\n\t" \
  "v_cvt_pk_bf16_f32 v27, v70, v71\n\t" \
  "v_smfmac_f32_16x16x64_bf16 v[48:51], a[12:15], v[126:133], v143 abid:1\n\t" \
  "s_waitcnt lgkmcnt(3)\n\t" \
  "ds_read_b128 v[68:71], v35 offset:512\n\t" \
  "v_add_u32_sdwa v0, s29, v108 dst_sel:DWORD dst_unused:UNUSED_PAD src0_sel:DWORD src1_sel:WORD_0\n\t" \
  "v_smfmac_f32_16x16x64_bf16 v[52:55], a[16:19], v[126:133], v144\n\t" \
  "s_waitcnt lgkmcnt(3)\n\t" \
  "ds_read_b64 v[0:1], v0 offset:0\n\t" \
  "v_add_u32_sdwa v2, s29, v108 dst_sel:DWORD dst_unused:UNUSED_PAD src0_sel:DWORD src1_sel:WORD_1\n\t" \
  "v_smfmac_f32_16x16x64_bf16 v[56:59], a[20:23], v[126:133], v144 abid:1\n\t" \
  "s_waitcnt lgkmcnt(3)\n\t" \
  "ds_write_b64 v24, v[8:9] offset:0\n\t" \
  "v_add_u32_sdwa v4, s29, v109 dst_sel:DWORD dst_unused:UNUSED_PAD src0_sel:DWORD src1_sel:WORD_0\n\t" \
  "v_smfmac_f32_16x16x64_bf16 v[60:63], a[24:27], v[126:133], v145\n\t" \
  "s_waitcnt lgkmcnt(3)\n\t" \
  "ds_read_b64 v[2:3], v2 offset:0\n\t" \
  "v_add_u32_sdwa v6, s29, v109 dst_sel:DWORD dst_unused:UNUSED_PAD src0_sel:DWORD src1_sel:WORD_1\n\t" \
  "v_smfmac_f32_16x16x64_bf16 v[64:67], a[28:31], v[126:133], v145 abid:1\n\t" \
  "s_waitcnt lgkmcnt(3)\n\t" \
  "s_bfe_i32 s47, s48, 0x10000\n\t" \
  "ds_write_b64 v24, v[10:11] offset:8\n\t" \
  "v_add_u32 v28, s38, v187\n\t" \
  "v_smfmac_f32_16x16x64_bf16 v[68:71], a[32:35], v[126:133], v178\n\t" \
  "s_sub_u32 s30, s30, s29\n\t" \
  "s_lshr_b32 s48, s48, 1\n\t" \
  "ds_read_b128 v[126:129], v183 offset:4096\n\t" \
  "ds_read_b128 v[28:31], v28 offset:8192\n\t" \
  "v_smfmac_f32_16x16x64_bf16 v[36:39], a[36:39], v[134:141], v146\n\t" \
  "s_mov_b32 s37, s29\n\t" \
  "s_add_u32 s36, s35, s39\n\t" \
  "ds_write_b64 v24, v[12:13] offset:128\n\t" \
  "v_smfmac_f32_16x16x64_bf16 v[40:43], a[40:43], v[134:141], v146 abid:1\n\t" \
  "s_andn2_b32 s44, 0x40000000, s47\n\t" \
  "ds_read_b128 v[130:133], v184 offset:4096\n\t" \
  "v_smfmac_f32_16x16x64_bf16 v[44:47], a[44:47], v[134:141], v147\n\t" \
  "s_or_b32 s36, s36, s44\n\t" \
  "ds_write_b64 v24, v[14:15] offset:136\n\t" \
  "v_xor_b32 v24, 64, v24\n\t" \
  "v_smfmac_f32_16x16x64_bf16 v[48:51], a[48:51], v[134:141], v147 abid:1\n\t" \
  "ds_read_b64 v[4:5], v4 offset:0\n\t" \
  "s_waitcnt lgkmcnt(4)\n\t" \
  "buffer_store_dwordx4 v[28:31], v34, s[20:23], s36 offen sc1\n\t" \
  "v_add_u32_sdwa v8, s29, v110 dst_sel:DWORD dst_unused:UNUSED_PAD src0_sel:DWORD src1_sel:WORD_0\n\t" \
  "v_smfmac_f32_16x16x64_bf16 v[52:55], a[52:55], v[134:141], v148\n\t" \
  "s_and_b32 s44, s47, 4718592\n\t" \
  "ds_write_b64 v24, v[16:17] offset:0\n\t" \
  "v_add_u32_sdwa v10, s29, v110 dst_sel:DWORD dst_unused:UNUSED_PAD src0_sel:DWORD src1_sel:WORD_1\n\t" \
  "v_xad_u32 v28, v187, 64, s38\n\t" \
  "v_smfmac_f32_16x16x64_bf16 v[56:59], a[56:59], v[134:141], v148 abid:1\n\t" \
  "s_add_u32 s35, s35, s44\n\t" \
  "ds_read_b64 v[6:7], v6 offset:0\n\t" \
  "ds_read_b128 v[28:31], v28 offset:9216\n\t" \
  "v_add_u32_sdwa v12, s29, v111 dst_sel:DWORD dst_unused:UNUSED_PAD src0_sel:DWORD src1_sel:WORD_0\n\t" \
  "v_smfmac_f32_16x16x64_bf16 v[60:63], a[60:63], v[134:141], v149\n\t" \
  "s_and_b32 s27, s64, 7\n\t" \
  "s_add_u32 s36, s36, 4608\n\t" \
  "ds_write_b64 v24, v[18:19] offset:8\n\t" \
  "v_add_u32_sdwa v14, s29, v111 dst_sel:DWORD dst_unused:UNUSED_PAD src0_sel:DWORD src1_sel:WORD_1\n\t" \
  "v_smfmac_f32_16x16x64_bf16 v[64:67], a[64:67], v[134:141], v149 abid:1\n\t" \
  "s_lshl2_add_u32 s66, s27, s68\n\t" \
  "ds_read_b64 v[8:9], v8 offset:0\n\t" \
  "v_smfmac_f32_16x16x64_bf16 v[68:71], a[68:71], v[134:141], v178 abid:1\n\t" \
  "s_waitcnt lgkmcnt(6)\n\t" \
  "ds_write_b64 v24, v[20:21] offset:128\n\t" \
  "v_smfmac_f32_16x16x64_bf16 v[36:39], a[72:75], v[126:133], v150\n\t" \
  "s_waitcnt lgkmcnt(3)\n\t" \
  "ds_read_b128 v[134:137], v183 offset:6144\n\t" \
  "s_waitcnt lgkmcnt(4)\n\t" \
  "buffer_store_dwordx4 v[28:31], v34, s[20:23], s36 offen sc1\n\t" \
  "v_smfmac_f32_16x16x64_bf16 v[36:39], v[72:75], v[0:7], v182 abid:1\n\t" \
  "s_and_b32 s27, s64, 8\n\t" \
  "s_and_b32 s44, s47, 18432\n\t" \
  "ds_write_b64 v24, v[22:23] offset:136\n\t" \
  "v_add_u32_sdwa v0, s29, v112 dst_sel:DWORD dst_unused:UNUSED_PAD src0_sel:DWORD src1_sel:WORD_0\n\t" \
  "v_add_u32 v28, s38, v187\n\t" \
  "v_smfmac_f32_16x16x64_bf16 v[40:43], a[76:79], v[126:133], v150 abid:1\n\t" \
  "s_mul_i32 s27, s27, 176\n\t" \
  "ds_read_b128 v[138:141], v184 offset:6144\n\t" \
  "ds_read_b128 v[28:31], v28 offset:10240\n\t" \
  "v_add_u32_sdwa v2, s29, v112 dst_sel:DWORD dst_unused:UNUSED_PAD src0_sel:DWORD src1_sel:WORD_1\n\t" \
  "v_smfmac_f32_16x16x64_bf16 v[44:47], a[80:83], v[126:133], v151\n\t" \
  "s_add_u32 s66, s66, s27\n\t" \
  "s_add_u32 s36, s36, 4608\n\t" \
  "ds_write_b64 v25, v[26:27] offset:16384\n\t" \
  "v_add_u32_sdwa v4, s29, v113 dst_sel:DWORD dst_unused:UNUSED_PAD src0_sel:DWORD src1_sel:WORD_0\n\t" \
  "v_smfmac_f32_16x16x64_bf16 v[48:51], a[84:87], v[126:133], v151 abid:1\n\t" \
  "s_lshr_b64 s[64:65], s[64:65], 4\n\t" \
  "ds_read_b64 v[10:11], v10 offset:0\n\t" \
  "v_add_u32_sdwa v6, s29, v113 dst_sel:DWORD dst_unused:UNUSED_PAD src0_sel:DWORD src1_sel:WORD_1\n\t" \
  "v_smfmac_f32_16x16x64_bf16 v[52:55], a[88:91], v[126:133], v152\n\t" \
  "ds_read_b64 v[12:13], v12 offset:0\n\t" \
  "v_add_u32_sdwa v16, s29, v114 dst_sel:DWORD dst_unused:UNUSED_PAD src0_sel:DWORD src1_sel:WORD_0\n\t" \
  "v_smfmac_f32_16x16x64_bf16 v[56:59], a[92:95], v[126:133], v152 abid:1\n\t" \
  "ds_read_b64 v[14:15], v14 offset:0\n\t" \
  "s_waitcnt lgkmcnt(4)\n\t" \
  "buffer_store_dwordx4 v[28:31], v34, s[20:23], s36 offen sc1\n\t" \
  "v_add_u32_sdwa v18, s29, v114 dst_sel:DWORD dst_unused:UNUSED_PAD src0_sel:DWORD src1_sel:WORD_1\n\t" \
  "v_smfmac_f32_16x16x64_bf16 v[60:63], a[96:99], v[126:133], v153\n\t" \
  "ds_read_b64 v[0:1], v0 offset:0\n\t" \
  "v_add_u32_sdwa v20, s29, v115 dst_sel:DWORD dst_unused:UNUSED_PAD src0_sel:DWORD src1_sel:WORD_0\n\t" \
  "v_xad_u32 v28, v187, 64, s38\n\t" \
  "v_smfmac_f32_16x16x64_bf16 v[64:67], a[100:103], v[126:133], v153 abid:1\n\t" \
  "s_add_u32 s38, s38, s44\n\t" \
  "ds_read_b64 v[2:3], v2 offset:0\n\t" \
  "ds_read_b128 v[28:31], v28 offset:11264\n\t" \
  "v_add_u32_sdwa v22, s29, v115 dst_sel:DWORD dst_unused:UNUSED_PAD src0_sel:DWORD src1_sel:WORD_1\n\t" \
  "v_smfmac_f32_16x16x64_bf16 v[68:71], a[104:107], v[126:133], v179\n\t" \
  "s_waitcnt lgkmcnt(6)\n\t" \
  "s_add_u32 s36, s36, 4608\n\t" \
  "ds_read_b128 v[126:129], v183 offset:8192\n\t" \
  "v_smfmac_f32_16x16x64_bf16 v[36:39], a[108:111], v[134:141], v154\n\t" \
  "s_waitcnt lgkmcnt(3)\n\t" \
  "s_sub_u32 s44, s38, 129024\n\t" \
  "ds_read_b128 v[130:133], v184 offset:8192\n\t" \
  "v_smfmac_f32_16x16x64_bf16 v[40:43], v[76:79], v[8:15], v182 abid:1\n\t" \
  "s_min_u32 s38, s38, s44\n\t" \
  "ds_read_b64 v[4:5], v4 offset:0\n\t" \
  "v_smfmac_f32_16x16x64_bf16 v[40:43], a[112:115], v[134:141], v154 abid:1\n\t" \
  "ds_read_b64 v[6:7], v6 offset:0\n\t" \
  "s_waitcnt lgkmcnt(4)\n\t" \
  "buffer_store_dwordx4 v[28:31], v34, s[20:23], s36 offen sc1\n\t" \
  "v_smfmac_f32_16x16x64_bf16 v[44:47], a[116:119], v[134:141], v155\n\t" \
  "ds_read_b64 v[16:17], v16 offset:0\n\t" \
  "v_add_u32_sdwa v8, s29, v32 dst_sel:DWORD dst_unused:UNUSED_PAD src0_sel:DWORD src1_sel:WORD_0\n\t" \
  "v_smfmac_f32_16x16x64_bf16 v[48:51], a[120:123], v[134:141], v155 abid:1\n\t" \
  "s_branch .Ljn22%=\n\t" \
  ".fill (.Lvar%=+30076-.)/4, 4, 0xbf800000\n\t" \
  "s_nop 0\n\t" \
  "s_branch .Lfin%=\n\t" \
  "s_branch .Lhe%=\n\t" \
  "s_waitcnt vmcnt(6)\n\t" \
  "s_waitcnt vmcnt(10)\n\t" \
  "s_waitcnt vmcnt(12)\n\t" \
  "s_waitcnt vmcnt(17)\n\t" \
  "s_waitcnt vmcnt(21) lgkmcnt(0)\n\t" \
  "s_barrier\n\t" \
  "v_smfmac_f32_16x16x64_bf16 v[36:39], v[220:223], v[0:7], v174\n\t" \
  "s_add_u32 m0, s33, 0\n\t" \
  "ds_read_b128 v[126:129], v183 offset:0\n\t" \
  "v_add_u32 v24, s37, v185\n\t" \
  "v_smfmac_f32_16x16x64_bf16 v[40:43], v[224:227], v[0:7], v174 abid:1\n\t" \
  "buffer_load_dwordx4 v186, s[16:19], s34 offen offset:0 nt lds\n\t" \
  "ds_read_b128 v[130:133], v184 offset:0\n\t" \
  "v_xor_b32 v25, s41, v183\n\t" \
  "v_smfmac_f32_16x16x64_bf16 v[44:47], v[228:231], v[0:7], v175\n\t" \
  "s_add_u32 m0, s33, 1920\n\t" \
  "ds_read_b128 v[134:137], v183 offset:2048\n\t" \
  "v_subrev_u32 v25, s30, v25\n\t" \
  "v_smfmac_f32_16x16x64_bf16 v[48:51], v[232:235], v[0:7], v175 abid:1\n\t" \
  "s_add_u32 s30, s29, 18432\n\t" \
  "buffer_load_dwordx4 v186, s[16:19], s34 offen offset:128 nt lds\n\t" \
  "ds_read_b128 v[138:141], v184 offset:2048\n\t" \
  "v_cvt_pk_bf16_f32 v8, v36, v37\n\t" \
  "v_cvt_pk_bf16_f32 v9, v38, v39\n\t" \
  "v_smfmac_f32_16x16x64_bf16 v[52:55], v[236:239], v[0:7], v176\n\t" \
  "s_add_u32 m0, s33, 3840\n\t" \
  "ds_read_b128 v[36:39], v35 offset:0\n\t" \
  "v_cvt_pk_bf16_f32 v10, v40, v41\n\t" \
  "v_cvt_pk_bf16_f32 v11, v42, v43\n\t" \
  "v_smfmac_f32_16x16x64_bf16 v[56:59], v[240:243], v[0:7], v176 abid:1\n\t" \
  "s_sub_u32 s25, s30, 129024\n\t" \
  "buffer_load_dwordx4 v186, s[16:19], s34 offen offset:256 nt lds\n\t" \
  "ds_read_b128 v[40:43], v35 offset:64\n\t" \
  "v_cvt_pk_bf16_f32 v16, v44, v45\n\t" \
  "v_cvt_pk_bf16_f32 v17, v46, v47\n\t" \
  "v_smfmac_f32_16x16x64_bf16 v[60:63], v[244:247], v[0:7], v177\n\t" \
  "s_add_u32 m0, s33, 5760\n\t" \
  "ds_read_b128 v[44:47], v35 offset:128\n\t" \
  "v_cvt_pk_bf16_f32 v18, v48, v49\n\t" \
  "v_cvt_pk_bf16_f32 v19, v50, v51\n\t" \
  "v_smfmac_f32_16x16x64_bf16 v[64:67], v[248:251], v[0:7], v177 abid:1\n\t" \
  "s_min_u32 s30, s30, s25\n\t" \
  "buffer_load_dwordx4 v186, s[16:19], s34 offen offset:384 nt lds\n\t" \
  "ds_read_b128 v[48:51], v35 offset:192\n\t" \
  "v_cvt_pk_bf16_f32 v12, v52, v53\n\t" \
  "v_cvt_pk_bf16_f32 v13, v54, v55\n\t" \
  "v_smfmac_f32_16x16x64_bf16 v[68:71], v[252:255], v[0:7], v182\n\t" \
  "s_add_u32 m0, s33, 7680\n\t" \
  "s_waitcnt lgkmcnt(3)\n\t" \
  "ds_read_b128 v[52:55], v35 offset:256\n\t" \
  "v_cvt_pk_bf16_f32 v14, v56, v57\n\t" \
  "v_cvt_pk_bf16_f32 v15, v58, v59\n\t" \
  "v_smfmac_f32_16x16x64_bf16 v[36:39], a[0:3], v[126:133], v142\n\t" \
  "s_waitcnt lgkmcnt(3)\n\t" \
  "buffer_load_dwordx4 v186, s[16:19], s34 offen offset:512 nt lds\n\t" \
  "ds_read_b128 v[56:59], v35 offset:320\n\t" \
  "v_cvt_pk_bf16_f32 v20, v60, v61\n\t" \
  "v_cvt_pk_bf16_f32 v21, v62, v63\n\t" \
  "v_smfmac_f32_16x16x64_bf16 v[40:43], a[4:7], v[126:133], v142 abid:1\n\t" \
  "s_waitcnt lgkmcnt(3)\n\t" \
  "ds_read_b128 v[60:63], v35 offset:384\n\t" \
  "v_cvt_pk_bf16_f32 v22, v64, v65\n\t" \
  "v_cvt_pk_bf16_f32 v23, v66, v67\n\t" \
  "v_smfmac_f32_16x16x64_bf16 v[44:47], a[8:11], v[126:133], v143\n\t" \
  "s_waitcnt lgkmcnt(3)\n\t" \
  "ds_read_b128 v[64:67], v35 offset:448\n\t" \
  "v_cvt_pk_bf16_f32 v26, v68, v69\n\t" \
  "v_cvt_pk_bf16_f32 v27, v70, v71\n\t" \
  "v_smfmac_f32_16x16x64_bf16 v[48:51], a[12:15], v[126:133], v143 abid:1\n\t" \
  "s_waitcnt lgkmcnt(3)\n\t" \
  "ds_read_b128 v[68:71], v35 offset:512\n\t" \
  "v_add_u32_sdwa v0, s29, v108 dst_sel:DWORD dst_unused:UNUSED_PAD src0_sel:DWORD src1_sel:WORD_0\n\t" \
  "v_smfmac_f32_16x16x64_bf16 v[52:55], a[16:19], v[126:133], v144\n\t" \
  "s_waitcnt lgkmcnt(3)\n\t" \
  "ds_read_b64 v[0:1], v0 offset:0\n\t" \
  "v_add_u32_sdwa v2, s29, v108 dst_sel:DWORD dst_unused:UNUSED_PAD src0_sel:DWORD src1_sel:WORD_1\n\t" \
  "v_smfmac_f32_16x16x64_bf16 v[56:59], a[20:23], v[126:133], v144 abid:1\n\t" \
  "s_waitcnt lgkmcnt(3)\n\t" \
  "ds_write_b64 v24, v[8:9] offset:0\n\t" \
  "v_add_u32_sdwa v4, s29, v109 dst_sel:DWORD dst_unused:UNUSED_PAD src0_sel:DWORD src1_sel:WORD_0\n\t" \
  "v_smfmac_f32_16x16x64_bf16 v[60:63], a[24:27], v[126:133], v145\n\t" \
  "s_waitcnt lgkmcnt(3)\n\t" \
  "ds_read_b64 v[2:3], v2 offset:0\n\t" \
  "v_add_u32_sdwa v6, s29, v109 dst_sel:DWORD dst_unused:UNUSED_PAD src0_sel:DWORD src1_sel:WORD_1\n\t" \
  "v_smfmac_f32_16x16x64_bf16 v[64:67], a[28:31], v[126:133], v145 abid:1\n\t" \
  "s_waitcnt lgkmcnt(3)\n\t" \
  "ds_write_b64 v24, v[10:11] offset:8\n\t" \
  "v_smfmac_f32_16x16x64_bf16 v[68:71], a[32:35], v[126:133], v178\n\t" \
  "s_sub_u32 s30, s30, s29\n\t" \
  "ds_read_b128 v[126:129], v183 offset:4096\n\t" \
  "v_smfmac_f32_16x16x64_bf16 v[36:39], a[36:39], v[134:141], v146\n\t" \
  "s_mov_b32 s37, s29\n\t" \
  "ds_write_b64 v24, v[12:13] offset:128\n\t" \
  "v_smfmac_f32_16x16x64_bf16 v[40:43], a[40:43], v[134:141], v146 abid:1\n\t" \
  "s_add_u32 s31, s31, 18432\n\t" \
  "ds_read_b128 v[130:133], v184 offset:4096\n\t" \
  "v_smfmac_f32_16x16x64_bf16 v[44:47], a[44:47], v[134:141], v147\n\t" \
  "s_sub_u32 s26, s31, 129024\n\t" \
  "ds_write_b64 v24, v[14:15] offset:136\n\t" \
  "v_xor_b32 v24, 64, v24\n\t" \
  "v_smfmac_f32_16x16x64_bf16 v[48:51], a[48:51], v[134:141], v147 abid:1\n\t" \
  "s_min_u32 s31, s31, s26\n\t" \
  "ds_read_b64 v[4:5], v4 offset:0\n\t" \
  "v_add_u32_sdwa v8, s29, v110 dst_sel:DWORD dst_unused:UNUSED_PAD src0_sel:DWORD src1_sel:WORD_0\n\t" \
  "v_smfmac_f32_16x16x64_bf16 v[52:55], a[52:55], v[134:141], v148\n\t" \
  "s_add_u32 s33, s31, s32\n\t" \
  "ds_write_b64 v24, v[16:17] offset:0\n\t" \
  "v_add_u32_sdwa v10, s29, v110 dst_sel:DWORD dst_unused:UNUSED_PAD src0_sel:DWORD src1_sel:WORD_1\n\t" \
  "v_smfmac_f32_16x16x64_bf16 v[56:59], a[56:59], v[134:141], v148 abid:1\n\t" \
  "s_add_u32 s34, s34, 4718592\n\t" \
  "ds_read_b64 v[6:7], v6 offset:0\n\t" \
  "v_add_u32_sdwa v12, s29, v111 dst_sel:DWORD dst_unused:UNUSED_PAD src0_sel:DWORD src1_sel:WORD_0\n\t" \
  "v_smfmac_f32_16x16x64_bf16 v[60:63], a[60:63], v[134:141], v149\n\t" \
  "s_and_b32 s27, s64, 7\n\t" \
  "ds_write_b64 v24, v[18:19] offset:8\n\t" \
  "v_add_u32_sdwa v14, s29, v111 dst_sel:DWORD dst_unused:UNUSED_PAD src0_sel:DWORD src1_sel:WORD_1\n\t" \
  "v_smfmac_f32_16x16x64_bf16 v[64:67], a[64:67], v[134:141], v149 abid:1\n\t" \
  "s_lshl2_add_u32 s66, s27, s68\n\t" \
  "ds_read_b64 v[8:9], v8 offset:0\n\t" \
  "v_smfmac_f32_16x16x64_bf16 v[68:71], a[68:71], v[134:141], v178 abid:1\n\t" \
  "s_waitcnt lgkmcnt(6)\n\t" \
  "ds_write_b64 v24, v[20:21] offset:128\n\t" \
  "v_smfmac_f32_16x16x64_bf16 v[36:39], a[72:75], v[126:133], v150\n\t" \
  "s_waitcnt lgkmcnt(3)\n\t" \
  "ds_read_b128 v[134:137], v183 offset:6144\n\t" \
  "v_smfmac_f32_16x16x64_bf16 v[36:39], v[72:75], v[0:7], v182 abid:1\n\t" \
  "s_and_b32 s27, s64, 8\n\t" \
  "ds_write_b64 v24, v[22:23] offset:136\n\t" \
  "v_add_u32_sdwa v0, s29, v112 dst_sel:DWORD dst_unused:UNUSED_PAD src0_sel:DWORD src1_sel:WORD_0\n\t" \
  "v_smfmac_f32_16x16x64_bf16 v[40:43], a[76:79], v[126:133], v150 abid:1\n\t" \
  "s_mul_i32 s27, s27, 176\n\t" \
  "ds_read_b128 v[138:141], v184 offset:6144\n\t" \
  "v_add_u32_sdwa v2, s29, v112 dst_sel:DWORD dst_unused:UNUSED_PAD src0_sel:DWORD src1_sel:WORD_1\n\t" \
  "v_smfmac_f32_16x16x64_bf16 v[44:47], a[80:83], v[126:133], v151\n\t" \
  "s_add_u32 s66, s66, s27\n\t" \
  "ds_write_b64 v25, v[26:27] offset:16384\n\t" \
  "v_add_u32_sdwa v4, s29, v113 dst_sel:DWORD dst_unused:UNUSED_PAD src0_sel:DWORD src1_sel:WORD_0\n\t" \
  "v_smfmac_f32_16x16x64_bf16 v[48:51], a[84:87], v[126:133], v151 abid:1\n\t" \
  "s_lshr_b64 s[64:65], s[64:65], 4\n\t" \
  "ds_read_b64 v[10:11], v10 offset:0\n\t" \
  "v_add_u32_sdwa v6, s29, v113 dst_sel:DWORD dst_unused:UNUSED_PAD src0_sel:DWORD src1_sel:WORD_1\n\t" \
  "v_smfmac_f32_16x16x64_bf16 v[52:55], a[88:91], v[126:133], v152\n\t" \
  "ds_read_b64 v[12:13], v12 offset:0\n\t" \
  "v_add_u32_sdwa v16, s29, v114 dst_sel:DWORD dst_unused:UNUSED_PAD src0_sel:DWORD src1_sel:WORD_0\n\t" \
  "v_smfmac_f32_16x16x64_bf16 v[56:59], a[92:95], v[126:133], v152 abid:1\n\t" \
  "ds_read_b64 v[14:15], v14 offset:0\n\t" \
  "v_add_u32_sdwa v18, s29, v114 dst_sel:DWORD dst_unused:UNUSED_PAD src0_sel:DWORD src1_sel:WORD_1\n\t" \
  "v_smfmac_f32_16x16x64_bf16 v[60:63], a[96:99], v[126:133], v153\n\t" \
  "ds_read_b64 v[0:1], v0 offset:0\n\t" \
  "v_add_u32_sdwa v20, s29, v115 dst_sel:DWORD dst_unused:UNUSED_PAD src0_sel:DWORD src1_sel:WORD_0\n\t" \
  "v_smfmac_f32_16x16x64_bf16 v[64:67], a[100:103], v[126:133], v153 abid:1\n\t" \
  "ds_read_b64 v[2:3], v2 offset:0\n\t" \
  "v_add_u32_sdwa v22, s29, v115 dst_sel:DWORD dst_unused:UNUSED_PAD src0_sel:DWORD src1_sel:WORD_1\n\t" \
  "v_smfmac_f32_16x16x64_bf16 v[68:71], a[104:107], v[126:133], v179\n\t" \
  "s_waitcnt lgkmcnt(6)\n\t" \
  "ds_read_b128 v[126:129], v183 offset:8192\n\t" \
  "v_smfmac_f32_16x16x64_bf16 v[36:39], a[108:111], v[134:141], v154\n\t" \
  "s_waitcnt lgkmcnt(3)\n\t" \
  "ds_read_b128 v[130:133], v184 offset:8192\n\t" \
  "v_smfmac_f32_16x16x64_bf16 v[40:43], v[76:79], v[8:15], v182 abid:1\n\t" \
  "ds_read_b64 v[4:5], v4 offset:0\n\t" \
  "v_smfmac_f32_16x16x64_bf16 v[40:43], a[112:115], v[134:141], v154 abid:1\n\t" \
  "ds_read_b64 v[6:7], v6 offset:0\n\t" \
  "v_smfmac_f32_16x16x64_bf16 v[44:47], a[116:119], v[134:141], v155\n\t" \
  "ds_read_b64 v[16:17], v16 offset:0\n\t" \
  "v_add_u32_sdwa v8, s29, v32 dst_sel:DWORD dst_unused:UNUSED_PAD src0_sel:DWORD src1_sel:WORD_0\n\t" \
  "v_smfmac_f32_16x16x64_bf16 v[48:51], a[120:123], v[134:141], v155 abid:1\n\t" \
  ".Ljn22%=:\n\t" \
  "ds_read_b64 v[18:19], v18 offset:0\n\t" \
  "v_add_u32_sdwa v10, s29, v32 dst_sel:DWORD dst_unused:UNUSED_PAD src0_sel:DWORD src1_sel:WORD_1\n\t" \
  "v_smfmac_f32_16x16x64_bf16 v[52:55], a[124:127], v[134:141], v156\n\t" \
  "ds_read_b64 v[20:21], v20 offset:0\n\t" \
  "v_add_u32 v28, s60, v187\n\t" \
  "v_smfmac_f32_16x16x64_bf16 v[56:59], a[128:131], v[134:141], v156 abid:1\n\t" \
  "s_waitcnt lgkmcnt(3)\n\t" \
  "ds_read_b64 v[22:23], v22 offset:0\n\t" \
  "v_smfmac_f32_16x16x64_bf16 v[44:47], v[80:83], v[0:7], v182 abid:1\n\t" \
  "ds_read_b64 v[8:9], v8 offset:0\n\t" \
  "v_add_u32_sdwa v0, s29, v116 dst_sel:DWORD dst_unused:UNUSED_PAD src0_sel:DWORD src1_sel:WORD_0\n\t" \
  "v_smfmac_f32_16x16x64_bf16 v[60:63], a[132:135], v[134:141], v157\n\t" \
  "ds_read_b64 v[10:11], v10 offset:0\n\t" \
  "v_add_u32_sdwa v2, s29, v116 dst_sel:DWORD dst_unused:UNUSED_PAD src0_sel:DWORD src1_sel:WORD_1\n\t" \
  "v_smfmac_f32_16x16x64_bf16 v[64:67], a[136:139], v[134:141], v157 abid:1\n\t" \
  "ds_read_b128 v[28:31], v28 offset:0\n\t" \
  "v_add_u32_sdwa v4, s29, v117 dst_sel:DWORD dst_unused:UNUSED_PAD src0_sel:DWORD src1_sel:WORD_0\n\t" \
  "v_smfmac_f32_16x16x64_bf16 v[68:71], a[140:143], v[134:141], v179 abid:1\n\t" \
  "s_waitcnt lgkmcnt(3)\n\t" \
  "ds_read_b128 v[134:137], v183 offset:10240\n\t" \
  "v_add_u32_sdwa v6, s29, v117 dst_sel:DWORD dst_unused:UNUSED_PAD src0_sel:DWORD src1_sel:WORD_1\n\t" \
  "v_smfmac_f32_16x16x64_bf16 v[48:51], v[84:87], v[16:23], v182 abid:1\n\t" \
  "ds_read_b128 v[138:141], v184 offset:10240\n\t" \
  "v_add_u32 v24, s38, v187\n\t" \
  "v_smfmac_f32_16x16x64_bf16 v[36:39], a[144:147], v[126:133], v158\n\t" \
  "ds_read_b128 v[24:27], v24 offset:8192\n\t" \
  "v_add_u32_sdwa v16, s29, v118 dst_sel:DWORD dst_unused:UNUSED_PAD src0_sel:DWORD src1_sel:WORD_0\n\t" \
  "v_smfmac_f32_16x16x64_bf16 v[40:43], a[148:151], v[126:133], v158 abid:1\n\t" \
  "s_waitcnt lgkmcnt(3)\n\t" \
  "ds_read_b64 v[0:1], v0 offset:0\n\t" \
  "v_add_u32_sdwa v18, s29, v118 dst_sel:DWORD dst_unused:UNUSED_PAD src0_sel:DWORD src1_sel:WORD_1\n\t" \
  "s_set_gpr_idx_on s6, gpr_idx(DST)\n\t" \
  "v_smfmac_f32_16x16x64_bf16 v[36:39], v[28:31], v[8:15], v182 abid:1\n\t" \
  "s_set_gpr_idx_off\n\t" \
  "ds_read_b64 v[2:3], v2 offset:0\n\t" \
  "v_add_u32_sdwa v20, s29, v119 dst_sel:DWORD dst_unused:UNUSED_PAD src0_sel:DWORD src1_sel:WORD_0\n\t" \
  "v_smfmac_f32_16x16x64_bf16 v[44:47], a[152:155], v[126:133], v159\n\t" \
  "s_add_u32 s36, s35, s39\n\t" \
  "ds_read_b64 v[4:5], v4 offset:0\n\t" \
  "v_add_u32_sdwa v22, s29, v119 dst_sel:DWORD dst_unused:UNUSED_PAD src0_sel:DWORD src1_sel:WORD_1\n\t" \
  "v_smfmac_f32_16x16x64_bf16 v[48:51], a[156:159], v[126:133], v159 abid:1\n\t" \
  "s_waitcnt lgkmcnt(3)\n\t" \
  "buffer_store_dwordx4 v[24:27], v34, s[20:23], s36 offen sc1\n\t" \
  "v_add_u32_sdwa v8, s29, v33 dst_sel:DWORD dst_unused:UNUSED_PAD src0_sel:DWORD src1_sel:WORD_0\n\t" \
  "v_smfmac_f32_16x16x64_bf16 v[52:55], a[160:163], v[126:133], v160\n\t" \
  "ds_read_b64 v[6:7], v6 offset:0\n\t" \
  "v_xad_u32 v24, v187, 64, s38\n\t" \
  "v_smfmac_f32_16x16x64_bf16 v[56:59], a[164:167], v[126:133], v160 abid:1\n\t" \
  "ds_read_b128 v[24:27], v24 offset:9216\n\t" \
  "v_add_u32_sdwa v10, s29, v33 dst_sel:DWORD dst_unused:UNUSED_PAD src0_sel:DWORD src1_sel:WORD_1\n\t" \
  "v_smfmac_f32_16x16x64_bf16 v[60:63], a[168:171], v[126:133], v161\n\t" \
  "ds_read_b64 v[16:17], v16 offset:0\n\t" \
  "v_add_u32 v28, s60, v187\n\t" \
  "v_smfmac_f32_16x16x64_bf16 v[64:67], a[172:175], v[126:133], v161 abid:1\n\t" \
  "ds_read_b64 v[18:19], v18 offset:0\n\t" \
  "v_smfmac_f32_16x16x64_bf16 v[68:71], a[176:179], v[126:133], v180\n\t" \
  "s_add_u32 s36, s36, 4608\n\t" \
  "s_waitcnt lgkmcnt(3)\n\t" \
  "ds_read_b128 v[126:129], v183 offset:12288\n\t" \
  "v_smfmac_f32_16x16x64_bf16 v[52:55], v[88:91], v[0:7], v182 abid:1\n\t" \
  "s_waitcnt lgkmcnt(3)\n\t" \
  "buffer_store_dwordx4 v[24:27], v34, s[20:23], s36 offen sc1\n\t" \
  "v_add_u32_sdwa v0, s29, v120 dst_sel:DWORD dst_unused:UNUSED_PAD src0_sel:DWORD src1_sel:WORD_0\n\t" \
  "v_smfmac_f32_16x16x64_bf16 v[36:39], a[180:183], v[134:141], v162\n\t" \
  "ds_read_b128 v[130:133], v184 offset:12288\n\t" \
  "v_add_u32 v24, s38, v187\n\t" \
  "v_smfmac_f32_16x16x64_bf16 v[40:43], a[184:187], v[134:141], v162 abid:1\n\t" \
  "ds_read_b128 v[24:27], v24 offset:10240\n\t" \
  "v_add_u32_sdwa v2, s29, v120 dst_sel:DWORD dst_unused:UNUSED_PAD src0_sel:DWORD src1_sel:WORD_1\n\t" \
  "v_smfmac_f32_16x16x64_bf16 v[44:47], a[188:191], v[134:141], v163\n\t" \
  "ds_read_b64 v[20:21], v20 offset:0\n\t" \
  "v_add_u32_sdwa v4, s29, v121 dst_sel:DWORD dst_unused:UNUSED_PAD src0_sel:DWORD src1_sel:WORD_0\n\t" \
  "v_smfmac_f32_16x16x64_bf16 v[48:51], a[192:195], v[134:141], v163 abid:1\n\t" \
  "ds_read_b64 v[22:23], v22 offset:0\n\t" \
  "v_add_u32_sdwa v6, s29, v121 dst_sel:DWORD dst_unused:UNUSED_PAD src0_sel:DWORD src1_sel:WORD_1\n\t" \
  "v_smfmac_f32_16x16x64_bf16 v[52:55], a[196:199], v[134:141], v164\n\t" \
  "s_add_u32 s36, s36, 4608\n\t" \
  "ds_read_b64 v[8:9], v8 offset:0\n\t" \
  "v_smfmac_f32_16x16x64_bf16 v[56:59], a[200:203], v[134:141], v164 abid:1\n\t" \
  "s_waitcnt lgkmcnt(3)\n\t" \
  "buffer_store_dwordx4 v[24:27], v34, s[20:23], s36 offen sc1\n\t" \
  "v_smfmac_f32_16x16x64_bf16 v[60:63], a[204:207], v[134:141], v165\n\t" \
  "ds_read_b64 v[10:11], v10 offset:0\n\t" \
  "v_xad_u32 v24, v187, 64, s38\n\t" \
  "v_smfmac_f32_16x16x64_bf16 v[64:67], a[208:211], v[134:141], v165 abid:1\n\t" \
  "s_waitcnt lgkmcnt(2)\n\t" \
  "ds_read_b128 v[24:27], v24 offset:11264\n\t" \
  "v_smfmac_f32_16x16x64_bf16 v[56:59], v[92:95], v[16:23], v182 abid:1\n\t" \
  "s_add_u32 s38, s38, 18432\n\t" \
  "ds_read_b128 v[28:31], v28 offset:1280\n\t" \
  "v_add_u32_sdwa v16, s29, v122 dst_sel:DWORD dst_unused:UNUSED_PAD src0_sel:DWORD src1_sel:WORD_0\n\t" \
  "v_smfmac_f32_16x16x64_bf16 v[68:71], a[212:215], v[134:141], v180 abid:1\n\t" \
  "s_sub_u32 s44, s38, 129024\n\t" \
  "ds_read_b128 v[134:137], v183 offset:14336\n\t" \
  "v_add_u32_sdwa v18, s29, v122 dst_sel:DWORD dst_unused:UNUSED_PAD src0_sel:DWORD src1_sel:WORD_1\n\t" \
  "v_smfmac_f32_16x16x64_bf16 v[36:39], a[216:219], v[126:133], v166\n\t" \
  "s_add_u32 s36, s36, 4608\n\t" \
  "ds_read_b128 v[138:141], v184 offset:14336\n\t" \
  "v_add_u32_sdwa v20, s29, v123 dst_sel:DWORD dst_unused:UNUSED_PAD src0_sel:DWORD src1_sel:WORD_0\n\t" \
  "v_smfmac_f32_16x16x64_bf16 v[40:43], a[220:223], v[126:133], v166 abid:1\n\t" \
  "s_waitcnt lgkmcnt(3)\n\t" \
  "buffer_store_dwordx4 v[24:27], v34, s[20:23], s36 offen sc1\n\t" \
  "v_add_u32_sdwa v22, s29, v123 dst_sel:DWORD dst_unused:UNUSED_PAD src0_sel:DWORD src1_sel:WORD_1\n\t" \
  "v_smfmac_f32_16x16x64_bf16 v[44:47], a[224:227], v[126:133], v167\n\t" \
  "s_waitcnt lgkmcnt(2)\n\t" \
  "ds_read_b64 v[0:1], v0 offset:0\n\t" \
  "s_set_gpr_idx_on s7, gpr_idx(DST)\n\t" \
  "v_smfmac_f32_16x16x64_bf16 v[36:39], v[28:31], v[8:15], v182 abid:1\n\t" \
  "s_set_gpr_idx_off\n\t" \
  "ds_read_b64 v[2:3], v2 offset:0\n\t" \
  "v_add_u32_sdwa v8, s29, v124 dst_sel:DWORD dst_unused:UNUSED_PAD src0_sel:DWORD src1_sel:WORD_0\n\t" \
  "v_smfmac_f32_16x16x64_bf16 v[48:51], a[228:231], v[126:133], v167 abid:1\n\t" \
  "s_min_u32 s38, s38, s44\n\t" \
  "ds_read_b64 v[4:5], v4 offset:0\n\t" \
  "v_add_u32_sdwa v10, s29, v124 dst_sel:DWORD dst_unused:UNUSED_PAD src0_sel:DWORD src1_sel:WORD_1\n\t" \
  "v_smfmac_f32_16x16x64_bf16 v[52:55], a[232:235], v[126:133], v168\n\t" \
  "s_add_u32 s35, s35, 4718592\n\t" \
  "ds_read_b64 v[6:7], v6 offset:0\n\t" \
  "v_add_u32_sdwa v12, s29, v125 dst_sel:DWORD dst_unused:UNUSED_PAD src0_sel:DWORD src1_sel:WORD_0\n\t" \
  "v_smfmac_f32_16x16x64_bf16 v[56:59], a[236:239], v[126:133], v168 abid:1\n\t" \
  "ds_read_b64 v[16:17], v16 offset:0\n\t" \
  "v_add_u32_sdwa v14, s29, v125 dst_sel:DWORD dst_unused:UNUSED_PAD src0_sel:DWORD src1_sel:WORD_1\n\t" \
  "v_smfmac_f32_16x16x64_bf16 v[60:63], a[240:243], v[126:133], v169\n\t" \
  "s_add_u32 s29, s29, s30\n\t" \
  "ds_read_b64 v[18:19], v18 offset:0\n\t" \
  "v_smfmac_f32_16x16x64_bf16 v[64:67], a[244:247], v[126:133], v169 abid:1\n\t" \
  "ds_read_b64 v[20:21], v20 offset:0\n\t" \
  "v_smfmac_f32_16x16x64_bf16 v[68:71], a[248:251], v[126:133], v181\n\t" \
  "s_waitcnt lgkmcnt(3)\n\t" \
  "ds_read_b64 v[22:23], v22 offset:0\n\t" \
  "v_smfmac_f32_16x16x64_bf16 v[60:63], v[96:99], v[0:7], v182 abid:1\n\t" \
  "ds_read_b128 v[0:3], v183 offset:16384\n\t" \
  "v_smfmac_f32_16x16x64_bf16 v[36:39], a[252:255], v[134:141], v170\n\t" \
  "ds_read_b128 v[4:7], v184 offset:16384\n\t" \
  "v_add_u32 v183, s30, v183\n\t" \
  "v_smfmac_f32_16x16x64_bf16 v[40:43], v[188:191], v[134:141], v170 abid:1\n\t" \
  "ds_read_b64 v[8:9], v8 offset:0\n\t" \
  "v_add_u32 v184, s30, v184\n\t" \
  "v_smfmac_f32_16x16x64_bf16 v[44:47], v[192:195], v[134:141], v171\n\t" \
  "s_waitcnt lgkmcnt(3)\n\t" \
  "ds_read_b64 v[10:11], v10 offset:0\n\t" \
  "v_smfmac_f32_16x16x64_bf16 v[64:67], v[100:103], v[16:23], v182 abid:1\n\t" \
  "ds_read_b64 v[12:13], v12 offset:0\n\t" \
  "v_smfmac_f32_16x16x64_bf16 v[48:51], v[196:199], v[134:141], v171 abid:1\n\t" \
  "ds_read_b64 v[14:15], v14 offset:0\n\t" \
  "v_smfmac_f32_16x16x64_bf16 v[52:55], v[200:203], v[134:141], v172\n\t" \
  "v_smfmac_f32_16x16x64_bf16 v[56:59], v[204:207], v[134:141], v172 abid:1\n\t" \
  "v_smfmac_f32_16x16x64_bf16 v[60:63], v[208:211], v[134:141], v173\n\t" \
  "v_smfmac_f32_16x16x64_bf16 v[64:67], v[212:215], v[134:141], v173 abid:1\n\t" \
  "s_waitcnt lgkmcnt(0)\n\t" \
  "v_smfmac_f32_16x16x64_bf16 v[68:71], v[104:107], v[8:15], v182 abid:1\n\t" \
  "v_smfmac_f32_16x16x64_bf16 v[68:71], v[216:219], v[134:141], v181 abid:1\n\t" \
  "s_setpc_b64 s[66:67]\n\t" \
  ".fill (.Lvar%=+32768-.)/4, 4, 0xbf800000\n\t" \
  "s_branch .Lfin%=\n\t" \
  "s_branch .Lhe%=\n\t" \
  "s_waitcnt vmcnt(6)\n\t" \
  "s_waitcnt vmcnt(10)\n\t" \
  "s_waitcnt vmcnt(12)\n\t" \
  "s_waitcnt vmcnt(17)\n\t" \
  "s_waitcnt vmcnt(21) lgkmcnt(0)\n\t" \
  "s_barrier\n\t" \
  "v_smfmac_f32_16x16x64_bf16 v[36:39], v[220:223], v[0:7], v174\n\t" \
  "ds_read_b128 v[126:129], v183 offset:0\n\t" \
  "v_add_u32 v24, s37, v185\n\t" \
  "v_lshrrev_b32 v28, 4, v187\n\t" \
  "v_smfmac_f32_16x16x64_bf16 v[40:43], v[224:227], v[0:7], v174 abid:1\n\t" \
  "ds_read_b128 v[130:133], v184 offset:0\n\t" \
  "v_xor_b32 v25, s41, v183\n\t" \
  "v_and_or_b32 v28, v28, 48, s45\n\t" \
  "v_smfmac_f32_16x16x64_bf16 v[44:47], v[228:231], v[0:7], v175\n\t" \
  "ds_read_b128 v[134:137], v183 offset:2048\n\t" \
  "v_subrev_u32 v25, s30, v25\n\t" \
  "v_smfmac_f32_16x16x64_bf16 v[48:51], v[232:235], v[0:7], v175 abid:1\n\t" \
  "s_add_u32 s30, s29, 18432\n\t" \
  "ds_read_b128 v[138:141], v184 offset:2048\n\t" \
  "v_cvt_pk_bf16_f32 v8, v36, v37\n\t" \
  "v_cvt_pk_bf16_f32 v9, v38, v39\n\t" \
  "v_smfmac_f32_16x16x64_bf16 v[52:55], v[236:239], v[0:7], v176\n\t" \
  "ds_read_b128 v[36:39], v28 offset:0\n\t" \
  "v_cvt_pk_bf16_f32 v10, v40, v41\n\t" \
  "v_cvt_pk_bf16_f32 v11, v42, v43\n\t" \
  "v_smfmac_f32_16x16x64_bf16 v[56:59], v[240:243], v[0:7], v176 abid:1\n\t" \
  "s_sub_u32 s25, s30, 129024\n\t" \
  "ds_read_b128 v[40:43], v28 offset:64\n\t" \
  "v_cvt_pk_bf16_f32 v16, v44, v45\n\t" \
  "v_cvt_pk_bf16_f32 v17, v46, v47\n\t" \
  "v_smfmac_f32_16x16x64_bf16 v[60:63], v[244:247], v[0:7], v177\n\t" \
  "ds_read_b128 v[44:47], v28 offset:128\n\t" \
  "v_cvt_pk_bf16_f32 v18, v48, v49\n\t" \
  "v_cvt_pk_bf16_f32 v19, v50, v51\n\t" \
  "v_smfmac_f32_16x16x64_bf16 v[64:67], v[248:251], v[0:7], v177 abid:1\n\t" \
  "s_min_u32 s30, s30, s25\n\t" \
  "ds_read_b128 v[48:51], v28 offset:192\n\t" \
  "v_cvt_pk_bf16_f32 v12, v52, v53\n\t" \
  "v_cvt_pk_bf16_f32 v13, v54, v55\n\t" \
  "v_smfmac_f32_16x16x64_bf16 v[68:71], v[252:255], v[0:7], v182\n\t" \
  "s_waitcnt lgkmcnt(3)\n\t" \
  "ds_read_b128 v[52:55], v28 offset:256\n\t" \
  "v_cvt_pk_bf16_f32 v14, v56, v57\n\t" \
  "v_cvt_pk_bf16_f32 v15, v58, v59\n\t" \
  "v_smfmac_f32_16x16x64_bf16 v[36:39], a[0:3], v[126:133], v142\n\t" \
  "s_waitcnt lgkmcnt(3)\n\t" \
  "ds_read_b128 v[56:59], v28 offset:320\n\t" \
  "v_cvt_pk_bf16_f32 v20, v60, v61\n\t" \
  "v_cvt_pk_bf16_f32 v21, v62, v63\n\t" \
  "v_smfmac_f32_16x16x64_bf16 v[40:43], a[4:7], v[126:133], v142 abid:1\n\t" \
  "s_waitcnt lgkmcnt(3)\n\t" \
  "ds_read_b128 v[60:63], v28 offset:384\n\t" \
  "v_cvt_pk_bf16_f32 v22, v64, v65\n\t" \
  "v_cvt_pk_bf16_f32 v23, v66, v67\n\t" \
  "v_smfmac_f32_16x16x64_bf16 v[44:47], a[8:11], v[126:133], v143\n\t" \
  "s_waitcnt lgkmcnt(3)\n\t" \
  "ds_read_b128 v[64:67], v28 offset:448\n\t" \
  "v_cvt_pk_bf16_f32 v26, v68, v69\n\t" \
  "v_cvt_pk_bf16_f32 v27, v70, v71\n\t" \
  "v_smfmac_f32_16x16x64_bf16 v[48:51], a[12:15], v[126:133], v143 abid:1\n\t" \
  "s_waitcnt lgkmcnt(3)\n\t" \
  "ds_read_b128 v[68:71], v28 offset:512\n\t" \
  "v_add_u32_sdwa v0, s29, v108 dst_sel:DWORD dst_unused:UNUSED_PAD src0_sel:DWORD src1_sel:WORD_0\n\t" \
  "v_smfmac_f32_16x16x64_bf16 v[52:55], a[16:19], v[126:133], v144\n\t" \
  "s_waitcnt lgkmcnt(3)\n\t" \
  "ds_read_b64 v[0:1], v0 offset:0\n\t" \
  "v_add_u32_sdwa v2, s29, v108 dst_sel:DWORD dst_unused:UNUSED_PAD src0_sel:DWORD src1_sel:WORD_1\n\t" \
  "v_smfmac_f32_16x16x64_bf16 v[56:59], a[20:23], v[126:133], v144 abid:1\n\t" \
  "s_waitcnt lgkmcnt(3)\n\t" \
  "ds_write_b64 v24, v[8:9] offset:0\n\t" \
  "v_add_u32_sdwa v4, s29, v109 dst_sel:DWORD dst_unused:UNUSED_PAD src0_sel:DWORD src1_sel:WORD_0\n\t" \
  "v_smfmac_f32_16x16x64_bf16 v[60:63], a[24:27], v[126:133], v145\n\t" \
  "s_waitcnt lgkmcnt(3)\n\t" \
  "ds_read_b64 v[2:3], v2 offset:0\n\t" \
  "v_add_u32_sdwa v6, s29, v109 dst_sel:DWORD dst_unused:UNUSED_PAD src0_sel:DWORD src1_sel:WORD_1\n\t" \
  "v_smfmac_f32_16x16x64_bf16 v[64:67], a[28:31], v[126:133], v145 abid:1\n\t" \
  "s_waitcnt lgkmcnt(3)\n\t" \
  "s_bfe_i32 s47, s48, 0x10000\n\t" \
  "ds_write_b64 v24, v[10:11] offset:8\n\t" \
  "v_add_u32 v28, s38, v187\n\t" \
  "v_smfmac_f32_16x16x64_bf16 v[68:71], a[32:35], v[126:133], v178\n\t" \
  "s_sub_u32 s30, s30, s29\n\t" \
  "s_lshr_b32 s48, s48, 1\n\t" \
  "ds_read_b128 v[126:129], v183 offset:4096\n\t" \
  "ds_read_b128 v[28:31], v28 offset:8192\n\t" \
  "v_smfmac_f32_16x16x64_bf16 v[36:39], a[36:39], v[134:141], v146\n\t" \
  "s_mov_b32 s37, s29\n\t" \
  "s_add_u32 s36, s35, s39\n\t" \
  "ds_write_b64 v24, v[12:13] offset:128\n\t" \
  "v_smfmac_f32_16x16x64_bf16 v[40:43], a[40:43], v[134:141], v146 abid:1\n\t" \
  "s_andn2_b32 s44, 0x40000000, s47\n\t" \
  "ds_read_b128 v[130:133], v184 offset:4096\n\t" \
  "v_smfmac_f32_16x16x64_bf16 v[44:47], a[44:47], v[134:141], v147\n\t" \
  "s_or_b32 s36, s36, s44\n\t" \
  "ds_write_b64 v24, v[14:15] offset:136\n\t" \
  "v_xor_b32 v24, 64, v24\n\t" \
  "v_smfmac_f32_16x16x64_bf16 v[48:51], a[48:51], v[134:141], v147 abid:1\n\t" \
  "ds_read_b64 v[4:5], v4 offset:0\n\t" \
  "s_waitcnt lgkmcnt(4)\n\t" \
  "buffer_store_dwordx4 v[28:31], v34, s[20:23], s36 offen sc1\n\t" \
  "v_add_u32_sdwa v8, s29, v110 dst_sel:DWORD dst_unused:UNUSED_PAD src0_sel:DWORD src1_sel:WORD_0\n\t" \
  "v_smfmac_f32_16x16x64_bf16 v[52:55], a[52:55], v[134:141], v148\n\t" \
  "s_and_b32 s44, s47, 4718592\n\t" \
  "ds_write_b64 v24, v[16:17] offset:0\n\t" \
  "v_add_u32_sdwa v10, s29, v110 dst_sel:DWORD dst_unused:UNUSED_PAD src0_sel:DWORD src1_sel:WORD_1\n\t" \
  "v_xad_u32 v28, v187, 64, s38\n\t" \
  "v_smfmac_f32_16x16x64_bf16 v[56:59], a[56:59], v[134:141], v148 abid:1\n\t" \
  "s_add_u32 s35, s35, s44\n\t" \
  "ds_read_b64 v[6:7], v6 offset:0\n\t" \
  "ds_read_b128 v[28:31], v28 offset:9216\n\t" \
  "v_add_u32_sdwa v12, s29, v111 dst_sel:DWORD dst_unused:UNUSED_PAD src0_sel:DWORD src1_sel:WORD_0\n\t" \
  "v_smfmac_f32_16x16x64_bf16 v[60:63], a[60:63], v[134:141], v149\n\t" \
  "s_and_b32 s27, s64, 7\n\t" \
  "s_add_u32 s36, s36, 4608\n\t" \
  "ds_write_b64 v24, v[18:19] offset:8\n\t" \
  "v_add_u32_sdwa v14, s29, v111 dst_sel:DWORD dst_unused:UNUSED_PAD src0_sel:DWORD src1_sel:WORD_1\n\t" \
  "v_smfmac_f32_16x16x64_bf16 v[64:67], a[64:67], v[134:141], v149 abid:1\n\t" \
  "s_lshl2_add_u32 s66, s27, s68\n\t" \
  "ds_read_b64 v[8:9], v8 offset:0\n\t" \
  "v_smfmac_f32_16x16x64_bf16 v[68:71], a[68:71], v[134:141], v178 abid:1\n\t" \
  "s_waitcnt lgkmcnt(6)\n\t" \
  "ds_write_b64 v24, v[20:21] offset:128\n\t" \
  "v_smfmac_f32_16x16x64_bf16 v[36:39], a[72:75], v[126:133], v150\n\t" \
  "s_waitcnt lgkmcnt(3)\n\t" \
  "ds_read_b128 v[134:137], v183 offset:6144\n\t" \
  "s_waitcnt lgkmcnt(4)\n\t" \
  "buffer_store_dwordx4 v[28:31], v34, s[20:23], s36 offen sc1\n\t" \
  "v_smfmac_f32_16x16x64_bf16 v[36:39], v[72:75], v[0:7], v182 abid:1\n\t" \
  "s_and_b32 s27, s64, 8\n\t" \
  "s_and_b32 s44, s47, 18432\n\t" \
  "ds_write_b64 v24, v[22:23] offset:136\n\t" \
  "v_add_u32_sdwa v0, s29, v112 dst_sel:DWORD dst_unused:UNUSED_PAD src0_sel:DWORD src1_sel:WORD_0\n\t" \
  "v_add_u32 v28, s38, v187\n\t" \
  "v_smfmac_f32_16x16x64_bf16 v[40:43], a[76:79], v[126:133], v150 abid:1\n\t" \
  "s_mul_i32 s27, s27, 176\n\t" \
  "ds_read_b128 v[138:141], v184 offset:6144\n\t" \
  "ds_read_b128 v[28:31], v28 offset:10240\n\t" \
  "v_add_u32_sdwa v2, s29, v112 dst_sel:DWORD dst_unused:UNUSED_PAD src0_sel:DWORD src1_sel:WORD_1\n\t" \
  "v_smfmac_f32_16x16x64_bf16 v[44:47], a[80:83], v[126:133], v151\n\t" \
  "s_add_u32 s66, s66, s27\n\t" \
  "s_add_u32 s36, s36, 4608\n\t" \
  "ds_write_b64 v25, v[26:27] offset:16384\n\t" \
  "v_add_u32_sdwa v4, s29, v113 dst_sel:DWORD dst_unused:UNUSED_PAD src0_sel:DWORD src1_sel:WORD_0\n\t" \
  "v_smfmac_f32_16x16x64_bf16 v[48:51], a[84:87], v[126:133], v151 abid:1\n\t" \
  "s_lshr_b64 s[64:65], s[64:65], 4\n\t" \
  "ds_read_b64 v[10:11], v10 offset:0\n\t" \
  "ds_read_b64 v[12:13], v12 offset:0\n\t" \
  "v_add_u32_sdwa v6, s29, v113 dst_sel:DWORD dst_unused:UNUSED_PAD src0_sel:DWORD src1_sel:WORD_1\n\t" \
  "v_smfmac_f32_16x16x64_bf16 v[52:55], a[88:91], v[126:133], v152\n\t" \
  "ds_read_b64 v[14:15], v14 offset:0\n\t" \
  "ds_read_b64 v[0:1], v0 offset:0\n\t" \
  "v_add_u32_sdwa v16, s29, v114 dst_sel:DWORD dst_unused:UNUSED_PAD src0_sel:DWORD src1_sel:WORD_0\n\t" \
  "v_smfmac_f32_16x16x64_bf16 v[56:59], a[92:95], v[126:133], v152 abid:1\n\t" \
  "ds_read_b64 v[2:3], v2 offset:0\n\t" \
  "s_waitcnt lgkmcnt(6)\n\t" \
  "buffer_store_dwordx4 v[28:31], v34, s[20:23], s36 offen sc1\n\t" \
  "v_add_u32_sdwa v18, s29, v114 dst_sel:DWORD dst_unused:UNUSED_PAD src0_sel:DWORD src1_sel:WORD_1\n\t" \
  "v_smfmac_f32_16x16x64_bf16 v[60:63], a[96:99], v[126:133], v153\n\t" \
  "ds_read_b64 v[4:5], v4 offset:0\n\t" \
  "v_add_u32_sdwa v20, s29, v115 dst_sel:DWORD dst_unused:UNUSED_PAD src0_sel:DWORD src1_sel:WORD_0\n\t" \
  "v_xad_u32 v28, v187, 64, s38\n\t" \
  "v_smfmac_f32_16x16x64_bf16 v[64:67], a[100:103], v[126:133], v153 abid:1\n\t" \
  "s_add_u32 s38, s38, s44\n\t" \
  "ds_read_b64 v[6:7], v6 offset:0\n\t" \
  "ds_read_b128 v[28:31], v28 offset:11264\n\t" \
  "v_add_u32_sdwa v22, s29, v115 dst_sel:DWORD dst_unused:UNUSED_PAD src0_sel:DWORD src1_sel:WORD_1\n\t" \
  "v_smfmac_f32_16x16x64_bf16 v[68:71], a[104:107], v[126:133], v179\n\t" \
  "s_waitcnt lgkmcnt(4)\n\t" \
  "s_add_u32 s36, s36, 4608\n\t" \
  "ds_read_b128 v[126:129], v183 offset:8192\n\t" \
  "v_smfmac_f32_16x16x64_bf16 v[40:43], v[76:79], v[8:15], v182 abid:1\n\t" \
  "s_sub_u32 s44, s38, 129024\n\t" \
  "ds_read_b128 v[130:133], v184 offset:8192\n\t" \
  "v_smfmac_f32_16x16x64_bf16 v[36:39], a[108:111], v[134:141], v154\n\t" \
  "s_min_u32 s38, s38, s44\n\t" \
  "ds_read_b64 v[16:17], v16 offset:0\n\t" \
  "v_smfmac_f32_16x16x64_bf16 v[40:43], a[112:115], v[134:141], v154 abid:1\n\t" \
  "s_waitcnt lgkmcnt(3)\n\t" \
  "ds_read_b64 v[18:19], v18 offset:0\n\t" \
  "s_waitcnt lgkmcnt(4)\n\t" \
  "buffer_store_dwordx4 v[28:31], v34, s[20:23], s36 offen sc1\n\t" \
  "v_smfmac_f32_16x16x64_bf16 v[44:47], v[80:83], v[0:7], v182 abid:1\n\t" \
  "ds_read_b64 v[20:21], v20 offset:0\n\t" \
  "v_add_u32_sdwa v0, s29, v32 dst_sel:DWORD dst_unused:UNUSED_PAD src0_sel:DWORD src1_sel:WORD_0\n\t" \
  "v_smfmac_f32_16x16x64_bf16 v[44:47], a[116:119], v[134:141], v155\n\t" \
  "s_branch .Ljn23%=\n\t" \
  ".fill (.Lvar%=+34172-.)/4, 4, 0xbf800000\n\t" \
  "s_nop 0\n\t" \
  "s_branch .Lfin%=\n\t" \
  "s_branch .Lhe%=\n\t" \
  "s_waitcnt vmcnt(6)\n\t" \
  "s_waitcnt vmcnt(10)\n\t" \
  "s_waitcnt vmcnt(12)\n\t" \
  "s_waitcnt vmcnt(17)\n\t" \
  "s_waitcnt vmcnt(21) lgkmcnt(0)\n\t" \
  "s_barrier\n\t" \
  "v_smfmac_f32_16x16x64_bf16 v[36:39], v[220:223], v[0:7], v174\n\t" \
  "s_add_u32 m0, s33, 0\n\t" \
  "ds_read_b128 v[126:129], v183 offset:0\n\t" \
  "v_add_u32 v24, s37, v185\n\t" \
  "v_lshrrev_b32 v28, 4, v187\n\t" \
  "v_smfmac_f32_16x16x64_bf16 v[40:43], v[224:227], v[0:7], v174 abid:1\n\t" \
  "buffer_load_dwordx4 v186, s[16:19], s34 offen offset:0 nt lds\n\t" \
  "ds_read_b128 v[130:133], v184 offset:0\n\t" \
  "v_xor_b32 v25, s41, v183\n\t" \
  "v_and_or_b32 v28, v28, 48, s45\n\t" \
  "v_smfmac_f32_16x16x64_bf16 v[44:47], v[228:231], v[0:7], v175\n\t" \
  "s_add_u32 m0, s33, 1920\n\t" \
  "ds_read_b128 v[134:137], v183 offset:2048\n\t" \
  "v_subrev_u32 v25, s30, v25\n\t" \
  "v_smfmac_f32_16x16x64_bf16 v[48:51], v[232:235], v[0:7], v175 abid:1\n\t" \
  "s_add_u32 s30, s29, 18432\n\t" \
  "buffer_load_dwordx4 v186, s[16:19], s34 offen offset:128 nt lds\n\t" \
  "ds_read_b128 v[138:141], v184 offset:2048\n\t" \
  "v_cvt_pk_bf16_f32 v8, v36, v37\n\t" \
  "v_cvt_pk_bf16_f32 v9, v38, v39\n\t" \
  "v_smfmac_f32_16x16x64_bf16 v[52:55], v[236:239], v[0:7], v176\n\t" \
  "s_add_u32 m0, s33, 3840\n\t" \
  "ds_read_b128 v[36:39], v28 offset:0\n\t" \
  "v_cvt_pk_bf16_f32 v10, v40, v41\n\t" \
  "v_cvt_pk_bf16_f32 v11, v42, v43\n\t" \
  "v_smfmac_f32_16x16x64_bf16 v[56:59], v[240:243], v[0:7], v176 abid:1\n\t" \
  "s_sub_u32 s25, s30, 129024\n\t" \
  "buffer_load_dwordx4 v186, s[16:19], s34 offen offset:256 nt lds\n\t" \
  "ds_read_b128 v[40:43], v28 offset:64\n\t" \
  "v_cvt_pk_bf16_f32 v16, v44, v45\n\t" \
  "v_cvt_pk_bf16_f32 v17, v46, v47\n\t" \
  "v_smfmac_f32_16x16x64_bf16 v[60:63], v[244:247], v[0:7], v177\n\t" \
  "s_add_u32 m0, s33, 5760\n\t" \
  "ds_read_b128 v[44:47], v28 offset:128\n\t" \
  "v_cvt_pk_bf16_f32 v18, v48, v49\n\t" \
  "v_cvt_pk_bf16_f32 v19, v50, v51\n\t" \
  "v_smfmac_f32_16x16x64_bf16 v[64:67], v[248:251], v[0:7], v177 abid:1\n\t" \
  "s_min_u32 s30, s30, s25\n\t" \
  "buffer_load_dwordx4 v186, s[16:19], s34 offen offset:384 nt lds\n\t" \
  "ds_read_b128 v[48:51], v28 offset:192\n\t" \
  "v_cvt_pk_bf16_f32 v12, v52, v53\n\t" \
  "v_cvt_pk_bf16_f32 v13, v54, v55\n\t" \
  "v_smfmac_f32_16x16x64_bf16 v[68:71], v[252:255], v[0:7], v182\n\t" \
  "s_add_u32 m0, s33, 7680\n\t" \
  "s_waitcnt lgkmcnt(3)\n\t" \
  "ds_read_b128 v[52:55], v28 offset:256\n\t" \
  "v_cvt_pk_bf16_f32 v14, v56, v57\n\t" \
  "v_cvt_pk_bf16_f32 v15, v58, v59\n\t" \
  "v_smfmac_f32_16x16x64_bf16 v[36:39], a[0:3], v[126:133], v142\n\t" \
  "s_waitcnt lgkmcnt(3)\n\t" \
  "buffer_load_dwordx4 v186, s[16:19], s34 offen offset:512 nt lds\n\t" \
  "ds_read_b128 v[56:59], v28 offset:320\n\t" \
  "v_cvt_pk_bf16_f32 v20, v60, v61\n\t" \
  "v_cvt_pk_bf16_f32 v21, v62, v63\n\t" \
  "v_smfmac_f32_16x16x64_bf16 v[40:43], a[4:7], v[126:133], v142 abid:1\n\t" \
  "s_waitcnt lgkmcnt(3)\n\t" \
  "ds_read_b128 v[60:63], v28 offset:384\n\t" \
  "v_cvt_pk_bf16_f32 v22, v64, v65\n\t" \
  "v_cvt_pk_bf16_f32 v23, v66, v67\n\t" \
  "v_smfmac_f32_16x16x64_bf16 v[44:47], a[8:11], v[126:133], v143\n\t" \
  "s_waitcnt lgkmcnt(3)\n\t" \
  "ds_read_b128 v[64:67], v28 offset:448\n\t" \
  "v_cvt_pk_bf16_f32 v26, v68, v69\n\t" \
  "v_cvt_pk_bf16_f32 v27, v70, v71\n\t" \
  "v_smfmac_f32_16x16x64_bf16 v[48:51], a[12:15], v[126:133], v143 abid:1\n\t" \
  "s_waitcnt lgkmcnt(3)\n\t" \
  "ds_read_b128 v[68:71], v28 offset:512\n\t" \
  "v_add_u32_sdwa v0, s29, v108 dst_sel:DWORD dst_unused:UNUSED_PAD src0_sel:DWORD src1_sel:WORD_0\n\t" \
  "v_smfmac_f32_16x16x64_bf16 v[52:55], a[16:19], v[126:133], v144\n\t" \
  "s_waitcnt lgkmcnt(3)\n\t" \
  "ds_read_b64 v[0:1], v0 offset:0\n\t" \
  "v_add_u32_sdwa v2, s29, v108 dst_sel:DWORD dst_unused:UNUSED_PAD src0_sel:DWORD src1_sel:WORD_1\n\t" \
  "v_smfmac_f32_16x16x64_bf16 v[56:59], a[20:23], v[126:133], v144 abid:1\n\t" \
  "s_waitcnt lgkmcnt(3)\n\t" \
  "ds_write_b64 v24, v[8:9] offset:0\n\t" \
  "v_add_u32_sdwa v4, s29, v109 dst_sel:DWORD dst_unused:UNUSED_PAD src0_sel:DWORD src1_sel:WORD_0\n\t" \
  "v_smfmac_f32_16x16x64_bf16 v[60:63], a[24:27], v[126:133], v145\n\t" \
  "s_waitcnt lgkmcnt(3)\n\t" \
  "ds_read_b64 v[2:3], v2 offset:0\n\t" \
  "v_add_u32_sdwa v6, s29, v109 dst_sel:DWORD dst_unused:UNUSED_PAD src0_sel:DWORD src1_sel:WORD_1\n\t" \
  "v_smfmac_f32_16x16x64_bf16 v[64:67], a[28:31], v[126:133], v145 abid:1\n\t" \
  "s_waitcnt lgkmcnt(3)\n\t" \
  "ds_write_b64 v24, v[10:11] offset:8\n\t" \
  "v_smfmac_f32_16x16x64_bf16 v[68:71], a[32:35], v[126:133], v178\n\t" \
  "s_sub_u32 s30, s30, s29\n\t" \
  "ds_read_b128 v[126:129], v183 offset:4096\n\t" \
  "v_smfmac_f32_16x16x64_bf16 v[36:39], a[36:39], v[134:141], v146\n\t" \
  "s_mov_b32 s37, s29\n\t" \
  "ds_write_b64 v24, v[12:13] offset:128\n\t" \
  "v_smfmac_f32_16x16x64_bf16 v[40:43], a[40:43], v[134:141], v146 abid:1\n\t" \
  "s_add_u32 s31, s31, 18432\n\t" \
  "ds_read_b128 v[130:133], v184 offset:4096\n\t" \
  "v_smfmac_f32_16x16x64_bf16 v[44:47], a[44:47], v[134:141], v147\n\t" \
  "s_sub_u32 s26, s31, 129024\n\t" \
  "ds_write_b64 v24, v[14:15] offset:136\n\t" \
  "v_xor_b32 v24, 64, v24\n\t" \
  "v_smfmac_f32_16x16x64_bf16 v[48:51], a[48:51], v[134:141], v147 abid:1\n\t" \
  "s_min_u32 s31, s31, s26\n\t" \
  "ds_read_b64 v[4:5], v4 offset:0\n\t" \
  "v_add_u32_sdwa v8, s29, v110 dst_sel:DWORD dst_unused:UNUSED_PAD src0_sel:DWORD src1_sel:WORD_0\n\t" \
  "v_smfmac_f32_16x16x64_bf16 v[52:55], a[52:55], v[134:141], v148\n\t" \
  "s_add_u32 s33, s31, s32\n\t" \
  "ds_write_b64 v24, v[16:17] offset:0\n\t" \
  "v_add_u32_sdwa v10, s29, v110 dst_sel:DWORD dst_unused:UNUSED_PAD src0_sel:DWORD src1_sel:WORD_1\n\t" \
  "v_smfmac_f32_16x16x64_bf16 v[56:59], a[56:59], v[134:141], v148 abid:1\n\t" \
  "s_add_u32 s34, s34, 4718592\n\t" \
  "ds_read_b64 v[6:7], v6 offset:0\n\t" \
  "v_add_u32_sdwa v12, s29, v111 dst_sel:DWORD dst_unused:UNUSED_PAD src0_sel:DWORD src1_sel:WORD_0\n\t" \
  "v_smfmac_f32_16x16x64_bf16 v[60:63], a[60:63], v[134:141], v149\n\t" \
  "s_and_b32 s27, s64, 7\n\t" \
  "ds_write_b64 v24, v[18:19] offset:8\n\t" \
  "v_add_u32_sdwa v14, s29, v111 dst_sel:DWORD dst_unused:UNUSED_PAD src0_sel:DWORD src1_sel:WORD_1\n\t" \
  "v_smfmac_f32_16x16x64_bf16 v[64:67], a[64:67], v[134:141], v149 abid:1\n\t" \
  "s_lshl2_add_u32 s66, s27, s68\n\t" \
  "ds_read_b64 v[8:9], v8 offset:0\n\t" \
  "v_smfmac_f32_16x16x64_bf16 v[68:71], a[68:71], v[134:141], v178 abid:1\n\t" \
  "s_waitcnt lgkmcnt(6)\n\t" \
  "ds_write_b64 v24, v[20:21] offset:128\n\t" \
  "v_smfmac_f32_16x16x64_bf16 v[36:39], a[72:75], v[126:133], v150\n\t" \
  "s_waitcnt lgkmcnt(3)\n\t" \
  "ds_read_b128 v[134:137], v183 offset:6144\n\t" \
  "v_smfmac_f32_16x16x64_bf16 v[36:39], v[72:75], v[0:7], v182 abid:1\n\t" \
  "s_and_b32 s27, s64, 8\n\t" \
  "ds_write_b64 v24, v[22:23] offset:136\n\t" \
  "v_add_u32_sdwa v0, s29, v112 dst_sel:DWORD dst_unused:UNUSED_PAD src0_sel:DWORD src1_sel:WORD_0\n\t" \
  "v_smfmac_f32_16x16x64_bf16 v[40:43], a[76:79], v[126:133], v150 abid:1\n\t" \
  "s_mul_i32 s27, s27, 176\n\t" \
  "ds_read_b128 v[138:141], v184 offset:6144\n\t" \
  "v_add_u32_sdwa v2, s29, v112 dst_sel:DWORD dst_unused:UNUSED_PAD src0_sel:DWORD src1_sel:WORD_1\n\t" \
  "v_smfmac_f32_16x16x64_bf16 v[44:47], a[80:83], v[126:133], v151\n\t" \
  "s_add_u32 s66, s66, s27\n\t" \
  "ds_write_b64 v25, v[26:27] offset:16384\n\t" \
  "v_add_u32_sdwa v4, s29, v113 dst_sel:DWORD dst_unused:UNUSED_PAD src0_sel:DWORD src1_sel:WORD_0\n\t" \
  "v_smfmac_f32_16x16x64_bf16 v[48:51], a[84:87], v[126:133], v151 abid:1\n\t" \
  "s_lshr_b64 s[64:65], s[64:65], 4\n\t" \
  "ds_read_b64 v[10:11], v10 offset:0\n\t" \
  "ds_read_b64 v[12:13], v12 offset:0\n\t" \
  "v_add_u32_sdwa v6, s29, v113 dst_sel:DWORD dst_unused:UNUSED_PAD src0_sel:DWORD src1_sel:WORD_1\n\t" \
  "v_smfmac_f32_16x16x64_bf16 v[52:55], a[88:91], v[126:133], v152\n\t" \
  "ds_read_b64 v[14:15], v14 offset:0\n\t" \
  "ds_read_b64 v[0:1], v0 offset:0\n\t" \
  "v_add_u32_sdwa v16, s29, v114 dst_sel:DWORD dst_unused:UNUSED_PAD src0_sel:DWORD src1_sel:WORD_0\n\t" \
  "v_smfmac_f32_16x16x64_bf16 v[56:59], a[92:95], v[126:133], v152 abid:1\n\t" \
  "ds_read_b64 v[2:3], v2 offset:0\n\t" \
  "v_add_u32_sdwa v18, s29, v114 dst_sel:DWORD dst_unused:UNUSED_PAD src0_sel:DWORD src1_sel:WORD_1\n\t" \
  "v_smfmac_f32_16x16x64_bf16 v[60:63], a[96:99], v[126:133], v153\n\t" \
  "ds_read_b64 v[4:5], v4 offset:0\n\t" \
  "v_add_u32_sdwa v20, s29, v115 dst_sel:DWORD dst_unused:UNUSED_PAD src0_sel:DWORD src1_sel:WORD_0\n\t" \
  "v_smfmac_f32_16x16x64_bf16 v[64:67], a[100:103], v[126:133], v153 abid:1\n\t" \
  "ds_read_b64 v[6:7], v6 offset:0\n\t" \
  "v_add_u32_sdwa v22, s29, v115 dst_sel:DWORD dst_unused:UNUSED_PAD src0_sel:DWORD src1_sel:WORD_1\n\t" \
  "v_smfmac_f32_16x16x64_bf16 v[68:71], a[104:107], v[126:133], v179\n\t" \
  "s_waitcnt lgkmcnt(4)\n\t" \
  "ds_read_b128 v[126:129], v183 offset:8192\n\t" \
  "v_smfmac_f32_16x16x64_bf16 v[40:43], v[76:79], v[8:15], v182 abid:1\n\t" \
  "ds_read_b128 v[130:133], v184 offset:8192\n\t" \
  "v_smfmac_f32_16x16x64_bf16 v[36:39], a[108:111], v[134:141], v154\n\t" \
  "ds_read_b64 v[16:17], v16 offset:0\n\t" \
  "v_smfmac_f32_16x16x64_bf16 v[40:43], a[112:115], v[134:141], v154 abid:1\n\t" \
  "s_waitcnt lgkmcnt(3)\n\t" \
  "ds_read_b64 v[18:19], v18 offset:0\n\t" \
  "v_smfmac_f32_16x16x64_bf16 v[44:47], v[80:83], v[0:7], v182 abid:1\n\t" \
  "ds_read_b64 v[20:21], v20 offset:0\n\t" \
  "v_add_u32_sdwa v0, s29, v32 dst_sel:DWORD dst_unused:UNUSED_PAD src0_sel:DWORD src1_sel:WORD_0\n\t" \
  "v_smfmac_f32_16x16x64_bf16 v[44:47], a[116:119], v[134:141], v155\n\t" \
  ".Ljn23%=:\n\t" \
  "ds_read_b64 v[22:23], v22 offset:0\n\t" \
  "v_add_u32_sdwa v2, s29, v32 dst_sel:DWORD dst_unused:UNUSED_PAD src0_sel:DWORD src1_sel:WORD_1\n\t" \
  "v_smfmac_f32_16x16x64_bf16 v[48:51], a[120:123], v[134:141], v155 abid:1\n\t" \
  "ds_read_b64 v[0:1], v0 offset:0\n\t" \
  "v_add_u32 v28, s60, v187\n\t" \
  "v_smfmac_f32_16x16x64_bf16 v[52:55], a[124:127], v[134:141], v156\n\t" \
  "ds_read_b64 v[2:3], v2 offset:0\n\t" \
  "v_add_u32_sdwa v8, s29, v116 dst_sel:DWORD dst_unused:UNUSED_PAD src0_sel:DWORD src1_sel:WORD_0\n\t" \
  "v_smfmac_f32_16x16x64_bf16 v[56:59], a[128:131], v[134:141], v156 abid:1\n\t" \
  "ds_read_b128 v[28:31], v28 offset:0\n\t" \
  "v_add_u32_sdwa v10, s29, v116 dst_sel:DWORD dst_unused:UNUSED_PAD src0_sel:DWORD src1_sel:WORD_1\n\t" \
  "v_smfmac_f32_16x16x64_bf16 v[60:63], a[132:135], v[134:141], v157\n\t" \
  "s_waitcnt lgkmcnt(3)\n\t" \
  "ds_read_b64 v[8:9], v8 offset:0\n\t" \
  "v_add_u32_sdwa v12, s29, v117 dst_sel:DWORD dst_unused:UNUSED_PAD src0_sel:DWORD src1_sel:WORD_0\n\t" \
  "v_smfmac_f32_16x16x64_bf16 v[48:51], v[84:87], v[16:23], v182 abid:1\n\t" \
  "ds_read_b64 v[10:11], v10 offset:0\n\t" \
  "v_add_u32_sdwa v14, s29, v117 dst_sel:DWORD dst_unused:UNUSED_PAD src0_sel:DWORD src1_sel:WORD_1\n\t" \
  "v_smfmac_f32_16x16x64_bf16 v[64:67], a[136:139], v[134:141], v157 abid:1\n\t" \
  "ds_read_b64 v[12:13], v12 offset:0\n\t" \
  "v_add_u32_sdwa v16, s29, v118 dst_sel:DWORD dst_unused:UNUSED_PAD src0_sel:DWORD src1_sel:WORD_0\n\t" \
  "v_smfmac_f32_16x16x64_bf16 v[68:71], a[140:143], v[134:141], v179 abid:1\n\t" \
  "s_waitcnt lgkmcnt(3)\n\t" \
  "ds_read_b128 v[134:137], v183 offset:10240\n\t" \
  "v_add_u32 v24, s38, v187\n\t" \
  "s_set_gpr_idx_on s6, gpr_idx(DST)\n\t" \
  "v_smfmac_f32_16x16x64_bf16 v[36:39], v[28:31], v[0:7], v182 abid:1\n\t" \
  "s_set_gpr_idx_off\n\t" \
  "ds_read_b128 v[24:27], v24 offset:8192\n\t" \
  "v_add_u32_sdwa v18, s29, v118 dst_sel:DWORD dst_unused:UNUSED_PAD src0_sel:DWORD src1_sel:WORD_1\n\t" \
  "v_smfmac_f32_16x16x64_bf16 v[36:39], a[144:147], v[126:133], v158\n\t" \
  "ds_read_b128 v[138:141], v184 offset:10240\n\t" \
  "v_add_u32_sdwa v20, s29, v119 dst_sel:DWORD dst_unused:UNUSED_PAD src0_sel:DWORD src1_sel:WORD_0\n\t" \
  "v_smfmac_f32_16x16x64_bf16 v[40:43], a[148:151], v[126:133], v158 abid:1\n\t" \
  "ds_read_b64 v[14:15], v14 offset:0\n\t" \
  "v_add_u32_sdwa v22, s29, v119 dst_sel:DWORD dst_unused:UNUSED_PAD src0_sel:DWORD src1_sel:WORD_1\n\t" \
  "v_smfmac_f32_16x16x64_bf16 v[44:47], a[152:155], v[126:133], v159\n\t" \
  "s_add_u32 s36, s35, s39\n\t" \
  "ds_read_b64 v[16:17], v16 offset:0\n\t" \
  "v_add_u32_sdwa v0, s29, v33 dst_sel:DWORD dst_unused:UNUSED_PAD src0_sel:DWORD src1_sel:WORD_0\n\t" \
  "v_smfmac_f32_16x16x64_bf16 v[48:51], a[156:159], v[126:133], v159 abid:1\n\t" \
  "s_waitcnt lgkmcnt(3)\n\t" \
  "buffer_store_dwordx4 v[24:27], v34, s[20:23], s36 offen sc1\n\t" \
  "v_add_u32_sdwa v2, s29, v33 dst_sel:DWORD dst_unused:UNUSED_PAD src0_sel:DWORD src1_sel:WORD_1\n\t" \
  "v_smfmac_f32_16x16x64_bf16 v[52:55], a[160:163], v[126:133], v160\n\t" \
  "ds_read_b64 v[18:19], v18 offset:0\n\t" \
  "v_xad_u32 v24, v187, 64, s38\n\t" \
  "v_smfmac_f32_16x16x64_bf16 v[56:59], a[164:167], v[126:133], v160 abid:1\n\t" \
  "s_waitcnt lgkmcnt(2)\n\t" \
  "ds_read_b128 v[24:27], v24 offset:9216\n\t" \
  "v_add_u32 v28, s60, v187\n\t" \
  "v_smfmac_f32_16x16x64_bf16 v[52:55], v[88:91], v[8:15], v182 abid:1\n\t" \
  "ds_read_b64 v[20:21], v20 offset:0\n\t" \
  "v_add_u32_sdwa v8, s29, v120 dst_sel:DWORD dst_unused:UNUSED_PAD src0_sel:DWORD src1_sel:WORD_0\n\t" \
  "v_smfmac_f32_16x16x64_bf16 v[60:63], a[168:171], v[126:133], v161\n\t" \
  "ds_read_b64 v[22:23], v22 offset:0\n\t" \
  "v_add_u32_sdwa v10, s29, v120 dst_sel:DWORD dst_unused:UNUSED_PAD src0_sel:DWORD src1_sel:WORD_1\n\t" \
  "v_smfmac_f32_16x16x64_bf16 v[64:67], a[172:175], v[126:133], v161 abid:1\n\t" \
  "s_add_u32 s36, s36, 4608\n\t" \
  "ds_read_b64 v[0:1], v0 offset:0\n\t" \
  "v_add_u32_sdwa v12, s29, v121 dst_sel:DWORD dst_unused:UNUSED_PAD src0_sel:DWORD src1_sel:WORD_0\n\t" \
  "v_smfmac_f32_16x16x64_bf16 v[68:71], a[176:179], v[126:133], v180\n\t" \
  "s_waitcnt lgkmcnt(3)\n\t" \
  "buffer_store_dwordx4 v[24:27], v34, s[20:23], s36 offen sc1\n\t" \
  "v_add_u32_sdwa v14, s29, v121 dst_sel:DWORD dst_unused:UNUSED_PAD src0_sel:DWORD src1_sel:WORD_1\n\t" \
  "v_smfmac_f32_16x16x64_bf16 v[36:39], a[180:183], v[134:141], v162\n\t" \
  "ds_read_b128 v[126:129], v183 offset:12288\n\t" \
  "v_add_u32 v24, s38, v187\n\t" \
  "v_smfmac_f32_16x16x64_bf16 v[40:43], a[184:187], v[134:141], v162 abid:1\n\t" \
  "s_waitcnt lgkmcnt(2)\n\t" \
  "ds_read_b128 v[24:27], v24 offset:10240\n\t" \
  "v_smfmac_f32_16x16x64_bf16 v[56:59], v[92:95], v[16:23], v182 abid:1\n\t" \
  "ds_read_b128 v[130:133], v184 offset:12288\n\t" \
  "v_add_u32_sdwa v16, s29, v122 dst_sel:DWORD dst_unused:UNUSED_PAD src0_sel:DWORD src1_sel:WORD_0\n\t" \
  "v_smfmac_f32_16x16x64_bf16 v[44:47], a[188:191], v[134:141], v163\n\t" \
  "ds_read_b64 v[2:3], v2 offset:0\n\t" \
  "v_add_u32_sdwa v18, s29, v122 dst_sel:DWORD dst_unused:UNUSED_PAD src0_sel:DWORD src1_sel:WORD_1\n\t" \
  "v_smfmac_f32_16x16x64_bf16 v[48:51], a[192:195], v[134:141], v163 abid:1\n\t" \
  "s_add_u32 s36, s36, 4608\n\t" \
  "ds_read_b128 v[28:31], v28 offset:1280\n\t" \
  "v_add_u32_sdwa v20, s29, v123 dst_sel:DWORD dst_unused:UNUSED_PAD src0_sel:DWORD src1_sel:WORD_0\n\t" \
  "v_smfmac_f32_16x16x64_bf16 v[52:55], a[196:199], v[134:141], v164\n\t" \
  "s_waitcnt lgkmcnt(3)\n\t" \
  "buffer_store_dwordx4 v[24:27], v34, s[20:23], s36 offen sc1\n\t" \
  "v_add_u32_sdwa v22, s29, v123 dst_sel:DWORD dst_unused:UNUSED_PAD src0_sel:DWORD src1_sel:WORD_1\n\t" \
  "v_smfmac_f32_16x16x64_bf16 v[56:59], a[200:203], v[134:141], v164 abid:1\n\t" \
  "ds_read_b64 v[8:9], v8 offset:0\n\t" \
  "v_xad_u32 v24, v187, 64, s38\n\t" \
  "v_smfmac_f32_16x16x64_bf16 v[60:63], a[204:207], v[134:141], v165\n\t" \
  "s_add_u32 s38, s38, 18432\n\t" \
  "ds_read_b128 v[24:27], v24 offset:11264\n\t" \
  "v_smfmac_f32_16x16x64_bf16 v[64:67], a[208:211], v[134:141], v165 abid:1\n\t" \
  "s_waitcnt lgkmcnt(2)\n\t" \
  "ds_read_b64 v[10:11], v10 offset:0\n\t" \
  "s_set_gpr_idx_on s7, gpr_idx(DST)\n\t" \
  "v_smfmac_f32_16x16x64_bf16 v[36:39], v[28:31], v[0:7], v182 abid:1\n\t" \
  "s_set_gpr_idx_off\n\t" \
  "ds_read_b64 v[12:13], v12 offset:0\n\t" \
  "v_smfmac_f32_16x16x64_bf16 v[68:71], a[212:215], v[134:141], v180 abid:1\n\t" \
  "s_add_u32 s36, s36, 4608\n\t" \
  "ds_read_b128 v[134:137], v183 offset:14336\n\t" \
  "v_smfmac_f32_16x16x64_bf16 v[36:39], a[216:219], v[126:133], v166\n\t" \
  "s_waitcnt lgkmcnt(3)\n\t" \
  "buffer_store_dwordx4 v[24:27], v34, s[20:23], s36 offen sc1\n\t" \
  "v_smfmac_f32_16x16x64_bf16 v[40:43], a[220:223], v[126:133], v166 abid:1\n\t" \
  "s_sub_u32 s44, s38, 129024\n\t" \
  "ds_read_b128 v[138:141], v184 offset:14336\n\t" \
  "v_smfmac_f32_16x16x64_bf16 v[44:47], a[224:227], v[126:133], v167\n\t" \
  "s_min_u32 s38, s38, s44\n\t" \
  "ds_read_b64 v[14:15], v14 offset:0\n\t" \
  "v_smfmac_f32_16x16x64_bf16 v[48:51], a[228:231], v[126:133], v167 abid:1\n\t" \
  "s_add_u32 s35, s35, 4718592\n\t" \
  "ds_read_b64 v[16:17], v16 offset:0\n\t" \
  "v_smfmac_f32_16x16x64_bf16 v[52:55], a[232:235], v[126:133], v168\n\t" \
  "ds_read_b64 v[18:19], v18 offset:0\n\t" \
  "v_smfmac_f32_16x16x64_bf16 v[56:59], a[236:239], v[126:133], v168 abid:1\n\t" \
  "ds_read_b64 v[20:21], v20 offset:0\n\t" \
  "v_smfmac_f32_16x16x64_bf16 v[60:63], a[240:243], v[126:133], v169\n\t" \
  "s_waitcnt lgkmcnt(3)\n\t" \
  "ds_read_b64 v[22:23], v22 offset:0\n\t" \
  "v_smfmac_f32_16x16x64_bf16 v[60:63], v[96:99], v[8:15], v182 abid:1\n\t" \
  "v_add_u32_sdwa v8, s29, v35 dst_sel:DWORD dst_unused:UNUSED_PAD src0_sel:DWORD src1_sel:WORD_0\n\t" \
  "v_smfmac_f32_16x16x64_bf16 v[64:67], a[244:247], v[126:133], v169 abid:1\n\t" \
  "ds_read_b64 v[8:9], v8 offset:0\n\t" \
  "v_add_u32_sdwa v10, s29, v35 dst_sel:DWORD dst_unused:UNUSED_PAD src0_sel:DWORD src1_sel:WORD_1\n\t" \
  "v_smfmac_f32_16x16x64_bf16 v[68:71], a[248:251], v[126:133], v181\n\t" \
  "ds_read_b128 v[0:3], v183 offset:16384\n\t" \
  "ds_read_b64 v[10:11], v10 offset:0\n\t" \
  "v_add_u32 v28, s60, v187\n\t" \
  "v_smfmac_f32_16x16x64_bf16 v[36:39], a[252:255], v[134:141], v170\n\t" \
  "s_waitcnt lgkmcnt(3)\n\t" \
  "ds_read_b128 v[4:7], v184 offset:16384\n\t" \
  "v_add_u32 v183, s30, v183\n\t" \
  "v_smfmac_f32_16x16x64_bf16 v[64:67], v[100:103], v[16:23], v182 abid:1\n\t" \
  "ds_read_b128 v[28:31], v28 offset:2560\n\t" \
  "v_add_u32_sdwa v16, s29, v124 dst_sel:DWORD dst_unused:UNUSED_PAD src0_sel:DWORD src1_sel:WORD_0\n\t" \
  "v_smfmac_f32_16x16x64_bf16 v[40:43], v[188:191], v[134:141], v170 abid:1\n\t" \
  "ds_read_b64 v[16:17], v16 offset:0\n\t" \
  "v_add_u32_sdwa v18, s29, v124 dst_sel:DWORD dst_unused:UNUSED_PAD src0_sel:DWORD src1_sel:WORD_1\n\t" \
  "v_smfmac_f32_16x16x64_bf16 v[44:47], v[192:195], v[134:141], v171\n\t" \
  "ds_read_b64 v[18:19], v18 offset:0\n\t" \
  "v_add_u32_sdwa v20, s29, v125 dst_sel:DWORD dst_unused:UNUSED_PAD src0_sel:DWORD src1_sel:WORD_0\n\t" \
  "v_smfmac_f32_16x16x64_bf16 v[48:51], v[196:199], v[134:141], v171 abid:1\n\t" \
  "ds_read_b64 v[20:21], v20 offset:0\n\t" \
  "v_add_u32_sdwa v22, s29, v125 dst_sel:DWORD dst_unused:UNUSED_PAD src0_sel:DWORD src1_sel:WORD_1\n\t" \
  "v_smfmac_f32_16x16x64_bf16 v[52:55], v[200:203], v[134:141], v172\n\t" \
  "s_waitcnt lgkmcnt(3)\n\t" \
  "ds_read_b64 v[22:23], v22 offset:0\n\t" \
  "v_add_u32 v184, s30, v184\n\t" \
  "s_set_gpr_idx_on s8, gpr_idx(DST)\n\t" \
  "v_smfmac_f32_16x16x64_bf16 v[36:39], v[28:31], v[8:15], v182 abid:1\n\t" \
  "s_set_gpr_idx_off\n\t" \
  "v_smfmac_f32_16x16x64_bf16 v[56:59], v[204:207], v[134:141], v172 abid:1\n\t" \
  "s_add_u32 s29, s29, s30\n\t" \
  "v_smfmac_f32_16x16x64_bf16 v[60:63], v[208:211], v[134:141], v173\n\t" \
  "v_smfmac_f32_16x16x64_bf16 v[64:67], v[212:215], v[134:141], v173 abid:1\n\t" \
  "s_waitcnt lgkmcnt(0)\n\t" \
  "v_smfmac_f32_16x16x64_bf16 v[68:71], v[104:107], v[16:23], v182 abid:1\n\t" \
  "v_smfmac_f32_16x16x64_bf16 v[68:71], v[216:219], v[134:141], v181 abid:1\n\t" \
  "s_setpc_b64 s[66:67]\n\t" \
  ".fill (.Lvar%=+36864-.)/4, 4, 0xbf800000\n\t" \
  "s_branch .Lfin%=\n\t" \
  "s_branch .Lhe%=\n\t" \
  "s_waitcnt vmcnt(6)\n\t" \
  "s_waitcnt vmcnt(10)\n\t" \
  "s_waitcnt vmcnt(12)\n\t" \
  "s_waitcnt vmcnt(17)\n\t" \
  "s_waitcnt vmcnt(21) lgkmcnt(0)\n\t" \
  "s_barrier\n\t" \
  "v_smfmac_f32_16x16x64_bf16 v[36:39], v[220:223], v[0:7], v174\n\t" \
  "ds_read_b128 v[126:129], v183 offset:0\n\t" \
  "v_add_u32 v24, s37, v185\n\t" \
  "v_smfmac_f32_16x16x64_bf16 v[40:43], v[224:227], v[0:7], v174 abid:1\n\t" \
  "ds_read_b128 v[130:133], v184 offset:0\n\t" \
  "v_xor_b32 v25, s41, v183\n\t" \
  "v_smfmac_f32_16x16x64_bf16 v[44:47], v[228:231], v[0:7], v175\n\t" \
  "ds_read_b128 v[134:137], v183 offset:2048\n\t" \
  "v_subrev_u32 v25, s30, v25\n\t" \
  "v_smfmac_f32_16x16x64_bf16 v[48:51], v[232:235], v[0:7], v175 abid:1\n\t" \
  "s_add_u32 s30, s29, 18432\n\t" \
  "ds_read_b128 v[138:141], v184 offset:2048\n\t" \
  "v_cvt_pk_bf16_f32 v8, v36, v37\n\t" \
  "v_cvt_pk_bf16_f32 v9, v38, v39\n\t" \
  "v_smfmac_f32_16x16x64_bf16 v[52:55], v[236:239], v[0:7], v176\n\t" \
  "ds_read_b128 v[36:39], v35 offset:0\n\t" \
  "v_cvt_pk_bf16_f32 v10, v40, v41\n\t" \
  "v_cvt_pk_bf16_f32 v11, v42, v43\n\t" \
  "v_smfmac_f32_16x16x64_bf16 v[56:59], v[240:243], v[0:7], v176 abid:1\n\t" \
  "s_sub_u32 s25, s30, 129024\n\t" \
  "ds_read_b128 v[40:43], v35 offset:64\n\t" \
  "v_cvt_pk_bf16_f32 v16, v44, v45\n\t" \
  "v_cvt_pk_bf16_f32 v17, v46, v47\n\t" \
  "v_smfmac_f32_16x16x64_bf16 v[60:63], v[244:247], v[0:7], v177\n\t" \
  "ds_read_b128 v[44:47], v35 offset:128\n\t" \
  "v_cvt_pk_bf16_f32 v18, v48, v49\n\t" \
  "v_cvt_pk_bf16_f32 v19, v50, v51\n\t" \
  "v_smfmac_f32_16x16x64_bf16 v[64:67], v[248:251], v[0:7], v177 abid:1\n\t" \
  "s_min_u32 s30, s30, s25\n\t" \
  "ds_read_b128 v[48:51], v35 offset:192\n\t" \
  "v_cvt_pk_bf16_f32 v12, v52, v53\n\t" \
  "v_cvt_pk_bf16_f32 v13, v54, v55\n\t" \
  "v_smfmac_f32_16x16x64_bf16 v[68:71], v[252:255], v[0:7], v182\n\t" \
  "s_waitcnt lgkmcnt(3)\n\t" \
  "ds_read_b128 v[52:55], v35 offset:256\n\t" \
  "v_cvt_pk_bf16_f32 v14, v56, v57\n\t" \
  "v_cvt_pk_bf16_f32 v15, v58, v59\n\t" \
  "v_smfmac_f32_16x16x64_bf16 v[36:39], a[0:3], v[126:133], v142\n\t" \
  "s_waitcnt lgkmcnt(3)\n\t" \
  "ds_read_b128 v[56:59], v35 offset:320\n\t" \
  "v_cvt_pk_bf16_f32 v20, v60, v61\n\t" \
  "v_cvt_pk_bf16_f32 v21, v62, v63\n\t" \
  "v_smfmac_f32_16x16x64_bf16 v[40:43], a[4:7], v[126:133], v142 abid:1\n\t" \
  "s_waitcnt lgkmcnt(3)\n\t" \
  "ds_read_b128 v[60:63], v35 offset:384\n\t" \
  "v_cvt_pk_bf16_f32 v22, v64, v65\n\t" \
  "v_cvt_pk_bf16_f32 v23, v66, v67\n\t" \
  "v_smfmac_f32_16x16x64_bf16 v[44:47], a[8:11], v[126:133], v143\n\t" \
  "s_waitcnt lgkmcnt(3)\n\t" \
  "ds_read_b128 v[64:67], v35 offset:448\n\t" \
  "v_cvt_pk_bf16_f32 v26, v68, v69\n\t" \
  "v_cvt_pk_bf16_f32 v27, v70, v71\n\t" \
  "v_smfmac_f32_16x16x64_bf16 v[48:51], a[12:15], v[126:133], v143 abid:1\n\t" \
  "s_waitcnt lgkmcnt(3)\n\t" \
  "ds_read_b128 v[68:71], v35 offset:512\n\t" \
  "v_add_u32_sdwa v0, s29, v108 dst_sel:DWORD dst_unused:UNUSED_PAD src0_sel:DWORD src1_sel:WORD_0\n\t" \
  "v_smfmac_f32_16x16x64_bf16 v[52:55], a[16:19], v[126:133], v144\n\t" \
  "s_waitcnt lgkmcnt(3)\n\t" \
  "ds_read_b64 v[0:1], v0 offset:0\n\t" \
  "v_add_u32_sdwa v2, s29, v108 dst_sel:DWORD dst_unused:UNUSED_PAD src0_sel:DWORD src1_sel:WORD_1\n\t" \
  "v_smfmac_f32_16x16x64_bf16 v[56:59], a[20:23], v[126:133], v144 abid:1\n\t" \
  "s_waitcnt lgkmcnt(3)\n\t" \
  "ds_write_b64 v24, v[8:9] offset:0\n\t" \
  "v_add_u32_sdwa v4, s29, v109 dst_sel:DWORD dst_unused:UNUSED_PAD src0_sel:DWORD src1_sel:WORD_0\n\t" \
  "v_smfmac_f32_16x16x64_bf16 v[60:63], a[24:27], v[126:133], v145\n\t" \
  "s_waitcnt lgkmcnt(3)\n\t" \
  "ds_read_b64 v[2:3], v2 offset:0\n\t" \
  "v_add_u32_sdwa v6, s29, v109 dst_sel:DWORD dst_unused:UNUSED_PAD src0_sel:DWORD src1_sel:WORD_1\n\t" \
  "v_smfmac_f32_16x16x64_bf16 v[64:67], a[28:31], v[126:133], v145 abid:1\n\t" \
  "s_waitcnt lgkmcnt(3)\n\t" \
  "ds_write_b64 v24, v[10:11] offset:8\n\t" \
  "v_smfmac_f32_16x16x64_bf16 v[68:71], a[32:35], v[126:133], v178\n\t" \
  "s_sub_u32 s30, s30, s29\n\t" \
  "s_bfe_i32 s47, s48, 0x10000\n\t" \
  "ds_read_b128 v[126:129], v183 offset:4096\n\t" \
  "v_add_u32 v28, s38, v187\n\t" \
  "v_smfmac_f32_16x16x64_bf16 v[36:39], a[36:39], v[134:141], v146\n\t" \
  "s_mov_b32 s37, s29\n\t" \
  "s_lshr_b32 s48, s48, 1\n\t" \
  "ds_write_b64 v24, v[12:13] offset:128\n\t" \
  "ds_read_b128 v[28:31], v28 offset:12288\n\t" \
  "v_smfmac_f32_16x16x64_bf16 v[40:43], a[40:43], v[134:141], v146 abid:1\n\t" \
  "s_add_u32 s36, s35, s39\n\t" \
  "ds_read_b128 v[130:133], v184 offset:4096\n\t" \
  "v_smfmac_f32_16x16x64_bf16 v[44:47], a[44:47], v[134:141], v147\n\t" \
  "s_andn2_b32 s44, 0x40000000, s47\n\t" \
  "ds_write_b64 v24, v[14:15] offset:136\n\t" \
  "v_xor_b32 v24, 64, v24\n\t" \
  "v_smfmac_f32_16x16x64_bf16 v[48:51], a[48:51], v[134:141], v147 abid:1\n\t" \
  "s_or_b32 s36, s36, s44\n\t" \
  "ds_read_b64 v[4:5], v4 offset:0\n\t" \
  "v_add_u32_sdwa v8, s29, v110 dst_sel:DWORD dst_unused:UNUSED_PAD src0_sel:DWORD src1_sel:WORD_0\n\t" \
  "v_smfmac_f32_16x16x64_bf16 v[52:55], a[52:55], v[134:141], v148\n\t" \
  "ds_write_b64 v24, v[16:17] offset:0\n\t" \
  "s_waitcnt lgkmcnt(4)\n\t" \
  "buffer_store_dwordx4 v[28:31], v34, s[20:23], s36 offen sc1\n\t" \
  "v_add_u32_sdwa v10, s29, v110 dst_sel:DWORD dst_unused:UNUSED_PAD src0_sel:DWORD src1_sel:WORD_1\n\t" \
  "v_smfmac_f32_16x16x64_bf16 v[56:59], a[56:59], v[134:141], v148 abid:1\n\t" \
  "s_and_b32 s44, s47, 4718592\n\t" \
  "ds_read_b64 v[6:7], v6 offset:0\n\t" \
  "v_add_u32_sdwa v12, s29, v111 dst_sel:DWORD dst_unused:UNUSED_PAD src0_sel:DWORD src1_sel:WORD_0\n\t" \
  "v_xad_u32 v28, v187, 64, s38\n\t" \
  "v_smfmac_f32_16x16x64_bf16 v[60:63], a[60:63], v[134:141], v149\n\t" \
  "s_and_b32 s27, s64, 7\n\t" \
  "s_add_u32 s35, s35, s44\n\t" \
  "ds_write_b64 v24, v[18:19] offset:8\n\t" \
  "ds_read_b128 v[28:31], v28 offset:13312\n\t" \
  "v_add_u32_sdwa v14, s29, v111 dst_sel:DWORD dst_unused:UNUSED_PAD src0_sel:DWORD src1_sel:WORD_1\n\t" \
  "v_smfmac_f32_16x16x64_bf16 v[64:67], a[64:67], v[134:141], v149 abid:1\n\t" \
  "s_lshl2_add_u32 s66, s27, s68\n\t" \
  "s_add_u32 s36, s36, 4608\n\t" \
  "ds_read_b64 v[8:9], v8 offset:0\n\t" \
  "v_smfmac_f32_16x16x64_bf16 v[68:71], a[68:71], v[134:141], v178 abid:1\n\t" \
  "s_waitcnt lgkmcnt(6)\n\t" \
  "ds_write_b64 v24, v[20:21] offset:128\n\t" \
  "v_smfmac_f32_16x16x64_bf16 v[36:39], a[72:75], v[126:133], v150\n\t" \
  "s_waitcnt lgkmcnt(3)\n\t" \
  "ds_read_b128 v[134:137], v183 offset:6144\n\t" \
  "v_smfmac_f32_16x16x64_bf16 v[36:39], v[72:75], v[0:7], v182 abid:1\n\t" \
  "s_and_b32 s27, s64, 8\n\t" \
  "ds_write_b64 v24, v[22:23] offset:136\n\t" \
  "s_waitcnt lgkmcnt(4)\n\t" \
  "buffer_store_dwordx4 v[28:31], v34, s[20:23], s36 offen sc1\n\t" \
  "v_add_u32_sdwa v0, s29, v112 dst_sel:DWORD dst_unused:UNUSED_PAD src0_sel:DWORD src1_sel:WORD_0\n\t" \
  "v_smfmac_f32_16x16x64_bf16 v[40:43], a[76:79], v[126:133], v150 abid:1\n\t" \
  "s_mul_i32 s27, s27, 176\n\t" \
  "s_and_b32 s44, s47, 18432\n\t" \
  "ds_read_b128 v[138:141], v184 offset:6144\n\t" \
  "v_add_u32_sdwa v2, s29, v112 dst_sel:DWORD dst_unused:UNUSED_PAD src0_sel:DWORD src1_sel:WORD_1\n\t" \
  "v_add_u32 v28, s38, v187\n\t" \
  "v_smfmac_f32_16x16x64_bf16 v[44:47], a[80:83], v[126:133], v151\n\t" \
  "s_add_u32 s66, s66, s27\n\t" \
  "ds_write_b64 v25, v[26:27] offset:16384\n\t" \
  "ds_read_b128 v[28:31], v28 offset:14336\n\t" \
  "v_add_u32_sdwa v4, s29, v113 dst_sel:DWORD dst_unused:UNUSED_PAD src0_sel:DWORD src1_sel:WORD_0\n\t" \
  "v_smfmac_f32_16x16x64_bf16 v[48:51], a[84:87], v[126:133], v151 abid:1\n\t" \
  "s_lshr_b64 s[64:65], s[64:65], 4\n\t" \
  "s_add_u32 s36, s36, 4608\n\t" \
  "ds_read_b64 v[10:11], v10 offset:0\n\t" \
  "v_add_u32_sdwa v6, s29, v113 dst_sel:DWORD dst_unused:UNUSED_PAD src0_sel:DWORD src1_sel:WORD_1\n\t" \
  "v_smfmac_f32_16x16x64_bf16 v[52:55], a[88:91], v[126:133], v152\n\t" \
  "ds_read_b64 v[12:13], v12 offset:0\n\t" \
  "v_add_u32_sdwa v16, s29, v114 dst_sel:DWORD dst_unused:UNUSED_PAD src0_sel:DWORD src1_sel:WORD_0\n\t" \
  "v_smfmac_f32_16x16x64_bf16 v[56:59], a[92:95], v[126:133], v152 abid:1\n\t" \
  "ds_read_b64 v[14:15], v14 offset:0\n\t" \
  "v_add_u32_sdwa v18, s29, v114 dst_sel:DWORD dst_unused:UNUSED_PAD src0_sel:DWORD src1_sel:WORD_1\n\t" \
  "v_smfmac_f32_16x16x64_bf16 v[60:63], a[96:99], v[126:133], v153\n\t" \
  "ds_read_b64 v[0:1], v0 offset:0\n\t" \
  "s_waitcnt lgkmcnt(4)\n\t" \
  "buffer_store_dwordx4 v[28:31], v34, s[20:23], s36 offen sc1\n\t" \
  "v_add_u32_sdwa v20, s29, v115 dst_sel:DWORD dst_unused:UNUSED_PAD src0_sel:DWORD src1_sel:WORD_0\n\t" \
  "v_smfmac_f32_16x16x64_bf16 v[64:67], a[100:103], v[126:133], v153 abid:1\n\t" \
  "ds_read_b64 v[2:3], v2 offset:0\n\t" \
  "v_add_u32_sdwa v22, s29, v115 dst_sel:DWORD dst_unused:UNUSED_PAD src0_sel:DWORD src1_sel:WORD_1\n\t" \
  "v_xad_u32 v28, v187, 64, s38\n\t" \
  "v_smfmac_f32_16x16x64_bf16 v[68:71], a[104:107], v[126:133], v179\n\t" \
  "s_waitcnt lgkmcnt(6)\n\t" \
  "s_add_u32 s38, s38, s44\n\t" \
  "ds_read_b128 v[126:129], v183 offset:8192\n\t" \
  "ds_read_b128 v[28:31], v28 offset:15360\n\t" \
  "v_smfmac_f32_16x16x64_bf16 v[36:39], a[108:111], v[134:141], v154\n\t" \
  "s_waitcnt lgkmcnt(3)\n\t" \
  "s_add_u32 s36, s36, 4608\n\t" \
  "ds_read_b128 v[130:133], v184 offset:8192\n\t" \
  "v_smfmac_f32_16x16x64_bf16 v[40:43], v[76:79], v[8:15], v182 abid:1\n\t" \
  "s_sub_u32 s44, s38, 129024\n\t" \
  "ds_read_b64 v[4:5], v4 offset:0\n\t" \
  "v_smfmac_f32_16x16x64_bf16 v[40:43], a[112:115], v[134:141], v154 abid:1\n\t" \
  "s_min_u32 s38, s38, s44\n\t" \
  "ds_read_b64 v[6:7], v6 offset:0\n\t" \
  "v_smfmac_f32_16x16x64_bf16 v[44:47], a[116:119], v[134:141], v155\n\t" \
  "ds_read_b64 v[16:17], v16 offset:0\n\t" \
  "s_waitcnt lgkmcnt(4)\n\t" \
  "buffer_store_dwordx4 v[28:31], v34, s[20:23], s36 offen sc1\n\t" \
  "v_smfmac_f32_16x16x64_bf16 v[48:51], a[120:123], v[134:141], v155 abid:1\n\t" \
  "ds_read_b64 v[18:19], v18 offset:0\n\t" \
  "v_add_u32_sdwa v8, s29, v32 dst_sel:DWORD dst_unused:UNUSED_PAD src0_sel:DWORD src1_sel:WORD_0\n\t" \
  "v_smfmac_f32_16x16x64_bf16 v[52:55], a[124:127], v[134:141], v156\n\t" \
  "s_branch .Ljn31%=\n\t" \
  ".fill (.Lvar%=+38268-.)/4, 4, 0xbf800000\n\t" \
  "s_nop 0\n\t" \
  "s_branch .Lfin%=\n\t" \
  "s_branch .Lhe%=\n\t" \
  "s_waitcnt vmcnt(6)\n\t" \
  "s_waitcnt vmcnt(10)\n\t" \
  "s_waitcnt vmcnt(12)\n\t" \
  "s_waitcnt vmcnt(17)\n\t" \
  "s_waitcnt vmcnt(21) lgkmcnt(0)\n\t" \
  "s_barrier\n\t" \
  "v_smfmac_f32_16x16x64_bf16 v[36:39], v[220:223], v[0:7], v174\n\t" \
  "s_add_u32 m0, s33, 0\n\t" \
  "ds_read_b128 v[126:129], v183 offset:0\n\t" \
  "v_add_u32 v24, s37, v185\n\t" \
  "v_smfmac_f32_16x16x64_bf16 v[40:43], v[224:227], v[0:7], v174 abid:1\n\t" \
  "buffer_load_dwordx4 v186, s[16:19], s34 offen offset:0 nt lds\n\t" \
  "ds_read_b128 v[130:133], v184 offset:0\n\t" \
  "v_xor_b32 v25, s41, v183\n\t" \
  "v_smfmac_f32_16x16x64_bf16 v[44:47], v[228:231], v[0:7], v175\n\t" \
  "s_add_u32 m0, s33, 1920\n\t" \
  "ds_read_b128 v[134:137], v183 offset:2048\n\t" \
  "v_subrev_u32 v25, s30, v25\n\t" \
  "v_smfmac_f32_16x16x64_bf16 v[48:51], v[232:235], v[0:7], v175 abid:1\n\t" \
  "s_add_u32 s30, s29, 18432\n\t" \
  "buffer_load_dwordx4 v186, s[16:19], s34 offen offset:128 nt lds\n\t" \
  "ds_read_b128 v[138:141], v184 offset:2048\n\t" \
  "v_cvt_pk_bf16_f32 v8, v36, v37\n\t" \
  "v_cvt_pk_bf16_f32 v9, v38, v39\n\t" \
  "v_smfmac_f32_16x16x64_bf16 v[52:55], v[236:239], v[0:7], v176\n\t" \
  "s_add_u32 m0, s33, 3840\n\t" \
  "ds_read_b128 v[36:39], v35 offset:0\n\t" \
  "v_cvt_pk_bf16_f32 v10, v40, v41\n\t" \
  "v_cvt_pk_bf16_f32 v11, v42, v43\n\t" \
  "v_smfmac_f32_16x16x64_bf16 v[56:59], v[240:243], v[0:7], v176 abid:1\n\t" \
  "s_sub_u32 s25, s30, 129024\n\t" \
  "buffer_load_dwordx4 v186, s[16:19], s34 offen offset:256 nt lds\n\t" \
  "ds_read_b128 v[40:43], v35 offset:64\n\t" \
  "v_cvt_pk_bf16_f32 v16, v44, v45\n\t" \
  "v_cvt_pk_bf16_f32 v17, v46, v47\n\t" \
  "v_smfmac_f32_16x16x64_bf16 v[60:63], v[244:247], v[0:7], v177\n\t" \
  "s_add_u32 m0, s33, 5760\n\t" \
  "ds_read_b128 v[44:47], v35 offset:128\n\t" \
  "v_cvt_pk_bf16_f32 v18, v48, v49\n\t" \
  "v_cvt_pk_bf16_f32 v19, v50, v51\n\t" \
  "v_smfmac_f32_16x16x64_bf16 v[64:67], v[248:251], v[0:7], v177 abid:1\n\t" \
  "s_min_u32 s30, s30, s25\n\t" \
  "buffer_load_dwordx4 v186, s[16:19], s34 offen offset:384 nt lds\n\t" \
  "ds_read_b128 v[48:51], v35 offset:192\n\t" \
  "v_cvt_pk_bf16_f32 v12, v52, v53\n\t" \
  "v_cvt_pk_bf16_f32 v13, v54, v55\n\t" \
  "v_smfmac_f32_16x16x64_bf16 v[68:71], v[252:255], v[0:7], v182\n\t" \
  "s_waitcnt lgkmcnt(3)\n\t" \
  "ds_read_b128 v[52:55], v35 offset:256\n\t" \
  "v_cvt_pk_bf16_f32 v14, v56, v57\n\t" \
  "v_cvt_pk_bf16_f32 v15, v58, v59\n\t" \
  "v_smfmac_f32_16x16x64_bf16 v[36:39], a[0:3], v[126:133], v142\n\t" \
  "s_waitcnt lgkmcnt(3)\n\t" \
  "ds_read_b128 v[56:59], v35 offset:320\n\t" \
  "v_cvt_pk_bf16_f32 v20, v60, v61\n\t" \
  "v_cvt_pk_bf16_f32 v21, v62, v63\n\t" \
  "v_smfmac_f32_16x16x64_bf16 v[40:43], a[4:7], v[126:133], v142 abid:1\n\t" \
  "s_waitcnt lgkmcnt(3)\n\t" \
  "ds_read_b128 v[60:63], v35 offset:384\n\t" \
  "v_cvt_pk_bf16_f32 v22, v64, v65\n\t" \
  "v_cvt_pk_bf16_f32 v23, v66, v67\n\t" \
  "v_smfmac_f32_16x16x64_bf16 v[44:47], a[8:11], v[126:133], v143\n\t" \
  "s_waitcnt lgkmcnt(3)\n\t" \
  "ds_read_b128 v[64:67], v35 offset:448\n\t" \
  "v_cvt_pk_bf16_f32 v26, v68, v69\n\t" \
  "v_cvt_pk_bf16_f32 v27, v70, v71\n\t" \
  "v_smfmac_f32_16x16x64_bf16 v[48:51], a[12:15], v[126:133], v143 abid:1\n\t" \
  "s_waitcnt lgkmcnt(3)\n\t" \
  "ds_read_b128 v[68:71], v35 offset:512\n\t" \
  "v_add_u32_sdwa v0, s29, v108 dst_sel:DWORD dst_unused:UNUSED_PAD src0_sel:DWORD src1_sel:WORD_0\n\t" \
  "v_smfmac_f32_16x16x64_bf16 v[52:55], a[16:19], v[126:133], v144\n\t" \
  "s_waitcnt lgkmcnt(3)\n\t" \
  "ds_read_b64 v[0:1], v0 offset:0\n\t" \
  "v_add_u32_sdwa v2, s29, v108 dst_sel:DWORD dst_unused:UNUSED_PAD src0_sel:DWORD src1_sel:WORD_1\n\t" \
  "v_smfmac_f32_16x16x64_bf16 v[56:59], a[20:23], v[126:133], v144 abid:1\n\t" \
  "s_waitcnt lgkmcnt(3)\n\t" \
  "ds_write_b64 v24, v[8:9] offset:0\n\t" \
  "v_add_u32_sdwa v4, s29, v109 dst_sel:DWORD dst_unused:UNUSED_PAD src0_sel:DWORD src1_sel:WORD_0\n\t" \
  "v_smfmac_f32_16x16x64_bf16 v[60:63], a[24:27], v[126:133], v145\n\t" \
  "s_waitcnt lgkmcnt(3)\n\t" \
  "ds_read_b64 v[2:3], v2 offset:0\n\t" \
  "v_add_u32_sdwa v6, s29, v109 dst_sel:DWORD dst_unused:UNUSED_PAD src0_sel:DWORD src1_sel:WORD_1\n\t" \
  "v_smfmac_f32_16x16x64_bf16 v[64:67], a[28:31], v[126:133], v145 abid:1\n\t" \
  "s_waitcnt lgkmcnt(3)\n\t" \
  "ds_write_b64 v24, v[10:11] offset:8\n\t" \
  "v_smfmac_f32_16x16x64_bf16 v[68:71], a[32:35], v[126:133], v178\n\t" \
  "s_sub_u32 s30, s30, s29\n\t" \
  "ds_read_b128 v[126:129], v183 offset:4096\n\t" \
  "v_smfmac_f32_16x16x64_bf16 v[36:39], a[36:39], v[134:141], v146\n\t" \
  "s_mov_b32 s37, s29\n\t" \
  "ds_write_b64 v24, v[12:13] offset:128\n\t" \
  "v_smfmac_f32_16x16x64_bf16 v[40:43], a[40:43], v[134:141], v146 abid:1\n\t" \
  "s_add_u32 s31, s31, 18432\n\t" \
  "ds_read_b128 v[130:133], v184 offset:4096\n\t" \
  "v_smfmac_f32_16x16x64_bf16 v[44:47], a[44:47], v[134:141], v147\n\t" \
  "s_sub_u32 s26, s31, 129024\n\t" \
  "ds_write_b64 v24, v[14:15] offset:136\n\t" \
  "v_xor_b32 v24, 64, v24\n\t" \
  "v_smfmac_f32_16x16x64_bf16 v[48:51], a[48:51], v[134:141], v147 abid:1\n\t" \
  "s_min_u32 s31, s31, s26\n\t" \
  "ds_read_b64 v[4:5], v4 offset:0\n\t" \
  "v_add_u32_sdwa v8, s29, v110 dst_sel:DWORD dst_unused:UNUSED_PAD src0_sel:DWORD src1_sel:WORD_0\n\t" \
  "v_smfmac_f32_16x16x64_bf16 v[52:55], a[52:55], v[134:141], v148\n\t" \
  "s_add_u32 s33, s31, s32\n\t" \
  "ds_write_b64 v24, v[16:17] offset:0\n\t" \
  "v_add_u32_sdwa v10, s29, v110 dst_sel:DWORD dst_unused:UNUSED_PAD src0_sel:DWORD src1_sel:WORD_1\n\t" \
  "v_smfmac_f32_16x16x64_bf16 v[56:59], a[56:59], v[134:141], v148 abid:1\n\t" \
  "s_add_u32 s34, s34, 4718592\n\t" \
  "ds_read_b64 v[6:7], v6 offset:0\n\t" \
  "v_add_u32_sdwa v12, s29, v111 dst_sel:DWORD dst_unused:UNUSED_PAD src0_sel:DWORD src1_sel:WORD_0\n\t" \
  "v_smfmac_f32_16x16x64_bf16 v[60:63], a[60:63], v[134:141], v149\n\t" \
  "s_and_b32 s27, s64, 7\n\t" \
  "ds_write_b64 v24, v[18:19] offset:8\n\t" \
  "v_add_u32_sdwa v14, s29, v111 dst_sel:DWORD dst_unused:UNUSED_PAD src0_sel:DWORD src1_sel:WORD_1\n\t" \
  "v_smfmac_f32_16x16x64_bf16 v[64:67], a[64:67], v[134:141], v149 abid:1\n\t" \
  "s_lshl2_add_u32 s66, s27, s68\n\t" \
  "ds_read_b64 v[8:9], v8 offset:0\n\t" \
  "v_smfmac_f32_16x16x64_bf16 v[68:71], a[68:71], v[134:141], v178 abid:1\n\t" \
  "s_waitcnt lgkmcnt(6)\n\t" \
  "ds_write_b64 v24, v[20:21] offset:128\n\t" \
  "v_smfmac_f32_16x16x64_bf16 v[36:39], a[72:75], v[126:133], v150\n\t" \
  "s_waitcnt lgkmcnt(3)\n\t" \
  "ds_read_b128 v[134:137], v183 offset:6144\n\t" \
  "v_smfmac_f32_16x16x64_bf16 v[36:39], v[72:75], v[0:7], v182 abid:1\n\t" \
  "s_and_b32 s27, s64, 8\n\t" \
  "ds_write_b64 v24, v[22:23] offset:136\n\t" \
  "v_add_u32_sdwa v0, s29, v112 dst_sel:DWORD dst_unused:UNUSED_PAD src0_sel:DWORD src1_sel:WORD_0\n\t" \
  "v_smfmac_f32_16x16x64_bf16 v[40:43], a[76:79], v[126:133], v150 abid:1\n\t" \
  "s_mul_i32 s27, s27, 176\n\t" \
  "ds_read_b128 v[138:141], v184 offset:6144\n\t" \
  "v_add_u32_sdwa v2, s29, v112 dst_sel:DWORD dst_unused:UNUSED_PAD src0_sel:DWORD src1_sel:WORD_1\n\t" \
  "v_smfmac_f32_16x16x64_bf16 v[44:47], a[80:83], v[126:133], v151\n\t" \
  "s_add_u32 s66, s66, s27\n\t" \
  "ds_write_b64 v25, v[26:27] offset:16384\n\t" \
  "v_add_u32_sdwa v4, s29, v113 dst_sel:DWORD dst_unused:UNUSED_PAD src0_sel:DWORD src1_sel:WORD_0\n\t" \
  "v_smfmac_f32_16x16x64_bf16 v[48:51], a[84:87], v[126:133], v151 abid:1\n\t" \
  "s_lshr_b64 s[64:65], s[64:65], 4\n\t" \
  "ds_read_b64 v[10:11], v10 offset:0\n\t" \
  "v_add_u32_sdwa v6, s29, v113 dst_sel:DWORD dst_unused:UNUSED_PAD src0_sel:DWORD src1_sel:WORD_1\n\t" \
  "v_smfmac_f32_16x16x64_bf16 v[52:55], a[88:91], v[126:133], v152\n\t" \
  "ds_read_b64 v[12:13], v12 offset:0\n\t" \
  "v_add_u32_sdwa v16, s29, v114 dst_sel:DWORD dst_unused:UNUSED_PAD src0_sel:DWORD src1_sel:WORD_0\n\t" \
  "v_smfmac_f32_16x16x64_bf16 v[56:59], a[92:95], v[126:133], v152 abid:1\n\t" \
  "ds_read_b64 v[14:15], v14 offset:0\n\t" \
  "v_add_u32_sdwa v18, s29, v114 dst_sel:DWORD dst_unused:UNUSED_PAD src0_sel:DWORD src1_sel:WORD_1\n\t" \
  "v_smfmac_f32_16x16x64_bf16 v[60:63], a[96:99], v[126:133], v153\n\t" \
  "ds_read_b64 v[0:1], v0 offset:0\n\t" \
  "v_add_u32_sdwa v20, s29, v115 dst_sel:DWORD dst_unused:UNUSED_PAD src0_sel:DWORD src1_sel:WORD_0\n\t" \
  "v_smfmac_f32_16x16x64_bf16 v[64:67], a[100:103], v[126:133], v153 abid:1\n\t" \
  "ds_read_b64 v[2:3], v2 offset:0\n\t" \
  "v_add_u32_sdwa v22, s29, v115 dst_sel:DWORD dst_unused:UNUSED_PAD src0_sel:DWORD src1_sel:WORD_1\n\t" \
  "v_smfmac_f32_16x16x64_bf16 v[68:71], a[104:107], v[126:133], v179\n\t" \
  "s_waitcnt lgkmcnt(6)\n\t" \
  "ds_read_b128 v[126:129], v183 offset:8192\n\t" \
  "v_smfmac_f32_16x16x64_bf16 v[36:39], a[108:111], v[134:141], v154\n\t" \
  "s_waitcnt lgkmcnt(3)\n\t" \
  "ds_read_b128 v[130:133], v184 offset:8192\n\t" \
  "v_smfmac_f32_16x16x64_bf16 v[40:43], v[76:79], v[8:15], v182 abid:1\n\t" \
  "ds_read_b64 v[4:5], v4 offset:0\n\t" \
  "v_smfmac_f32_16x16x64_bf16 v[40:43], a[112:115], v[134:141], v154 abid:1\n\t" \
  "ds_read_b64 v[6:7], v6 offset:0\n\t" \
  "v_smfmac_f32_16x16x64_bf16 v[44:47], a[116:119], v[134:141], v155\n\t" \
  "ds_read_b64 v[16:17], v16 offset:0\n\t" \
  "v_smfmac_f32_16x16x64_bf16 v[48:51], a[120:123], v[134:141], v155 abid:1\n\t" \
  "ds_read_b64 v[18:19], v18 offset:0\n\t" \
  "v_add_u32_sdwa v8, s29, v32 dst_sel:DWORD dst_unused:UNUSED_PAD src0_sel:DWORD src1_sel:WORD_0\n\t" \
  "v_smfmac_f32_16x16x64_bf16 v[52:55], a[124:127], v[134:141], v156\n\t" \
  ".Ljn31%=:\n\t" \
  "ds_read_b64 v[20:21], v20 offset:0\n\t" \
  "v_add_u32_sdwa v10, s29, v32 dst_sel:DWORD dst_unused:UNUSED_PAD src0_sel:DWORD src1_sel:WORD_1\n\t" \
  "v_smfmac_f32_16x16x64_bf16 v[56:59], a[128:131], v[134:141], v156 abid:1\n\t" \
  "s_waitcnt lgkmcnt(3)\n\t" \
  "ds_read_b64 v[22:23], v22 offset:0\n\t" \
  "v_add_u32 v28, s60, v187\n\t" \
  "v_smfmac_f32_16x16x64_bf16 v[44:47], v[80:83], v[0:7], v182 abid:1\n\t" \
  "ds_read_b64 v[8:9], v8 offset:0\n\t" \
  "v_add_u32_sdwa v0, s29, v116 dst_sel:DWORD dst_unused:UNUSED_PAD src0_sel:DWORD src1_sel:WORD_0\n\t" \
  "v_smfmac_f32_16x16x64_bf16 v[60:63], a[132:135], v[134:141], v157\n\t" \
  "ds_read_b64 v[10:11], v10 offset:0\n\t" \
  "v_add_u32_sdwa v2, s29, v116 dst_sel:DWORD dst_unused:UNUSED_PAD src0_sel:DWORD src1_sel:WORD_1\n\t" \
  "v_smfmac_f32_16x16x64_bf16 v[64:67], a[136:139], v[134:141], v157 abid:1\n\t" \
  "ds_read_b128 v[28:31], v28 offset:0\n\t" \
  "v_add_u32_sdwa v4, s29, v117 dst_sel:DWORD dst_unused:UNUSED_PAD src0_sel:DWORD src1_sel:WORD_0\n\t" \
  "v_smfmac_f32_16x16x64_bf16 v[68:71], a[140:143], v[134:141], v179 abid:1\n\t" \
  "s_waitcnt lgkmcnt(3)\n\t" \
  "ds_read_b128 v[134:137], v183 offset:10240\n\t" \
  "v_add_u32_sdwa v6, s29, v117 dst_sel:DWORD dst_unused:UNUSED_PAD src0_sel:DWORD src1_sel:WORD_1\n\t" \
  "v_smfmac_f32_16x16x64_bf16 v[48:51], v[84:87], v[16:23], v182 abid:1\n\t" \
  "ds_read_b128 v[138:141], v184 offset:10240\n\t" \
  "v_add_u32_sdwa v16, s29, v118 dst_sel:DWORD dst_unused:UNUSED_PAD src0_sel:DWORD src1_sel:WORD_0\n\t" \
  "v_smfmac_f32_16x16x64_bf16 v[36:39], a[144:147], v[126:133], v158\n\t" \
  "ds_read_b64 v[0:1], v0 offset:0\n\t" \
  "v_add_u32 v24, s38, v187\n\t" \
  "v_smfmac_f32_16x16x64_bf16 v[40:43], a[148:151], v[126:133], v158 abid:1\n\t" \
  "s_waitcnt lgkmcnt(3)\n\t" \
  "ds_read_b128 v[24:27], v24 offset:12288\n\t" \
  "v_add_u32_sdwa v18, s29, v118 dst_sel:DWORD dst_unused:UNUSED_PAD src0_sel:DWORD src1_sel:WORD_1\n\t" \
  "s_set_gpr_idx_on s6, gpr_idx(DST)\n\t" \
  "v_smfmac_f32_16x16x64_bf16 v[36:39], v[28:31], v[8:15], v182 abid:1\n\t" \
  "s_set_gpr_idx_off\n\t" \
  "ds_read_b64 v[2:3], v2 offset:0\n\t" \
  "v_add_u32_sdwa v20, s29, v119 dst_sel:DWORD dst_unused:UNUSED_PAD src0_sel:DWORD src1_sel:WORD_0\n\t" \
  "v_smfmac_f32_16x16x64_bf16 v[44:47], a[152:155], v[126:133], v159\n\t" \
  "ds_read_b64 v[4:5], v4 offset:0\n\t" \
  "v_add_u32_sdwa v22, s29, v119 dst_sel:DWORD dst_unused:UNUSED_PAD src0_sel:DWORD src1_sel:WORD_1\n\t" \
  "v_smfmac_f32_16x16x64_bf16 v[48:51], a[156:159], v[126:133], v159 abid:1\n\t" \
  "s_add_u32 s36, s35, s39\n\t" \
  "ds_read_b64 v[6:7], v6 offset:0\n\t" \
  "v_add_u32_sdwa v8, s29, v120 dst_sel:DWORD dst_unused:UNUSED_PAD src0_sel:DWORD src1_sel:WORD_0\n\t" \
  "v_smfmac_f32_16x16x64_bf16 v[52:55], a[160:163], v[126:133], v160\n\t" \
  "s_waitcnt lgkmcnt(3)\n\t" \
  "buffer_store_dwordx4 v[24:27], v34, s[20:23], s36 offen sc1\n\t" \
  "v_add_u32_sdwa v10, s29, v120 dst_sel:DWORD dst_unused:UNUSED_PAD src0_sel:DWORD src1_sel:WORD_1\n\t" \
  "v_smfmac_f32_16x16x64_bf16 v[56:59], a[164:167], v[126:133], v160 abid:1\n\t" \
  "ds_read_b64 v[16:17], v16 offset:0\n\t" \
  "v_xad_u32 v24, v187, 64, s38\n\t" \
  "v_smfmac_f32_16x16x64_bf16 v[60:63], a[168:171], v[126:133], v161\n\t" \
  "ds_read_b128 v[24:27], v24 offset:13312\n\t" \
  "v_add_u32_sdwa v12, s29, v121 dst_sel:DWORD dst_unused:UNUSED_PAD src0_sel:DWORD src1_sel:WORD_0\n\t" \
  "v_smfmac_f32_16x16x64_bf16 v[64:67], a[172:175], v[126:133], v161 abid:1\n\t" \
  "s_waitcnt lgkmcnt(2)\n\t" \
  "ds_read_b64 v[18:19], v18 offset:0\n\t" \
  "v_add_u32_sdwa v14, s29, v121 dst_sel:DWORD dst_unused:UNUSED_PAD src0_sel:DWORD src1_sel:WORD_1\n\t" \
  "v_smfmac_f32_16x16x64_bf16 v[52:55], v[88:91], v[0:7], v182 abid:1\n\t" \
  "ds_read_b64 v[20:21], v20 offset:0\n\t" \
  "v_add_u32_sdwa v0, s29, v122 dst_sel:DWORD dst_unused:UNUSED_PAD src0_sel:DWORD src1_sel:WORD_0\n\t" \
  "v_smfmac_f32_16x16x64_bf16 v[68:71], a[176:179], v[126:133], v180\n\t" \
  "s_add_u32 s36, s36, 4608\n\t" \
  "ds_read_b128 v[126:129], v183 offset:12288\n\t" \
  "v_add_u32_sdwa v2, s29, v122 dst_sel:DWORD dst_unused:UNUSED_PAD src0_sel:DWORD src1_sel:WORD_1\n\t" \
  "v_smfmac_f32_16x16x64_bf16 v[36:39], a[180:183], v[134:141], v162\n\t" \
  "s_waitcnt lgkmcnt(3)\n\t" \
  "buffer_store_dwordx4 v[24:27], v34, s[20:23], s36 offen sc1\n\t" \
  "v_add_u32_sdwa v4, s29, v123 dst_sel:DWORD dst_unused:UNUSED_PAD src0_sel:DWORD src1_sel:WORD_0\n\t" \
  "v_smfmac_f32_16x16x64_bf16 v[40:43], a[184:187], v[134:141], v162 abid:1\n\t" \
  "ds_read_b128 v[130:133], v184 offset:12288\n\t" \
  "v_add_u32 v24, s38, v187\n\t" \
  "v_smfmac_f32_16x16x64_bf16 v[44:47], a[188:191], v[134:141], v163\n\t" \
  "ds_read_b128 v[24:27], v24 offset:14336\n\t" \
  "v_add_u32_sdwa v6, s29, v123 dst_sel:DWORD dst_unused:UNUSED_PAD src0_sel:DWORD src1_sel:WORD_1\n\t" \
  "v_smfmac_f32_16x16x64_bf16 v[48:51], a[192:195], v[134:141], v163 abid:1\n\t" \
  "ds_read_b64 v[22:23], v22 offset:0\n\t" \
  "v_smfmac_f32_16x16x64_bf16 v[52:55], a[196:199], v[134:141], v164\n\t" \
  "ds_read_b64 v[8:9], v8 offset:0\n\t" \
  "v_smfmac_f32_16x16x64_bf16 v[56:59], a[200:203], v[134:141], v164 abid:1\n\t" \
  "s_add_u32 s36, s36, 4608\n\t" \
  "ds_read_b64 v[10:11], v10 offset:0\n\t" \
  "v_smfmac_f32_16x16x64_bf16 v[60:63], a[204:207], v[134:141], v165\n\t" \
  "s_waitcnt lgkmcnt(3)\n\t" \
  "buffer_store_dwordx4 v[24:27], v34, s[20:23], s36 offen sc1\n\t" \
  "v_smfmac_f32_16x16x64_bf16 v[64:67], a[208:211], v[134:141], v165 abid:1\n\t" \
  "s_waitcnt lgkmcnt(2)\n\t" \
  "ds_read_b64 v[12:13], v12 offset:0\n\t" \
  "v_xad_u32 v24, v187, 64, s38\n\t" \
  "v_smfmac_f32_16x16x64_bf16 v[56:59], v[92:95], v[16:23], v182 abid:1\n\t" \
  "s_add_u32 s38, s38, 18432\n\t" \
  "ds_read_b128 v[24:27], v24 offset:15360\n\t" \
  "v_add_u32_sdwa v16, s29, v124 dst_sel:DWORD dst_unused:UNUSED_PAD src0_sel:DWORD src1_sel:WORD_0\n\t" \
  "v_smfmac_f32_16x16x64_bf16 v[68:71], a[212:215], v[134:141], v180 abid:1\n\t" \
  "s_sub_u32 s44, s38, 129024\n\t" \
  "ds_read_b128 v[134:137], v183 offset:14336\n\t" \
  "v_add_u32_sdwa v18, s29, v124 dst_sel:DWORD dst_unused:UNUSED_PAD src0_sel:DWORD src1_sel:WORD_1\n\t" \
  "v_smfmac_f32_16x16x64_bf16 v[36:39], a[216:219], v[126:133], v166\n\t" \
  "s_min_u32 s38, s38, s44\n\t" \
  "ds_read_b128 v[138:141], v184 offset:14336\n\t" \
  "v_add_u32_sdwa v20, s29, v125 dst_sel:DWORD dst_unused:UNUSED_PAD src0_sel:DWORD src1_sel:WORD_0\n\t" \
  "v_smfmac_f32_16x16x64_bf16 v[40:43], a[220:223], v[126:133], v166 abid:1\n\t" \
  "s_add_u32 s36, s36, 4608\n\t" \
  "ds_read_b64 v[14:15], v14 offset:0\n\t" \
  "v_add_u32_sdwa v22, s29, v125 dst_sel:DWORD dst_unused:UNUSED_PAD src0_sel:DWORD src1_sel:WORD_1\n\t" \
  "v_smfmac_f32_16x16x64_bf16 v[44:47], a[224:227], v[126:133], v167\n\t" \
  "s_waitcnt lgkmcnt(3)\n\t" \
  "buffer_store_dwordx4 v[24:27], v34, s[20:23], s36 offen sc1\n\t" \
  "v_smfmac_f32_16x16x64_bf16 v[48:51], a[228:231], v[126:133], v167 abid:1\n\t" \
  "s_add_u32 s29, s29, s30\n\t" \
  "ds_read_b64 v[0:1], v0 offset:0\n\t" \
  "v_add_u32 v24, s43, v187\n\t" \
  "v_smfmac_f32_16x16x64_bf16 v[52:55], a[232:235], v[126:133], v168\n\t" \
  "s_add_u32 s35, s35, 4718592\n\t" \
  "ds_read_b128 v[24:27], v24 offset:17408\n\t" \
  "v_smfmac_f32_16x16x64_bf16 v[56:59], a[236:239], v[126:133], v168 abid:1\n\t" \
  "s_waitcnt lgkmcnt(2)\n\t" \
  "ds_read_b64 v[2:3], v2 offset:0\n\t" \
  "v_smfmac_f32_16x16x64_bf16 v[60:63], v[96:99], v[8:15], v182 abid:1\n\t" \
  "ds_read_b64 v[4:5], v4 offset:0\n\t" \
  "v_smfmac_f32_16x16x64_bf16 v[60:63], a[240:243], v[126:133], v169\n\t" \
  "ds_read_b64 v[6:7], v6 offset:0\n\t" \
  "v_smfmac_f32_16x16x64_bf16 v[64:67], a[244:247], v[126:133], v169 abid:1\n\t" \
  "s_waitcnt lgkmcnt(3)\n\t" \
  "buffer_store_dwordx4 v[24:27], v186, s[20:23], s40 offen offset:384 sc1\n\t" \
  "v_smfmac_f32_16x16x64_bf16 v[68:71], a[248:251], v[126:133], v181\n\t" \
  "s_add_u32 s43, s43, 18432\n\t" \
  "ds_read_b64 v[16:17], v16 offset:0\n\t" \
  "v_smfmac_f32_16x16x64_bf16 v[36:39], a[252:255], v[134:141], v170\n\t" \
  "s_sub_u32 s42, s43, 129024\n\t" \
  "ds_read_b64 v[18:19], v18 offset:0\n\t" \
  "v_smfmac_f32_16x16x64_bf16 v[40:43], v[188:191], v[134:141], v170 abid:1\n\t" \
  "s_waitcnt lgkmcnt(2)\n\t" \
  "ds_read_b64 v[20:21], v20 offset:0\n\t" \
  "v_smfmac_f32_16x16x64_bf16 v[64:67], v[100:103], v[0:7], v182 abid:1\n\t" \
  "s_min_u32 s43, s43, s42\n\t" \
  "ds_read_b128 v[0:3], v183 offset:16384\n\t" \
  "v_smfmac_f32_16x16x64_bf16 v[44:47], v[192:195], v[134:141], v171\n\t" \
  "s_add_u32 s40, s40, 4718592\n\t" \
  "ds_read_b128 v[4:7], v184 offset:16384\n\t" \
  "v_add_u32 v183, s30, v183\n\t" \
  "v_smfmac_f32_16x16x64_bf16 v[48:51], v[196:199], v[134:141], v171 abid:1\n\t" \
  "ds_read_b64 v[22:23], v22 offset:0\n\t" \
  "v_add_u32 v184, s30, v184\n\t" \
  "v_smfmac_f32_16x16x64_bf16 v[52:55], v[200:203], v[134:141], v172\n\t" \
  "v_smfmac_f32_16x16x64_bf16 v[56:59], v[204:207], v[134:141], v172 abid:1\n\t" \
  "v_smfmac_f32_16x16x64_bf16 v[60:63], v[208:211], v[134:141], v173\n\t" \
  "v_smfmac_f32_16x16x64_bf16 v[64:67], v[212:215], v[134:141], v173 abid:1\n\t" \
  "s_waitcnt lgkmcnt(0)\n\t" \
  "v_smfmac_f32_16x16x64_bf16 v[68:71], v[104:107], v[16:23], v182 abid:1\n\t" \
  "v_smfmac_f32_16x16x64_bf16 v[68:71], v[216:219], v[134:141], v181 abid:1\n\t" \
  "s_setpc_b64 s[66:67]\n\t" \
  ".fill (.Lvar%=+40960-.)/4, 4, 0xbf800000\n\t" \
  "s_branch .Lfin%=\n\t" \
  "s_branch .Lhe%=\n\t" \
  "s_waitcnt vmcnt(6)\n\t" \
  "s_waitcnt vmcnt(10)\n\t" \
  "s_waitcnt vmcnt(12)\n\t" \
  "s_waitcnt vmcnt(17)\n\t" \
  "s_waitcnt vmcnt(21) lgkmcnt(0)\n\t" \
  "s_barrier\n\t" \
  "v_smfmac_f32_16x16x64_bf16 v[36:39], v[220:223], v[0:7], v174\n\t" \
  "ds_read_b128 v[126:129], v183 offset:0\n\t" \
  "v_add_u32 v24, s37, v185\n\t" \
  "v_smfmac_f32_16x16x64_bf16 v[40:43], v[224:227], v[0:7], v174 abid:1\n\t" \
  "ds_read_b128 v[130:133], v184 offset:0\n\t" \
  "v_xor_b32 v25, s41, v183\n\t" \
  "v_smfmac_f32_16x16x64_bf16 v[44:47], v[228:231], v[0:7], v175\n\t" \
  "ds_read_b128 v[134:137], v183 offset:2048\n\t" \
  "v_subrev_u32 v25, s30, v25\n\t" \
  "v_smfmac_f32_16x16x64_bf16 v[48:51], v[232:235], v[0:7], v175 abid:1\n\t" \
  "s_add_u32 s30, s29, 18432\n\t" \
  "ds_read_b128 v[138:141], v184 offset:2048\n\t" \
  "v_cvt_pk_bf16_f32 v8, v36, v37\n\t" \
  "v_cvt_pk_bf16_f32 v9, v38, v39\n\t" \
  "v_smfmac_f32_16x16x64_bf16 v[52:55], v[236:239], v[0:7], v176\n\t" \
  "ds_read_b128 v[36:39], v35 offset:0\n\t" \
  "v_cvt_pk_bf16_f32 v10, v40, v41\n\t" \
  "v_cvt_pk_bf16_f32 v11, v42, v43\n\t" \
  "v_smfmac_f32_16x16x64_bf16 v[56:59], v[240:243], v[0:7], v176 abid:1\n\t" \
  "s_sub_u32 s25, s30, 129024\n\t" \
  "ds_read_b128 v[40:43], v35 offset:64\n\t" \
  "v_cvt_pk_bf16_f32 v16, v44, v45\n\t" \
  "v_cvt_pk_bf16_f32 v17, v46, v47\n\t" \
  "v_smfmac_f32_16x16x64_bf16 v[60:63], v[244:247], v[0:7], v177\n\t" \
  "ds_read_b128 v[44:47], v35 offset:128\n\t" \
  "v_cvt_pk_bf16_f32 v18, v48, v49\n\t" \
  "v_cvt_pk_bf16_f32 v19, v50, v51\n\t" \
  "v_smfmac_f32_16x16x64_bf16 v[64:67], v[248:251], v[0:7], v177 abid:1\n\t" \
  "s_min_u32 s30, s30, s25\n\t" \
  "ds_read_b128 v[48:51], v35 offset:192\n\t" \
  "v_cvt_pk_bf16_f32 v12, v52, v53\n\t" \
  "v_cvt_pk_bf16_f32 v13, v54, v55\n\t" \
  "v_smfmac_f32_16x16x64_bf16 v[68:71], v[252:255], v[0:7], v182\n\t" \
  "s_waitcnt lgkmcnt(3)\n\t" \
  "ds_read_b128 v[52:55], v35 offset:256\n\t" \
  "v_cvt_pk_bf16_f32 v14, v56, v57\n\t" \
  "v_cvt_pk_bf16_f32 v15, v58, v59\n\t" \
  "v_smfmac_f32_16x16x64_bf16 v[36:39], a[0:3], v[126:133], v142\n\t" \
  "s_waitcnt lgkmcnt(3)\n\t" \
  "ds_read_b128 v[56:59], v35 offset:320\n\t" \
  "v_cvt_pk_bf16_f32 v20, v60, v61\n\t" \
  "v_cvt_pk_bf16_f32 v21, v62, v63\n\t" \
  "v_smfmac_f32_16x16x64_bf16 v[40:43], a[4:7], v[126:133], v142 abid:1\n\t" \
  "s_waitcnt lgkmcnt(3)\n\t" \
  "ds_read_b128 v[60:63], v35 offset:384\n\t" \
  "v_cvt_pk_bf16_f32 v22, v64, v65\n\t" \
  "v_cvt_pk_bf16_f32 v23, v66, v67\n\t" \
  "v_smfmac_f32_16x16x64_bf16 v[44:47], a[8:11], v[126:133], v143\n\t" \
  "s_waitcnt lgkmcnt(3)\n\t" \
  "ds_read_b128 v[64:67], v35 offset:448\n\t" \
  "v_cvt_pk_bf16_f32 v26, v68, v69\n\t" \
  "v_cvt_pk_bf16_f32 v27, v70, v71\n\t" \
  "v_smfmac_f32_16x16x64_bf16 v[48:51], a[12:15], v[126:133], v143 abid:1\n\t" \
  "s_waitcnt lgkmcnt(3)\n\t" \
  "ds_read_b128 v[68:71], v35 offset:512\n\t" \
  "v_add_u32_sdwa v0, s29, v108 dst_sel:DWORD dst_unused:UNUSED_PAD src0_sel:DWORD src1_sel:WORD_0\n\t" \
  "v_smfmac_f32_16x16x64_bf16 v[52:55], a[16:19], v[126:133], v144\n\t" \
  "s_waitcnt lgkmcnt(3)\n\t" \
  "ds_read_b64 v[0:1], v0 offset:0\n\t" \
  "v_add_u32_sdwa v2, s29, v108 dst_sel:DWORD dst_unused:UNUSED_PAD src0_sel:DWORD src1_sel:WORD_1\n\t" \
  "v_smfmac_f32_16x16x64_bf16 v[56:59], a[20:23], v[126:133], v144 abid:1\n\t" \
  "s_waitcnt lgkmcnt(3)\n\t" \
  "ds_write_b64 v24, v[8:9] offset:0\n\t" \
  "v_add_u32_sdwa v4, s29, v109 dst_sel:DWORD dst_unused:UNUSED_PAD src0_sel:DWORD src1_sel:WORD_0\n\t" \
  "v_smfmac_f32_16x16x64_bf16 v[60:63], a[24:27], v[126:133], v145\n\t" \
  "s_waitcnt lgkmcnt(3)\n\t" \
  "ds_read_b64 v[2:3], v2 offset:0\n\t" \
  "v_add_u32_sdwa v6, s29, v109 dst_sel:DWORD dst_unused:UNUSED_PAD src0_sel:DWORD src1_sel:WORD_1\n\t" \
  "v_smfmac_f32_16x16x64_bf16 v[64:67], a[28:31], v[126:133], v145 abid:1\n\t" \
  "s_waitcnt lgkmcnt(3)\n\t" \
  "ds_write_b64 v24, v[10:11] offset:8\n\t" \
  "v_smfmac_f32_16x16x64_bf16 v[68:71], a[32:35], v[126:133], v178\n\t" \
  "s_sub_u32 s30, s30, s29\n\t" \
  "s_bfe_i32 s47, s48, 0x10000\n\t" \
  "ds_read_b128 v[126:129], v183 offset:4096\n\t" \
  "v_add_u32 v28, s38, v187\n\t" \
  "v_smfmac_f32_16x16x64_bf16 v[36:39], a[36:39], v[134:141], v146\n\t" \
  "s_mov_b32 s37, s29\n\t" \
  "s_lshr_b32 s48, s48, 1\n\t" \
  "ds_write_b64 v24, v[12:13] offset:128\n\t" \
  "ds_read_b128 v[28:31], v28 offset:12288\n\t" \
  "v_smfmac_f32_16x16x64_bf16 v[40:43], a[40:43], v[134:141], v146 abid:1\n\t" \
  "s_add_u32 s36, s35, s39\n\t" \
  "ds_read_b128 v[130:133], v184 offset:4096\n\t" \
  "v_smfmac_f32_16x16x64_bf16 v[44:47], a[44:47], v[134:141], v147\n\t" \
  "s_andn2_b32 s44, 0x40000000, s47\n\t" \
  "ds_write_b64 v24, v[14:15] offset:136\n\t" \
  "v_xor_b32 v24, 64, v24\n\t" \
  "v_smfmac_f32_16x16x64_bf16 v[48:51], a[48:51], v[134:141], v147 abid:1\n\t" \
  "s_or_b32 s36, s36, s44\n\t" \
  "ds_read_b64 v[4:5], v4 offset:0\n\t" \
  "v_add_u32_sdwa v8, s29, v110 dst_sel:DWORD dst_unused:UNUSED_PAD src0_sel:DWORD src1_sel:WORD_0\n\t" \
  "v_smfmac_f32_16x16x64_bf16 v[52:55], a[52:55], v[134:141], v148\n\t" \
  "ds_write_b64 v24, v[16:17] offset:0\n\t" \
  "s_waitcnt lgkmcnt(4)\n\t" \
  "buffer_store_dwordx4 v[28:31], v34, s[20:23], s36 offen sc1\n\t" \
  "v_add_u32_sdwa v10, s29, v110 dst_sel:DWORD dst_unused:UNUSED_PAD src0_sel:DWORD src1_sel:WORD_1\n\t" \
  "v_smfmac_f32_16x16x64_bf16 v[56:59], a[56:59], v[134:141], v148 abid:1\n\t" \
  "s_and_b32 s44, s47, 4718592\n\t" \
  "ds_read_b64 v[6:7], v6 offset:0\n\t" \
  "v_add_u32_sdwa v12, s29, v111 dst_sel:DWORD dst_unused:UNUSED_PAD src0_sel:DWORD src1_sel:WORD_0\n\t" \
  "v_xad_u32 v28, v187, 64, s38\n\t" \
  "v_smfmac_f32_16x16x64_bf16 v[60:63], a[60:63], v[134:141], v149\n\t" \
  "s_and_b32 s27, s64, 7\n\t" \
  "s_add_u32 s35, s35, s44\n\t" \
  "ds_write_b64 v24, v[18:19] offset:8\n\t" \
  "ds_read_b128 v[28:31], v28 offset:13312\n\t" \
  "v_add_u32_sdwa v14, s29, v111 dst_sel:DWORD dst_unused:UNUSED_PAD src0_sel:DWORD src1_sel:WORD_1\n\t" \
  "v_smfmac_f32_16x16x64_bf16 v[64:67], a[64:67], v[134:141], v149 abid:1\n\t" \
  "s_lshl2_add_u32 s66, s27, s68\n\t" \
  "s_add_u32 s36, s36, 4608\n\t" \
  "ds_read_b64 v[8:9], v8 offset:0\n\t" \
  "v_smfmac_f32_16x16x64_bf16 v[68:71], a[68:71], v[134:141], v178 abid:1\n\t" \
  "s_waitcnt lgkmcnt(6)\n\t" \
  "ds_write_b64 v24, v[20:21] offset:128\n\t" \
  "v_smfmac_f32_16x16x64_bf16 v[36:39], a[72:75], v[126:133], v150\n\t" \
  "s_waitcnt lgkmcnt(3)\n\t" \
  "ds_read_b128 v[134:137], v183 offset:6144\n\t" \
  "v_smfmac_f32_16x16x64_bf16 v[36:39], v[72:75], v[0:7], v182 abid:1\n\t" \
  "s_and_b32 s27, s64, 8\n\t" \
  "ds_write_b64 v24, v[22:23] offset:136\n\t" \
  "s_waitcnt lgkmcnt(4)\n\t" \
  "buffer_store_dwordx4 v[28:31], v34, s[20:23], s36 offen sc1\n\t" \
  "v_add_u32_sdwa v0, s29, v112 dst_sel:DWORD dst_unused:UNUSED_PAD src0_sel:DWORD src1_sel:WORD_0\n\t" \
  "v_smfmac_f32_16x16x64_bf16 v[40:43], a[76:79], v[126:133], v150 abid:1\n\t" \
  "s_mul_i32 s27, s27, 176\n\t" \
  "s_and_b32 s44, s47, 18432\n\t" \
  "ds_read_b128 v[138:141], v184 offset:6144\n\t" \
  "v_add_u32_sdwa v2, s29, v112 dst_sel:DWORD dst_unused:UNUSED_PAD src0_sel:DWORD src1_sel:WORD_1\n\t" \
  "v_add_u32 v28, s38, v187\n\t" \
  "v_smfmac_f32_16x16x64_bf16 v[44:47], a[80:83], v[126:133], v151\n\t" \
  "s_add_u32 s66, s66, s27\n\t" \
  "ds_write_b64 v25, v[26:27] offset:16384\n\t" \
  "ds_read_b128 v[28:31], v28 offset:14336\n\t" \
  "v_add_u32_sdwa v4, s29, v113 dst_sel:DWORD dst_unused:UNUSED_PAD src0_sel:DWORD src1_sel:WORD_0\n\t" \
  "v_smfmac_f32_16x16x64_bf16 v[48:51], a[84:87], v[126:133], v151 abid:1\n\t" \
  "s_lshr_b64 s[64:65], s[64:65], 4\n\t" \
  "s_add_u32 s36, s36, 4608\n\t" \
  "ds_read_b64 v[10:11], v10 offset:0\n\t" \
  "ds_read_b64 v[12:13], v12 offset:0\n\t" \
  "v_add_u32_sdwa v6, s29, v113 dst_sel:DWORD dst_unused:UNUSED_PAD src0_sel:DWORD src1_sel:WORD_1\n\t" \
  "v_smfmac_f32_16x16x64_bf16 v[52:55], a[88:91], v[126:133], v152\n\t" \
  "ds_read_b64 v[14:15], v14 offset:0\n\t" \
  "ds_read_b64 v[0:1], v0 offset:0\n\t" \
  "v_add_u32_sdwa v16, s29, v114 dst_sel:DWORD dst_unused:UNUSED_PAD src0_sel:DWORD src1_sel:WORD_0\n\t" \
  "v_smfmac_f32_16x16x64_bf16 v[56:59], a[92:95], v[126:133], v152 abid:1\n\t" \
  "ds_read_b64 v[2:3], v2 offset:0\n\t" \
  "v_add_u32_sdwa v18, s29, v114 dst_sel:DWORD dst_unused:UNUSED_PAD src0_sel:DWORD src1_sel:WORD_1\n\t" \
  "v_smfmac_f32_16x16x64_bf16 v[60:63], a[96:99], v[126:133], v153\n\t" \
  "ds_read_b64 v[4:5], v4 offset:0\n\t" \
  "s_waitcnt lgkmcnt(6)\n\t" \
  "buffer_store_dwordx4 v[28:31], v34, s[20:23], s36 offen sc1\n\t" \
  "v_add_u32_sdwa v20, s29, v115 dst_sel:DWORD dst_unused:UNUSED_PAD src0_sel:DWORD src1_sel:WORD_0\n\t" \
  "v_smfmac_f32_16x16x64_bf16 v[64:67], a[100:103], v[126:133], v153 abid:1\n\t" \
  "ds_read_b64 v[6:7], v6 offset:0\n\t" \
  "v_add_u32_sdwa v22, s29, v115 dst_sel:DWORD dst_unused:UNUSED_PAD src0_sel:DWORD src1_sel:WORD_1\n\t" \
  "v_xad_u32 v28, v187, 64, s38\n\t" \
  "v_smfmac_f32_16x16x64_bf16 v[68:71], a[104:107], v[126:133], v179\n\t" \
  "s_waitcnt lgkmcnt(4)\n\t" \
  "s_add_u32 s38, s38, s44\n\t" \
  "ds_read_b128 v[126:129], v183 offset:8192\n\t" \
  "ds_read_b128 v[28:31], v28 offset:15360\n\t" \
  "v_smfmac_f32_16x16x64_bf16 v[40:43], v[76:79], v[8:15], v182 abid:1\n\t" \
  "s_add_u32 s36, s36, 4608\n\t" \
  "ds_read_b128 v[130:133], v184 offset:8192\n\t" \
  "v_smfmac_f32_16x16x64_bf16 v[36:39], a[108:111], v[134:141], v154\n\t" \
  "s_sub_u32 s44, s38, 129024\n\t" \
  "ds_read_b64 v[16:17], v16 offset:0\n\t" \
  "v_smfmac_f32_16x16x64_bf16 v[40:43], a[112:115], v[134:141], v154 abid:1\n\t" \
  "s_waitcnt lgkmcnt(3)\n\t" \
  "s_min_u32 s38, s38, s44\n\t" \
  "ds_read_b64 v[18:19], v18 offset:0\n\t" \
  "v_smfmac_f32_16x16x64_bf16 v[44:47], v[80:83], v[0:7], v182 abid:1\n\t" \
  "ds_read_b64 v[20:21], v20 offset:0\n\t" \
  "s_waitcnt lgkmcnt(4)\n\t" \
  "buffer_store_dwordx4 v[28:31], v34, s[20:23], s36 offen sc1\n\t" \
  "v_smfmac_f32_16x16x64_bf16 v[44:47], a[116:119], v[134:141], v155\n\t" \
  "ds_read_b64 v[22:23], v22 offset:0\n\t" \
  "v_add_u32_sdwa v0, s29, v32 dst_sel:DWORD dst_unused:UNUSED_PAD src0_sel:DWORD src1_sel:WORD_0\n\t" \
  "v_smfmac_f32_16x16x64_bf16 v[48:51], a[120:123], v[134:141], v155 abid:1\n\t" \
  "s_branch .Ljn32%=\n\t" \
  ".fill (.Lvar%=+42364-.)/4, 4, 0xbf800000\n\t" \
  "s_nop 0\n\t" \
  "s_branch .Lfin%=\n\t" \
  "s_branch .Lhe%=\n\t" \
  "s_waitcnt vmcnt(6)\n\t" \
  "s_waitcnt vmcnt(10)\n\t" \
  "s_waitcnt vmcnt(12)\n\t" \
  "s_waitcnt vmcnt(17)\n\t" \
  "s_waitcnt vmcnt(21) lgkmcnt(0)\n\t" \
  "s_barrier\n\t" \
  "v_smfmac_f32_16x16x64_bf16 v[36:39], v[220:223], v[0:7], v174\n\t" \
  "s_add_u32 m0, s33, 0\n\t" \
  "ds_read_b128 v[126:129], v183 offset:0\n\t" \
  "v_add_u32 v24, s37, v185\n\t" \
  "v_smfmac_f32_16x16x64_bf16 v[40:43], v[224:227], v[0:7], v174 abid:1\n\t" \
  "buffer_load_dwordx4 v186, s[16:19], s34 offen offset:0 nt lds\n\t" \
  "ds_read_b128 v[130:133], v184 offset:0\n\t" \
  "v_xor_b32 v25, s41, v183\n\t" \
  "v_smfmac_f32_16x16x64_bf16 v[44:47], v[228:231], v[0:7], v175\n\t" \
  "s_add_u32 m0, s33, 1920\n\t" \
  "ds_read_b128 v[134:137], v183 offset:2048\n\t" \
  "v_subrev_u32 v25, s30, v25\n\t" \
  "v_smfmac_f32_16x16x64_bf16 v[48:51], v[232:235], v[0:7], v175 abid:1\n\t" \
  "s_add_u32 s30, s29, 18432\n\t" \
  "buffer_load_dwordx4 v186, s[16:19], s34 offen offset:128 nt lds\n\t" \
  "ds_read_b128 v[138:141], v184 offset:2048\n\t" \
  "v_cvt_pk_bf16_f32 v8, v36, v37\n\t" \
  "v_cvt_pk_bf16_f32 v9, v38, v39\n\t" \
  "v_smfmac_f32_16x16x64_bf16 v[52:55], v[236:239], v[0:7], v176\n\t" \
  "s_add_u32 m0, s33, 3840\n\t" \
  "ds_read_b128 v[36:39], v35 offset:0\n\t" \
  "v_cvt_pk_bf16_f32 v10, v40, v41\n\t" \
  "v_cvt_pk_bf16_f32 v11, v42, v43\n\t" \
  "v_smfmac_f32_16x16x64_bf16 v[56:59], v[240:243], v[0:7], v176 abid:1\n\t" \
  "s_sub_u32 s25, s30, 129024\n\t" \
  "buffer_load_dwordx4 v186, s[16:19], s34 offen offset:256 nt lds\n\t" \
  "ds_read_b128 v[40:43], v35 offset:64\n\t" \
  "v_cvt_pk_bf16_f32 v16, v44, v45\n\t" \
  "v_cvt_pk_bf16_f32 v17, v46, v47\n\t" \
  "v_smfmac_f32_16x16x64_bf16 v[60:63], v[244:247], v[0:7], v177\n\t" \
  "s_add_u32 m0, s33, 5760\n\t" \
  "ds_read_b128 v[44:47], v35 offset:128\n\t" \
  "v_cvt_pk_bf16_f32 v18, v48, v49\n\t" \
  "v_cvt_pk_bf16_f32 v19, v50, v51\n\t" \
  "v_smfmac_f32_16x16x64_bf16 v[64:67], v[248:251], v[0:7], v177 abid:1\n\t" \
  "s_min_u32 s30, s30, s25\n\t" \
  "buffer_load_dwordx4 v186, s[16:19], s34 offen offset:384 nt lds\n\t" \
  "ds_read_b128 v[48:51], v35 offset:192\n\t" \
  "v_cvt_pk_bf16_f32 v12, v52, v53\n\t" \
  "v_cvt_pk_bf16_f32 v13, v54, v55\n\t" \
  "v_smfmac_f32_16x16x64_bf16 v[68:71], v[252:255], v[0:7], v182\n\t" \
  "s_waitcnt lgkmcnt(3)\n\t" \
  "ds_read_b128 v[52:55], v35 offset:256\n\t" \
  "v_cvt_pk_bf16_f32 v14, v56, v57\n\t" \
  "v_cvt_pk_bf16_f32 v15, v58, v59\n\t" \
  "v_smfmac_f32_16x16x64_bf16 v[36:39], a[0:3], v[126:133], v142\n\t" \
  "s_waitcnt lgkmcnt(3)\n\t" \
  "ds_read_b128 v[56:59], v35 offset:320\n\t" \
  "v_cvt_pk_bf16_f32 v20, v60, v61\n\t" \
  "v_cvt_pk_bf16_f32 v21, v62, v63\n\t" \
  "v_smfmac_f32_16x16x64_bf16 v[40:43], a[4:7], v[126:133], v142 abid:1\n\t" \
  "s_waitcnt lgkmcnt(3)\n\t" \
  "ds_read_b128 v[60:63], v35 offset:384\n\t" \
  "v_cvt_pk_bf16_f32 v22, v64, v65\n\t" \
  "v_cvt_pk_bf16_f32 v23, v66, v67\n\t" \
  "v_smfmac_f32_16x16x64_bf16 v[44:47], a[8:11], v[126:133], v143\n\t" \
  "s_waitcnt lgkmcnt(3)\n\t" \
  "ds_read_b128 v[64:67], v35 offset:448\n\t" \
  "v_cvt_pk_bf16_f32 v26, v68, v69\n\t" \
  "v_cvt_pk_bf16_f32 v27, v70, v71\n\t" \
  "v_smfmac_f32_16x16x64_bf16 v[48:51], a[12:15], v[126:133], v143 abid:1\n\t" \
  "s_waitcnt lgkmcnt(3)\n\t" \
  "ds_read_b128 v[68:71], v35 offset:512\n\t" \
  "v_add_u32_sdwa v0, s29, v108 dst_sel:DWORD dst_unused:UNUSED_PAD src0_sel:DWORD src1_sel:WORD_0\n\t" \
  "v_smfmac_f32_16x16x64_bf16 v[52:55], a[16:19], v[126:133], v144\n\t" \
  "s_waitcnt lgkmcnt(3)\n\t" \
  "ds_read_b64 v[0:1], v0 offset:0\n\t" \
  "v_add_u32_sdwa v2, s29, v108 dst_sel:DWORD dst_unused:UNUSED_PAD src0_sel:DWORD src1_sel:WORD_1\n\t" \
  "v_smfmac_f32_16x16x64_bf16 v[56:59], a[20:23], v[126:133], v144 abid:1\n\t" \
  "s_waitcnt lgkmcnt(3)\n\t" \
  "ds_write_b64 v24, v[8:9] offset:0\n\t" \
  "v_add_u32_sdwa v4, s29, v109 dst_sel:DWORD dst_unused:UNUSED_PAD src0_sel:DWORD src1_sel:WORD_0\n\t" \
  "v_smfmac_f32_16x16x64_bf16 v[60:63], a[24:27], v[126:133], v145\n\t" \
  "s_waitcnt lgkmcnt(3)\n\t" \
  "ds_read_b64 v[2:3], v2 offset:0\n\t" \
  "v_add_u32_sdwa v6, s29, v109 dst_sel:DWORD dst_unused:UNUSED_PAD src0_sel:DWORD src1_sel:WORD_1\n\t" \
  "v_smfmac_f32_16x16x64_bf16 v[64:67], a[28:31], v[126:133], v145 abid:1\n\t" \
  "s_waitcnt lgkmcnt(3)\n\t" \
  "ds_write_b64 v24, v[10:11] offset:8\n\t" \
  "v_smfmac_f32_16x16x64_bf16 v[68:71], a[32:35], v[126:133], v178\n\t" \
  "s_sub_u32 s30, s30, s29\n\t" \
  "ds_read_b128 v[126:129], v183 offset:4096\n\t" \
  "v_smfmac_f32_16x16x64_bf16 v[36:39], a[36:39], v[134:141], v146\n\t" \
  "s_mov_b32 s37, s29\n\t" \
  "ds_write_b64 v24, v[12:13] offset:128\n\t" \
  "v_smfmac_f32_16x16x64_bf16 v[40:43], a[40:43], v[134:141], v146 abid:1\n\t" \
  "s_add_u32 s31, s31, 18432\n\t" \
  "ds_read_b128 v[130:133], v184 offset:4096\n\t" \
  "v_smfmac_f32_16x16x64_bf16 v[44:47], a[44:47], v[134:141], v147\n\t" \
  "s_sub_u32 s26, s31, 129024\n\t" \
  "ds_write_b64 v24, v[14:15] offset:136\n\t" \
  "v_xor_b32 v24, 64, v24\n\t" \
  "v_smfmac_f32_16x16x64_bf16 v[48:51], a[48:51], v[134:141], v147 abid:1\n\t" \
  "s_min_u32 s31, s31, s26\n\t" \
  "ds_read_b64 v[4:5], v4 offset:0\n\t" \
  "v_add_u32_sdwa v8, s29, v110 dst_sel:DWORD dst_unused:UNUSED_PAD src0_sel:DWORD src1_sel:WORD_0\n\t" \
  "v_smfmac_f32_16x16x64_bf16 v[52:55], a[52:55], v[134:141], v148\n\t" \
  "s_add_u32 s33, s31, s32\n\t" \
  "ds_write_b64 v24, v[16:17] offset:0\n\t" \
  "v_add_u32_sdwa v10, s29, v110 dst_sel:DWORD dst_unused:UNUSED_PAD src0_sel:DWORD src1_sel:WORD_1\n\t" \
  "v_smfmac_f32_16x16x64_bf16 v[56:59], a[56:59], v[134:141], v148 abid:1\n\t" \
  "s_add_u32 s34, s34, 4718592\n\t" \
  "ds_read_b64 v[6:7], v6 offset:0\n\t" \
  "v_add_u32_sdwa v12, s29, v111 dst_sel:DWORD dst_unused:UNUSED_PAD src0_sel:DWORD src1_sel:WORD_0\n\t" \
  "v_smfmac_f32_16x16x64_bf16 v[60:63], a[60:63], v[134:141], v149\n\t" \
  "s_and_b32 s27, s64, 7\n\t" \
  "ds_write_b64 v24, v[18:19] offset:8\n\t" \
  "v_add_u32_sdwa v14, s29, v111 dst_sel:DWORD dst_unused:UNUSED_PAD src0_sel:DWORD src1_sel:WORD_1\n\t" \
  "v_smfmac_f32_16x16x64_bf16 v[64:67], a[64:67], v[134:141], v149 abid:1\n\t" \
  "s_lshl2_add_u32 s66, s27, s68\n\t" \
  "ds_read_b64 v[8:9], v8 offset:0\n\t" \
  "v_smfmac_f32_16x16x64_bf16 v[68:71], a[68:71], v[134:141], v178 abid:1\n\t" \
  "s_waitcnt lgkmcnt(6)\n\t" \
  "ds_write_b64 v24, v[20:21] offset:128\n\t" \
  "v_smfmac_f32_16x16x64_bf16 v[36:39], a[72:75], v[126:133], v150\n\t" \
  "s_waitcnt lgkmcnt(3)\n\t" \
  "ds_read_b128 v[134:137], v183 offset:6144\n\t" \
  "v_smfmac_f32_16x16x64_bf16 v[36:39], v[72:75], v[0:7], v182 abid:1\n\t" \
  "s_and_b32 s27, s64, 8\n\t" \
  "ds_write_b64 v24, v[22:23] offset:136\n\t" \
  "v_add_u32_sdwa v0, s29, v112 dst_sel:DWORD dst_unused:UNUSED_PAD src0_sel:DWORD src1_sel:WORD_0\n\t" \
  "v_smfmac_f32_16x16x64_bf16 v[40:43], a[76:79], v[126:133], v150 abid:1\n\t" \
  "s_mul_i32 s27, s27, 176\n\t" \
  "ds_read_b128 v[138:141], v184 offset:6144\n\t" \
  "v_add_u32_sdwa v2, s29, v112 dst_sel:DWORD dst_unused:UNUSED_PAD src0_sel:DWORD src1_sel:WORD_1\n\t" \
  "v_smfmac_f32_16x16x64_bf16 v[44:47], a[80:83], v[126:133], v151\n\t" \
  "s_add_u32 s66, s66, s27\n\t" \
  "ds_write_b64 v25, v[26:27] offset:16384\n\t" \
  "v_add_u32_sdwa v4, s29, v113 dst_sel:DWORD dst_unused:UNUSED_PAD src0_sel:DWORD src1_sel:WORD_0\n\t" \
  "v_smfmac_f32_16x16x64_bf16 v[48:51], a[84:87], v[126:133], v151 abid:1\n\t" \
  "s_lshr_b64 s[64:65], s[64:65], 4\n\t" \
  "ds_read_b64 v[10:11], v10 offset:0\n\t" \
  "ds_read_b64 v[12:13], v12 offset:0\n\t" \
  "v_add_u32_sdwa v6, s29, v113 dst_sel:DWORD dst_unused:UNUSED_PAD src0_sel:DWORD src1_sel:WORD_1\n\t" \
  "v_smfmac_f32_16x16x64_bf16 v[52:55], a[88:91], v[126:133], v152\n\t" \
  "ds_read_b64 v[14:15], v14 offset:0\n\t" \
  "ds_read_b64 v[0:1], v0 offset:0\n\t" \
  "v_add_u32_sdwa v16, s29, v114 dst_sel:DWORD dst_unused:UNUSED_PAD src0_sel:DWORD src1_sel:WORD_0\n\t" \
  "v_smfmac_f32_16x16x64_bf16 v[56:59], a[92:95], v[126:133], v152 abid:1\n\t" \
  "ds_read_b64 v[2:3], v2 offset:0\n\t" \
  "v_add_u32_sdwa v18, s29, v114 dst_sel:DWORD dst_unused:UNUSED_PAD src0_sel:DWORD src1_sel:WORD_1\n\t" \
  "v_smfmac_f32_16x16x64_bf16 v[60:63], a[96:99], v[126:133], v153\n\t" \
  "ds_read_b64 v[4:5], v4 offset:0\n\t" \
  "v_add_u32_sdwa v20, s29, v115 dst_sel:DWORD dst_unused:UNUSED_PAD src0_sel:DWORD src1_sel:WORD_0\n\t" \
  "v_smfmac_f32_16x16x64_bf16 v[64:67], a[100:103], v[126:133], v153 abid:1\n\t" \
  "ds_read_b64 v[6:7], v6 offset:0\n\t" \
  "v_add_u32_sdwa v22, s29, v115 dst_sel:DWORD dst_unused:UNUSED_PAD src0_sel:DWORD src1_sel:WORD_1\n\t" \
  "v_smfmac_f32_16x16x64_bf16 v[68:71], a[104:107], v[126:133], v179\n\t" \
  "s_waitcnt lgkmcnt(4)\n\t" \
  "ds_read_b128 v[126:129], v183 offset:8192\n\t" \
  "v_smfmac_f32_16x16x64_bf16 v[40:43], v[76:79], v[8:15], v182 abid:1\n\t" \
  "ds_read_b128 v[130:133], v184 offset:8192\n\t" \
  "v_smfmac_f32_16x16x64_bf16 v[36:39], a[108:111], v[134:141], v154\n\t" \
  "ds_read_b64 v[16:17], v16 offset:0\n\t" \
  "v_smfmac_f32_16x16x64_bf16 v[40:43], a[112:115], v[134:141], v154 abid:1\n\t" \
  "s_waitcnt lgkmcnt(3)\n\t" \
  "ds_read_b64 v[18:19], v18 offset:0\n\t" \
  "v_smfmac_f32_16x16x64_bf16 v[44:47], v[80:83], v[0:7], v182 abid:1\n\t" \
  "ds_read_b64 v[20:21], v20 offset:0\n\t" \
  "v_smfmac_f32_16x16x64_bf16 v[44:47], a[116:119], v[134:141], v155\n\t" \
  "ds_read_b64 v[22:23], v22 offset:0\n\t" \
  "v_add_u32_sdwa v0, s29, v32 dst_sel:DWORD dst_unused:UNUSED_PAD src0_sel:DWORD src1_sel:WORD_0\n\t" \
  "v_smfmac_f32_16x16x64_bf16 v[48:51], a[120:123], v[134:141], v155 abid:1\n\t" \
  ".Ljn32%=:\n\t" \
  "ds_read_b64 v[0:1], v0 offset:0\n\t" \
  "v_add_u32_sdwa v2, s29, v32 dst_sel:DWORD dst_unused:UNUSED_PAD src0_sel:DWORD src1_sel:WORD_1\n\t" \
  "v_smfmac_f32_16x16x64_bf16 v[52:55], a[124:127], v[134:141], v156\n\t" \
  "ds_read_b64 v[2:3], v2 offset:0\n\t" \
  "v_add_u32 v28, s60, v187\n\t" \
  "v_smfmac_f32_16x16x64_bf16 v[56:59], a[128:131], v[134:141], v156 abid:1\n\t" \
  "ds_read_b128 v[28:31], v28 offset:0\n\t" \
  "v_add_u32_sdwa v8, s29, v116 dst_sel:DWORD dst_unused:UNUSED_PAD src0_sel:DWORD src1_sel:WORD_0\n\t" \
  "v_smfmac_f32_16x16x64_bf16 v[60:63], a[132:135], v[134:141], v157\n\t" \
  "s_waitcnt lgkmcnt(3)\n\t" \
  "ds_read_b64 v[8:9], v8 offset:0\n\t" \
  "v_add_u32_sdwa v10, s29, v116 dst_sel:DWORD dst_unused:UNUSED_PAD src0_sel:DWORD src1_sel:WORD_1\n\t" \
  "v_smfmac_f32_16x16x64_bf16 v[48:51], v[84:87], v[16:23], v182 abid:1\n\t" \
  "ds_read_b64 v[10:11], v10 offset:0\n\t" \
  "v_add_u32_sdwa v12, s29, v117 dst_sel:DWORD dst_unused:UNUSED_PAD src0_sel:DWORD src1_sel:WORD_0\n\t" \
  "v_smfmac_f32_16x16x64_bf16 v[64:67], a[136:139], v[134:141], v157 abid:1\n\t" \
  "ds_read_b64 v[12:13], v12 offset:0\n\t" \
  "v_add_u32_sdwa v14, s29, v117 dst_sel:DWORD dst_unused:UNUSED_PAD src0_sel:DWORD src1_sel:WORD_1\n\t" \
  "v_smfmac_f32_16x16x64_bf16 v[68:71], a[140:143], v[134:141], v179 abid:1\n\t" \
  "s_waitcnt lgkmcnt(3)\n\t" \
  "ds_read_b128 v[134:137], v183 offset:10240\n\t" \
  "v_add_u32_sdwa v16, s29, v118 dst_sel:DWORD dst_unused:UNUSED_PAD src0_sel:DWORD src1_sel:WORD_0\n\t" \
  "s_set_gpr_idx_on s6, gpr_idx(DST)\n\t" \
  "v_smfmac_f32_16x16x64_bf16 v[36:39], v[28:31], v[0:7], v182 abid:1\n\t" \
  "s_set_gpr_idx_off\n\t" \
  "ds_read_b128 v[138:141], v184 offset:10240\n\t" \
  "v_add_u32 v24, s38, v187\n\t" \
  "v_smfmac_f32_16x16x64_bf16 v[36:39], a[144:147], v[126:133], v158\n\t" \
  "ds_read_b128 v[24:27], v24 offset:12288\n\t" \
  "v_add_u32_sdwa v18, s29, v118 dst_sel:DWORD dst_unused:UNUSED_PAD src0_sel:DWORD src1_sel:WORD_1\n\t" \
  "v_smfmac_f32_16x16x64_bf16 v[40:43], a[148:151], v[126:133], v158 abid:1\n\t" \
  "ds_read_b64 v[14:15], v14 offset:0\n\t" \
  "v_add_u32_sdwa v20, s29, v119 dst_sel:DWORD dst_unused:UNUSED_PAD src0_sel:DWORD src1_sel:WORD_0\n\t" \
  "v_smfmac_f32_16x16x64_bf16 v[44:47], a[152:155], v[126:133], v159\n\t" \
  "ds_read_b64 v[16:17], v16 offset:0\n\t" \
  "v_add_u32_sdwa v22, s29, v119 dst_sel:DWORD dst_unused:UNUSED_PAD src0_sel:DWORD src1_sel:WORD_1\n\t" \
  "v_smfmac_f32_16x16x64_bf16 v[48:51], a[156:159], v[126:133], v159 abid:1\n\t" \
  "s_add_u32 s36, s35, s39\n\t" \
  "ds_read_b64 v[18:19], v18 offset:0\n\t" \
  "v_add_u32_sdwa v0, s29, v33 dst_sel:DWORD dst_unused:UNUSED_PAD src0_sel:DWORD src1_sel:WORD_0\n\t" \
  "v_smfmac_f32_16x16x64_bf16 v[52:55], a[160:163], v[126:133], v160\n\t" \
  "s_waitcnt lgkmcnt(3)\n\t" \
  "buffer_store_dwordx4 v[24:27], v34, s[20:23], s36 offen sc1\n\t" \
  "v_add_u32_sdwa v2, s29, v33 dst_sel:DWORD dst_unused:UNUSED_PAD src0_sel:DWORD src1_sel:WORD_1\n\t" \
  "v_smfmac_f32_16x16x64_bf16 v[56:59], a[164:167], v[126:133], v160 abid:1\n\t" \
  "s_waitcnt lgkmcnt(2)\n\t" \
  "ds_read_b64 v[20:21], v20 offset:0\n\t" \
  "v_xad_u32 v24, v187, 64, s38\n\t" \
  "v_smfmac_f32_16x16x64_bf16 v[52:55], v[88:91], v[8:15], v182 abid:1\n\t" \
  "ds_read_b128 v[24:27], v24 offset:13312\n\t" \
  "v_add_u32 v28, s60, v187\n\t" \
  "v_smfmac_f32_16x16x64_bf16 v[60:63], a[168:171], v[126:133], v161\n\t" \
  "ds_read_b64 v[22:23], v22 offset:0\n\t" \
  "v_add_u32_sdwa v8, s29, v120 dst_sel:DWORD dst_unused:UNUSED_PAD src0_sel:DWORD src1_sel:WORD_0\n\t" \
  "v_smfmac_f32_16x16x64_bf16 v[64:67], a[172:175], v[126:133], v161 abid:1\n\t" \
  "ds_read_b64 v[0:1], v0 offset:0\n\t" \
  "v_add_u32_sdwa v10, s29, v120 dst_sel:DWORD dst_unused:UNUSED_PAD src0_sel:DWORD src1_sel:WORD_1\n\t" \
  "v_smfmac_f32_16x16x64_bf16 v[68:71], a[176:179], v[126:133], v180\n\t" \
  "s_add_u32 s36, s36, 4608\n\t" \
  "ds_read_b128 v[126:129], v183 offset:12288\n\t" \
  "v_add_u32_sdwa v12, s29, v121 dst_sel:DWORD dst_unused:UNUSED_PAD src0_sel:DWORD src1_sel:WORD_0\n\t" \
  "v_smfmac_f32_16x16x64_bf16 v[36:39], a[180:183], v[134:141], v162\n\t" \
  "s_waitcnt lgkmcnt(3)\n\t" \
  "buffer_store_dwordx4 v[24:27], v34, s[20:23], s36 offen sc1\n\t" \
  "v_add_u32_sdwa v14, s29, v121 dst_sel:DWORD dst_unused:UNUSED_PAD src0_sel:DWORD src1_sel:WORD_1\n\t" \
  "v_smfmac_f32_16x16x64_bf16 v[40:43], a[184:187], v[134:141], v162 abid:1\n\t" \
  "s_waitcnt lgkmcnt(2)\n\t" \
  "ds_read_b128 v[130:133], v184 offset:12288\n\t" \
  "v_add_u32 v24, s38, v187\n\t" \
  "v_smfmac_f32_16x16x64_bf16 v[56:59], v[92:95], v[16:23], v182 abid:1\n\t" \
  "ds_read_b128 v[24:27], v24 offset:14336\n\t" \
  "v_add_u32_sdwa v16, s29, v122 dst_sel:DWORD dst_unused:UNUSED_PAD src0_sel:DWORD src1_sel:WORD_0\n\t" \
  "v_smfmac_f32_16x16x64_bf16 v[44:47], a[188:191], v[134:141], v163\n\t" \
  "ds_read_b64 v[2:3], v2 offset:0\n\t" \
  "v_add_u32_sdwa v18, s29, v122 dst_sel:DWORD dst_unused:UNUSED_PAD src0_sel:DWORD src1_sel:WORD_1\n\t" \
  "v_smfmac_f32_16x16x64_bf16 v[48:51], a[192:195], v[134:141], v163 abid:1\n\t" \
  "ds_read_b128 v[28:31], v28 offset:1280\n\t" \
  "v_add_u32_sdwa v20, s29, v123 dst_sel:DWORD dst_unused:UNUSED_PAD src0_sel:DWORD src1_sel:WORD_0\n\t" \
  "v_smfmac_f32_16x16x64_bf16 v[52:55], a[196:199], v[134:141], v164\n\t" \
  "s_add_u32 s36, s36, 4608\n\t" \
  "ds_read_b64 v[8:9], v8 offset:0\n\t" \
  "v_add_u32_sdwa v22, s29, v123 dst_sel:DWORD dst_unused:UNUSED_PAD src0_sel:DWORD src1_sel:WORD_1\n\t" \
  "v_smfmac_f32_16x16x64_bf16 v[56:59], a[200:203], v[134:141], v164 abid:1\n\t" \
  "s_waitcnt lgkmcnt(3)\n\t" \
  "buffer_store_dwordx4 v[24:27], v34, s[20:23], s36 offen sc1\n\t" \
  "v_smfmac_f32_16x16x64_bf16 v[60:63], a[204:207], v[134:141], v165\n\t" \
  "ds_read_b64 v[10:11], v10 offset:0\n\t" \
  "v_xad_u32 v24, v187, 64, s38\n\t" \
  "v_smfmac_f32_16x16x64_bf16 v[64:67], a[208:211], v[134:141], v165 abid:1\n\t" \
  "s_waitcnt lgkmcnt(2)\n\t" \
  "ds_read_b128 v[24:27], v24 offset:15360\n\t" \
  "s_set_gpr_idx_on s7, gpr_idx(DST)\n\t" \
  "v_smfmac_f32_16x16x64_bf16 v[36:39], v[28:31], v[0:7], v182 abid:1\n\t" \
  "s_set_gpr_idx_off\n\t" \
  "ds_read_b64 v[12:13], v12 offset:0\n\t" \
  "v_smfmac_f32_16x16x64_bf16 v[68:71], a[212:215], v[134:141], v180 abid:1\n\t" \
  "s_add_u32 s38, s38, 18432\n\t" \
  "ds_read_b128 v[134:137], v183 offset:14336\n\t" \
  "v_smfmac_f32_16x16x64_bf16 v[36:39], a[216:219], v[126:133], v166\n\t" \
  "s_add_u32 s36, s36, 4608\n\t" \
  "ds_read_b128 v[138:141], v184 offset:14336\n\t" \
  "v_smfmac_f32_16x16x64_bf16 v[40:43], a[220:223], v[126:133], v166 abid:1\n\t" \
  "s_waitcnt lgkmcnt(3)\n\t" \
  "buffer_store_dwordx4 v[24:27], v34, s[20:23], s36 offen sc1\n\t" \
  "v_smfmac_f32_16x16x64_bf16 v[44:47], a[224:227], v[126:133], v167\n\t" \
  "s_sub_u32 s44, s38, 129024\n\t" \
  "ds_read_b64 v[14:15], v14 offset:0\n\t" \
  "v_add_u32 v24, s43, v187\n\t" \
  "v_smfmac_f32_16x16x64_bf16 v[48:51], a[228:231], v[126:133], v167 abid:1\n\t" \
  "s_min_u32 s38, s38, s44\n\t" \
  "ds_read_b128 v[24:27], v24 offset:17408\n\t" \
  "v_smfmac_f32_16x16x64_bf16 v[52:55], a[232:235], v[126:133], v168\n\t" \
  "s_add_u32 s35, s35, 4718592\n\t" \
  "ds_read_b64 v[16:17], v16 offset:0\n\t" \
  "v_smfmac_f32_16x16x64_bf16 v[56:59], a[236:239], v[126:133], v168 abid:1\n\t" \
  "ds_read_b64 v[18:19], v18 offset:0\n\t" \
  "v_smfmac_f32_16x16x64_bf16 v[60:63], a[240:243], v[126:133], v169\n\t" \
  "s_waitcnt lgkmcnt(3)\n\t" \
  "ds_read_b64 v[20:21], v20 offset:0\n\t" \
  "v_smfmac_f32_16x16x64_bf16 v[60:63], v[96:99], v[8:15], v182 abid:1\n\t" \
  "s_waitcnt lgkmcnt(3)\n\t" \
  "buffer_store_dwordx4 v[24:27], v186, s[20:23], s40 offen offset:384 sc1\n\t" \
  "v_add_u32_sdwa v8, s29, v124 dst_sel:DWORD dst_unused:UNUSED_PAD src0_sel:DWORD src1_sel:WORD_0\n\t" \
  "v_smfmac_f32_16x16x64_bf16 v[64:67], a[244:247], v[126:133], v169 abid:1\n\t" \
  "s_add_u32 s43, s43, 18432\n\t" \
  "ds_read_b64 v[22:23], v22 offset:0\n\t" \
  "v_add_u32_sdwa v10, s29, v124 dst_sel:DWORD dst_unused:UNUSED_PAD src0_sel:DWORD src1_sel:WORD_1\n\t" \
  "v_smfmac_f32_16x16x64_bf16 v[68:71], a[248:251], v[126:133], v181\n\t" \
  "s_sub_u32 s42, s43, 129024\n\t" \
  "ds_read_b128 v[0:3], v183 offset:16384\n\t" \
  "v_add_u32_sdwa v12, s29, v125 dst_sel:DWORD dst_unused:UNUSED_PAD src0_sel:DWORD src1_sel:WORD_0\n\t" \
  "v_smfmac_f32_16x16x64_bf16 v[36:39], a[252:255], v[134:141], v170\n\t" \
  "s_min_u32 s43, s43, s42\n\t" \
  "ds_read_b128 v[4:7], v184 offset:16384\n\t" \
  "v_add_u32_sdwa v14, s29, v125 dst_sel:DWORD dst_unused:UNUSED_PAD src0_sel:DWORD src1_sel:WORD_1\n\t" \
  "v_smfmac_f32_16x16x64_bf16 v[40:43], v[188:191], v[134:141], v170 abid:1\n\t" \
  "s_add_u32 s29, s29, s30\n\t" \
  "ds_read_b64 v[8:9], v8 offset:0\n\t" \
  "v_add_u32 v183, s30, v183\n\t" \
  "v_smfmac_f32_16x16x64_bf16 v[44:47], v[192:195], v[134:141], v171\n\t" \
  "s_waitcnt lgkmcnt(3)\n\t" \
  "ds_read_b64 v[10:11], v10 offset:0\n\t" \
  "v_add_u32 v184, s30, v184\n\t" \
  "v_smfmac_f32_16x16x64_bf16 v[64:67], v[100:103], v[16:23], v182 abid:1\n\t" \
  "s_add_u32 s40, s40, 4718592\n\t" \
  "ds_read_b64 v[12:13], v12 offset:0\n\t" \
  "v_smfmac_f32_16x16x64_bf16 v[48:51], v[196:199], v[134:141], v171 abid:1\n\t" \
  "ds_read_b64 v[14:15], v14 offset:0\n\t" \
  "v_smfmac_f32_16x16x64_bf16 v[52:55], v[200:203], v[134:141], v172\n\t" \
  "v_smfmac_f32_16x16x64_bf16 v[56:59], v[204:207], v[134:141], v172 abid:1\n\t" \
  "v_smfmac_f32_16x16x64_bf16 v[60:63], v[208:211], v[134:141], v173\n\t" \
  "v_smfmac_f32_16x16x64_bf16 v[64:67], v[212:215], v[134:141], v173 abid:1\n\t" \
  "s_waitcnt lgkmcnt(0)\n\t" \
  "v_smfmac_f32_16x16x64_bf16 v[68:71], v[104:107], v[8:15], v182 abid:1\n\t" \
  "v_smfmac_f32_16x16x64_bf16 v[68:71], v[216:219], v[134:141], v181 abid:1\n\t" \
  "s_setpc_b64 s[66:67]\n\t" \
  ".fill (.Lvar%=+45056-.)/4, 4, 0xbf800000\n\t" \
  "s_branch .Lfin%=\n\t" \
  "s_branch .Lhe%=\n\t" \
  "s_waitcnt vmcnt(6)\n\t" \
  "s_waitcnt vmcnt(10)\n\t" \
  "s_waitcnt vmcnt(12)\n\t" \
  "s_waitcnt vmcnt(17)\n\t" \
  "s_waitcnt vmcnt(21) lgkmcnt(0)\n\t" \
  "s_barrier\n\t" \
  "v_smfmac_f32_16x16x64_bf16 v[36:39], v[220:223], v[0:7], v174\n\t" \
  "ds_read_b128 v[126:129], v183 offset:0\n\t" \
  "v_add_u32 v24, s37, v185\n\t" \
  "v_lshrrev_b32 v28, 4, v187\n\t" \
  "v_smfmac_f32_16x16x64_bf16 v[40:43], v[224:227], v[0:7], v174 abid:1\n\t" \
  "ds_read_b128 v[130:133], v184 offset:0\n\t" \
  "v_xor_b32 v25, s41, v183\n\t" \
  "v_and_or_b32 v28, v28, 48, s45\n\t" \
  "v_smfmac_f32_16x16x64_bf16 v[44:47], v[228:231], v[0:7], v175\n\t" \
  "ds_read_b128 v[134:137], v183 offset:2048\n\t" \
  "v_subrev_u32 v25, s30, v25\n\t" \
  "v_smfmac_f32_16x16x64_bf16 v[48:51], v[232:235], v[0:7], v175 abid:1\n\t" \
  "s_add_u32 s30, s29, 18432\n\t" \
  "ds_read_b128 v[138:141], v184 offset:2048\n\t" \
  "v_cvt_pk_bf16_f32 v8, v36, v37\n\t" \
  "v_cvt_pk_bf16_f32 v9, v38, v39\n\t" \
  "v_smfmac_f32_16x16x64_bf16 v[52:55], v[236:239], v[0:7], v176\n\t" \
  "ds_read_b128 v[36:39], v28 offset:0\n\t" \
  "v_cvt_pk_bf16_f32 v10, v40, v41\n\t" \
  "v_cvt_pk_bf16_f32 v11, v42, v43\n\t" \
  "v_smfmac_f32_16x16x64_bf16 v[56:59], v[240:243], v[0:7], v176 abid:1\n\t" \
  "s_sub_u32 s25, s30, 129024\n\t" \
  "ds_read_b128 v[40:43], v28 offset:64\n\t" \
  "v_cvt_pk_bf16_f32 v16, v44, v45\n\t" \
  "v_cvt_pk_bf16_f32 v17, v46, v47\n\t" \
  "v_smfmac_f32_16x16x64_bf16 v[60:63], v[244:247], v[0:7], v177\n\t" \
  "ds_read_b128 v[44:47], v28 offset:128\n\t" \
  "v_cvt_pk_bf16_f32 v18, v48, v49\n\t" \
  "v_cvt_pk_bf16_f32 v19, v50, v51\n\t" \
  "v_smfmac_f32_16x16x64_bf16 v[64:67], v[248:251], v[0:7], v177 abid:1\n\t" \
  "s_min_u32 s30, s30, s25\n\t" \
  "ds_read_b128 v[48:51], v28 offset:192\n\t" \
  "v_cvt_pk_bf16_f32 v12, v52, v53\n\t" \
  "v_cvt_pk_bf16_f32 v13, v54, v55\n\t" \
  "v_smfmac_f32_16x16x64_bf16 v[68:71], v[252:255], v[0:7], v182\n\t" \
  "s_waitcnt lgkmcnt(3)\n\t" \
  "ds_read_b128 v[52:55], v28 offset:256\n\t" \
  "v_cvt_pk_bf16_f32 v14, v56, v57\n\t" \
  "v_cvt_pk_bf16_f32 v15, v58, v59\n\t" \
  "v_smfmac_f32_16x16x64_bf16 v[36:39], a[0:3], v[126:133], v142\n\t" \
  "s_waitcnt lgkmcnt(3)\n\t" \
  "ds_read_b128 v[56:59], v28 offset:320\n\t" \
  "v_cvt_pk_bf16_f32 v20, v60, v61\n\t" \
  "v_cvt_pk_bf16_f32 v21, v62, v63\n\t" \
  "v_smfmac_f32_16x16x64_bf16 v[40:43], a[4:7], v[126:133], v142 abid:1\n\t" \
  "s_waitcnt lgkmcnt(3)\n\t" \
  "ds_read_b128 v[60:63], v28 offset:384\n\t" \
  "v_cvt_pk_bf16_f32 v22, v64, v65\n\t" \
  "v_cvt_pk_bf16_f32 v23, v66, v67\n\t" \
  "v_smfmac_f32_16x16x64_bf16 v[44:47], a[8:11], v[126:133], v143\n\t" \
  "s_waitcnt lgkmcnt(3)\n\t" \
  "ds_read_b128 v[64:67], v28 offset:448\n\t" \
  "v_cvt_pk_bf16_f32 v26, v68, v69\n\t" \
  "v_cvt_pk_bf16_f32 v27, v70, v71\n\t" \
  "v_smfmac_f32_16x16x64_bf16 v[48:51], a[12:15], v[126:133], v143 abid:1\n\t" \
  "s_waitcnt lgkmcnt(3)\n\t" \
  "ds_read_b128 v[68:71], v28 offset:512\n\t" \
  "v_add_u32_sdwa v0, s29, v108 dst_sel:DWORD dst_unused:UNUSED_PAD src0_sel:DWORD src1_sel:WORD_0\n\t" \
  "v_smfmac_f32_16x16x64_bf16 v[52:55], a[16:19], v[126:133], v144\n\t" \
  "s_waitcnt lgkmcnt(3)\n\t" \
  "ds_read_b64 v[0:1], v0 offset:0\n\t" \
  "v_add_u32_sdwa v2, s29, v108 dst_sel:DWORD dst_unused:UNUSED_PAD src0_sel:DWORD src1_sel:WORD_1\n\t" \
  "v_smfmac_f32_16x16x64_bf16 v[56:59], a[20:23], v[126:133], v144 abid:1\n\t" \
  "s_waitcnt lgkmcnt(3)\n\t" \
  "ds_write_b64 v24, v[8:9] offset:0\n\t" \
  "v_add_u32_sdwa v4, s29, v109 dst_sel:DWORD dst_unused:UNUSED_PAD src0_sel:DWORD src1_sel:WORD_0\n\t" \
  "v_smfmac_f32_16x16x64_bf16 v[60:63], a[24:27], v[126:133], v145\n\t" \
  "s_waitcnt lgkmcnt(3)\n\t" \
  "ds_read_b64 v[2:3], v2 offset:0\n\t" \
  "v_add_u32_sdwa v6, s29, v109 dst_sel:DWORD dst_unused:UNUSED_PAD src0_sel:DWORD src1_sel:WORD_1\n\t" \
  "v_smfmac_f32_16x16x64_bf16 v[64:67], a[28:31], v[126:133], v145 abid:1\n\t" \
  "s_waitcnt lgkmcnt(3)\n\t" \
  "ds_write_b64 v24, v[10:11] offset:8\n\t" \
  "v_smfmac_f32_16x16x64_bf16 v[68:71], a[32:35], v[126:133], v178\n\t" \
  "s_sub_u32 s30, s30, s29\n\t" \
  "s_bfe_i32 s47, s48, 0x10000\n\t" \
  "ds_read_b128 v[126:129], v183 offset:4096\n\t" \
  "v_add_u32 v28, s38, v187\n\t" \
  "v_smfmac_f32_16x16x64_bf16 v[36:39], a[36:39], v[134:141], v146\n\t" \
  "s_mov_b32 s37, s29\n\t" \
  "s_lshr_b32 s48, s48, 1\n\t" \
  "ds_write_b64 v24, v[12:13] offset:128\n\t" \
  "ds_read_b128 v[28:31], v28 offset:12288\n\t" \
  "v_smfmac_f32_16x16x64_bf16 v[40:43], a[40:43], v[134:141], v146 abid:1\n\t" \
  "s_add_u32 s36, s35, s39\n\t" \
  "ds_read_b128 v[130:133], v184 offset:4096\n\t" \
  "v_smfmac_f32_16x16x64_bf16 v[44:47], a[44:47], v[134:141], v147\n\t" \
  "s_andn2_b32 s44, 0x40000000, s47\n\t" \
  "ds_write_b64 v24, v[14:15] offset:136\n\t" \
  "v_xor_b32 v24, 64, v24\n\t" \
  "v_smfmac_f32_16x16x64_bf16 v[48:51], a[48:51], v[134:141], v147 abid:1\n\t" \
  "s_or_b32 s36, s36, s44\n\t" \
  "ds_read_b64 v[4:5], v4 offset:0\n\t" \
  "v_add_u32_sdwa v8, s29, v110 dst_sel:DWORD dst_unused:UNUSED_PAD src0_sel:DWORD src1_sel:WORD_0\n\t" \
  "v_smfmac_f32_16x16x64_bf16 v[52:55], a[52:55], v[134:141], v148\n\t" \
  "ds_write_b64 v24, v[16:17] offset:0\n\t" \
  "s_waitcnt lgkmcnt(4)\n\t" \
  "buffer_store_dwordx4 v[28:31], v34, s[20:23], s36 offen sc1\n\t" \
  "v_add_u32_sdwa v10, s29, v110 dst_sel:DWORD dst_unused:UNUSED_PAD src0_sel:DWORD src1_sel:WORD_1\n\t" \
  "v_smfmac_f32_16x16x64_bf16 v[56:59], a[56:59], v[134:141], v148 abid:1\n\t" \
  "s_and_b32 s44, s47, 4718592\n\t" \
  "ds_read_b64 v[6:7], v6 offset:0\n\t" \
  "v_add_u32_sdwa v12, s29, v111 dst_sel:DWORD dst_unused:UNUSED_PAD src0_sel:DWORD src1_sel:WORD_0\n\t" \
  "v_xad_u32 v28, v187, 64, s38\n\t" \
  "v_smfmac_f32_16x16x64_bf16 v[60:63], a[60:63], v[134:141], v149\n\t" \
  "s_and_b32 s27, s64, 7\n\t" \
  "s_add_u32 s35, s35, s44\n\t" \
  "ds_write_b64 v24, v[18:19] offset:8\n\t" \
  "ds_read_b128 v[28:31], v28 offset:13312\n\t" \
  "v_add_u32_sdwa v14, s29, v111 dst_sel:DWORD dst_unused:UNUSED_PAD src0_sel:DWORD src1_sel:WORD_1\n\t" \
  "v_smfmac_f32_16x16x64_bf16 v[64:67], a[64:67], v[134:141], v149 abid:1\n\t" \
  "s_lshl2_add_u32 s66, s27, s68\n\t" \
  "s_add_u32 s36, s36, 4608\n\t" \
  "ds_read_b64 v[8:9], v8 offset:0\n\t" \
  "v_smfmac_f32_16x16x64_bf16 v[68:71], a[68:71], v[134:141], v178 abid:1\n\t" \
  "s_waitcnt lgkmcnt(6)\n\t" \
  "ds_write_b64 v24, v[20:21] offset:128\n\t" \
  "v_smfmac_f32_16x16x64_bf16 v[36:39], a[72:75], v[126:133], v150\n\t" \
  "s_waitcnt lgkmcnt(3)\n\t" \
  "ds_read_b128 v[134:137], v183 offset:6144\n\t" \
  "v_smfmac_f32_16x16x64_bf16 v[36:39], v[72:75], v[0:7], v182 abid:1\n\t" \
  "s_and_b32 s27, s64, 8\n\t" \
  "ds_write_b64 v24, v[22:23] offset:136\n\t" \
  "s_waitcnt lgkmcnt(4)\n\t" \
  "buffer_store_dwordx4 v[28:31], v34, s[20:23], s36 offen sc1\n\t" \
  "v_add_u32_sdwa v0, s29, v112 dst_sel:DWORD dst_unused:UNUSED_PAD src0_sel:DWORD src1_sel:WORD_0\n\t" \
  "v_smfmac_f32_16x16x64_bf16 v[40:43], a[76:79], v[126:133], v150 abid:1\n\t" \
  "s_mul_i32 s27, s27, 176\n\t" \
  "s_and_b32 s44, s47, 18432\n\t" \
  "ds_read_b128 v[138:141], v184 offset:6144\n\t" \
  "v_add_u32_sdwa v2, s29, v112 dst_sel:DWORD dst_unused:UNUSED_PAD src0_sel:DWORD src1_sel:WORD_1\n\t" \
  "v_add_u32 v28, s38, v187\n\t" \
  "v_smfmac_f32_16x16x64_bf16 v[44:47], a[80:83], v[126:133], v151\n\t" \
  "s_add_u32 s66, s66, s27\n\t" \
  "ds_write_b64 v25, v[26:27] offset:16384\n\t" \
  "ds_read_b128 v[28:31], v28 offset:14336\n\t" \
  "v_add_u32_sdwa v4, s29, v113 dst_sel:DWORD dst_unused:UNUSED_PAD src0_sel:DWORD src1_sel:WORD_0\n\t" \
  "v_smfmac_f32_16x16x64_bf16 v[48:51], a[84:87], v[126:133], v151 abid:1\n\t" \
  "s_lshr_b64 s[64:65], s[64:65], 4\n\t" \
  "s_add_u32 s36, s36, 4608\n\t" \
  "ds_read_b64 v[10:11], v10 offset:0\n\t" \
  "ds_read_b64 v[12:13], v12 offset:0\n\t" \
  "v_add_u32_sdwa v6, s29, v113 dst_sel:DWORD dst_unused:UNUSED_PAD src0_sel:DWORD src1_sel:WORD_1\n\t" \
  "v_smfmac_f32_16x16x64_bf16 v[52:55], a[88:91], v[126:133], v152\n\t" \
  "ds_read_b64 v[14:15], v14 offset:0\n\t" \
  "ds_read_b64 v[0:1], v0 offset:0\n\t" \
  "v_add_u32_sdwa v16, s29, v114 dst_sel:DWORD dst_unused:UNUSED_PAD src0_sel:DWORD src1_sel:WORD_0\n\t" \
  "v_smfmac_f32_16x16x64_bf16 v[56:59], a[92:95], v[126:133], v152 abid:1\n\t" \
  "ds_read_b64 v[2:3], v2 offset:0\n\t" \
  "ds_read_b64 v[4:5], v4 offset:0\n\t" \
  "v_add_u32_sdwa v18, s29, v114 dst_sel:DWORD dst_unused:UNUSED_PAD src0_sel:DWORD src1_sel:WORD_1\n\t" \
  "v_smfmac_f32_16x16x64_bf16 v[60:63], a[96:99], v[126:133], v153\n\t" \
  "ds_read_b64 v[6:7], v6 offset:0\n\t" \
  "ds_read_b64 v[16:17], v16 offset:0\n\t" \
  "s_waitcnt lgkmcnt(8)\n\t" \
  "buffer_store_dwordx4 v[28:31], v34, s[20:23], s36 offen sc1\n\t" \
  "v_add_u32_sdwa v20, s29, v115 dst_sel:DWORD dst_unused:UNUSED_PAD src0_sel:DWORD src1_sel:WORD_0\n\t" \
  "v_smfmac_f32_16x16x64_bf16 v[64:67], a[100:103], v[126:133], v153 abid:1\n\t" \
  "ds_read_b64 v[18:19], v18 offset:0\n\t" \
  "v_add_u32_sdwa v22, s29, v115 dst_sel:DWORD dst_unused:UNUSED_PAD src0_sel:DWORD src1_sel:WORD_1\n\t" \
  "v_xad_u32 v28, v187, 64, s38\n\t" \
  "v_smfmac_f32_16x16x64_bf16 v[68:71], a[104:107], v[126:133], v179\n\t" \
  "s_waitcnt lgkmcnt(6)\n\t" \
  "s_add_u32 s38, s38, s44\n\t" \
  "ds_read_b128 v[126:129], v183 offset:8192\n\t" \
  "ds_read_b128 v[28:31], v28 offset:15360\n\t" \
  "v_smfmac_f32_16x16x64_bf16 v[40:43], v[76:79], v[8:15], v182 abid:1\n\t" \
  "s_add_u32 s36, s36, 4608\n\t" \
  "ds_read_b128 v[130:133], v184 offset:8192\n\t" \
  "v_smfmac_f32_16x16x64_bf16 v[36:39], a[108:111], v[134:141], v154\n\t" \
  "s_waitcnt lgkmcnt(4)\n\t" \
  "s_sub_u32 s44, s38, 129024\n\t" \
  "ds_read_b64 v[20:21], v20 offset:0\n\t" \
  "v_smfmac_f32_16x16x64_bf16 v[44:47], v[80:83], v[0:7], v182 abid:1\n\t" \
  "s_min_u32 s38, s38, s44\n\t" \
  "ds_read_b64 v[22:23], v22 offset:0\n\t" \
  "v_smfmac_f32_16x16x64_bf16 v[40:43], a[112:115], v[134:141], v154 abid:1\n\t" \
  "s_waitcnt lgkmcnt(3)\n\t" \
  "buffer_store_dwordx4 v[28:31], v34, s[20:23], s36 offen sc1\n\t" \
  "v_smfmac_f32_16x16x64_bf16 v[44:47], a[116:119], v[134:141], v155\n\t" \
  "v_add_u32_sdwa v0, s29, v32 dst_sel:DWORD dst_unused:UNUSED_PAD src0_sel:DWORD src1_sel:WORD_0\n\t" \
  "v_smfmac_f32_16x16x64_bf16 v[48:51], a[120:123], v[134:141], v155 abid:1\n\t" \
  "s_branch .Ljn33%=\n\t" \
  ".fill (.Lvar%=+46460-.)/4, 4, 0xbf800000\n\t" \
  "s_nop 0\n\t" \
  "s_branch .Lfin%=\n\t" \
  "s_branch .Lhe%=\n\t" \
  "s_waitcnt vmcnt(6)\n\t" \
  "s_waitcnt vmcnt(10)\n\t" \
  "s_waitcnt vmcnt(12)\n\t" \
  "s_waitcnt vmcnt(17)\n\t" \
  "s_waitcnt vmcnt(21) lgkmcnt(0)\n\t" \
  "s_barrier\n\t" \
  "v_smfmac_f32_16x16x64_bf16 v[36:39], v[220:223], v[0:7], v174\n\t" \
  "s_add_u32 m0, s33, 0\n\t" \
  "ds_read_b128 v[126:129], v183 offset:0\n\t" \
  "v_add_u32 v24, s37, v185\n\t" \
  "v_lshrrev_b32 v28, 4, v187\n\t" \
  "v_smfmac_f32_16x16x64_bf16 v[40:43], v[224:227], v[0:7], v174 abid:1\n\t" \
  "buffer_load_dwordx4 v186, s[16:19], s34 offen offset:0 nt lds\n\t" \
  "ds_read_b128 v[130:133], v184 offset:0\n\t" \
  "v_xor_b32 v25, s41, v183\n\t" \
  "v_and_or_b32 v28, v28, 48, s45\n\t" \
  "v_smfmac_f32_16x16x64_bf16 v[44:47], v[228:231], v[0:7], v175\n\t" \
  "s_add_u32 m0, s33, 1920\n\t" \
  "ds_read_b128 v[134:137], v183 offset:2048\n\t" \
  "v_subrev_u32 v25, s30, v25\n\t" \
  "v_smfmac_f32_16x16x64_bf16 v[48:51], v[232:235], v[0:7], v175 abid:1\n\t" \
  "s_add_u32 s30, s29, 18432\n\t" \
  "buffer_load_dwordx4 v186, s[16:19], s34 offen offset:128 nt lds\n\t" \
  "ds_read_b128 v[138:141], v184 offset:2048\n\t" \
  "v_cvt_pk_bf16_f32 v8, v36, v37\n\t" \
  "v_cvt_pk_bf16_f32 v9, v38, v39\n\t" \
  "v_smfmac_f32_16x16x64_bf16 v[52:55], v[236:239], v[0:7], v176\n\t" \
  "s_add_u32 m0, s33, 3840\n\t" \
  "ds_read_b128 v[36:39], v28 offset:0\n\t" \
  "v_cvt_pk_bf16_f32 v10, v40, v41\n\t" \
  "v_cvt_pk_bf16_f32 v11, v42, v43\n\t" \
  "v_smfmac_f32_16x16x64_bf16 v[56:59], v[240:243], v[0:7], v176 abid:1\n\t" \
  "s_sub_u32 s25, s30, 129024\n\t" \
  "buffer_load_dwordx4 v186, s[16:19], s34 offen offset:256 nt lds\n\t" \
  "ds_read_b128 v[40:43], v28 offset:64\n\t" \
  "v_cvt_pk_bf16_f32 v16, v44, v45\n\t" \
  "v_cvt_pk_bf16_f32 v17, v46, v47\n\t" \
  "v_smfmac_f32_16x16x64_bf16 v[60:63], v[244:247], v[0:7], v177\n\t" \
  "s_add_u32 m0, s33, 5760\n\t" \
  "ds_read_b128 v[44:47], v28 offset:128\n\t" \
  "v_cvt_pk_bf16_f32 v18, v48, v49\n\t" \
  "v_cvt_pk_bf16_f32 v19, v50, v51\n\t" \
  "v_smfmac_f32_16x16x64_bf16 v[64:67], v[248:251], v[0:7], v177 abid:1\n\t" \
  "s_min_u32 s30, s30, s25\n\t" \
  "buffer_load_dwordx4 v186, s[16:19], s34 offen offset:384 nt lds\n\t" \
  "ds_read_b128 v[48:51], v28 offset:192\n\t" \
  "v_cvt_pk_bf16_f32 v12, v52, v53\n\t" \
  "v_cvt_pk_bf16_f32 v13, v54, v55\n\t" \
  "v_smfmac_f32_16x16x64_bf16 v[68:71], v[252:255], v[0:7], v182\n\t" \
  "s_waitcnt lgkmcnt(3)\n\t" \
  "ds_read_b128 v[52:55], v28 offset:256\n\t" \
  "v_cvt_pk_bf16_f32 v14, v56, v57\n\t" \
  "v_cvt_pk_bf16_f32 v15, v58, v59\n\t" \
  "v_smfmac_f32_16x16x64_bf16 v[36:39], a[0:3], v[126:133], v142\n\t" \
  "s_waitcnt lgkmcnt(3)\n\t" \
  "ds_read_b128 v[56:59], v28 offset:320\n\t" \
  "v_cvt_pk_bf16_f32 v20, v60, v61\n\t" \
  "v_cvt_pk_bf16_f32 v21, v62, v63\n\t" \
  "v_smfmac_f32_16x16x64_bf16 v[40:43], a[4:7], v[126:133], v142 abid:1\n\t" \
  "s_waitcnt lgkmcnt(3)\n\t" \
  "ds_read_b128 v[60:63], v28 offset:384\n\t" \
  "v_cvt_pk_bf16_f32 v22, v64, v65\n\t" \
  "v_cvt_pk_bf16_f32 v23, v66, v67\n\t" \
  "v_smfmac_f32_16x16x64_bf16 v[44:47], a[8:11], v[126:133], v143\n\t" \
  "s_waitcnt lgkmcnt(3)\n\t" \
  "ds_read_b128 v[64:67], v28 offset:448\n\t" \
  "v_cvt_pk_bf16_f32 v26, v68, v69\n\t" \
  "v_cvt_pk_bf16_f32 v27, v70, v71\n\t" \
  "v_smfmac_f32_16x16x64_bf16 v[48:51], a[12:15], v[126:133], v143 abid:1\n\t" \
  "s_waitcnt lgkmcnt(3)\n\t" \
  "ds_read_b128 v[68:71], v28 offset:512\n\t" \
  "v_add_u32_sdwa v0, s29, v108 dst_sel:DWORD dst_unused:UNUSED_PAD src0_sel:DWORD src1_sel:WORD_0\n\t" \
  "v_smfmac_f32_16x16x64_bf16 v[52:55], a[16:19], v[126:133], v144\n\t" \
  "s_waitcnt lgkmcnt(3)\n\t" \
  "ds_read_b64 v[0:1], v0 offset:0\n\t" \
  "v_add_u32_sdwa v2, s29, v108 dst_sel:DWORD dst_unused:UNUSED_PAD src0_sel:DWORD src1_sel:WORD_1\n\t" \
  "v_smfmac_f32_16x16x64_bf16 v[56:59], a[20:23], v[126:133], v144 abid:1\n\t" \
  "s_waitcnt lgkmcnt(3)\n\t" \
  "ds_write_b64 v24, v[8:9] offset:0\n\t" \
  "v_add_u32_sdwa v4, s29, v109 dst_sel:DWORD dst_unused:UNUSED_PAD src0_sel:DWORD src1_sel:WORD_0\n\t" \
  "v_smfmac_f32_16x16x64_bf16 v[60:63], a[24:27], v[126:133], v145\n\t" \
  "s_waitcnt lgkmcnt(3)\n\t" \
  "ds_read_b64 v[2:3], v2 offset:0\n\t" \
  "v_add_u32_sdwa v6, s29, v109 dst_sel:DWORD dst_unused:UNUSED_PAD src0_sel:DWORD src1_sel:WORD_1\n\t" \
  "v_smfmac_f32_16x16x64_bf16 v[64:67], a[28:31], v[126:133], v145 abid:1\n\t" \
  "s_waitcnt lgkmcnt(3)\n\t" \
  "ds_write_b64 v24, v[10:11] offset:8\n\t" \
  "v_smfmac_f32_16x16x64_bf16 v[68:71], a[32:35], v[126:133], v178\n\t" \
  "s_sub_u32 s30, s30, s29\n\t" \
  "ds_read_b128 v[126:129], v183 offset:4096\n\t" \
  "v_smfmac_f32_16x16x64_bf16 v[36:39], a[36:39], v[134:141], v146\n\t" \
  "s_mov_b32 s37, s29\n\t" \
  "ds_write_b64 v24, v[12:13] offset:128\n\t" \
  "v_smfmac_f32_16x16x64_bf16 v[40:43], a[40:43], v[134:141], v146 abid:1\n\t" \
  "s_add_u32 s31, s31, 18432\n\t" \
  "ds_read_b128 v[130:133], v184 offset:4096\n\t" \
  "v_smfmac_f32_16x16x64_bf16 v[44:47], a[44:47], v[134:141], v147\n\t" \
  "s_sub_u32 s26, s31, 129024\n\t" \
  "ds_write_b64 v24, v[14:15] offset:136\n\t" \
  "v_xor_b32 v24, 64, v24\n\t" \
  "v_smfmac_f32_16x16x64_bf16 v[48:51], a[48:51], v[134:141], v147 abid:1\n\t" \
  "s_min_u32 s31, s31, s26\n\t" \
  "ds_read_b64 v[4:5], v4 offset:0\n\t" \
  "v_add_u32_sdwa v8, s29, v110 dst_sel:DWORD dst_unused:UNUSED_PAD src0_sel:DWORD src1_sel:WORD_0\n\t" \
  "v_smfmac_f32_16x16x64_bf16 v[52:55], a[52:55], v[134:141], v148\n\t" \
  "s_add_u32 s33, s31, s32\n\t" \
  "ds_write_b64 v24, v[16:17] offset:0\n\t" \
  "v_add_u32_sdwa v10, s29, v110 dst_sel:DWORD dst_unused:UNUSED_PAD src0_sel:DWORD src1_sel:WORD_1\n\t" \
  "v_smfmac_f32_16x16x64_bf16 v[56:59], a[56:59], v[134:141], v148 abid:1\n\t" \
  "s_add_u32 s34, s34, 4718592\n\t" \
  "ds_read_b64 v[6:7], v6 offset:0\n\t" \
  "v_add_u32_sdwa v12, s29, v111 dst_sel:DWORD dst_unused:UNUSED_PAD src0_sel:DWORD src1_sel:WORD_0\n\t" \
  "v_smfmac_f32_16x16x64_bf16 v[60:63], a[60:63], v[134:141], v149\n\t" \
  "s_and_b32 s27, s64, 7\n\t" \
  "ds_write_b64 v24, v[18:19] offset:8\n\t" \
  "v_add_u32_sdwa v14, s29, v111 dst_sel:DWORD dst_unused:UNUSED_PAD src0_sel:DWORD src1_sel:WORD_1\n\t" \
  "v_smfmac_f32_16x16x64_bf16 v[64:67], a[64:67], v[134:141], v149 abid:1\n\t" \
  "s_lshl2_add_u32 s66, s27, s68\n\t" \
  "ds_read_b64 v[8:9], v8 offset:0\n\t" \
  "v_smfmac_f32_16x16x64_bf16 v[68:71], a[68:71], v[134:141], v178 abid:1\n\t" \
  "s_waitcnt lgkmcnt(6)\n\t" \
  "ds_write_b64 v24, v[20:21] offset:128\n\t" \
  "v_smfmac_f32_16x16x64_bf16 v[36:39], a[72:75], v[126:133], v150\n\t" \
  "s_waitcnt lgkmcnt(3)\n\t" \
  "ds_read_b128 v[134:137], v183 offset:6144\n\t" \
  "v_smfmac_f32_16x16x64_bf16 v[36:39], v[72:75], v[0:7], v182 abid:1\n\t" \
  "s_and_b32 s27, s64, 8\n\t" \
  "ds_write_b64 v24, v[22:23] offset:136\n\t" \
  "v_add_u32_sdwa v0, s29, v112 dst_sel:DWORD dst_unused:UNUSED_PAD src0_sel:DWORD src1_sel:WORD_0\n\t" \
  "v_smfmac_f32_16x16x64_bf16 v[40:43], a[76:79], v[126:133], v150 abid:1\n\t" \
  "s_mul_i32 s27, s27, 176\n\t" \
  "ds_read_b128 v[138:141], v184 offset:6144\n\t" \
  "v_add_u32_sdwa v2, s29, v112 dst_sel:DWORD dst_unused:UNUSED_PAD src0_sel:DWORD src1_sel:WORD_1\n\t" \
  "v_smfmac_f32_16x16x64_bf16 v[44:47], a[80:83], v[126:133], v151\n\t" \
  "s_add_u32 s66, s66, s27\n\t" \
  "ds_write_b64 v25, v[26:27] offset:16384\n\t" \
  "v_add_u32_sdwa v4, s29, v113 dst_sel:DWORD dst_unused:UNUSED_PAD src0_sel:DWORD src1_sel:WORD_0\n\t" \
  "v_smfmac_f32_16x16x64_bf16 v[48:51], a[84:87], v[126:133], v151 abid:1\n\t" \
  "s_lshr_b64 s[64:65], s[64:65], 4\n\t" \
  "ds_read_b64 v[10:11], v10 offset:0\n\t" \
  "ds_read_b64 v[12:13], v12 offset:0\n\t" \
  "v_add_u32_sdwa v6, s29, v113 dst_sel:DWORD dst_unused:UNUSED_PAD src0_sel:DWORD src1_sel:WORD_1\n\t" \
  "v_smfmac_f32_16x16x64_bf16 v[52:55], a[88:91], v[126:133], v152\n\t" \
  "ds_read_b64 v[14:15], v14 offset:0\n\t" \
  "ds_read_b64 v[0:1], v0 offset:0\n\t" \
  "v_add_u32_sdwa v16, s29, v114 dst_sel:DWORD dst_unused:UNUSED_PAD src0_sel:DWORD src1_sel:WORD_0\n\t" \
  "v_smfmac_f32_16x16x64_bf16 v[56:59], a[92:95], v[126:133], v152 abid:1\n\t" \
  "ds_read_b64 v[2:3], v2 offset:0\n\t" \
  "ds_read_b64 v[4:5], v4 offset:0\n\t" \
  "v_add_u32_sdwa v18, s29, v114 dst_sel:DWORD dst_unused:UNUSED_PAD src0_sel:DWORD src1_sel:WORD_1\n\t" \
  "v_smfmac_f32_16x16x64_bf16 v[60:63], a[96:99], v[126:133], v153\n\t" \
  "ds_read_b64 v[6:7], v6 offset:0\n\t" \
  "ds_read_b64 v[16:17], v16 offset:0\n\t" \
  "v_add_u32_sdwa v20, s29, v115 dst_sel:DWORD dst_unused:UNUSED_PAD src0_sel:DWORD src1_sel:WORD_0\n\t" \
  "v_smfmac_f32_16x16x64_bf16 v[64:67], a[100:103], v[126:133], v153 abid:1\n\t" \
  "ds_read_b64 v[18:19], v18 offset:0\n\t" \
  "v_add_u32_sdwa v22, s29, v115 dst_sel:DWORD dst_unused:UNUSED_PAD src0_sel:DWORD src1_sel:WORD_1\n\t" \
  "v_smfmac_f32_16x16x64_bf16 v[68:71], a[104:107], v[126:133], v179\n\t" \
  "s_waitcnt lgkmcnt(6)\n\t" \
  "ds_read_b128 v[126:129], v183 offset:8192\n\t" \
  "v_smfmac_f32_16x16x64_bf16 v[40:43], v[76:79], v[8:15], v182 abid:1\n\t" \
  "ds_read_b128 v[130:133], v184 offset:8192\n\t" \
  "v_smfmac_f32_16x16x64_bf16 v[36:39], a[108:111], v[134:141], v154\n\t" \
  "s_waitcnt lgkmcnt(4)\n\t" \
  "ds_read_b64 v[20:21], v20 offset:0\n\t" \
  "v_smfmac_f32_16x16x64_bf16 v[44:47], v[80:83], v[0:7], v182 abid:1\n\t" \
  "ds_read_b64 v[22:23], v22 offset:0\n\t" \
  "v_smfmac_f32_16x16x64_bf16 v[40:43], a[112:115], v[134:141], v154 abid:1\n\t" \
  "v_smfmac_f32_16x16x64_bf16 v[44:47], a[116:119], v[134:141], v155\n\t" \
  "v_add_u32_sdwa v0, s29, v32 dst_sel:DWORD dst_unused:UNUSED_PAD src0_sel:DWORD src1_sel:WORD_0\n\t" \
  "v_smfmac_f32_16x16x64_bf16 v[48:51], a[120:123], v[134:141], v155 abid:1\n\t" \
  ".Ljn33%=:\n\t" \
  "ds_read_b64 v[0:1], v0 offset:0\n\t" \
  "v_add_u32_sdwa v2, s29, v32 dst_sel:DWORD dst_unused:UNUSED_PAD src0_sel:DWORD src1_sel:WORD_1\n\t" \
  "v_smfmac_f32_16x16x64_bf16 v[52:55], a[124:127], v[134:141], v156\n\t" \
  "s_waitcnt lgkmcnt(1)\n\t" \
  "ds_read_b64 v[2:3], v2 offset:0\n\t" \
  "v_add_u32 v28, s60, v187\n\t" \
  "v_smfmac_f32_16x16x64_bf16 v[48:51], v[84:87], v[16:23], v182 abid:1\n\t" \
  "ds_read_b128 v[28:31], v28 offset:0\n\t" \
  "v_add_u32_sdwa v8, s29, v116 dst_sel:DWORD dst_unused:UNUSED_PAD src0_sel:DWORD src1_sel:WORD_0\n\t" \
  "v_smfmac_f32_16x16x64_bf16 v[56:59], a[128:131], v[134:141], v156 abid:1\n\t" \
  "ds_read_b64 v[8:9], v8 offset:0\n\t" \
  "v_add_u32_sdwa v10, s29, v116 dst_sel:DWORD dst_unused:UNUSED_PAD src0_sel:DWORD src1_sel:WORD_1\n\t" \
  "v_smfmac_f32_16x16x64_bf16 v[60:63], a[132:135], v[134:141], v157\n\t" \
  "ds_read_b64 v[10:11], v10 offset:0\n\t" \
  "v_add_u32_sdwa v12, s29, v117 dst_sel:DWORD dst_unused:UNUSED_PAD src0_sel:DWORD src1_sel:WORD_0\n\t" \
  "v_smfmac_f32_16x16x64_bf16 v[64:67], a[136:139], v[134:141], v157 abid:1\n\t" \
  "ds_read_b64 v[12:13], v12 offset:0\n\t" \
  "v_add_u32_sdwa v14, s29, v117 dst_sel:DWORD dst_unused:UNUSED_PAD src0_sel:DWORD src1_sel:WORD_1\n\t" \
  "v_smfmac_f32_16x16x64_bf16 v[68:71], a[140:143], v[134:141], v179 abid:1\n\t" \
  "s_waitcnt lgkmcnt(3)\n\t" \
  "ds_read_b128 v[134:137], v183 offset:10240\n\t" \
  "ds_read_b64 v[14:15], v14 offset:0\n\t" \
  "v_add_u32_sdwa v16, s29, v118 dst_sel:DWORD dst_unused:UNUSED_PAD src0_sel:DWORD src1_sel:WORD_0\n\t" \
  "s_set_gpr_idx_on s6, gpr_idx(DST)\n\t" \
  "v_smfmac_f32_16x16x64_bf16 v[36:39], v[28:31], v[0:7], v182 abid:1\n\t" \
  "s_set_gpr_idx_off\n\t" \
  "ds_read_b128 v[138:141], v184 offset:10240\n\t" \
  "ds_read_b64 v[16:17], v16 offset:0\n\t" \
  "v_add_u32 v24, s38, v187\n\t" \
  "v_smfmac_f32_16x16x64_bf16 v[36:39], a[144:147], v[126:133], v158\n\t" \
  "ds_read_b128 v[24:27], v24 offset:12288\n\t" \
  "v_add_u32_sdwa v18, s29, v118 dst_sel:DWORD dst_unused:UNUSED_PAD src0_sel:DWORD src1_sel:WORD_1\n\t" \
  "v_smfmac_f32_16x16x64_bf16 v[40:43], a[148:151], v[126:133], v158 abid:1\n\t" \
  "ds_read_b64 v[18:19], v18 offset:0\n\t" \
  "v_add_u32_sdwa v20, s29, v119 dst_sel:DWORD dst_unused:UNUSED_PAD src0_sel:DWORD src1_sel:WORD_0\n\t" \
  "v_smfmac_f32_16x16x64_bf16 v[44:47], a[152:155], v[126:133], v159\n\t" \
  "s_waitcnt lgkmcnt(4)\n\t" \
  "ds_read_b64 v[20:21], v20 offset:0\n\t" \
  "v_add_u32_sdwa v22, s29, v119 dst_sel:DWORD dst_unused:UNUSED_PAD src0_sel:DWORD src1_sel:WORD_1\n\t" \
  "v_smfmac_f32_16x16x64_bf16 v[52:55], v[88:91], v[8:15], v182 abid:1\n\t" \
  "s_add_u32 s36, s35, s39\n\t" \
  "ds_read_b64 v[22:23], v22 offset:0\n\t" \
  "v_add_u32_sdwa v0, s29, v33 dst_sel:DWORD dst_unused:UNUSED_PAD src0_sel:DWORD src1_sel:WORD_0\n\t" \
  "v_smfmac_f32_16x16x64_bf16 v[48:51], a[156:159], v[126:133], v159 abid:1\n\t" \
  "s_waitcnt lgkmcnt(3)\n\t" \
  "buffer_store_dwordx4 v[24:27], v34, s[20:23], s36 offen sc1\n\t" \
  "v_add_u32_sdwa v2, s29, v33 dst_sel:DWORD dst_unused:UNUSED_PAD src0_sel:DWORD src1_sel:WORD_1\n\t" \
  "v_smfmac_f32_16x16x64_bf16 v[52:55], a[160:163], v[126:133], v160\n\t" \
  "ds_read_b64 v[0:1], v0 offset:0\n\t" \
  "v_xad_u32 v24, v187, 64, s38\n\t" \
  "v_smfmac_f32_16x16x64_bf16 v[56:59], a[164:167], v[126:133], v160 abid:1\n\t" \
  "ds_read_b128 v[24:27], v24 offset:13312\n\t" \
  "v_add_u32 v28, s60, v187\n\t" \
  "v_smfmac_f32_16x16x64_bf16 v[60:63], a[168:171], v[126:133], v161\n\t" \
  "s_waitcnt lgkmcnt(2)\n\t" \
  "ds_read_b64 v[2:3], v2 offset:0\n\t" \
  "v_add_u32_sdwa v8, s29, v120 dst_sel:DWORD dst_unused:UNUSED_PAD src0_sel:DWORD src1_sel:WORD_0\n\t" \
  "v_smfmac_f32_16x16x64_bf16 v[56:59], v[92:95], v[16:23], v182 abid:1\n\t" \
  "ds_read_b128 v[28:31], v28 offset:1280\n\t" \
  "v_add_u32_sdwa v10, s29, v120 dst_sel:DWORD dst_unused:UNUSED_PAD src0_sel:DWORD src1_sel:WORD_1\n\t" \
  "v_smfmac_f32_16x16x64_bf16 v[64:67], a[172:175], v[126:133], v161 abid:1\n\t" \
  "s_add_u32 s36, s36, 4608\n\t" \
  "ds_read_b64 v[8:9], v8 offset:0\n\t" \
  "v_add_u32_sdwa v12, s29, v121 dst_sel:DWORD dst_unused:UNUSED_PAD src0_sel:DWORD src1_sel:WORD_0\n\t" \
  "v_smfmac_f32_16x16x64_bf16 v[68:71], a[176:179], v[126:133], v180\n\t" \
  "s_waitcnt lgkmcnt(3)\n\t" \
  "buffer_store_dwordx4 v[24:27], v34, s[20:23], s36 offen sc1\n\t" \
  "v_add_u32_sdwa v14, s29, v121 dst_sel:DWORD dst_unused:UNUSED_PAD src0_sel:DWORD src1_sel:WORD_1\n\t" \
  "v_smfmac_f32_16x16x64_bf16 v[36:39], a[180:183], v[134:141], v162\n\t" \
  "ds_read_b128 v[126:129], v183 offset:12288\n\t" \
  "v_add_u32 v24, s38, v187\n\t" \
  "v_smfmac_f32_16x16x64_bf16 v[40:43], a[184:187], v[134:141], v162 abid:1\n\t" \
  "s_waitcnt lgkmcnt(2)\n\t" \
  "ds_read_b128 v[24:27], v24 offset:14336\n\t" \
  "v_add_u32_sdwa v16, s29, v122 dst_sel:DWORD dst_unused:UNUSED_PAD src0_sel:DWORD src1_sel:WORD_0\n\t" \
  "s_set_gpr_idx_on s7, gpr_idx(DST)\n\t" \
  "v_smfmac_f32_16x16x64_bf16 v[36:39], v[28:31], v[0:7], v182 abid:1\n\t" \
  "s_set_gpr_idx_off\n\t" \
  "ds_read_b128 v[130:133], v184 offset:12288\n\t" \
  "v_add_u32_sdwa v18, s29, v122 dst_sel:DWORD dst_unused:UNUSED_PAD src0_sel:DWORD src1_sel:WORD_1\n\t" \
  "v_smfmac_f32_16x16x64_bf16 v[44:47], a[188:191], v[134:141], v163\n\t" \
  "ds_read_b64 v[10:11], v10 offset:0\n\t" \
  "v_add_u32_sdwa v20, s29, v123 dst_sel:DWORD dst_unused:UNUSED_PAD src0_sel:DWORD src1_sel:WORD_0\n\t" \
  "v_smfmac_f32_16x16x64_bf16 v[48:51], a[192:195], v[134:141], v163 abid:1\n\t" \
  "s_add_u32 s36, s36, 4608\n\t" \
  "ds_read_b64 v[12:13], v12 offset:0\n\t" \
  "v_add_u32_sdwa v22, s29, v123 dst_sel:DWORD dst_unused:UNUSED_PAD src0_sel:DWORD src1_sel:WORD_1\n\t" \
  "v_smfmac_f32_16x16x64_bf16 v[52:55], a[196:199], v[134:141], v164\n\t" \
  "s_waitcnt lgkmcnt(3)\n\t" \
  "buffer_store_dwordx4 v[24:27], v34, s[20:23], s36 offen sc1\n\t" \
  "v_smfmac_f32_16x16x64_bf16 v[56:59], a[200:203], v[134:141], v164 abid:1\n\t" \
  "ds_read_b64 v[14:15], v14 offset:0\n\t" \
  "v_xad_u32 v24, v187, 64, s38\n\t" \
  "v_smfmac_f32_16x16x64_bf16 v[60:63], a[204:207], v[134:141], v165\n\t" \
  "s_add_u32 s38, s38, 18432\n\t" \
  "ds_read_b128 v[24:27], v24 offset:15360\n\t" \
  "v_smfmac_f32_16x16x64_bf16 v[64:67], a[208:211], v[134:141], v165 abid:1\n\t" \
  "s_sub_u32 s44, s38, 129024\n\t" \
  "ds_read_b64 v[16:17], v16 offset:0\n\t" \
  "v_smfmac_f32_16x16x64_bf16 v[68:71], a[212:215], v[134:141], v180 abid:1\n\t" \
  "s_waitcnt lgkmcnt(5)\n\t" \
  "ds_read_b128 v[134:137], v183 offset:14336\n\t" \
  "v_smfmac_f32_16x16x64_bf16 v[36:39], a[216:219], v[126:133], v166\n\t" \
  "s_add_u32 s36, s36, 4608\n\t" \
  "s_waitcnt lgkmcnt(3)\n\t" \
  "ds_read_b128 v[138:141], v184 offset:14336\n\t" \
  "v_smfmac_f32_16x16x64_bf16 v[60:63], v[96:99], v[8:15], v182 abid:1\n\t" \
  "s_waitcnt lgkmcnt(3)\n\t" \
  "buffer_store_dwordx4 v[24:27], v34, s[20:23], s36 offen sc1\n\t" \
  "v_add_u32_sdwa v8, s29, v35 dst_sel:DWORD dst_unused:UNUSED_PAD src0_sel:DWORD src1_sel:WORD_0\n\t" \
  "v_smfmac_f32_16x16x64_bf16 v[40:43], a[220:223], v[126:133], v166 abid:1\n\t" \
  "s_min_u32 s38, s38, s44\n\t" \
  "ds_read_b64 v[18:19], v18 offset:0\n\t" \
  "v_add_u32 v24, s43, v187\n\t" \
  "v_smfmac_f32_16x16x64_bf16 v[44:47], a[224:227], v[126:133], v167\n\t" \
  "s_add_u32 s35, s35, 4718592\n\t" \
  "ds_read_b128 v[24:27], v24 offset:17408\n\t" \
  "v_add_u32_sdwa v10, s29, v35 dst_sel:DWORD dst_unused:UNUSED_PAD src0_sel:DWORD src1_sel:WORD_1\n\t" \
  "v_smfmac_f32_16x16x64_bf16 v[48:51], a[228:231], v[126:133], v167 abid:1\n\t" \
  "ds_read_b64 v[20:21], v20 offset:0\n\t" \
  "v_add_u32 v28, s60, v187\n\t" \
  "v_smfmac_f32_16x16x64_bf16 v[52:55], a[232:235], v[126:133], v168\n\t" \
  "ds_read_b64 v[22:23], v22 offset:0\n\t" \
  "v_smfmac_f32_16x16x64_bf16 v[56:59], a[236:239], v[126:133], v168 abid:1\n\t" \
  "ds_read_b64 v[8:9], v8 offset:0\n\t" \
  "v_smfmac_f32_16x16x64_bf16 v[60:63], a[240:243], v[126:133], v169\n\t" \
  "s_waitcnt lgkmcnt(3)\n\t" \
  "buffer_store_dwordx4 v[24:27], v186, s[20:23], s40 offen offset:384 sc1\n\t" \
  "v_smfmac_f32_16x16x64_bf16 v[64:67], a[244:247], v[126:133], v169 abid:1\n\t" \
  "s_add_u32 s43, s43, 18432\n\t" \
  "ds_read_b64 v[10:11], v10 offset:0\n\t" \
  "v_smfmac_f32_16x16x64_bf16 v[68:71], a[248:251], v[126:133], v181\n\t" \
  "s_waitcnt lgkmcnt(2)\n\t" \
  "ds_read_b128 v[0:3], v183 offset:16384\n\t" \
  "v_smfmac_f32_16x16x64_bf16 v[64:67], v[100:103], v[16:23], v182 abid:1\n\t" \
  "s_sub_u32 s42, s43, 129024\n\t" \
  "ds_read_b128 v[4:7], v184 offset:16384\n\t" \
  "v_add_u32_sdwa v16, s29, v124 dst_sel:DWORD dst_unused:UNUSED_PAD src0_sel:DWORD src1_sel:WORD_0\n\t" \
  "v_smfmac_f32_16x16x64_bf16 v[36:39], a[252:255], v[134:141], v170\n\t" \
  "s_min_u32 s43, s43, s42\n\t" \
  "ds_read_b128 v[28:31], v28 offset:2560\n\t" \
  "v_add_u32_sdwa v18, s29, v124 dst_sel:DWORD dst_unused:UNUSED_PAD src0_sel:DWORD src1_sel:WORD_1\n\t" \
  "v_smfmac_f32_16x16x64_bf16 v[40:43], v[188:191], v[134:141], v170 abid:1\n\t" \
  "s_add_u32 s40, s40, 4718592\n\t" \
  "ds_read_b64 v[16:17], v16 offset:0\n\t" \
  "v_add_u32_sdwa v20, s29, v125 dst_sel:DWORD dst_unused:UNUSED_PAD src0_sel:DWORD src1_sel:WORD_0\n\t" \
  "v_smfmac_f32_16x16x64_bf16 v[44:47], v[192:195], v[134:141], v171\n\t" \
  "ds_read_b64 v[18:19], v18 offset:0\n\t" \
  "v_add_u32_sdwa v22, s29, v125 dst_sel:DWORD dst_unused:UNUSED_PAD src0_sel:DWORD src1_sel:WORD_1\n\t" \
  "v_smfmac_f32_16x16x64_bf16 v[48:51], v[196:199], v[134:141], v171 abid:1\n\t" \
  "s_add_u32 s29, s29, s30\n\t" \
  "ds_read_b64 v[20:21], v20 offset:0\n\t" \
  "v_add_u32 v183, s30, v183\n\t" \
  "v_smfmac_f32_16x16x64_bf16 v[52:55], v[200:203], v[134:141], v172\n\t" \
  "s_waitcnt lgkmcnt(3)\n\t" \
  "ds_read_b64 v[22:23], v22 offset:0\n\t" \
  "v_add_u32 v184, s30, v184\n\t" \
  "s_set_gpr_idx_on s8, gpr_idx(DST)\n\t" \
  "v_smfmac_f32_16x16x64_bf16 v[36:39], v[28:31], v[8:15], v182 abid:1\n\t" \
  "s_set_gpr_idx_off\n\t" \
  "v_smfmac_f32_16x16x64_bf16 v[56:59], v[204:207], v[134:141], v172 abid:1\n\t" \
  "v_smfmac_f32_16x16x64_bf16 v[60:63], v[208:211], v[134:141], v173\n\t" \
  "v_smfmac_f32_16x16x64_bf16 v[64:67], v[212:215], v[134:141], v173 abid:1\n\t" \
  "s_waitcnt lgkmcnt(0)\n\t" \
  "v_smfmac_f32_16x16x64_bf16 v[68:71], v[104:107], v[16:23], v182 abid:1\n\t" \
  "v_smfmac_f32_16x16x64_bf16 v[68:71], v[216:219], v[134:141], v181 abid:1\n\t" \
  "s_setpc_b64 s[66:67]\n\t" \
  ".Lstop%=:\n\t" \
  ""
#define SPMM_CLOBBERS "memory", "scc", "vcc", \
  "v1", "v2", "v3", "v4", "v5", "v6", "v7", "v8", "v9", "v10", "v11", "v12", "v13", "v14", "v15", "v16", \
  "v17", "v18", "v19", "v20", "v21", "v22", "v23", "v24", "v25", "v26", "v27", "v28", "v29", "v30", "v31", "v32", \
  "v33", "v34", "v35", "v36", "v37", "v38", "v39", "v40", "v41", "v42", "v43", "v44", "v45", "v46", "v47", "v48", \
  "v49", "v50", "v51", "v52", "v53", "v54", "v55", "v56", "v57", "v58", "v59", "v60", "v61", "v62", "v63", "v64", \
  "v65", "v66", "v67", "v68", "v69", "v70", "v71", "v72", "v73", "v74", "v75", "v76", "v77", "v78", "v79", "v80", \
  "v81", "v82", "v83", "v84", "v85", "v86", "v87", "v88", "v89", "v90", "v91", "v92", "v93", "v94", "v95", "v96", \
  "v97", "v98", "v99", "v100", "v101", "v102", "v103", "v104", "v105", "v106", "v107", "v108", "v109", "v110", "v111", "v112", \
  "v113", "v114", "v115", "v116", "v117", "v118", "v119", "v120", "v121", "v122", "v123", "v124", "v125", "v126", "v127", "v128", \
  "v129", "v130", "v131", "v132", "v133", "v134", "v135", "v136", "v137", "v138", "v139", "v140", "v141", "v142", "v143", "v144", \
  "v145", "v146", "v147", "v148", "v149", "v150", "v151", "v152", "v153", "v154", "v155", "v156", "v157", "v158", "v159", "v160", \
  "v161", "v162", "v163", "v164", "v165", "v166", "v167", "v168", "v169", "v170", "v171", "v172", "v173", "v174", "v175", "v176", \
  "v177", "v178", "v179", "v180", "v181", "v182", "v183", "v184", "v185", "v186", "v187", "v188", "v189", "v190", "v191", "v192", \
  "v193", "v194", "v195", "v196", "v197", "v198", "v199", "v200", "v201", "v202", "v203", "v204", "v205", "v206", "v207", "v208", \
  "v209", "v210", "v211", "v212", "v213", "v214", "v215", "v216", "v217", "v218", "v219", "v220", "v221", "v222", "v223", "v224", \
  "v225", "v226", "v227", "v228", "v229", "v230", "v231", "v232", "v233", "v234", "v235", "v236", "v237", "v238", "v239", "v240", \
  "v241", "v242", "v243", "v244", "v245", "v246", "v247", "v248", "v249", "v250", "v251", "v252", "v253", "v254", "v255", "a0", \
  "a1", "a2", "a3", "a4", "a5", "a6", "a7", "a8", "a9", "a10", "a11", "a12", "a13", "a14", "a15", "a16", \
  "a17", "a18", "a19", "a20", "a21", "a22", "a23", "a24", "a25", "a26", "a27", "a28", "a29", "a30", "a31", "a32", \
  "a33", "a34", "a35", "a36", "a37", "a38", "a39", "a40", "a41", "a42", "a43", "a44", "a45", "a46", "a47", "a48", \
  "a49", "a50", "a51", "a52", "a53", "a54", "a55", "a56", "a57", "a58", "a59", "a60", "a61", "a62", "a63", "a64", \
  "a65", "a66", "a67", "a68", "a69", "a70", "a71", "a72", "a73", "a74", "a75", "a76", "a77", "a78", "a79", "a80", \
  "a81", "a82", "a83", "a84", "a85", "a86", "a87", "a88", "a89", "a90", "a91", "a92", "a93", "a94", "a95", "a96", \
  "a97", "a98", "a99", "a100", "a101", "a102", "a103", "a104", "a105", "a106", "a107", "a108", "a109", "a110", "a111", "a112", \
  "a113", "a114", "a115", "a116", "a117", "a118", "a119", "a120", "a121", "a122", "a123", "a124", "a125", "a126", "a127", "a128", \
  "a129", "a130", "a131", "a132", "a133", "a134", "a135", "a136", "a137", "a138", "a139", "a140", "a141", "a142", "a143", "a144", \
  "a145", "a146", "a147", "a148", "a149", "a150", "a151", "a152", "a153", "a154", "a155", "a156", "a157", "a158", "a159", "a160", \
  "a161", "a162", "a163", "a164", "a165", "a166", "a167", "a168", "a169", "a170", "a171", "a172", "a173", "a174", "a175", "a176", \
  "a177", "a178", "a179", "a180", "a181", "a182", "a183", "a184", "a185", "a186", "a187", "a188", "a189", "a190", "a191", "a192", \
  "a193", "a194", "a195", "a196", "a197", "a198", "a199", "a200", "a201", "a202", "a203", "a204", "a205", "a206", "a207", "a208", \
  "a209", "a210", "a211", "a212", "a213", "a214", "a215", "a216", "a217", "a218", "a219", "a220", "a221", "a222", "a223", "a224", \
  "a225", "a226", "a227", "a228", "a229", "a230", "a231", "a232", "a233", "a234", "a235", "a236", "a237", "a238", "a239", "a240", \
  "a241", "a242", "a243", "a244", "a245", "a246", "a247", "a248", "a249", "a250", "a251", "a252", "a253", "a254", "a255", "s4", \
  "s5", "s6", "s7", "s8", "s9", "s10", "s11", "s12", "s13", "s14", "s15", "s16", "s17", "s18", "s19", "s20", \
  "s21", "s22", "s23", "s24", "s25", "s26", "s27", "s28", "s29", "s30", "s31", "s32", "s33", "s34", "s35", "s36", \
  "s37", "s38", "s39", "s40", "s41", "s42", "s43", "s44", "s45", "s46", "s47", "s48", "s49", "s50", "s51", "s52", \
  "s53", "s54", "s55", "s56", "s57", "s58", "s59", "s60", "s61", "s62", "s63", "s64", "s65", "s66", "s67", "s68", \
  "s69", "s70", "s71", "s72", "s73", "s74", "s75", "s76", "s77", "s78", "s79", "s80", "s81", "s82", "s83", "s84", \
  "s85", "s86", "s87", "s88", "s89", "s90", "s91", "s92", "s93", "s94", "s95", "s96", "s97", "s98", "s99", "s100", \
  "s101"
#define SPMM_LDS 163840

__global__ __launch_bounds__(256, 1) void spmm_main(const uint16_t* x, const uint16_t* w, const uint16_t* b, uint16_t* out, char* ws) {
  __shared__ __attribute__((aligned(128))) char smem[SPMM_LDS];
  int tid = threadIdx.x;
  asm volatile(SPMM_ASM
               : "+{v0}"(tid)
               : [sKA] "s"(__builtin_amdgcn_kernarg_segment_ptr()), [sBID] "s"(blockIdx.x),
                 [sL0] "s"((unsigned)(size_t)(__attribute__((address_space(3))) char*)smem)
               : SPMM_CLOBBERS);
}

}

extern "C" void kernel_launch(void* const* d_in, const int* in_sizes, int n_in, void* d_out, int out_size,
                              void* d_ws, size_t ws_size, hipStream_t stream) {
  static_assert(SPMM_LDS == 160 * 1024, "LDS budget");
  spmm_main<<<256, 256, 0, stream>>>((const uint16_t*)d_in[0], (const uint16_t*)d_in[1], (const uint16_t*)d_in[2], (uint16_t*)d_out, (char*)d_ws);
}
```
